```python
import torch

def _softmax(x, axis=-1):
    return torch.softmax(x, dim=axis)
def _gelu(x):
    return torch.nn.functional.gelu(x, approximate="tanh")
def _silu(x):
    return torch.nn.functional.silu(x)
def _sigmoid(x):
    return torch.sigmoid(x)
def _layernorm(x, axis=-1, eps=1e-5):
    m = x.mean(dim=axis, keepdim=True)
    v = x.var(dim=axis, keepdim=True, unbiased=False)
    return (x - m) / torch.sqrt(v + eps)
def _scatter_add(tgt, idx, src, axis=0):
    return tgt.clone().index_add_(axis, idx, src)
def _segment_sum(src, seg, n):
    out = torch.zeros((n,) + src.shape[1:], dtype=src.dtype)
    return out.index_add_(0, seg, src)
def _pad(x, pw):
    flat = tuple(v for pair in reversed(pw) for v in pair)
    return torch.nn.functional.pad(x, flat)

kernel_name = 'standalone__dcn_v2_cross__recsys'

def setup_inputs():

    _w = (lambda a: (lambda t: t.to(torch.bfloat16) if t.is_floating_point() else t)(torch.as_tensor(a)))
    B, F, D, L = 16384, 100, 64, 3
    Din = min(F * D, 1024)
    return {
        "x0": _w((lambda *s: torch.randn(*s, dtype=torch.bfloat16) * 0.05)(B, Din)),
        "ws": _w((lambda *s: torch.randn(*s, dtype=torch.bfloat16) * 0.05)(L, Din, Din)),
        "bs": _w((lambda *s: torch.randn(*s, dtype=torch.bfloat16) * 0.05)(L, Din)),
    }

def reference(x0, ws, bs):
    x = x0
    for l in range(ws.shape[0]):
        x = x0 * (x @ ws[l] + bs[l]) + x
    return x
```

```cpp
#include <hip/hip_runtime.h>
#include <hip/hip_bf16.h>
typedef unsigned short u16;
typedef short bf16x8 __attribute__((ext_vector_type(8)));
typedef float f32x4 __attribute__((ext_vector_type(4)));
typedef unsigned u32x4 __attribute__((ext_vector_type(4)));
typedef unsigned u32x2 __attribute__((ext_vector_type(2)));
typedef __bf16 bf2 __attribute__((ext_vector_type(2)));
typedef float f32x2 __attribute__((ext_vector_type(2)));
constexpr int B_ = 16384, D_ = 1024, L_ = 3;

__global__ void k_tile(const u16* __restrict__ W, u16* __restrict__ Wt) {
  int gid = blockIdx.x * blockDim.x + threadIdx.x;
  int lane = gid & 63, f = gid >> 6;
  int ks = f & 31, nt = (f >> 5) & 63, l = f >> 11;
  int n = nt * 16 + (lane & 15), k0 = ks * 32 + (lane >> 4) * 8;
  const u16* src = W + (size_t)l * D_ * D_ + (size_t)k0 * D_ + n;
  u16 v[8];
#pragma unroll
  for (int j = 0; j < 8; ++j) v[j] = src[(size_t)j * D_];
  u32x4 o;
#pragma unroll
  for (int j = 0; j < 4; ++j) o[j] = (unsigned)v[2 * j] | ((unsigned)v[2 * j + 1] << 16);
  *(u32x4*)(Wt + (size_t)gid * 8) = o;
}
__device__ __forceinline__ float bf2f(unsigned short h) { return __uint_as_float((unsigned)h << 16); }
__device__ __forceinline__ unsigned pk(float a, float b) { f32x2 v = {a, b}; bf2 r = __builtin_convertvector(v, bf2); return __builtin_bit_cast(unsigned, r); }

__global__ __launch_bounds__(512, 2) void k_main(const u16* __restrict__ x0, const u16* __restrict__ Wt, const u16* __restrict__ bs, u16* __restrict__ out) {
  extern __shared__ __attribute__((aligned(16))) char lds[];
  const int tid = threadIdx.x, lane = tid & 63, wid = __builtin_amdgcn_readfirstlane(tid >> 6);
  const int row0 = blockIdx.x * 64;
  for (int i = tid; i < 64 * 128; i += 512) {
    int r = i >> 7, c = i & 127;
    u32x4 v = *(const u32x4*)(x0 + (size_t)(row0 + r) * D_ + c * 8);
    *(u32x4*)(lds + r * 2048 + ((c ^ (r & 15)) << 4)) = v;
  }
  __syncthreads();
  f32x4 acc[4][8];
  const int fr = lane & 15, fq = lane >> 4;
#pragma unroll
  for (int m = 0; m < 4; ++m)
#pragma unroll
    for (int n = 0; n < 8; ++n) acc[m][n] = (f32x4){1.f, 1.f, 1.f, 1.f};
  for (int l = 0; l < L_; ++l) {
    const bf16x8* wp = (const bf16x8*)Wt + ((size_t)(l * 64 + wid * 8) * 32) * 64 + lane;
    for (int ks = 0; ks < 32; ++ks) {
      bf16x8 a[4], b[8];
#pragma unroll
      for (int n = 0; n < 8; ++n) b[n] = wp[(n * 32 + ks) * 64];
#pragma unroll
      for (int m = 0; m < 4; ++m) { int r = m * 16 + fr; a[m] = *(const bf16x8*)(lds + r * 2048 + (((ks * 4 + fq) ^ (r & 15)) << 4)); }
#pragma unroll
      for (int n = 0; n < 8; ++n)
#pragma unroll
        for (int m = 0; m < 4; ++m) acc[m][n] = __builtin_amdgcn_mfma_f32_16x16x32_bf16(b[n], a[m], acc[m][n], 0, 0, 0);
    }
    __syncthreads();
#pragma unroll
    for (int n = 0; n < 8; ++n) {
      int col = wid * 128 + n * 16 + fq * 4;
      u32x2 bb = *(const u32x2*)(bs + l * D_ + col);
      f32x4 bv = {__uint_as_float(bb[0] << 16), __uint_as_float(bb[0] & 0xffff0000u), __uint_as_float(bb[1] << 16), __uint_as_float(bb[1] & 0xffff0000u)};
#pragma unroll
      for (int m = 0; m < 4; ++m) {
        acc[m][n] += bv;
        u32x2 xx = *(const u32x2*)(x0 + (size_t)(row0 + m * 16 + fr) * D_ + col);
        float y0 = __uint_as_float(xx[0] << 16) * acc[m][n][0], y1 = __uint_as_float(xx[0] & 0xffff0000u) * acc[m][n][1];
        float y2 = __uint_as_float(xx[1] << 16) * acc[m][n][2], y3 = __uint_as_float(xx[1] & 0xffff0000u) * acc[m][n][3];
        u32x2 o = {pk(y0, y1), pk(y2, y3)};
        int r = m * 16 + fr;
        if (l < L_ - 1) *(u32x2*)(lds + r * 2048 + (((col >> 3) ^ (r & 15)) << 4) + (col & 7) * 2) = o;
        else *(u32x2*)(out + (size_t)(row0 + r) * D_ + col) = o;
      }
      asm volatile("" ::: "memory");
    }
    __syncthreads();
  }
}

extern "C" void kernel_launch(void* const* d_in, const int* in_sizes, int n_in, void* d_out, int out_size, void* d_ws, size_t ws_size, hipStream_t stream) {
  const u16* x0 = (const u16*)d_in[0]; const u16* W = (const u16*)d_in[1]; const u16* bs = (const u16*)d_in[2];
  u16* Wt = (u16*)d_ws;
  hipFuncSetAttribute((const void*)k_main, hipFuncAttributeMaxDynamicSharedMemorySize, 131072);
  k_tile<<<L_ * 64 * 32 * 64 / 256, 256, 0, stream>>>(W, Wt);
  k_main<<<B_ / 64, 512, 131072, stream>>>(x0, Wt, bs, (u16*)d_out);
}
```

```cpp
#include <hip/hip_runtime.h>
typedef unsigned long long u64;
typedef unsigned u32x4 __attribute__((ext_vector_type(4)));

__global__ __launch_bounds__(256, 1) __attribute__((aligned(4096))) void k_dcn(const void* x0, const void* w, const void* b, void* out, void* ws) {
  __shared__ __attribute__((aligned(16))) char lds[163840];
  const unsigned wid = __builtin_amdgcn_readfirstlane(threadIdx.x >> 6);
  const u64 ka = (u64)__builtin_amdgcn_kernarg_segment_ptr();
  asm volatile("" :: "s"((unsigned)(size_t)(__attribute__((address_space(3))) char*)lds));
  asm volatile(
"s_load_dwordx8 s[4:11], %0, 0\n"
"s_load_dwordx2 s[12:13], %0, 32\n"
".p2align 6\n"
"s_getpc_b64 s[60:61]\n"
".Lpc_%=:\n"
"v_mbcnt_lo_u32_b32 v249, -1, 0\n"
"v_mbcnt_hi_u32_b32 v249, -1, v249\n"
"s_bfe_u32 s49, %2, 0x10003\n"
"v_lshrrev_b32 v16, 3, v249\n"
"v_xor_b32 v17, v16, v249\n"
"v_and_b32 v17, 7, v17\n"
"v_lshlrev_b32 v18, 1, v16\n"
"v_and_b32 v18, 4, v18\n"
"v_xor_b32 v18, v18, v249\n"
"v_and_b32 v18, 7, v18\n"
"v_lshlrev_b32 v18, 4, v18\n"
"v_lshl_add_u32 v251, v16, 11, v18\n"
"s_lshl_b32 s72, s49, 2\n"
"s_add_u32 s72, s72, %1\n"
"v_lshlrev_b32 v17, 4, v17\n"
"v_lshl_add_u32 v248, v16, 11, v17\n"
"s_lshl_b32 s46, s72, 8\n"
"s_lshl_b32 s55, %1, 15\n"
"s_and_b32 s60, s60, 0xfffff000\n"
"v_min_u32 v16, 22, v249\n"
"v_lshlrev_b32 v16, 12, v16\n"
"global_load_dword a53, v16, s[60:61]\n"
"s_waitcnt lgkmcnt(0)\n"
"s_add_u32 s58, s6, s46\n"
"s_addc_u32 s59, s7, 0\n"
"s_movk_i32 s66, 15360\n"
"s_add_u32 m0, s55, 0\n"
"v_add_u32 v238, 131072, v251\n"
"global_load_lds_dwordx4 v238, s[58:59] offset:0\n"
"s_add_u32 s18, s8, s46\n"
"s_addc_u32 s19, s9, 0\n"
"v_lshrrev_b32 v20, 4, v249\n"
"v_lshlrev_b32 v237, 4, v20\n"
"s_lshr_b32 s62, %2, 4\n"
"s_lshl_b32 s62, s62, 3\n"
"s_and_b32 s46, %2, 7\n"
"s_or_b32 s62, s62, s46\n"
"v_add_u32 v238, s66, v238\n"
"global_load_lds_dwordx4 v238, s[58:59] offset:1024\n"
"s_xor_b32 s50, s49, 1\n"
"s_lshl_b32 s42, %1, 12\n"
"s_lshl_b32 s46, s62, 18\n"
"s_lshl_b32 s47, %1, 16\n"
"s_add_u32 s46, s46, s47\n"
"s_add_u32 s20, s4, s46\n"
"s_addc_u32 s21, s5, 0\n"
"s_lshr_b32 s52, s62, 2\n"
"v_add_u32 v238, s66, v238\n"
"global_load_lds_dwordx4 v238, s[58:59] offset:2048\n"
"s_lshl_b32 s53, s50, 2\n"
"s_and_b32 s46, s62, 3\n"
"s_or_b32 s53, s53, s46\n"
"s_lshl_b32 s46, s52, 16\n"
"s_lshl_b32 s47, %1, 14\n"
"s_add_u32 s46, s46, s47\n"
"s_lshl_b32 s47, s53, 8\n"
"v_add_u32 v238, s66, v238\n"
"global_load_lds_dwordx4 v238, s[58:59] offset:3072\n"
"s_add_u32 s46, s46, s47\n"
"s_add_u32 s40, s6, s46\n"
"s_addc_u32 s41, s7, 0\n"
"v_and_b32 v234, 15, v249\n"
"v_lshlrev_b32 v234, 4, v234\n"
"v_lshl_add_u32 v234, v20, 11, v234\n"
"s_lshl_b32 s57, %1, 11\n"
"s_mul_i32 s73, s49, -98304\n"
"s_add_u32 m0, s55, 4096\n"
"v_add_u32 v238, 196608, v251\n"
"global_load_lds_dwordx4 v238, s[58:59] offset:0\n"
"s_add_u32 s73, s73, 147456\n"
"v_and_b32 v228, 31, v249\n"
"v_lshlrev_b32 v228, 6, v228\n"
"v_mov_b32 v250, 0\n"
"s_lshl_b32 s46, s72, 11\n"
"s_add_u32 s46, s46, 6291456\n"
"s_add_u32 s36, s12, s46\n"
"s_addc_u32 s37, s13, 0\n"
"v_add_u32 v238, s66, v238\n"
"global_load_lds_dwordx4 v238, s[58:59] offset:1024\n"
"s_xor_b32 s46, %2, 8\n"
"s_lshl_b32 s46, s46, 2\n"
"s_add_u32 s46, s46, %1\n"
"s_lshl_b32 s46, s46, 6\n"
"s_add_u32 s46, s46, 6356992\n"
"s_add_u32 s34, s12, s46\n"
"s_addc_u32 s35, s13, 0\n"
"s_lshr_b32 s46, %1, 1\n"
"v_add_u32 v238, s66, v238\n"
"global_load_lds_dwordx4 v238, s[58:59] offset:2048\n"
"s_lshl_b32 s46, s46, 15\n"
"s_and_b32 s56, %1, 1\n"
"s_lshl_b32 s56, s56, 13\n"
"s_add_u32 s56, s56, s46\n"
"s_add_u32 s56, s56, 81920\n"
"v_and_b32 v16, 7, v249\n"
"v_xor_b32 v18, v16, v20\n"
"v_add_u32 v238, s66, v238\n"
"global_load_lds_dwordx4 v238, s[58:59] offset:3072\n"
"v_lshlrev_b32 v18, 4, v18\n"
"v_lshl_add_u32 v18, v16, 7, v18\n"
"v_bfe_u32 v19, v249, 3, 1\n"
"v_lshl_add_u32 v240, v19, 10, v18\n"
"v_xor_b32 v241, 64, v240\n"
"v_bfe_u32 v16, v249, 2, 2\n"
"v_lshl_add_u32 v22, v20, 3, v16\n"
"s_mul_i32 s45, s50, -114688\n"
"s_add_u32 s45, s45, 131072\n"
"s_add_u32 s45, s45, s42\n"
"s_add_u32 m0, s45, 0\n"
"v_add_u32 v238, 128, v248\n"
"global_load_lds_dwordx4 v238, s[20:21] offset:0\n"
"v_and_b32 v23, 3, v249\n"
"v_lshlrev_b32 v21, 1, v16\n"
"v_and_b32 v21, 4, v21\n"
"v_or_b32 v21, v21, v23\n"
"v_lshlrev_b32 v21, 4, v21\n"
"v_lshl_add_u32 v21, v22, 7, v21\n"
"v_add_u32 v232, s55, v21\n"
"v_add_u32 v238, s66, v238\n"
"global_load_lds_dwordx4 v238, s[20:21] offset:1024\n"
"v_add_u32 v233, s56, v21\n"
"v_xor_b32 v254, 64, v232\n"
"v_xor_b32 v255, 64, v233\n"
"v_lshlrev_b32 v23, 4, v23\n"
"v_lshl_add_u32 v22, v22, 8, v23\n"
"s_lshl_b32 s47, %1, 6\n"
"s_add_u32 s47, s47, s73\n"
"v_add_u32 v238, s66, v238\n"
"global_load_lds_dwordx4 v238, s[20:21] offset:2048\n"
"v_add_u32 v224, s47, v22\n"
"s_sub_u32 s43, 98304, s73\n"
"s_add_u32 s73, s73, s57\n"
"s_mov_b32 s67, 0xffff0000\n"
"v_lshlrev_b32 v246, 4, v249\n"
"s_lshr_b32 s46, %2, 3\n"
"s_lshl_b32 s46, s46, 2\n"
"v_add_u32 v238, s66, v238\n"
"global_load_lds_dwordx4 v238, s[20:21] offset:3072\n"
"s_add_u32 s46, s46, %1\n"
"s_bfe_u32 s47, s46, 0x10005\n"
"s_lshl_b32 s68, s47, 20\n"
"s_lshr_b32 s47, s46, 6\n"
"s_lshl_b32 s47, s47, 18\n"
"s_add_u32 s68, s68, s47\n"
"s_and_b32 s46, s46, 31\n"
"global_load_dwordx4 v[192:195], v237, s[18:19] offset:0\n"
"global_load_dwordx4 v[196:199], v237, s[18:19] offset:64\n"
"global_load_dwordx4 v[200:203], v237, s[18:19] offset:128\n"
"global_load_dwordx4 v[204:207], v237, s[18:19] offset:192\n"
"s_lshr_b32 s45, %2, 3\n"
"s_lshl_b32 s45, s45, 2\n"
"s_add_u32 s45, s45, %1\n"
"s_min_u32 s45, s45, (.Lfin_%=-.Lpc_%=)/1024-1\n"
"s_lshl_b32 s45, s45, 10\n"
"v_lshl_add_u32 v25, v249, 4, s45\n"
"global_load_dwordx4 a[48:51], v25, s[60:61]\n"
"s_lshl_b32 s46, s46, 12\n"
"s_add_u32 s68, s68, s46\n"
"s_add_u32 s68, s68, 131072\n"
"v_lshl_add_u32 v24, v228, 1, s68\n"
"global_load_dword a54, v24, s[6:7]\n"
"s_bfe_u32 s46, s52, 0x20002\n"
"s_bitcmp1_b32 s52, 1\n"
"s_cselect_b32 s47, 0, 4\n"
"s_add_u32 s46, s46, s47\n"
"s_lshr_b32 s47, s52, 4\n"
"s_lshl_b32 s68, s46, 1\n"
"s_add_u32 s68, s68, s47\n"
"s_xor_b32 s47, s47, s50\n"
"s_lshl_b32 s69, s47, 3\n"
"s_add_u32 s69, s69, s46\n"
"s_lshr_b32 s71, %1, 1\n"
"s_lshl_b32 s71, s71, 4\n"
"s_and_b32 s46, %1, 1\n"
"s_lshl_b32 s46, s46, 1\n"
"s_add_u32 s71, s71, s46\n"
"s_xor_b32 s70, s71, 16\n"
"s_mul_i32 s63, s53, 768\n"
"s_and_b32 s47, s52, 1\n"
"s_add_u32 s63, s63, s47\n"
"s_lshl_b32 s63, s63, 10\n"
"s_and_b32 s46, s68, 3\n"
"s_lshl_b32 s46, s46, 2\n"
"s_add_u32 s46, s46, s70\n"
"s_add_u32 s46, s46, 96\n"
"s_lshl_b32 s47, s68, 2\n"
"s_add_u32 s47, s47, %1\n"
"s_lshl_b32 s47, s47, 1\n"
"s_cmp_ge_u32 s68, 12\n"
"s_cselect_b32 s68, s46, s47\n"
"s_lshl_b32 s68, s68, 11\n"
"s_add_u32 s68, s68, s63\n"
"v_lshl_add_u32 v225, v249, 4, s68\n"
"s_mul_i32 s48, s50, -114688\n"
"s_add_u32 s48, s48, 131072\n"
"v_add_u32 v242, s48, v240\n"
"v_add_u32 v243, s48, v241\n"
"s_mov_b64 exec, 0\n"
"s_cmp_eq_u32 %1, 0\n"
"s_cbranch_scc1 .Lpg1_%=\n"
"s_cmp_eq_u32 %1, 1\n"
"s_cbranch_scc1 .Lpg2_%=\n"
"s_cmp_eq_u32 %1, 2\n"
"s_cbranch_scc1 .Lpg3_%=\n"
"s_cmp_eq_u32 %1, 3\n"
"s_cbranch_scc1 .Lpg4_%=\n"
".Lreta_%=:\n"
"s_mov_b64 exec, -1\n"
"s_waitcnt vmcnt(10)\n"
"ds_read_b64_tr_b16 a[160:161], v232 offset:0\n"
"ds_read_b64_tr_b16 a[162:163], v232 offset:512\n"
"s_add_u32 m0, s55, 8192\n"
"v_add_u32 v238, 131200, v251\n"
"global_load_lds_dwordx4 v238, s[58:59] offset:0\n"
"ds_read_b64_tr_b16 a[164:165], v232 offset:4096\n"
"ds_read_b64_tr_b16 a[166:167], v232 offset:4608\n"
"v_add_u32 v238, s66, v238\n"
"global_load_lds_dwordx4 v238, s[58:59] offset:1024\n"
"ds_read_b64_tr_b16 a[168:169], v232 offset:8\n"
"ds_read_b64_tr_b16 a[170:171], v232 offset:520\n"
"v_add_u32 v238, s66, v238\n"
"global_load_lds_dwordx4 v238, s[58:59] offset:2048\n"
"ds_read_b64_tr_b16 a[172:173], v232 offset:4104\n"
"ds_read_b64_tr_b16 a[174:175], v232 offset:4616\n"
"v_add_u32 v238, s66, v238\n"
"global_load_lds_dwordx4 v238, s[58:59] offset:3072\n"
"ds_read_b64_tr_b16 a[176:177], v254 offset:0\n"
"ds_read_b64_tr_b16 a[178:179], v254 offset:512\n"
"s_add_u32 m0, s55, 12288\n"
"v_add_u32 v238, 196736, v251\n"
"global_load_lds_dwordx4 v238, s[58:59] offset:0\n"
"ds_read_b64_tr_b16 a[180:181], v254 offset:4096\n"
"ds_read_b64_tr_b16 a[182:183], v254 offset:4608\n"
"v_add_u32 v238, s66, v238\n"
"global_load_lds_dwordx4 v238, s[58:59] offset:1024\n"
"ds_read_b64_tr_b16 a[184:185], v254 offset:8\n"
"ds_read_b64_tr_b16 a[186:187], v254 offset:520\n"
"v_add_u32 v238, s66, v238\n"
"global_load_lds_dwordx4 v238, s[58:59] offset:2048\n"
"ds_read_b64_tr_b16 a[188:189], v254 offset:4104\n"
"ds_read_b64_tr_b16 a[190:191], v254 offset:4616\n"
"v_add_u32 v238, s66, v238\n"
"global_load_lds_dwordx4 v238, s[58:59] offset:3072\n"
"s_waitcnt vmcnt(14)\n"
"s_barrier\n"
"ds_read_b128 a[96:99], v242 offset:0\n"
"ds_read_b128 a[100:103], v243 offset:0\n"
"ds_read_b128 a[104:107], v242 offset:2048\n"
"ds_read_b128 a[108:111], v243 offset:2048\n"
"ds_read_b128 a[112:115], v242 offset:4096\n"
"ds_read_b128 a[116:119], v243 offset:4096\n"
"ds_read_b128 a[120:123], v242 offset:6144\n"
"ds_read_b128 a[124:127], v243 offset:6144\n"
"ds_read_b128 a[128:131], v242 offset:8192\n"
"ds_read_b128 a[132:135], v243 offset:8192\n"
"ds_read_b128 a[136:139], v242 offset:10240\n"
"ds_read_b128 a[140:143], v243 offset:10240\n"
"ds_read_b128 a[144:147], v242 offset:12288\n"
"ds_read_b128 a[148:151], v243 offset:12288\n"
"ds_read_b128 a[152:155], v242 offset:14336\n"
"ds_read_b128 a[156:159], v243 offset:14336\n"
"s_mul_i32 s45, s49, -114688\n"
"s_add_u32 s45, s45, 131072\n"
"s_add_u32 s45, s45, s42\n"
"s_waitcnt vmcnt(13)\n"
"v_lshlrev_b32 v28, 16, v192\n"
"v_add_f32 v28, 1.0, v28\n"
"v_and_b32 v29, s67, v192\n"
"v_add_f32 v29, 1.0, v29\n"
"v_lshlrev_b32 v30, 16, v193\n"
"v_add_f32 v30, 1.0, v30\n"
"v_and_b32 v31, s67, v193\n"
"v_add_f32 v31, 1.0, v31\n"
"v_lshlrev_b32 v60, 16, v194\n"
"v_add_f32 v60, 1.0, v60\n"
"v_and_b32 v61, s67, v194\n"
"v_add_f32 v61, 1.0, v61\n"
"v_lshlrev_b32 v62, 16, v195\n"
"v_add_f32 v62, 1.0, v62\n"
"v_and_b32 v63, s67, v195\n"
"v_add_f32 v63, 1.0, v63\n"
"s_waitcnt lgkmcnt(14)\n"
"v_mfma_f32_16x16x32_bf16 v[0:3], a[160:163], a[96:99], v[28:31]\n"
"s_add_u32 m0, s45, 0\n"
"v_add_u32 v238, 1152, v248\n"
"v_mfma_f32_16x16x32_bf16 v[0:3], a[164:167], a[100:103], v[0:3]\n"
"global_load_lds_dwordx4 v238, s[20:21] offset:0\n"
"s_waitcnt lgkmcnt(12)\n"
"v_mfma_f32_16x16x32_bf16 v[4:7], a[160:163], a[104:107], v[28:31]\n"
"v_add_u32 v238, s66, v238\n"
"global_load_lds_dwordx4 v238, s[20:21] offset:1024\n"
"v_mfma_f32_16x16x32_bf16 v[4:7], a[164:167], a[108:111], v[4:7]\n"
"v_add_u32 v238, s66, v238\n"
"s_waitcnt lgkmcnt(10)\n"
"v_mfma_f32_16x16x32_bf16 v[8:11], a[160:163], a[112:115], v[28:31]\n"
"global_load_lds_dwordx4 v238, s[20:21] offset:2048\n"
"v_mfma_f32_16x16x32_bf16 v[8:11], a[164:167], a[116:119], v[8:11]\n"
"v_add_u32 v238, s66, v238\n"
"s_waitcnt lgkmcnt(8)\n"
"v_mfma_f32_16x16x32_bf16 v[12:15], a[160:163], a[120:123], v[28:31]\n"
"global_load_lds_dwordx4 v238, s[20:21] offset:3072\n"
"v_mfma_f32_16x16x32_bf16 v[12:15], a[164:167], a[124:127], v[12:15]\n"
"s_waitcnt lgkmcnt(6)\n"
"v_mfma_f32_16x16x32_bf16 v[16:19], a[160:163], a[128:131], v[28:31]\n"
"s_waitcnt vmcnt(16)\n"
"v_lshlrev_b32 v92, 16, v196\n"
"v_mfma_f32_16x16x32_bf16 v[16:19], a[164:167], a[132:135], v[16:19]\n"
"v_add_f32 v92, 1.0, v92\n"
"s_waitcnt lgkmcnt(4)\n"
"v_mfma_f32_16x16x32_bf16 v[20:23], a[160:163], a[136:139], v[28:31]\n"
"v_and_b32 v93, s67, v196\n"
"v_mfma_f32_16x16x32_bf16 v[20:23], a[164:167], a[140:143], v[20:23]\n"
"v_add_f32 v93, 1.0, v93\n"
"s_waitcnt lgkmcnt(2)\n"
"v_mfma_f32_16x16x32_bf16 v[24:27], a[160:163], a[144:147], v[28:31]\n"
"v_lshlrev_b32 v94, 16, v197\n"
"v_mfma_f32_16x16x32_bf16 v[24:27], a[164:167], a[148:151], v[24:27]\n"
"v_add_f32 v94, 1.0, v94\n"
"s_waitcnt lgkmcnt(0)\n"
"v_mfma_f32_16x16x32_bf16 v[28:31], a[160:163], a[152:155], v[28:31]\n"
"v_and_b32 v95, s67, v197\n"
"v_mfma_f32_16x16x32_bf16 v[28:31], a[164:167], a[156:159], v[28:31]\n"
"v_add_f32 v95, 1.0, v95\n"
"v_mfma_f32_16x16x32_bf16 v[32:35], a[168:171], a[96:99], v[60:63]\n"
"s_add_u32 m0, s56, 0\n"
"v_add_u32 v238, 1179648, v251\n"
"v_mfma_f32_16x16x32_bf16 v[32:35], a[172:175], a[100:103], v[32:35]\n"
"global_load_lds_dwordx4 v238, s[58:59] offset:0\n"
"v_mfma_f32_16x16x32_bf16 v[36:39], a[168:171], a[104:107], v[60:63]\n"
"v_add_u32 v238, s66, v238\n"
"global_load_lds_dwordx4 v238, s[58:59] offset:1024\n"
"v_mfma_f32_16x16x32_bf16 v[36:39], a[172:175], a[108:111], v[36:39]\n"
"v_add_u32 v238, s66, v238\n"
"global_load_lds_dwordx4 v238, s[58:59] offset:2048\n"
"v_mfma_f32_16x16x32_bf16 v[40:43], a[168:171], a[112:115], v[60:63]\n"
"v_add_u32 v238, s66, v238\n"
"global_load_lds_dwordx4 v238, s[58:59] offset:3072\n"
"v_mfma_f32_16x16x32_bf16 v[40:43], a[172:175], a[116:119], v[40:43]\n"
"s_add_u32 m0, s56, 4096\n"
"v_add_u32 v238, 1245184, v251\n"
"v_mfma_f32_16x16x32_bf16 v[44:47], a[168:171], a[120:123], v[60:63]\n"
"global_load_lds_dwordx4 v238, s[58:59] offset:0\n"
"v_bfe_u32 v236, v249, 2, 2\n"
"v_mfma_f32_16x16x32_bf16 v[44:47], a[172:175], a[124:127], v[44:47]\n"
"v_add_u32 v238, s66, v238\n"
"global_load_lds_dwordx4 v238, s[58:59] offset:1024\n"
"v_mfma_f32_16x16x32_bf16 v[48:51], a[168:171], a[128:131], v[60:63]\n"
"v_lshlrev_b32 v124, 16, v198\n"
"v_add_u32 v238, s66, v238\n"
"v_mfma_f32_16x16x32_bf16 v[48:51], a[172:175], a[132:135], v[48:51]\n"
"v_add_f32 v124, 1.0, v124\n"
"global_load_lds_dwordx4 v238, s[58:59] offset:2048\n"
"v_mfma_f32_16x16x32_bf16 v[52:55], a[168:171], a[136:139], v[60:63]\n"
"v_and_b32 v125, s67, v198\n"
"v_add_u32 v238, s66, v238\n"
"v_mfma_f32_16x16x32_bf16 v[52:55], a[172:175], a[140:143], v[52:55]\n"
"v_add_f32 v125, 1.0, v125\n"
"global_load_lds_dwordx4 v238, s[58:59] offset:3072\n"
"v_mfma_f32_16x16x32_bf16 v[56:59], a[168:171], a[144:147], v[60:63]\n"
"v_lshlrev_b32 v126, 16, v199\n"
"v_lshrrev_b32 v237, 4, v249\n"
"v_mfma_f32_16x16x32_bf16 v[56:59], a[172:175], a[148:151], v[56:59]\n"
"v_add_f32 v126, 1.0, v126\n"
"v_and_b32 v239, 1, v249\n"
"v_mfma_f32_16x16x32_bf16 v[60:63], a[168:171], a[152:155], v[60:63]\n"
"v_and_b32 v127, s67, v199\n"
"v_lshlrev_b32 v239, 4, v239\n"
"v_mfma_f32_16x16x32_bf16 v[60:63], a[172:175], a[156:159], v[60:63]\n"
"v_add_f32 v127, 1.0, v127\n"
"v_mfma_f32_16x16x32_bf16 v[64:67], a[176:179], a[96:99], v[92:95]\n"
"s_movk_i32 s47, 0x3f80\n"
"v_lshlrev_b32_e64 v239, v239, s47\n"
"v_mfma_f32_16x16x32_bf16 v[64:67], a[180:183], a[100:103], v[64:67]\n"
"v_xor_b32 v236, v236, v237\n"
"v_add_u32 v236, -1, v236\n"
"v_mfma_f32_16x16x32_bf16 v[68:71], a[176:179], a[104:107], v[92:95]\n"
"v_ashrrev_i32 v236, 31, v236\n"
"v_and_b32 v239, v236, v239\n"
"v_mfma_f32_16x16x32_bf16 v[68:71], a[180:183], a[108:111], v[68:71]\n"
"v_and_b32 v236, 2, v249\n"
"v_add_u32 v236, -1, v236\n"
"v_mfma_f32_16x16x32_bf16 v[72:75], a[176:179], a[112:115], v[92:95]\n"
"global_load_dwordx2 v[230:231], v228, s[36:37] sc1\n"
"v_ashrrev_i32 v236, 31, v236\n"
"v_mfma_f32_16x16x32_bf16 v[72:75], a[180:183], a[116:119], v[72:75]\n"
"global_load_dword v229, v250, s[34:35] sc1\n"
"v_and_b32 v252, v236, v239\n"
"v_mfma_f32_16x16x32_bf16 v[76:79], a[176:179], a[120:123], v[92:95]\n"
"v_xor_b32 v253, v252, v239\n"
"s_lshl_b32 s46, %2, 2\n"
"v_mfma_f32_16x16x32_bf16 v[76:79], a[180:183], a[124:127], v[76:79]\n"
"s_add_u32 m0, s73, 0\n"
"v_add_u32 v238, 0, v234\n"
"v_mfma_f32_16x16x32_bf16 v[80:83], a[176:179], a[128:131], v[92:95]\n"
"s_waitcnt vmcnt(25)\n"
"v_lshlrev_b32 v156, 16, v200\n"
"global_load_lds_dwordx4 v238, s[40:41] offset:0\n"
"v_mfma_f32_16x16x32_bf16 v[80:83], a[180:183], a[132:135], v[80:83]\n"
"v_add_f32 v156, 1.0, v156\n"
"s_add_u32 s46, s46, %1\n"
"v_mfma_f32_16x16x32_bf16 v[84:87], a[176:179], a[136:139], v[92:95]\n"
"v_and_b32 v157, s67, v200\n"
"s_waitcnt vmcnt(15)\n"
"ds_read_b64_tr_b16 a[192:193], v232 offset:8192\n"
"v_mfma_f32_16x16x32_bf16 v[84:87], a[180:183], a[140:143], v[84:87]\n"
"v_add_f32 v157, 1.0, v157\n"
"ds_read_b64_tr_b16 a[194:195], v232 offset:8704\n"
"v_mfma_f32_16x16x32_bf16 v[88:91], a[176:179], a[144:147], v[92:95]\n"
"v_lshlrev_b32 v158, 16, v201\n"
"ds_read_b64_tr_b16 a[196:197], v232 offset:12288\n"
"v_mfma_f32_16x16x32_bf16 v[88:91], a[180:183], a[148:151], v[88:91]\n"
"v_add_f32 v158, 1.0, v158\n"
"ds_read_b64_tr_b16 a[198:199], v232 offset:12800\n"
"v_mfma_f32_16x16x32_bf16 v[92:95], a[176:179], a[152:155], v[92:95]\n"
"v_and_b32 v159, s67, v201\n"
"ds_read_b64_tr_b16 a[200:201], v232 offset:8200\n"
"v_mfma_f32_16x16x32_bf16 v[92:95], a[180:183], a[156:159], v[92:95]\n"
"v_add_f32 v159, 1.0, v159\n"
"v_mfma_f32_16x16x32_bf16 v[96:99], a[184:187], a[96:99], v[124:127]\n"
"ds_read_b64_tr_b16 a[202:203], v232 offset:8712\n"
"v_add_u32 v238, 7168, v238\n"
"v_mfma_f32_16x16x32_bf16 v[96:99], a[188:191], a[100:103], v[96:99]\n"
"ds_read_b64_tr_b16 a[204:205], v232 offset:12296\n"
"global_load_lds_dwordx4 v238, s[40:41] offset:1024\n"
"v_mfma_f32_16x16x32_bf16 v[100:103], a[184:187], a[104:107], v[124:127]\n"
"ds_read_b64_tr_b16 a[206:207], v232 offset:12808\n"
"v_lshlrev_b32 v235, 16, v206\n"
"v_mfma_f32_16x16x32_bf16 v[100:103], a[188:191], a[108:111], v[100:103]\n"
"ds_read_b64_tr_b16 a[208:209], v254 offset:8192\n"
"v_add_f32 v235, 1.0, v235\n"
"v_mfma_f32_16x16x32_bf16 v[104:107], a[184:187], a[112:115], v[124:127]\n"
"ds_read_b64_tr_b16 a[210:211], v254 offset:8704\n"
"v_accvgpr_write_b32 a28, v235\n"
"v_mfma_f32_16x16x32_bf16 v[104:107], a[188:191], a[116:119], v[104:107]\n"
"ds_read_b64_tr_b16 a[212:213], v254 offset:12288\n"
"v_and_b32 v235, s67, v206\n"
"v_mfma_f32_16x16x32_bf16 v[108:111], a[184:187], a[120:123], v[124:127]\n"
"ds_read_b64_tr_b16 a[214:215], v254 offset:12800\n"
"v_add_f32 v235, 1.0, v235\n"
"v_mfma_f32_16x16x32_bf16 v[108:111], a[188:191], a[124:127], v[108:111]\n"
"ds_read_b64_tr_b16 a[216:217], v254 offset:8200\n"
"v_accvgpr_write_b32 a29, v235\n"
"v_mfma_f32_16x16x32_bf16 v[112:115], a[184:187], a[128:131], v[124:127]\n"
"v_lshlrev_b32 v188, 16, v202\n"
"ds_read_b64_tr_b16 a[218:219], v254 offset:8712\n"
"v_mfma_f32_16x16x32_bf16 v[112:115], a[188:191], a[132:135], v[112:115]\n"
"v_add_f32 v188, 1.0, v188\n"
"ds_read_b64_tr_b16 a[220:221], v254 offset:12296\n"
"v_mfma_f32_16x16x32_bf16 v[116:119], a[184:187], a[136:139], v[124:127]\n"
"v_and_b32 v189, s67, v202\n"
"ds_read_b64_tr_b16 a[222:223], v254 offset:12808\n"
"v_mfma_f32_16x16x32_bf16 v[116:119], a[188:191], a[140:143], v[116:119]\n"
"v_add_f32 v189, 1.0, v189\n"
"v_lshlrev_b32 v235, 16, v207\n"
"v_mfma_f32_16x16x32_bf16 v[120:123], a[184:187], a[144:147], v[124:127]\n"
"v_lshlrev_b32 v190, 16, v203\n"
"v_add_f32 v235, 1.0, v235\n"
"v_mfma_f32_16x16x32_bf16 v[120:123], a[188:191], a[148:151], v[120:123]\n"
"v_add_f32 v190, 1.0, v190\n"
"v_accvgpr_write_b32 a30, v235\n"
"v_mfma_f32_16x16x32_bf16 v[124:127], a[184:187], a[152:155], v[124:127]\n"
"v_and_b32 v191, s67, v203\n"
"v_and_b32 v235, s67, v207\n"
"v_mfma_f32_16x16x32_bf16 v[124:127], a[188:191], a[156:159], v[124:127]\n"
"v_add_f32 v191, 1.0, v191\n"
"s_waitcnt lgkmcnt(12)\n"
"v_mfma_f32_16x16x32_bf16 v[128:131], a[192:195], a[96:99], v[156:159]\n"
"s_mul_i32 s45, s50, -98304\n"
"s_add_u32 s45, s45, 147456\n"
"s_add_u32 s45, s45, s42\n"
"v_mfma_f32_16x16x32_bf16 v[128:131], a[196:199], a[100:103], v[128:131]\n"
"s_add_u32 m0, s45, 0\n"
"v_add_u32 v238, 384, v248\n"
"v_mfma_f32_16x16x32_bf16 v[132:135], a[192:195], a[104:107], v[156:159]\n"
"global_load_lds_dwordx4 v238, s[20:21] offset:0\n"
"v_add_f32 v235, 1.0, v235\n"
"v_mfma_f32_16x16x32_bf16 v[132:135], a[196:199], a[108:111], v[132:135]\n"
"v_accvgpr_write_b32 a31, v235\n"
"s_lshl_b32 s46, s46, 6\n"
"v_mfma_f32_16x16x32_bf16 v[136:139], a[192:195], a[112:115], v[156:159]\n"
"s_add_u32 s47, s46, 6356992\n"
"v_mfma_f32_16x16x32_bf16 v[136:139], a[196:199], a[116:119], v[136:139]\n"
"v_add_u32 v238, s66, v238\n"
"global_load_lds_dwordx4 v238, s[20:21] offset:1024\n"
"v_mfma_f32_16x16x32_bf16 v[140:143], a[192:195], a[120:123], v[156:159]\n"
"s_add_u32 s30, s12, s47\n"
"s_addc_u32 s31, s13, 0\n"
"v_mfma_f32_16x16x32_bf16 v[140:143], a[196:199], a[124:127], v[140:143]\n"
"s_add_u32 s46, s46, 6422528\n"
"s_lshl_b32 s47, s53, 5\n"
"v_mfma_f32_16x16x32_bf16 v[144:147], a[192:195], a[128:131], v[156:159]\n"
"v_lshlrev_b32 v220, 16, v204\n"
"v_add_u32 v238, s66, v238\n"
"v_mfma_f32_16x16x32_bf16 v[144:147], a[196:199], a[132:135], v[144:147]\n"
"v_add_f32 v220, 1.0, v220\n"
"global_load_lds_dwordx4 v238, s[20:21] offset:2048\n"
"v_mfma_f32_16x16x32_bf16 v[148:151], a[192:195], a[136:139], v[156:159]\n"
"v_and_b32 v221, s67, v204\n"
"s_add_u32 s47, s47, s52\n"
"v_mfma_f32_16x16x32_bf16 v[148:151], a[196:199], a[140:143], v[148:151]\n"
"v_add_f32 v221, 1.0, v221\n"
"s_lshl_b32 s47, s47, 6\n"
"v_mfma_f32_16x16x32_bf16 v[152:155], a[192:195], a[144:147], v[156:159]\n"
"v_lshlrev_b32 v222, 16, v205\n"
"v_add_u32 v238, s66, v238\n"
"v_mfma_f32_16x16x32_bf16 v[152:155], a[196:199], a[148:151], v[152:155]\n"
"v_add_f32 v222, 1.0, v222\n"
"global_load_lds_dwordx4 v238, s[20:21] offset:3072\n"
"v_mfma_f32_16x16x32_bf16 v[156:159], a[192:195], a[152:155], v[156:159]\n"
"v_and_b32 v223, s67, v205\n"
"s_add_u32 s47, s47, 6291456\n"
"v_mfma_f32_16x16x32_bf16 v[156:159], a[196:199], a[156:159], v[156:159]\n"
"v_add_f32 v223, 1.0, v223\n"
"s_waitcnt lgkmcnt(8)\n"
"v_mfma_f32_16x16x32_bf16 v[160:163], a[200:203], a[96:99], v[188:191]\n"
"s_add_u32 m0, s55, 0\n"
"v_add_u32 v238, 1179776, v251\n"
"v_mfma_f32_16x16x32_bf16 v[160:163], a[204:207], a[100:103], v[160:163]\n"
"global_load_lds_dwordx4 v238, s[58:59] offset:0\n"
"v_mfma_f32_16x16x32_bf16 v[164:167], a[200:203], a[104:107], v[188:191]\n"
"s_cmp_eq_u32 %1, 0\n"
"s_cselect_b32 s46, s47, s46\n"
"v_mfma_f32_16x16x32_bf16 v[164:167], a[204:207], a[108:111], v[164:167]\n"
"s_add_u32 s38, s12, s46\n"
"s_addc_u32 s39, s13, 0\n"
"v_mfma_f32_16x16x32_bf16 v[168:171], a[200:203], a[112:115], v[188:191]\n"
"v_add_u32 v238, s66, v238\n"
"global_load_lds_dwordx4 v238, s[58:59] offset:1024\n"
"v_mfma_f32_16x16x32_bf16 v[168:171], a[204:207], a[116:119], v[168:171]\n"
"s_lshr_b32 s68, %2, 3\n"
"s_lshl_b32 s68, s68, 2\n"
"v_mfma_f32_16x16x32_bf16 v[172:175], a[200:203], a[120:123], v[188:191]\n"
"s_add_u32 s68, s68, %1\n"
"s_min_u32 s68, s68, (.Lfin_%=-.Lpc_%=)/1024-1\n"
"v_mfma_f32_16x16x32_bf16 v[172:175], a[204:207], a[124:127], v[172:175]\n"
"v_add_u32 v238, s66, v238\n"
"global_load_lds_dwordx4 v238, s[58:59] offset:2048\n"
"v_mfma_f32_16x16x32_bf16 v[176:179], a[200:203], a[128:131], v[188:191]\n"
"s_lshl_b32 s68, s68, 10\n"
"v_mfma_f32_16x16x32_bf16 v[176:179], a[204:207], a[132:135], v[176:179]\n"
"s_add_u32 s64, s60, s68\n"
"s_addc_u32 s65, s61, 0\n"
"v_mfma_f32_16x16x32_bf16 v[180:183], a[200:203], a[136:139], v[188:191]\n"
"v_add_u32 v238, s66, v238\n"
"global_load_lds_dwordx4 v238, s[58:59] offset:3072\n"
"v_mfma_f32_16x16x32_bf16 v[180:183], a[204:207], a[140:143], v[180:183]\n"
"s_mul_i32 s46, s72, 786432\n"
"s_add_u32 s46, s46, 69632\n"
"v_mfma_f32_16x16x32_bf16 v[184:187], a[200:203], a[144:147], v[188:191]\n"
"s_add_u32 s16, s12, s46\n"
"s_addc_u32 s17, s13, 0\n"
"v_mfma_f32_16x16x32_bf16 v[184:187], a[204:207], a[148:151], v[184:187]\n"
"s_waitcnt vmcnt(12)\n"
"ds_read_b64_tr_b16 a[160:161], v233 offset:0\n"
"v_mfma_f32_16x16x32_bf16 v[188:191], a[200:203], a[152:155], v[188:191]\n"
"ds_read_b64_tr_b16 a[162:163], v233 offset:512\n"
"v_mfma_f32_16x16x32_bf16 v[188:191], a[204:207], a[156:159], v[188:191]\n"
"ds_read_b64_tr_b16 a[164:165], v233 offset:4096\n"
"s_waitcnt lgkmcnt(7)\n"
"v_mfma_f32_16x16x32_bf16 v[192:195], a[208:211], a[96:99], v[220:223]\n"
"ds_read_b64_tr_b16 a[166:167], v233 offset:4608\n"
"v_mfma_f32_16x16x32_bf16 v[192:195], a[212:215], a[100:103], v[192:195]\n"
"s_mul_i32 s48, s49, -114688\n"
"s_add_u32 s48, s48, 131072\n"
"v_mfma_f32_16x16x32_bf16 v[196:199], a[208:211], a[104:107], v[220:223]\n"
"v_add_u32 v242, s48, v240\n"
"ds_read_b64_tr_b16 a[168:169], v233 offset:8\n"
"v_mfma_f32_16x16x32_bf16 v[196:199], a[212:215], a[108:111], v[196:199]\n"
"v_add_u32 v243, s48, v241\n"
"ds_read_b64_tr_b16 a[170:171], v233 offset:520\n"
"v_mfma_f32_16x16x32_bf16 v[200:203], a[208:211], a[112:115], v[220:223]\n"
"ds_read_b64_tr_b16 a[172:173], v233 offset:4104\n"
"v_mfma_f32_16x16x32_bf16 v[200:203], a[212:215], a[116:119], v[200:203]\n"
"ds_read_b64_tr_b16 a[174:175], v233 offset:4616\n"
"v_mfma_f32_16x16x32_bf16 v[204:207], a[208:211], a[120:123], v[220:223]\n"
"ds_read_b64_tr_b16 a[176:177], v255 offset:0\n"
"v_mfma_f32_16x16x32_bf16 v[204:207], a[212:215], a[124:127], v[204:207]\n"
"ds_read_b64_tr_b16 a[178:179], v255 offset:512\n"
"v_mfma_f32_16x16x32_bf16 v[208:211], a[208:211], a[128:131], v[220:223]\n"
"ds_read_b64_tr_b16 a[180:181], v255 offset:4096\n"
"v_mfma_f32_16x16x32_bf16 v[208:211], a[212:215], a[132:135], v[208:211]\n"
"ds_read_b64_tr_b16 a[182:183], v255 offset:4608\n"
"v_mfma_f32_16x16x32_bf16 v[212:215], a[208:211], a[136:139], v[220:223]\n"
"ds_read_b64_tr_b16 a[184:185], v255 offset:8\n"
"v_mfma_f32_16x16x32_bf16 v[212:215], a[212:215], a[140:143], v[212:215]\n"
"ds_read_b64_tr_b16 a[186:187], v255 offset:520\n"
"v_mfma_f32_16x16x32_bf16 v[216:219], a[208:211], a[144:147], v[220:223]\n"
"ds_read_b64_tr_b16 a[188:189], v255 offset:4104\n"
"v_mfma_f32_16x16x32_bf16 v[216:219], a[212:215], a[148:151], v[216:219]\n"
"ds_read_b64_tr_b16 a[190:191], v255 offset:4616\n"
"s_barrier\n"
"v_mfma_f32_16x16x32_bf16 v[220:223], a[208:211], a[152:155], v[220:223]\n"
"s_add_u32 m0, s55, 4096\n"
"v_add_u32 v238, 1245312, v251\n"
"v_mfma_f32_16x16x32_bf16 v[220:223], a[212:215], a[156:159], v[220:223]\n"
"global_load_lds_dwordx4 v238, s[58:59] offset:0\n"
"s_waitcnt lgkmcnt(15)\n"
"v_mfma_f32_16x16x32_bf16 a[0:3], a[216:219], a[96:99], a[28:31]\n"
"ds_read_b128 a[96:99], v242 offset:0\n"
"v_mfma_f32_16x16x32_bf16 a[0:3], a[220:223], a[100:103], a[0:3]\n"
"ds_read_b128 a[100:103], v243 offset:0\n"
"v_mfma_f32_16x16x32_bf16 a[4:7], a[216:219], a[104:107], a[28:31]\n"
"ds_read_b128 a[104:107], v242 offset:2048\n"
"v_add_u32 v238, s66, v238\n"
"v_mfma_f32_16x16x32_bf16 a[4:7], a[220:223], a[108:111], a[4:7]\n"
"ds_read_b128 a[108:111], v243 offset:2048\n"
"global_load_lds_dwordx4 v238, s[58:59] offset:1024\n"
"v_mfma_f32_16x16x32_bf16 a[8:11], a[216:219], a[112:115], a[28:31]\n"
"ds_read_b128 a[112:115], v242 offset:4096\n"
"v_mfma_f32_16x16x32_bf16 a[8:11], a[220:223], a[116:119], a[8:11]\n"
"ds_read_b128 a[116:119], v243 offset:4096\n"
"v_mfma_f32_16x16x32_bf16 a[12:15], a[216:219], a[120:123], a[28:31]\n"
"ds_read_b128 a[120:123], v242 offset:6144\n"
"v_add_u32 v238, s66, v238\n"
"v_mfma_f32_16x16x32_bf16 a[12:15], a[220:223], a[124:127], a[12:15]\n"
"ds_read_b128 a[124:127], v243 offset:6144\n"
"global_load_lds_dwordx4 v238, s[58:59] offset:2048\n"
"v_mfma_f32_16x16x32_bf16 a[16:19], a[216:219], a[128:131], a[28:31]\n"
"ds_read_b128 a[128:131], v242 offset:8192\n"
"v_mfma_f32_16x16x32_bf16 a[16:19], a[220:223], a[132:135], a[16:19]\n"
"ds_read_b128 a[132:135], v243 offset:8192\n"
"v_mfma_f32_16x16x32_bf16 a[20:23], a[216:219], a[136:139], a[28:31]\n"
"ds_read_b128 a[136:139], v242 offset:10240\n"
"v_add_u32 v238, s66, v238\n"
"v_mfma_f32_16x16x32_bf16 a[20:23], a[220:223], a[140:143], a[20:23]\n"
"ds_read_b128 a[140:143], v243 offset:10240\n"
"global_load_lds_dwordx4 v238, s[58:59] offset:3072\n"
"v_mfma_f32_16x16x32_bf16 a[24:27], a[216:219], a[144:147], a[28:31]\n"
"ds_read_b128 a[144:147], v242 offset:12288\n"
"v_mfma_f32_16x16x32_bf16 a[24:27], a[220:223], a[148:151], a[24:27]\n"
"ds_read_b128 a[148:151], v243 offset:12288\n"
"v_mfma_f32_16x16x32_bf16 a[28:31], a[216:219], a[152:155], a[28:31]\n"
"ds_read_b128 a[152:155], v242 offset:14336\n"
"v_mfma_f32_16x16x32_bf16 a[28:31], a[220:223], a[156:159], a[28:31]\n"
"ds_read_b128 a[156:159], v243 offset:14336\n"
"s_waitcnt lgkmcnt(14)\n"
"v_mfma_f32_16x16x32_bf16 v[0:3], a[160:163], a[96:99], v[0:3]\n"
"s_add_u32 m0, s55, 8192\n"
"v_add_u32 v238, 393216, v251\n"
"v_mfma_f32_16x16x32_bf16 v[0:3], a[164:167], a[100:103], v[0:3]\n"
"global_load_lds_dwordx4 v238, s[58:59] offset:0\n"
"s_waitcnt lgkmcnt(12)\n"
"v_mfma_f32_16x16x32_bf16 v[4:7], a[160:163], a[104:107], v[4:7]\n"
"v_mfma_f32_16x16x32_bf16 v[4:7], a[164:167], a[108:111], v[4:7]\n"
"s_waitcnt lgkmcnt(10)\n"
"v_mfma_f32_16x16x32_bf16 v[8:11], a[160:163], a[112:115], v[8:11]\n"
"v_add_u32 v238, s66, v238\n"
"global_load_lds_dwordx4 v238, s[58:59] offset:1024\n"
"v_mfma_f32_16x16x32_bf16 v[8:11], a[164:167], a[116:119], v[8:11]\n"
"s_waitcnt lgkmcnt(8)\n"
"v_mfma_f32_16x16x32_bf16 v[12:15], a[160:163], a[120:123], v[12:15]\n"
"v_mfma_f32_16x16x32_bf16 v[12:15], a[164:167], a[124:127], v[12:15]\n"
"v_add_u32 v238, s66, v238\n"
"s_waitcnt lgkmcnt(6)\n"
"v_mfma_f32_16x16x32_bf16 v[16:19], a[160:163], a[128:131], v[16:19]\n"
"global_load_lds_dwordx4 v238, s[58:59] offset:2048\n"
"v_mfma_f32_16x16x32_bf16 v[16:19], a[164:167], a[132:135], v[16:19]\n"
"s_waitcnt lgkmcnt(4)\n"
"v_mfma_f32_16x16x32_bf16 v[20:23], a[160:163], a[136:139], v[20:23]\n"
"s_nop 0\n"
".Lpg1_%=:\n"
"s_cbranch_execz .Lpg5_%=\n"
"v_mfma_f32_16x16x32_bf16 v[20:23], a[164:167], a[140:143], v[20:23]\n"
"v_add_u32 v238, s66, v238\n"
"s_waitcnt lgkmcnt(2)\n"
"v_mfma_f32_16x16x32_bf16 v[24:27], a[160:163], a[144:147], v[24:27]\n"
"global_load_lds_dwordx4 v238, s[58:59] offset:3072\n"
"v_mfma_f32_16x16x32_bf16 v[24:27], a[164:167], a[148:151], v[24:27]\n"
"s_waitcnt lgkmcnt(0)\n"
"v_mfma_f32_16x16x32_bf16 v[28:31], a[160:163], a[152:155], v[28:31]\n"
"v_mfma_f32_16x16x32_bf16 v[28:31], a[164:167], a[156:159], v[28:31]\n"
"v_mfma_f32_16x16x32_bf16 v[32:35], a[168:171], a[96:99], v[32:35]\n"
"s_add_u32 m0, s55, 12288\n"
"v_add_u32 v238, 458752, v251\n"
"v_mfma_f32_16x16x32_bf16 v[32:35], a[172:175], a[100:103], v[32:35]\n"
"global_load_lds_dwordx4 v238, s[58:59] offset:0\n"
"v_mfma_f32_16x16x32_bf16 v[36:39], a[168:171], a[104:107], v[36:39]\n"
"s_waitcnt vmcnt(17)\n"
"s_barrier\n"
"v_mfma_f32_16x16x32_bf16 v[36:39], a[172:175], a[108:111], v[36:39]\n"
"ds_read_b64_tr_b16 a[32:33], v224 offset:0\n"
"v_mfma_f32_16x16x32_bf16 v[40:43], a[168:171], a[112:115], v[40:43]\n"
"v_add_u32 v238, s66, v238\n"
"global_load_lds_dwordx4 v238, s[58:59] offset:1024\n"
"v_mfma_f32_16x16x32_bf16 v[40:43], a[172:175], a[116:119], v[40:43]\n"
"ds_read_b64_tr_b16 a[34:35], v224 offset:1024\n"
"v_mfma_f32_16x16x32_bf16 v[44:47], a[168:171], a[120:123], v[44:47]\n"
"ds_read_b64_tr_b16 a[36:37], v224 offset:8\n"
"v_mfma_f32_16x16x32_bf16 v[44:47], a[172:175], a[124:127], v[44:47]\n"
"v_add_u32 v238, s66, v238\n"
"global_load_lds_dwordx4 v238, s[58:59] offset:2048\n"
"v_mfma_f32_16x16x32_bf16 v[48:51], a[168:171], a[128:131], v[48:51]\n"
"ds_read_b64_tr_b16 a[38:39], v224 offset:1032\n"
"v_mfma_f32_16x16x32_bf16 v[48:51], a[172:175], a[132:135], v[48:51]\n"
"v_readfirstlane_b32 s44, v229\n"
"v_mfma_f32_16x16x32_bf16 v[52:55], a[168:171], a[136:139], v[52:55]\n"
"v_add_u32 v238, s66, v238\n"
"global_load_lds_dwordx4 v238, s[58:59] offset:3072\n"
"v_mfma_f32_16x16x32_bf16 v[52:55], a[172:175], a[140:143], v[52:55]\n"
"v_mfma_f32_16x16x32_bf16 v[56:59], a[168:171], a[144:147], v[56:59]\n"
"v_mfma_f32_16x16x32_bf16 v[56:59], a[172:175], a[148:151], v[56:59]\n"
"v_mfma_f32_16x16x32_bf16 v[60:63], a[168:171], a[152:155], v[60:63]\n"
"v_mfma_f32_16x16x32_bf16 v[60:63], a[172:175], a[156:159], v[60:63]\n"
"v_mfma_f32_16x16x32_bf16 v[64:67], a[176:179], a[96:99], v[64:67]\n"
"s_waitcnt lgkmcnt(2)\n"
"global_store_dwordx4 v225, a[32:35], s[12:13] offset:0 sc1\n"
"v_mfma_f32_16x16x32_bf16 v[64:67], a[180:183], a[100:103], v[64:67]\n"
"v_mfma_f32_16x16x32_bf16 v[68:71], a[176:179], a[104:107], v[68:71]\n"
"s_mul_i32 s48, s50, -98304\n"
"s_add_u32 s48, s48, 147456\n"
"v_mfma_f32_16x16x32_bf16 v[68:71], a[180:183], a[108:111], v[68:71]\n"
"v_add_u32 v242, s48, v240\n"
"v_mfma_f32_16x16x32_bf16 v[72:75], a[176:179], a[112:115], v[72:75]\n"
"v_add_u32 v243, s48, v241\n"
"v_mfma_f32_16x16x32_bf16 v[72:75], a[180:183], a[116:119], v[72:75]\n"
"s_waitcnt lgkmcnt(0)\n"
"global_store_dwordx4 v225, a[36:39], s[12:13] offset:2048 sc1\n"
"v_mfma_f32_16x16x32_bf16 v[76:79], a[176:179], a[120:123], v[76:79]\n"
"v_mfma_f32_16x16x32_bf16 v[76:79], a[180:183], a[124:127], v[76:79]\n"
"s_barrier\n"
"v_mfma_f32_16x16x32_bf16 v[80:83], a[176:179], a[128:131], v[80:83]\n"
"s_mul_i32 s45, s49, -98304\n"
"s_add_u32 s45, s45, 147456\n"
"s_add_u32 s45, s45, s42\n"
"v_mfma_f32_16x16x32_bf16 v[80:83], a[180:183], a[132:135], v[80:83]\n"
"s_add_u32 m0, s45, 0\n"
"v_add_u32 v238, 1408, v248\n"
"v_mfma_f32_16x16x32_bf16 v[84:87], a[176:179], a[136:139], v[84:87]\n"
"s_waitcnt vmcnt(10)\n"
"ds_read_b64_tr_b16 a[192:193], v232 offset:0\n"
"global_load_lds_dwordx4 v238, s[20:21] offset:0\n"
"v_mfma_f32_16x16x32_bf16 v[84:87], a[180:183], a[140:143], v[84:87]\n"
"ds_read_b64_tr_b16 a[194:195], v232 offset:512\n"
"v_mfma_f32_16x16x32_bf16 v[88:91], a[176:179], a[144:147], v[88:91]\n"
"ds_read_b64_tr_b16 a[196:197], v232 offset:4096\n"
"v_mfma_f32_16x16x32_bf16 v[88:91], a[180:183], a[148:151], v[88:91]\n"
"ds_read_b64_tr_b16 a[198:199], v232 offset:4608\n"
"v_add_u32 v238, s66, v238\n"
"v_mfma_f32_16x16x32_bf16 v[92:95], a[176:179], a[152:155], v[92:95]\n"
"ds_read_b64_tr_b16 a[200:201], v232 offset:8\n"
"global_load_lds_dwordx4 v238, s[20:21] offset:1024\n"
"v_mfma_f32_16x16x32_bf16 v[92:95], a[180:183], a[156:159], v[92:95]\n"
"ds_read_b64_tr_b16 a[202:203], v232 offset:520\n"
"v_mfma_f32_16x16x32_bf16 v[96:99], a[184:187], a[96:99], v[96:99]\n"
"ds_read_b64_tr_b16 a[204:205], v232 offset:4104\n"
"v_mfma_f32_16x16x32_bf16 v[96:99], a[188:191], a[100:103], v[96:99]\n"
"ds_read_b64_tr_b16 a[206:207], v232 offset:4616\n"
"v_add_u32 v238, s66, v238\n"
"v_mfma_f32_16x16x32_bf16 v[100:103], a[184:187], a[104:107], v[100:103]\n"
"ds_read_b64_tr_b16 a[208:209], v254 offset:0\n"
"global_load_lds_dwordx4 v238, s[20:21] offset:2048\n"
"v_mfma_f32_16x16x32_bf16 v[100:103], a[188:191], a[108:111], v[100:103]\n"
"ds_read_b64_tr_b16 a[210:211], v254 offset:512\n"
"v_mfma_f32_16x16x32_bf16 v[104:107], a[184:187], a[112:115], v[104:107]\n"
"ds_read_b64_tr_b16 a[212:213], v254 offset:4096\n"
"v_mfma_f32_16x16x32_bf16 v[104:107], a[188:191], a[116:119], v[104:107]\n"
"ds_read_b64_tr_b16 a[214:215], v254 offset:4608\n"
"v_add_u32 v238, s66, v238\n"
"v_mfma_f32_16x16x32_bf16 v[108:111], a[184:187], a[120:123], v[108:111]\n"
"ds_read_b64_tr_b16 a[216:217], v254 offset:8\n"
"global_load_lds_dwordx4 v238, s[20:21] offset:3072\n"
"v_mfma_f32_16x16x32_bf16 v[108:111], a[188:191], a[124:127], v[108:111]\n"
"ds_read_b64_tr_b16 a[218:219], v254 offset:520\n"
"v_mfma_f32_16x16x32_bf16 v[112:115], a[184:187], a[128:131], v[112:115]\n"
"ds_read_b64_tr_b16 a[220:221], v254 offset:4104\n"
"v_mfma_f32_16x16x32_bf16 v[112:115], a[188:191], a[132:135], v[112:115]\n"
"ds_read_b64_tr_b16 a[222:223], v254 offset:4616\n"
"v_mfma_f32_16x16x32_bf16 v[116:119], a[184:187], a[136:139], v[116:119]\n"
"v_mfma_f32_16x16x32_bf16 v[116:119], a[188:191], a[140:143], v[116:119]\n"
"v_mfma_f32_16x16x32_bf16 v[120:123], a[184:187], a[144:147], v[120:123]\n"
"s_waitcnt lgkmcnt(0)\n"
"s_add_u32 m0, s55, 0\n"
"v_add_u32 v238, 393344, v251\n"
"v_mfma_f32_16x16x32_bf16 v[120:123], a[188:191], a[148:151], v[120:123]\n"
"global_load_lds_dwordx4 v238, s[58:59] offset:0\n"
"v_mfma_f32_16x16x32_bf16 v[124:127], a[184:187], a[152:155], v[124:127]\n"
"v_mfma_f32_16x16x32_bf16 v[124:127], a[188:191], a[156:159], v[124:127]\n"
"v_mfma_f32_16x16x32_bf16 v[128:131], a[192:195], a[96:99], v[128:131]\n"
"v_add_u32 v238, s66, v238\n"
"global_load_lds_dwordx4 v238, s[58:59] offset:1024\n"
"v_mfma_f32_16x16x32_bf16 v[128:131], a[196:199], a[100:103], v[128:131]\n"
"v_mfma_f32_16x16x32_bf16 v[132:135], a[192:195], a[104:107], v[132:135]\n"
"v_mfma_f32_16x16x32_bf16 v[132:135], a[196:199], a[108:111], v[132:135]\n"
"v_add_u32 v238, s66, v238\n"
"global_load_lds_dwordx4 v238, s[58:59] offset:2048\n"
"v_mfma_f32_16x16x32_bf16 v[136:139], a[192:195], a[112:115], v[136:139]\n"
"v_mfma_f32_16x16x32_bf16 v[136:139], a[196:199], a[116:119], v[136:139]\n"
"v_mfma_f32_16x16x32_bf16 v[140:143], a[192:195], a[120:123], v[140:143]\n"
"v_add_u32 v238, s66, v238\n"
"global_load_lds_dwordx4 v238, s[58:59] offset:3072\n"
"v_mfma_f32_16x16x32_bf16 v[140:143], a[196:199], a[124:127], v[140:143]\n"
"v_mfma_f32_16x16x32_bf16 v[144:147], a[192:195], a[128:131], v[144:147]\n"
"v_mfma_f32_16x16x32_bf16 v[144:147], a[196:199], a[132:135], v[144:147]\n"
"s_add_u32 m0, s55, 4096\n"
"v_add_u32 v238, 458880, v251\n"
"v_mfma_f32_16x16x32_bf16 v[148:151], a[192:195], a[136:139], v[148:151]\n"
"global_load_lds_dwordx4 v238, s[58:59] offset:0\n"
"v_mfma_f32_16x16x32_bf16 v[148:151], a[196:199], a[140:143], v[148:151]\n"
"v_mfma_f32_16x16x32_bf16 v[152:155], a[192:195], a[144:147], v[152:155]\n"
"v_mfma_f32_16x16x32_bf16 v[152:155], a[196:199], a[148:151], v[152:155]\n"
"v_add_u32 v238, s66, v238\n"
"global_load_lds_dwordx4 v238, s[58:59] offset:1024\n"
"v_mfma_f32_16x16x32_bf16 v[156:159], a[192:195], a[152:155], v[156:159]\n"
"v_mfma_f32_16x16x32_bf16 v[156:159], a[196:199], a[156:159], v[156:159]\n"
"v_mfma_f32_16x16x32_bf16 v[160:163], a[200:203], a[96:99], v[160:163]\n"
"v_add_u32 v238, s66, v238\n"
"global_load_lds_dwordx4 v238, s[58:59] offset:2048\n"
"v_mfma_f32_16x16x32_bf16 v[160:163], a[204:207], a[100:103], v[160:163]\n"
"v_mfma_f32_16x16x32_bf16 v[164:167], a[200:203], a[104:107], v[164:167]\n"
"v_mfma_f32_16x16x32_bf16 v[164:167], a[204:207], a[108:111], v[164:167]\n"
"v_add_u32 v238, s66, v238\n"
"global_load_lds_dwordx4 v238, s[58:59] offset:3072\n"
"v_mfma_f32_16x16x32_bf16 v[168:171], a[200:203], a[112:115], v[168:171]\n"
"v_mfma_f32_16x16x32_bf16 v[168:171], a[204:207], a[116:119], v[168:171]\n"
"v_mfma_f32_16x16x32_bf16 v[172:175], a[200:203], a[120:123], v[172:175]\n"
"v_mfma_f32_16x16x32_bf16 v[172:175], a[204:207], a[124:127], v[172:175]\n"
"v_mfma_f32_16x16x32_bf16 v[176:179], a[200:203], a[128:131], v[176:179]\n"
"v_mfma_f32_16x16x32_bf16 v[176:179], a[204:207], a[132:135], v[176:179]\n"
"v_mfma_f32_16x16x32_bf16 v[180:183], a[200:203], a[136:139], v[180:183]\n"
"v_mfma_f32_16x16x32_bf16 v[180:183], a[204:207], a[140:143], v[180:183]\n"
"v_mfma_f32_16x16x32_bf16 v[184:187], a[200:203], a[144:147], v[184:187]\n"
"s_waitcnt vmcnt(14)\n"
"ds_read_b64_tr_b16 a[160:161], v232 offset:8192\n"
"v_mfma_f32_16x16x32_bf16 v[184:187], a[204:207], a[148:151], v[184:187]\n"
"ds_read_b64_tr_b16 a[162:163], v232 offset:8704\n"
"v_mfma_f32_16x16x32_bf16 v[188:191], a[200:203], a[152:155], v[188:191]\n"
"ds_read_b64_tr_b16 a[164:165], v232 offset:12288\n"
"v_mfma_f32_16x16x32_bf16 v[188:191], a[204:207], a[156:159], v[188:191]\n"
"ds_read_b64_tr_b16 a[166:167], v232 offset:12800\n"
"v_mfma_f32_16x16x32_bf16 v[192:195], a[208:211], a[96:99], v[192:195]\n"
"ds_read_b64_tr_b16 a[168:169], v232 offset:8200\n"
"v_mfma_f32_16x16x32_bf16 v[192:195], a[212:215], a[100:103], v[192:195]\n"
"ds_read_b64_tr_b16 a[170:171], v232 offset:8712\n"
"v_mfma_f32_16x16x32_bf16 v[196:199], a[208:211], a[104:107], v[196:199]\n"
"ds_read_b64_tr_b16 a[172:173], v232 offset:12296\n"
"v_mfma_f32_16x16x32_bf16 v[196:199], a[212:215], a[108:111], v[196:199]\n"
"ds_read_b64_tr_b16 a[174:175], v232 offset:12808\n"
"v_mfma_f32_16x16x32_bf16 v[200:203], a[208:211], a[112:115], v[200:203]\n"
"ds_read_b64_tr_b16 a[176:177], v254 offset:8192\n"
"v_mfma_f32_16x16x32_bf16 v[200:203], a[212:215], a[116:119], v[200:203]\n"
"ds_read_b64_tr_b16 a[178:179], v254 offset:8704\n"
"v_mfma_f32_16x16x32_bf16 v[204:207], a[208:211], a[120:123], v[204:207]\n"
"ds_read_b64_tr_b16 a[180:181], v254 offset:12288\n"
"v_mfma_f32_16x16x32_bf16 v[204:207], a[212:215], a[124:127], v[204:207]\n"
"ds_read_b64_tr_b16 a[182:183], v254 offset:12800\n"
"v_mfma_f32_16x16x32_bf16 v[208:211], a[208:211], a[128:131], v[208:211]\n"
"ds_read_b64_tr_b16 a[184:185], v254 offset:8200\n"
"v_mfma_f32_16x16x32_bf16 v[208:211], a[212:215], a[132:135], v[208:211]\n"
"ds_read_b64_tr_b16 a[186:187], v254 offset:8712\n"
"v_mfma_f32_16x16x32_bf16 v[212:215], a[208:211], a[136:139], v[212:215]\n"
"ds_read_b64_tr_b16 a[188:189], v254 offset:12296\n"
"v_mfma_f32_16x16x32_bf16 v[212:215], a[212:215], a[140:143], v[212:215]\n"
"ds_read_b64_tr_b16 a[190:191], v254 offset:12808\n"
"v_mfma_f32_16x16x32_bf16 v[216:219], a[208:211], a[144:147], v[216:219]\n"
"v_mfma_f32_16x16x32_bf16 v[216:219], a[212:215], a[148:151], v[216:219]\n"
"s_barrier\n"
"v_mfma_f32_16x16x32_bf16 v[220:223], a[208:211], a[152:155], v[220:223]\n"
"v_mfma_f32_16x16x32_bf16 v[220:223], a[212:215], a[156:159], v[220:223]\n"
"v_mfma_f32_16x16x32_bf16 a[0:3], a[216:219], a[96:99], a[0:3]\n"
"ds_read_b128 a[96:99], v242 offset:0\n"
"v_mfma_f32_16x16x32_bf16 a[0:3], a[220:223], a[100:103], a[0:3]\n"
"ds_read_b128 a[100:103], v243 offset:0\n"
"v_mfma_f32_16x16x32_bf16 a[4:7], a[216:219], a[104:107], a[4:7]\n"
"ds_read_b128 a[104:107], v242 offset:2048\n"
"v_mfma_f32_16x16x32_bf16 a[4:7], a[220:223], a[108:111], a[4:7]\n"
"ds_read_b128 a[108:111], v243 offset:2048\n"
"v_mfma_f32_16x16x32_bf16 a[8:11], a[216:219], a[112:115], a[8:11]\n"
"ds_read_b128 a[112:115], v242 offset:4096\n"
"v_mfma_f32_16x16x32_bf16 a[8:11], a[220:223], a[116:119], a[8:11]\n"
"ds_read_b128 a[116:119], v243 offset:4096\n"
"v_mfma_f32_16x16x32_bf16 a[12:15], a[216:219], a[120:123], a[12:15]\n"
"ds_read_b128 a[120:123], v242 offset:6144\n"
"v_mfma_f32_16x16x32_bf16 a[12:15], a[220:223], a[124:127], a[12:15]\n"
"ds_read_b128 a[124:127], v243 offset:6144\n"
"v_mfma_f32_16x16x32_bf16 a[16:19], a[216:219], a[128:131], a[16:19]\n"
"ds_read_b128 a[128:131], v242 offset:8192\n"
"v_mfma_f32_16x16x32_bf16 a[16:19], a[220:223], a[132:135], a[16:19]\n"
"ds_read_b128 a[132:135], v243 offset:8192\n"
"v_mfma_f32_16x16x32_bf16 a[20:23], a[216:219], a[136:139], a[20:23]\n"
"ds_read_b128 a[136:139], v242 offset:10240\n"
"v_mfma_f32_16x16x32_bf16 a[20:23], a[220:223], a[140:143], a[20:23]\n"
"ds_read_b128 a[140:143], v243 offset:10240\n"
"v_mfma_f32_16x16x32_bf16 a[24:27], a[216:219], a[144:147], a[24:27]\n"
"ds_read_b128 a[144:147], v242 offset:12288\n"
"v_mfma_f32_16x16x32_bf16 a[24:27], a[220:223], a[148:151], a[24:27]\n"
"ds_read_b128 a[148:151], v243 offset:12288\n"
"v_mfma_f32_16x16x32_bf16 a[28:31], a[216:219], a[152:155], a[28:31]\n"
"ds_read_b128 a[152:155], v242 offset:14336\n"
"v_mfma_f32_16x16x32_bf16 a[28:31], a[220:223], a[156:159], a[28:31]\n"
"ds_read_b128 a[156:159], v243 offset:14336\n"
"s_waitcnt lgkmcnt(14)\n"
"v_mfma_f32_16x16x32_bf16 v[0:3], a[160:163], a[96:99], v[0:3]\n"
"s_mul_i32 s45, s50, -49152\n"
"s_add_u32 s45, s45, 131072\n"
"s_add_u32 s45, s45, s42\n"
"v_mfma_f32_16x16x32_bf16 v[0:3], a[164:167], a[100:103], v[0:3]\n"
"s_waitcnt lgkmcnt(12)\n"
"v_mfma_f32_16x16x32_bf16 v[4:7], a[160:163], a[104:107], v[4:7]\n"
"s_add_u32 m0, s45, 0\n"
"v_add_u32 v238, 640, v248\n"
"v_mfma_f32_16x16x32_bf16 v[4:7], a[164:167], a[108:111], v[4:7]\n"
"global_load_lds_dwordx4 v238, s[20:21] offset:0\n"
"s_waitcnt lgkmcnt(10)\n"
"v_mfma_f32_16x16x32_bf16 v[8:11], a[160:163], a[112:115], v[8:11]\n"
"v_mfma_f32_16x16x32_bf16 v[8:11], a[164:167], a[116:119], v[8:11]\n"
"s_waitcnt lgkmcnt(8)\n"
"v_mfma_f32_16x16x32_bf16 v[12:15], a[160:163], a[120:123], v[12:15]\n"
"v_add_u32 v238, s66, v238\n"
"global_load_lds_dwordx4 v238, s[20:21] offset:1024\n"
"s_waitcnt vmcnt(14)\n"
"s_barrier\n"
"s_mov_b64 exec, 1\n"
"global_atomic_add v250, v252, s[38:39] offset:0 sc1\n"
"s_mov_b64 exec, -1\n"
"v_mfma_f32_16x16x32_bf16 v[12:15], a[164:167], a[124:127], v[12:15]\n"
"s_waitcnt lgkmcnt(6)\n"
"v_mfma_f32_16x16x32_bf16 v[16:19], a[160:163], a[128:131], v[16:19]\n"
"v_mfma_f32_16x16x32_bf16 v[16:19], a[164:167], a[132:135], v[16:19]\n"
"v_add_u32 v238, s66, v238\n"
"s_waitcnt lgkmcnt(4)\n"
"v_mfma_f32_16x16x32_bf16 v[20:23], a[160:163], a[136:139], v[20:23]\n"
"global_load_lds_dwordx4 v238, s[20:21] offset:2048\n"
"v_mfma_f32_16x16x32_bf16 v[20:23], a[164:167], a[140:143], v[20:23]\n"
"s_waitcnt lgkmcnt(2)\n"
"v_mfma_f32_16x16x32_bf16 v[24:27], a[160:163], a[144:147], v[24:27]\n"
"v_mfma_f32_16x16x32_bf16 v[24:27], a[164:167], a[148:151], v[24:27]\n"
"v_add_u32 v238, s66, v238\n"
"s_waitcnt lgkmcnt(0)\n"
"v_mfma_f32_16x16x32_bf16 v[28:31], a[160:163], a[152:155], v[28:31]\n"
"global_load_lds_dwordx4 v238, s[20:21] offset:3072\n"
"v_mfma_f32_16x16x32_bf16 v[28:31], a[164:167], a[156:159], v[28:31]\n"
"v_mfma_f32_16x16x32_bf16 v[32:35], a[168:171], a[96:99], v[32:35]\n"
"v_mfma_f32_16x16x32_bf16 v[32:35], a[172:175], a[100:103], v[32:35]\n"
"s_add_u32 m0, s55, 8192\n"
"v_add_u32 v238, 1441792, v251\n"
"v_mfma_f32_16x16x32_bf16 v[36:39], a[168:171], a[104:107], v[36:39]\n"
"global_load_lds_dwordx4 v238, s[58:59] offset:0\n"
"v_mfma_f32_16x16x32_bf16 v[36:39], a[172:175], a[108:111], v[36:39]\n"
"v_mfma_f32_16x16x32_bf16 v[40:43], a[168:171], a[112:115], v[40:43]\n"
"v_mfma_f32_16x16x32_bf16 v[40:43], a[172:175], a[116:119], v[40:43]\n"
"v_add_u32 v238, s66, v238\n"
"global_load_lds_dwordx4 v238, s[58:59] offset:1024\n"
"v_mfma_f32_16x16x32_bf16 v[44:47], a[168:171], a[120:123], v[44:47]\n"
"v_mfma_f32_16x16x32_bf16 v[44:47], a[172:175], a[124:127], v[44:47]\n"
"v_mfma_f32_16x16x32_bf16 v[48:51], a[168:171], a[128:131], v[48:51]\n"
"v_add_u32 v238, s66, v238\n"
"global_load_lds_dwordx4 v238, s[58:59] offset:2048\n"
"v_mfma_f32_16x16x32_bf16 v[48:51], a[172:175], a[132:135], v[48:51]\n"
"v_mfma_f32_16x16x32_bf16 v[52:55], a[168:171], a[136:139], v[52:55]\n"
"v_mfma_f32_16x16x32_bf16 v[52:55], a[172:175], a[140:143], v[52:55]\n"
"v_add_u32 v238, s66, v238\n"
"global_load_lds_dwordx4 v238, s[58:59] offset:3072\n"
"v_mfma_f32_16x16x32_bf16 v[56:59], a[168:171], a[144:147], v[56:59]\n"
"v_mfma_f32_16x16x32_bf16 v[56:59], a[172:175], a[148:151], v[56:59]\n"
"v_mfma_f32_16x16x32_bf16 v[60:63], a[168:171], a[152:155], v[60:63]\n"
"s_add_u32 m0, s55, 12288\n"
"v_add_u32 v238, 1507328, v251\n"
"v_mfma_f32_16x16x32_bf16 v[60:63], a[172:175], a[156:159], v[60:63]\n"
"global_load_lds_dwordx4 v238, s[58:59] offset:0\n"
"v_mfma_f32_16x16x32_bf16 v[64:67], a[176:179], a[96:99], v[64:67]\n"
"v_mfma_f32_16x16x32_bf16 v[64:67], a[180:183], a[100:103], v[64:67]\n"
"v_mfma_f32_16x16x32_bf16 v[68:71], a[176:179], a[104:107], v[68:71]\n"
"s_mul_i32 s48, s49, -98304\n"
"s_add_u32 s48, s48, 147456\n"
"v_mfma_f32_16x16x32_bf16 v[68:71], a[180:183], a[108:111], v[68:71]\n"
"v_add_u32 v242, s48, v240\n"
"v_add_u32 v238, s66, v238\n"
"v_mfma_f32_16x16x32_bf16 v[72:75], a[176:179], a[112:115], v[72:75]\n"
"v_add_u32 v243, s48, v241\n"
"global_load_lds_dwordx4 v238, s[58:59] offset:1024\n"
"v_mfma_f32_16x16x32_bf16 v[72:75], a[180:183], a[116:119], v[72:75]\n"
"v_mfma_f32_16x16x32_bf16 v[76:79], a[176:179], a[120:123], v[76:79]\n"
"v_mfma_f32_16x16x32_bf16 v[76:79], a[180:183], a[124:127], v[76:79]\n"
"v_add_u32 v238, s66, v238\n"
"global_load_lds_dwordx4 v238, s[58:59] offset:2048\n"
"v_mfma_f32_16x16x32_bf16 v[80:83], a[176:179], a[128:131], v[80:83]\n"
"v_mfma_f32_16x16x32_bf16 v[80:83], a[180:183], a[132:135], v[80:83]\n"
"v_mfma_f32_16x16x32_bf16 v[84:87], a[176:179], a[136:139], v[84:87]\n"
"v_add_u32 v238, s66, v238\n"
"global_load_lds_dwordx4 v238, s[58:59] offset:3072\n"
"v_mfma_f32_16x16x32_bf16 v[84:87], a[180:183], a[140:143], v[84:87]\n"
"s_waitcnt vmcnt(13)\n"
"ds_read_b64_tr_b16 a[192:193], v232 offset:0\n"
"v_mfma_f32_16x16x32_bf16 v[88:91], a[176:179], a[144:147], v[88:91]\n"
"ds_read_b64_tr_b16 a[194:195], v232 offset:512\n"
"v_mfma_f32_16x16x32_bf16 v[88:91], a[180:183], a[148:151], v[88:91]\n"
"ds_read_b64_tr_b16 a[196:197], v232 offset:4096\n"
"v_mfma_f32_16x16x32_bf16 v[92:95], a[176:179], a[152:155], v[92:95]\n"
"ds_read_b64_tr_b16 a[198:199], v232 offset:4608\n"
"v_mfma_f32_16x16x32_bf16 v[92:95], a[180:183], a[156:159], v[92:95]\n"
"ds_read_b64_tr_b16 a[200:201], v232 offset:8\n"
"v_mfma_f32_16x16x32_bf16 v[96:99], a[184:187], a[96:99], v[96:99]\n"
"ds_read_b64_tr_b16 a[202:203], v232 offset:520\n"
"v_mfma_f32_16x16x32_bf16 v[96:99], a[188:191], a[100:103], v[96:99]\n"
"ds_read_b64_tr_b16 a[204:205], v232 offset:4104\n"
"v_mfma_f32_16x16x32_bf16 v[100:103], a[184:187], a[104:107], v[100:103]\n"
"ds_read_b64_tr_b16 a[206:207], v232 offset:4616\n"
"v_mfma_f32_16x16x32_bf16 v[100:103], a[188:191], a[108:111], v[100:103]\n"
"ds_read_b64_tr_b16 a[208:209], v254 offset:0\n"
"v_mfma_f32_16x16x32_bf16 v[104:107], a[184:187], a[112:115], v[104:107]\n"
"ds_read_b64_tr_b16 a[210:211], v254 offset:512\n"
"v_mfma_f32_16x16x32_bf16 v[104:107], a[188:191], a[116:119], v[104:107]\n"
"ds_read_b64_tr_b16 a[212:213], v254 offset:4096\n"
"v_mfma_f32_16x16x32_bf16 v[108:111], a[184:187], a[120:123], v[108:111]\n"
"ds_read_b64_tr_b16 a[214:215], v254 offset:4608\n"
"v_mfma_f32_16x16x32_bf16 v[108:111], a[188:191], a[124:127], v[108:111]\n"
"ds_read_b64_tr_b16 a[216:217], v254 offset:8\n"
"v_mfma_f32_16x16x32_bf16 v[112:115], a[184:187], a[128:131], v[112:115]\n"
"ds_read_b64_tr_b16 a[218:219], v254 offset:520\n"
"v_mfma_f32_16x16x32_bf16 v[112:115], a[188:191], a[132:135], v[112:115]\n"
"ds_read_b64_tr_b16 a[220:221], v254 offset:4104\n"
"v_mfma_f32_16x16x32_bf16 v[116:119], a[184:187], a[136:139], v[116:119]\n"
"ds_read_b64_tr_b16 a[222:223], v254 offset:4616\n"
"v_mfma_f32_16x16x32_bf16 v[116:119], a[188:191], a[140:143], v[116:119]\n"
"v_mfma_f32_16x16x32_bf16 v[120:123], a[184:187], a[144:147], v[120:123]\n"
"v_mfma_f32_16x16x32_bf16 v[120:123], a[188:191], a[148:151], v[120:123]\n"
"s_waitcnt lgkmcnt(0)\n"
"s_add_u32 m0, s55, 0\n"
"v_add_u32 v238, 1441920, v251\n"
"v_mfma_f32_16x16x32_bf16 v[124:127], a[184:187], a[152:155], v[124:127]\n"
"global_load_lds_dwordx4 v238, s[58:59] offset:0\n"
"v_mfma_f32_16x16x32_bf16 v[124:127], a[188:191], a[156:159], v[124:127]\n"
"v_mfma_f32_16x16x32_bf16 v[128:131], a[192:195], a[96:99], v[128:131]\n"
"v_mfma_f32_16x16x32_bf16 v[128:131], a[196:199], a[100:103], v[128:131]\n"
"v_add_u32 v238, s66, v238\n"
"global_load_lds_dwordx4 v238, s[58:59] offset:1024\n"
"v_mfma_f32_16x16x32_bf16 v[132:135], a[192:195], a[104:107], v[132:135]\n"
"v_mfma_f32_16x16x32_bf16 v[132:135], a[196:199], a[108:111], v[132:135]\n"
"v_mfma_f32_16x16x32_bf16 v[136:139], a[192:195], a[112:115], v[136:139]\n"
"v_add_u32 v238, s66, v238\n"
"global_load_lds_dwordx4 v238, s[58:59] offset:2048\n"
"v_mfma_f32_16x16x32_bf16 v[136:139], a[196:199], a[116:119], v[136:139]\n"
"v_mfma_f32_16x16x32_bf16 v[140:143], a[192:195], a[120:123], v[140:143]\n"
"v_mfma_f32_16x16x32_bf16 v[140:143], a[196:199], a[124:127], v[140:143]\n"
"v_add_u32 v238, s66, v238\n"
"global_load_lds_dwordx4 v238, s[58:59] offset:3072\n"
"v_mfma_f32_16x16x32_bf16 v[144:147], a[192:195], a[128:131], v[144:147]\n"
"v_mfma_f32_16x16x32_bf16 v[144:147], a[196:199], a[132:135], v[144:147]\n"
"v_mfma_f32_16x16x32_bf16 v[148:151], a[192:195], a[136:139], v[148:151]\n"
"s_add_u32 m0, s55, 4096\n"
"v_add_u32 v238, 1507456, v251\n"
"v_mfma_f32_16x16x32_bf16 v[148:151], a[196:199], a[140:143], v[148:151]\n"
"global_load_lds_dwordx4 v238, s[58:59] offset:0\n"
"v_mfma_f32_16x16x32_bf16 v[152:155], a[192:195], a[144:147], v[152:155]\n"
"v_mfma_f32_16x16x32_bf16 v[152:155], a[196:199], a[148:151], v[152:155]\n"
"v_mfma_f32_16x16x32_bf16 v[156:159], a[192:195], a[152:155], v[156:159]\n"
"v_add_u32 v238, s66, v238\n"
"global_load_lds_dwordx4 v238, s[58:59] offset:1024\n"
"v_mfma_f32_16x16x32_bf16 v[156:159], a[196:199], a[156:159], v[156:159]\n"
"v_mfma_f32_16x16x32_bf16 v[160:163], a[200:203], a[96:99], v[160:163]\n"
"v_mfma_f32_16x16x32_bf16 v[160:163], a[204:207], a[100:103], v[160:163]\n"
"v_add_u32 v238, s66, v238\n"
"global_load_lds_dwordx4 v238, s[58:59] offset:2048\n"
"v_mfma_f32_16x16x32_bf16 v[164:167], a[200:203], a[104:107], v[164:167]\n"
"v_mfma_f32_16x16x32_bf16 v[164:167], a[204:207], a[108:111], v[164:167]\n"
"v_mfma_f32_16x16x32_bf16 v[168:171], a[200:203], a[112:115], v[168:171]\n"
"v_add_u32 v238, s66, v238\n"
"global_load_lds_dwordx4 v238, s[58:59] offset:3072\n"
"v_mfma_f32_16x16x32_bf16 v[168:171], a[204:207], a[116:119], v[168:171]\n"
"global_load_dword v235, v228, s[36:37] offset:0 sc1\n"
"v_mfma_f32_16x16x32_bf16 v[172:175], a[200:203], a[120:123], v[172:175]\n"
"v_mfma_f32_16x16x32_bf16 v[172:175], a[204:207], a[124:127], v[172:175]\n"
"v_mfma_f32_16x16x32_bf16 v[176:179], a[200:203], a[128:131], v[176:179]\n"
"v_mfma_f32_16x16x32_bf16 v[176:179], a[204:207], a[132:135], v[176:179]\n"
"v_mfma_f32_16x16x32_bf16 v[180:183], a[200:203], a[136:139], v[180:183]\n"
"v_mfma_f32_16x16x32_bf16 v[180:183], a[204:207], a[140:143], v[180:183]\n"
"v_mfma_f32_16x16x32_bf16 v[184:187], a[200:203], a[144:147], v[184:187]\n"
"v_mfma_f32_16x16x32_bf16 v[184:187], a[204:207], a[148:151], v[184:187]\n"
"v_mfma_f32_16x16x32_bf16 v[188:191], a[200:203], a[152:155], v[188:191]\n"
"s_waitcnt vmcnt(9)\n"
"ds_read_b64_tr_b16 a[160:161], v232 offset:8192\n"
"v_mfma_f32_16x16x32_bf16 v[188:191], a[204:207], a[156:159], v[188:191]\n"
"ds_read_b64_tr_b16 a[162:163], v232 offset:8704\n"
"v_mfma_f32_16x16x32_bf16 v[192:195], a[208:211], a[96:99], v[192:195]\n"
"ds_read_b64_tr_b16 a[164:165], v232 offset:12288\n"
"v_mfma_f32_16x16x32_bf16 v[192:195], a[212:215], a[100:103], v[192:195]\n"
"ds_read_b64_tr_b16 a[166:167], v232 offset:12800\n"
"v_mfma_f32_16x16x32_bf16 v[196:199], a[208:211], a[104:107], v[196:199]\n"
"ds_read_b64_tr_b16 a[168:169], v232 offset:8200\n"
"v_mfma_f32_16x16x32_bf16 v[196:199], a[212:215], a[108:111], v[196:199]\n"
"ds_read_b64_tr_b16 a[170:171], v232 offset:8712\n"
"v_mfma_f32_16x16x32_bf16 v[200:203], a[208:211], a[112:115], v[200:203]\n"
"ds_read_b64_tr_b16 a[172:173], v232 offset:12296\n"
"v_mfma_f32_16x16x32_bf16 v[200:203], a[212:215], a[116:119], v[200:203]\n"
"ds_read_b64_tr_b16 a[174:175], v232 offset:12808\n"
"v_mfma_f32_16x16x32_bf16 v[204:207], a[208:211], a[120:123], v[204:207]\n"
"ds_read_b64_tr_b16 a[176:177], v254 offset:8192\n"
"v_mfma_f32_16x16x32_bf16 v[204:207], a[212:215], a[124:127], v[204:207]\n"
"ds_read_b64_tr_b16 a[178:179], v254 offset:8704\n"
"v_mfma_f32_16x16x32_bf16 v[208:211], a[208:211], a[128:131], v[208:211]\n"
"ds_read_b64_tr_b16 a[180:181], v254 offset:12288\n"
"v_mfma_f32_16x16x32_bf16 v[208:211], a[212:215], a[132:135], v[208:211]\n"
"ds_read_b64_tr_b16 a[182:183], v254 offset:12800\n"
"v_mfma_f32_16x16x32_bf16 v[212:215], a[208:211], a[136:139], v[212:215]\n"
"ds_read_b64_tr_b16 a[184:185], v254 offset:8200\n"
"v_mfma_f32_16x16x32_bf16 v[212:215], a[212:215], a[140:143], v[212:215]\n"
"ds_read_b64_tr_b16 a[186:187], v254 offset:8712\n"
"v_mfma_f32_16x16x32_bf16 v[216:219], a[208:211], a[144:147], v[216:219]\n"
"ds_read_b64_tr_b16 a[188:189], v254 offset:12296\n"
"v_mfma_f32_16x16x32_bf16 v[216:219], a[212:215], a[148:151], v[216:219]\n"
"ds_read_b64_tr_b16 a[190:191], v254 offset:12808\n"
"s_barrier\n"
"v_mfma_f32_16x16x32_bf16 v[220:223], a[208:211], a[152:155], v[220:223]\n"
"v_mfma_f32_16x16x32_bf16 v[220:223], a[212:215], a[156:159], v[220:223]\n"
"v_mfma_f32_16x16x32_bf16 a[0:3], a[216:219], a[96:99], a[0:3]\n"
"ds_read_b128 a[96:99], v242 offset:0\n"
"v_mfma_f32_16x16x32_bf16 a[0:3], a[220:223], a[100:103], a[0:3]\n"
"ds_read_b128 a[100:103], v243 offset:0\n"
"v_mfma_f32_16x16x32_bf16 a[4:7], a[216:219], a[104:107], a[4:7]\n"
"ds_read_b128 a[104:107], v242 offset:2048\n"
"v_mfma_f32_16x16x32_bf16 a[4:7], a[220:223], a[108:111], a[4:7]\n"
"ds_read_b128 a[108:111], v243 offset:2048\n"
"v_mfma_f32_16x16x32_bf16 a[8:11], a[216:219], a[112:115], a[8:11]\n"
"ds_read_b128 a[112:115], v242 offset:4096\n"
"v_mfma_f32_16x16x32_bf16 a[8:11], a[220:223], a[116:119], a[8:11]\n"
"ds_read_b128 a[116:119], v243 offset:4096\n"
"v_mfma_f32_16x16x32_bf16 a[12:15], a[216:219], a[120:123], a[12:15]\n"
"ds_read_b128 a[120:123], v242 offset:6144\n"
"v_mfma_f32_16x16x32_bf16 a[12:15], a[220:223], a[124:127], a[12:15]\n"
"ds_read_b128 a[124:127], v243 offset:6144\n"
"v_mfma_f32_16x16x32_bf16 a[16:19], a[216:219], a[128:131], a[16:19]\n"
"ds_read_b128 a[128:131], v242 offset:8192\n"
"v_mfma_f32_16x16x32_bf16 a[16:19], a[220:223], a[132:135], a[16:19]\n"
"ds_read_b128 a[132:135], v243 offset:8192\n"
"v_mfma_f32_16x16x32_bf16 a[20:23], a[216:219], a[136:139], a[20:23]\n"
"ds_read_b128 a[136:139], v242 offset:10240\n"
"v_mfma_f32_16x16x32_bf16 a[20:23], a[220:223], a[140:143], a[20:23]\n"
"ds_read_b128 a[140:143], v243 offset:10240\n"
"v_mfma_f32_16x16x32_bf16 a[24:27], a[216:219], a[144:147], a[24:27]\n"
"ds_read_b128 a[144:147], v242 offset:12288\n"
"v_mfma_f32_16x16x32_bf16 a[24:27], a[220:223], a[148:151], a[24:27]\n"
"ds_read_b128 a[148:151], v243 offset:12288\n"
"v_mfma_f32_16x16x32_bf16 a[28:31], a[216:219], a[152:155], a[28:31]\n"
"ds_read_b128 a[152:155], v242 offset:14336\n"
"v_mfma_f32_16x16x32_bf16 a[28:31], a[220:223], a[156:159], a[28:31]\n"
"ds_read_b128 a[156:159], v243 offset:14336\n"
"s_waitcnt lgkmcnt(14)\n"
"v_mfma_f32_16x16x32_bf16 v[0:3], a[160:163], a[96:99], v[0:3]\n"
"v_mfma_f32_16x16x32_bf16 v[0:3], a[164:167], a[100:103], v[0:3]\n"
"s_waitcnt lgkmcnt(12)\n"
"v_mfma_f32_16x16x32_bf16 v[4:7], a[160:163], a[104:107], v[4:7]\n"
"v_mfma_f32_16x16x32_bf16 v[4:7], a[164:167], a[108:111], v[4:7]\n"
"s_waitcnt lgkmcnt(10)\n"
"v_mfma_f32_16x16x32_bf16 v[8:11], a[160:163], a[112:115], v[8:11]\n"
"v_mfma_f32_16x16x32_bf16 v[8:11], a[164:167], a[116:119], v[8:11]\n"
"s_waitcnt lgkmcnt(8)\n"
"v_mfma_f32_16x16x32_bf16 v[12:15], a[160:163], a[120:123], v[12:15]\n"
"v_mfma_f32_16x16x32_bf16 v[12:15], a[164:167], a[124:127], v[12:15]\n"
"s_waitcnt lgkmcnt(6)\n"
"v_mfma_f32_16x16x32_bf16 v[16:19], a[160:163], a[128:131], v[16:19]\n"
"v_mfma_f32_16x16x32_bf16 v[16:19], a[164:167], a[132:135], v[16:19]\n"
"s_waitcnt lgkmcnt(4)\n"
"v_mfma_f32_16x16x32_bf16 v[20:23], a[160:163], a[136:139], v[20:23]\n"
"s_waitcnt vmcnt(0)\n"
"v_cmp_ne_u32 vcc, v235, v230\n"
"s_nop 0\n"
"s_cmp_eq_u64 vcc, -1\n"
"s_cbranch_scc0 .Lwslow0_%=\n"
".Lwback0_%=:\n"
"v_mfma_f32_16x16x32_bf16 v[20:23], a[164:167], a[140:143], v[20:23]\n"
"s_waitcnt lgkmcnt(2)\n"
"v_mfma_f32_16x16x32_bf16 v[24:27], a[160:163], a[144:147], v[24:27]\n"
"v_mfma_f32_16x16x32_bf16 v[24:27], a[164:167], a[148:151], v[24:27]\n"
"s_waitcnt lgkmcnt(0)\n"
"v_mfma_f32_16x16x32_bf16 v[28:31], a[160:163], a[152:155], v[28:31]\n"
"global_load_dwordx4 a[160:163], v246, s[16:17] offset:-4096 sc1\n"
"v_mfma_f32_16x16x32_bf16 v[28:31], a[164:167], a[156:159], v[28:31]\n"
"v_mfma_f32_16x16x32_bf16 v[32:35], a[168:171], a[96:99], v[32:35]\n"
"global_load_dwordx4 a[164:167], v246, s[16:17] offset:-3072 sc1\n"
"v_mfma_f32_16x16x32_bf16 v[32:35], a[172:175], a[100:103], v[32:35]\n"
"v_mfma_f32_16x16x32_bf16 v[36:39], a[168:171], a[104:107], v[36:39]\n"
"s_mul_i32 s45, s49, -49152\n"
"s_add_u32 s45, s45, 131072\n"
"s_add_u32 s45, s45, s42\n"
"v_mfma_f32_16x16x32_bf16 v[36:39], a[172:175], a[108:111], v[36:39]\n"
"s_add_u32 m0, s45, 0\n"
"v_add_u32 v238, 1664, v248\n"
"v_mfma_f32_16x16x32_bf16 v[40:43], a[168:171], a[112:115], v[40:43]\n"
"global_load_lds_dwordx4 v238, s[20:21] offset:0\n"
"v_mfma_f32_16x16x32_bf16 v[40:43], a[172:175], a[116:119], v[40:43]\n"
"v_mfma_f32_16x16x32_bf16 v[44:47], a[168:171], a[120:123], v[44:47]\n"
"v_mfma_f32_16x16x32_bf16 v[44:47], a[172:175], a[124:127], v[44:47]\n"
"v_add_u32 v238, s66, v238\n"
"global_load_lds_dwordx4 v238, s[20:21] offset:1024\n"
"v_mfma_f32_16x16x32_bf16 v[48:51], a[168:171], a[128:131], v[48:51]\n"
"v_mfma_f32_16x16x32_bf16 v[48:51], a[172:175], a[132:135], v[48:51]\n"
"v_mfma_f32_16x16x32_bf16 v[52:55], a[168:171], a[136:139], v[52:55]\n"
"v_add_u32 v238, s66, v238\n"
"global_load_lds_dwordx4 v238, s[20:21] offset:2048\n"
"v_mfma_f32_16x16x32_bf16 v[52:55], a[172:175], a[140:143], v[52:55]\n"
"v_mfma_f32_16x16x32_bf16 v[56:59], a[168:171], a[144:147], v[56:59]\n"
"v_mfma_f32_16x16x32_bf16 v[56:59], a[172:175], a[148:151], v[56:59]\n"
"v_add_u32 v238, s66, v238\n"
"global_load_lds_dwordx4 v238, s[20:21] offset:3072\n"
"v_mfma_f32_16x16x32_bf16 v[60:63], a[168:171], a[152:155], v[60:63]\n"
"global_load_dwordx4 a[168:171], v246, s[16:17] offset:-2048 sc1\n"
"v_mfma_f32_16x16x32_bf16 v[60:63], a[172:175], a[156:159], v[60:63]\n"
"v_mfma_f32_16x16x32_bf16 v[64:67], a[176:179], a[96:99], v[64:67]\n"
"global_load_dwordx4 a[172:175], v246, s[16:17] offset:-1024 sc1\n"
"v_mfma_f32_16x16x32_bf16 v[64:67], a[180:183], a[100:103], v[64:67]\n"
"v_mfma_f32_16x16x32_bf16 v[68:71], a[176:179], a[104:107], v[68:71]\n"
"s_mul_i32 s48, s50, -49152\n"
"s_add_u32 s48, s48, 131072\n"
"v_mfma_f32_16x16x32_bf16 v[68:71], a[180:183], a[108:111], v[68:71]\n"
"v_add_u32 v242, s48, v240\n"
"v_mfma_f32_16x16x32_bf16 v[72:75], a[176:179], a[112:115], v[72:75]\n"
"v_add_u32 v243, s48, v241\n"
"v_mfma_f32_16x16x32_bf16 v[72:75], a[180:183], a[116:119], v[72:75]\n"
"v_mfma_f32_16x16x32_bf16 v[76:79], a[176:179], a[120:123], v[76:79]\n"
"v_mfma_f32_16x16x32_bf16 v[76:79], a[180:183], a[124:127], v[76:79]\n"
"v_mfma_f32_16x16x32_bf16 v[80:83], a[176:179], a[128:131], v[80:83]\n"
"v_mfma_f32_16x16x32_bf16 v[80:83], a[180:183], a[132:135], v[80:83]\n"
"v_mfma_f32_16x16x32_bf16 v[84:87], a[176:179], a[136:139], v[84:87]\n"
"ds_read_b64_tr_b16 a[192:193], v232 offset:0\n"
"v_mfma_f32_16x16x32_bf16 v[84:87], a[180:183], a[140:143], v[84:87]\n"
"ds_read_b64_tr_b16 a[194:195], v232 offset:512\n"
"v_mfma_f32_16x16x32_bf16 v[88:91], a[176:179], a[144:147], v[88:91]\n"
"s_nop 0\n"
".Lpg2_%=:\n"
"s_cbranch_execz .Lpg6_%=\n"
"ds_read_b64_tr_b16 a[196:197], v232 offset:4096\n"
"v_mfma_f32_16x16x32_bf16 v[88:91], a[180:183], a[148:151], v[88:91]\n"
"ds_read_b64_tr_b16 a[198:199], v232 offset:4608\n"
"v_mfma_f32_16x16x32_bf16 v[92:95], a[176:179], a[152:155], v[92:95]\n"
"global_load_dwordx4 a[176:179], v246, s[16:17] offset:0 sc1\n"
"ds_read_b64_tr_b16 a[200:201], v232 offset:8\n"
"v_mfma_f32_16x16x32_bf16 v[92:95], a[180:183], a[156:159], v[92:95]\n"
"ds_read_b64_tr_b16 a[202:203], v232 offset:520\n"
"v_mfma_f32_16x16x32_bf16 v[96:99], a[184:187], a[96:99], v[96:99]\n"
"global_load_dwordx4 a[180:183], v246, s[16:17] offset:1024 sc1\n"
"ds_read_b64_tr_b16 a[204:205], v232 offset:4104\n"
"v_mfma_f32_16x16x32_bf16 v[96:99], a[188:191], a[100:103], v[96:99]\n"
"ds_read_b64_tr_b16 a[206:207], v232 offset:4616\n"
"v_mfma_f32_16x16x32_bf16 v[100:103], a[184:187], a[104:107], v[100:103]\n"
"ds_read_b64_tr_b16 a[208:209], v254 offset:0\n"
"v_mfma_f32_16x16x32_bf16 v[100:103], a[188:191], a[108:111], v[100:103]\n"
"ds_read_b64_tr_b16 a[210:211], v254 offset:512\n"
"v_mfma_f32_16x16x32_bf16 v[104:107], a[184:187], a[112:115], v[104:107]\n"
"ds_read_b64_tr_b16 a[212:213], v254 offset:4096\n"
"v_mfma_f32_16x16x32_bf16 v[104:107], a[188:191], a[116:119], v[104:107]\n"
"ds_read_b64_tr_b16 a[214:215], v254 offset:4608\n"
"v_mfma_f32_16x16x32_bf16 v[108:111], a[184:187], a[120:123], v[108:111]\n"
"ds_read_b64_tr_b16 a[216:217], v254 offset:8\n"
"v_mfma_f32_16x16x32_bf16 v[108:111], a[188:191], a[124:127], v[108:111]\n"
"ds_read_b64_tr_b16 a[218:219], v254 offset:520\n"
"v_mfma_f32_16x16x32_bf16 v[112:115], a[184:187], a[128:131], v[112:115]\n"
"ds_read_b64_tr_b16 a[220:221], v254 offset:4104\n"
"v_mfma_f32_16x16x32_bf16 v[112:115], a[188:191], a[132:135], v[112:115]\n"
"ds_read_b64_tr_b16 a[222:223], v254 offset:4616\n"
"v_mfma_f32_16x16x32_bf16 v[116:119], a[184:187], a[136:139], v[116:119]\n"
"v_mfma_f32_16x16x32_bf16 v[116:119], a[188:191], a[140:143], v[116:119]\n"
"v_mfma_f32_16x16x32_bf16 v[120:123], a[184:187], a[144:147], v[120:123]\n"
"v_mfma_f32_16x16x32_bf16 v[120:123], a[188:191], a[148:151], v[120:123]\n"
"v_mfma_f32_16x16x32_bf16 v[124:127], a[184:187], a[152:155], v[124:127]\n"
"global_load_dwordx4 a[184:187], v246, s[16:17] offset:2048 sc1\n"
"v_mfma_f32_16x16x32_bf16 v[124:127], a[188:191], a[156:159], v[124:127]\n"
"s_waitcnt lgkmcnt(12)\n"
"v_mfma_f32_16x16x32_bf16 v[128:131], a[192:195], a[96:99], v[128:131]\n"
"global_load_dwordx4 a[188:191], v246, s[16:17] offset:3072 sc1\n"
"v_add_u32 v246, 0x2000, v246\n"
"v_mfma_f32_16x16x32_bf16 v[128:131], a[196:199], a[100:103], v[128:131]\n"
"v_mfma_f32_16x16x32_bf16 v[132:135], a[192:195], a[104:107], v[132:135]\n"
"s_mul_i32 s45, s50, -32768\n"
"s_add_u32 s45, s45, 147456\n"
"s_add_u32 s45, s45, s42\n"
"v_mfma_f32_16x16x32_bf16 v[132:135], a[196:199], a[108:111], v[132:135]\n"
"s_add_u32 m0, s45, 0\n"
"v_add_u32 v238, 896, v248\n"
"v_mfma_f32_16x16x32_bf16 v[136:139], a[192:195], a[112:115], v[136:139]\n"
"s_and_b32 s46, s69, 3\n"
"global_load_lds_dwordx4 v238, s[20:21] offset:0\n"
"v_mfma_f32_16x16x32_bf16 v[136:139], a[196:199], a[116:119], v[136:139]\n"
"s_lshl_b32 s46, s46, 2\n"
"v_mfma_f32_16x16x32_bf16 v[140:143], a[192:195], a[120:123], v[140:143]\n"
"s_add_u32 s46, s46, s70\n"
"v_mfma_f32_16x16x32_bf16 v[140:143], a[196:199], a[124:127], v[140:143]\n"
"s_lshl_b32 s47, s69, 2\n"
"v_mfma_f32_16x16x32_bf16 v[144:147], a[192:195], a[128:131], v[144:147]\n"
"s_add_u32 s47, s47, %1\n"
"v_add_u32 v238, s66, v238\n"
"v_mfma_f32_16x16x32_bf16 v[144:147], a[196:199], a[132:135], v[144:147]\n"
"s_lshl_b32 s47, s47, 1\n"
"global_load_lds_dwordx4 v238, s[20:21] offset:1024\n"
"v_mfma_f32_16x16x32_bf16 v[148:151], a[192:195], a[136:139], v[148:151]\n"
"s_cmp_lt_u32 s69, 4\n"
"s_cselect_b32 s47, s46, s47\n"
"v_mfma_f32_16x16x32_bf16 v[148:151], a[196:199], a[140:143], v[148:151]\n"
"s_add_u32 s46, s46, 96\n"
"v_mfma_f32_16x16x32_bf16 v[152:155], a[192:195], a[144:147], v[152:155]\n"
"s_and_b32 s70, s69, 3\n"
"v_mfma_f32_16x16x32_bf16 v[152:155], a[196:199], a[148:151], v[152:155]\n"
"s_lshl_b32 s70, s70, 2\n"
"v_add_u32 v238, s66, v238\n"
"v_mfma_f32_16x16x32_bf16 v[156:159], a[192:195], a[152:155], v[156:159]\n"
"s_add_u32 s70, s70, s71\n"
"global_load_dwordx4 a[192:195], v246, s[16:17] offset:-4096 sc1\n"
"v_mfma_f32_16x16x32_bf16 v[156:159], a[196:199], a[156:159], v[156:159]\n"
"global_load_lds_dwordx4 v238, s[20:21] offset:2048\n"
"s_waitcnt lgkmcnt(8)\n"
"v_mfma_f32_16x16x32_bf16 v[160:163], a[200:203], a[96:99], v[160:163]\n"
"s_add_u32 s70, s70, 96\n"
"v_mfma_f32_16x16x32_bf16 v[160:163], a[204:207], a[100:103], v[160:163]\n"
"s_cmp_ge_u32 s69, 12\n"
"s_cselect_b32 s68, s46, s47\n"
"s_cselect_b32 s69, s70, s47\n"
"v_mfma_f32_16x16x32_bf16 v[164:167], a[200:203], a[104:107], v[164:167]\n"
"s_lshl_b32 s68, s68, 11\n"
"global_load_dwordx4 a[196:199], v246, s[16:17] offset:-3072 sc1\n"
"v_mfma_f32_16x16x32_bf16 v[164:167], a[204:207], a[108:111], v[164:167]\n"
"s_add_u32 s68, s68, s63\n"
"v_add_u32 v238, s66, v238\n"
"v_mfma_f32_16x16x32_bf16 v[168:171], a[200:203], a[112:115], v[168:171]\n"
"s_add_u32 s68, s68, 262144\n"
"global_load_lds_dwordx4 v238, s[20:21] offset:3072\n"
"v_mfma_f32_16x16x32_bf16 v[168:171], a[204:207], a[116:119], v[168:171]\n"
"v_lshl_add_u32 v226, v249, 4, s68\n"
"v_mfma_f32_16x16x32_bf16 v[172:175], a[200:203], a[120:123], v[172:175]\n"
"s_lshl_b32 s69, s69, 11\n"
"v_mfma_f32_16x16x32_bf16 v[172:175], a[204:207], a[124:127], v[172:175]\n"
"s_add_u32 s69, s69, s63\n"
"v_mfma_f32_16x16x32_bf16 v[176:179], a[200:203], a[128:131], v[176:179]\n"
"s_add_u32 s69, s69, 524288\n"
"v_mfma_f32_16x16x32_bf16 v[176:179], a[204:207], a[132:135], v[176:179]\n"
"v_lshl_add_u32 v227, v249, 4, s69\n"
"v_mfma_f32_16x16x32_bf16 v[180:183], a[200:203], a[136:139], v[180:183]\n"
"v_add_u32 v244, s55, v240\n"
"v_mfma_f32_16x16x32_bf16 v[180:183], a[204:207], a[140:143], v[180:183]\n"
"v_add_u32 v245, s55, v241\n"
"v_mfma_f32_16x16x32_bf16 v[184:187], a[200:203], a[144:147], v[184:187]\n"
"v_lshl_add_u32 v247, v249, 4, s42\n"
"v_mfma_f32_16x16x32_bf16 v[184:187], a[204:207], a[148:151], v[184:187]\n"
"s_lshl_b32 s46, s62, 18\n"
"v_mfma_f32_16x16x32_bf16 v[188:191], a[200:203], a[152:155], v[188:191]\n"
"s_lshl_b32 s47, s49, 10\n"
"global_load_dwordx4 a[200:203], v246, s[16:17] offset:-2048 sc1\n"
"v_mfma_f32_16x16x32_bf16 v[188:191], a[204:207], a[156:159], v[188:191]\n"
"s_waitcnt lgkmcnt(4)\n"
"v_mfma_f32_16x16x32_bf16 v[192:195], a[208:211], a[96:99], v[192:195]\n"
"s_add_u32 s46, s46, s47\n"
"v_mfma_f32_16x16x32_bf16 v[192:195], a[212:215], a[100:103], v[192:195]\n"
"s_lshl_b32 s47, %1, 8\n"
"global_load_dwordx4 a[204:207], v246, s[16:17] offset:-1024 sc1\n"
"v_mfma_f32_16x16x32_bf16 v[196:199], a[208:211], a[104:107], v[196:199]\n"
"s_add_u32 s47, s47, s46\n"
"v_mfma_f32_16x16x32_bf16 v[196:199], a[212:215], a[108:111], v[196:199]\n"
"s_add_u32 s28, s10, s47\n"
"s_addc_u32 s29, s11, 0\n"
"v_mfma_f32_16x16x32_bf16 v[200:203], a[208:211], a[112:115], v[200:203]\n"
"s_add_u32 s47, s47, 128\n"
"v_mfma_f32_16x16x32_bf16 v[200:203], a[212:215], a[116:119], v[200:203]\n"
"s_add_u32 s22, s4, s47\n"
"s_addc_u32 s23, s5, 0\n"
"v_mfma_f32_16x16x32_bf16 v[204:207], a[208:211], a[120:123], v[204:207]\n"
"s_lshl_b32 s47, %1, 16\n"
"v_mfma_f32_16x16x32_bf16 v[204:207], a[212:215], a[124:127], v[204:207]\n"
"s_add_u32 s46, s46, s47\n"
"v_mfma_f32_16x16x32_bf16 v[208:211], a[208:211], a[128:131], v[208:211]\n"
"s_add_u32 s24, s10, s46\n"
"s_addc_u32 s25, s11, 0\n"
"v_mfma_f32_16x16x32_bf16 v[208:211], a[212:215], a[132:135], v[208:211]\n"
"s_xor_b32 s46, s46, 0x400\n"
"v_mfma_f32_16x16x32_bf16 v[212:215], a[208:211], a[136:139], v[212:215]\n"
"s_add_u32 s26, s10, s46\n"
"s_addc_u32 s27, s11, 0\n"
"v_mfma_f32_16x16x32_bf16 v[212:215], a[212:215], a[140:143], v[212:215]\n"
"s_add_u32 s51, s42, 131072\n"
"v_mfma_f32_16x16x32_bf16 v[216:219], a[208:211], a[144:147], v[216:219]\n"
"s_lshl_b32 s46, %1, 7\n"
"v_mfma_f32_16x16x32_bf16 v[216:219], a[212:215], a[148:151], v[216:219]\n"
"s_add_u32 s60, s60, s46\n"
"s_addc_u32 s61, s61, 0\n"
"s_waitcnt lgkmcnt(0)\n"
"s_barrier\n"
"s_mov_b64 exec, 1\n"
"global_atomic_add v250, v252, s[38:39] offset:0 sc1\n"
"s_mov_b64 exec, -1\n"
"v_mfma_f32_16x16x32_bf16 v[220:223], a[208:211], a[152:155], v[220:223]\n"
"global_load_dwordx4 a[208:211], v246, s[16:17] offset:0 sc1\n"
"v_mfma_f32_16x16x32_bf16 v[220:223], a[212:215], a[156:159], v[220:223]\n"
"v_mfma_f32_16x16x32_bf16 a[0:3], a[216:219], a[96:99], a[0:3]\n"
"ds_read_b128 a[96:99], v242 offset:0\n"
"v_mfma_f32_16x16x32_bf16 a[0:3], a[220:223], a[100:103], a[0:3]\n"
"ds_read_b128 a[100:103], v243 offset:0\n"
"global_load_dwordx4 a[212:215], v246, s[16:17] offset:1024 sc1\n"
"v_mfma_f32_16x16x32_bf16 a[4:7], a[216:219], a[104:107], a[4:7]\n"
"ds_read_b128 a[104:107], v242 offset:2048\n"
"v_mfma_f32_16x16x32_bf16 a[4:7], a[220:223], a[108:111], a[4:7]\n"
"ds_read_b128 a[108:111], v243 offset:2048\n"
"v_mfma_f32_16x16x32_bf16 a[8:11], a[216:219], a[112:115], a[8:11]\n"
"ds_read_b128 a[112:115], v242 offset:4096\n"
"v_mfma_f32_16x16x32_bf16 a[8:11], a[220:223], a[116:119], a[8:11]\n"
"ds_read_b128 a[116:119], v243 offset:4096\n"
"v_mfma_f32_16x16x32_bf16 a[12:15], a[216:219], a[120:123], a[12:15]\n"
"ds_read_b128 a[120:123], v242 offset:6144\n"
"v_mfma_f32_16x16x32_bf16 a[12:15], a[220:223], a[124:127], a[12:15]\n"
"ds_read_b128 a[124:127], v243 offset:6144\n"
"v_mfma_f32_16x16x32_bf16 a[16:19], a[216:219], a[128:131], a[16:19]\n"
"ds_read_b128 a[128:131], v242 offset:8192\n"
"v_mfma_f32_16x16x32_bf16 a[16:19], a[220:223], a[132:135], a[16:19]\n"
"ds_read_b128 a[132:135], v243 offset:8192\n"
"v_mfma_f32_16x16x32_bf16 a[20:23], a[216:219], a[136:139], a[20:23]\n"
"ds_read_b128 a[136:139], v242 offset:10240\n"
"v_mfma_f32_16x16x32_bf16 a[20:23], a[220:223], a[140:143], a[20:23]\n"
"ds_read_b128 a[140:143], v243 offset:10240\n"
"v_mfma_f32_16x16x32_bf16 a[24:27], a[216:219], a[144:147], a[24:27]\n"
"ds_read_b128 a[144:147], v242 offset:12288\n"
"v_mfma_f32_16x16x32_bf16 a[24:27], a[220:223], a[148:151], a[24:27]\n"
"ds_read_b128 a[148:151], v243 offset:12288\n"
"v_mfma_f32_16x16x32_bf16 a[28:31], a[216:219], a[152:155], a[28:31]\n"
"ds_read_b128 a[152:155], v242 offset:14336\n"
"global_load_dwordx4 a[216:219], v246, s[16:17] offset:2048 sc1\n"
"v_mfma_f32_16x16x32_bf16 a[28:31], a[220:223], a[156:159], a[28:31]\n"
"ds_read_b128 a[156:159], v243 offset:14336\n"
"s_waitcnt vmcnt(22) lgkmcnt(14)\n"
"v_mfma_f32_16x16x32_bf16 v[0:3], a[160:163], a[96:99], v[0:3]\n"
"v_mfma_f32_16x16x32_bf16 v[0:3], a[164:167], a[100:103], v[0:3]\n"
"global_load_dwordx4 a[220:223], v246, s[16:17] offset:3072 sc1\n"
"s_waitcnt lgkmcnt(12)\n"
"v_mfma_f32_16x16x32_bf16 v[4:7], a[160:163], a[104:107], v[4:7]\n"
"s_mul_i32 s45, s49, -32768\n"
"s_add_u32 s45, s45, 147456\n"
"s_add_u32 s45, s45, s42\n"
"v_mfma_f32_16x16x32_bf16 v[4:7], a[164:167], a[108:111], v[4:7]\n"
"v_add_u32 v246, 0x2000, v246\n"
"s_waitcnt lgkmcnt(10)\n"
"v_mfma_f32_16x16x32_bf16 v[8:11], a[160:163], a[112:115], v[8:11]\n"
"v_add_u32 v224, s43, v224\n"
"v_mfma_f32_16x16x32_bf16 v[8:11], a[164:167], a[116:119], v[8:11]\n"
"s_waitcnt lgkmcnt(8)\n"
"v_mfma_f32_16x16x32_bf16 v[12:15], a[160:163], a[120:123], v[12:15]\n"
"s_add_u32 m0, s45, 0\n"
"v_add_u32 v238, 1920, v248\n"
"v_mfma_f32_16x16x32_bf16 v[12:15], a[164:167], a[124:127], v[12:15]\n"
"global_load_lds_dwordx4 v238, s[20:21] offset:0\n"
"s_waitcnt lgkmcnt(6)\n"
"v_mfma_f32_16x16x32_bf16 v[16:19], a[160:163], a[128:131], v[16:19]\n"
"v_mov_b32 v251, 0\n"
"v_mfma_f32_16x16x32_bf16 v[16:19], a[164:167], a[132:135], v[16:19]\n"
"s_waitcnt lgkmcnt(4)\n"
"v_mfma_f32_16x16x32_bf16 v[20:23], a[160:163], a[136:139], v[20:23]\n"
"v_mov_b32 v254, 0\n"
"v_mfma_f32_16x16x32_bf16 v[20:23], a[164:167], a[140:143], v[20:23]\n"
"v_add_u32 v238, s66, v238\n"
"s_waitcnt lgkmcnt(2)\n"
"v_mfma_f32_16x16x32_bf16 v[24:27], a[160:163], a[144:147], v[24:27]\n"
"v_mov_b32 v255, 0\n"
"global_load_lds_dwordx4 v238, s[20:21] offset:1024\n"
"v_mfma_f32_16x16x32_bf16 v[24:27], a[164:167], a[148:151], v[24:27]\n"
"s_waitcnt lgkmcnt(0)\n"
"v_mfma_f32_16x16x32_bf16 v[28:31], a[160:163], a[152:155], v[28:31]\n"
"global_load_dwordx4 a[160:163], v246, s[16:17] offset:-4096 sc1\n"
"v_mfma_f32_16x16x32_bf16 v[28:31], a[164:167], a[156:159], v[28:31]\n"
"s_waitcnt vmcnt(20)\n"
"v_mfma_f32_16x16x32_bf16 v[32:35], a[168:171], a[96:99], v[32:35]\n"
"v_add_u32 v238, s66, v238\n"
"global_load_lds_dwordx4 v238, s[20:21] offset:2048\n"
"v_mfma_f32_16x16x32_bf16 v[32:35], a[172:175], a[100:103], v[32:35]\n"
"global_load_dwordx4 a[164:167], v246, s[16:17] offset:-3072 sc1\n"
"v_mfma_f32_16x16x32_bf16 v[36:39], a[168:171], a[104:107], v[36:39]\n"
"v_mfma_f32_16x16x32_bf16 v[36:39], a[172:175], a[108:111], v[36:39]\n"
"v_mfma_f32_16x16x32_bf16 v[40:43], a[168:171], a[112:115], v[40:43]\n"
"v_add_u32 v238, s66, v238\n"
"global_load_lds_dwordx4 v238, s[20:21] offset:3072\n"
"v_mfma_f32_16x16x32_bf16 v[40:43], a[172:175], a[116:119], v[40:43]\n"
"v_mfma_f32_16x16x32_bf16 v[44:47], a[168:171], a[120:123], v[44:47]\n"
"v_mfma_f32_16x16x32_bf16 v[44:47], a[172:175], a[124:127], v[44:47]\n"
"v_mfma_f32_16x16x32_bf16 v[48:51], a[168:171], a[128:131], v[48:51]\n"
"v_mfma_f32_16x16x32_bf16 v[48:51], a[172:175], a[132:135], v[48:51]\n"
"v_mfma_f32_16x16x32_bf16 v[52:55], a[168:171], a[136:139], v[52:55]\n"
"v_mfma_f32_16x16x32_bf16 v[52:55], a[172:175], a[140:143], v[52:55]\n"
"v_mfma_f32_16x16x32_bf16 v[56:59], a[168:171], a[144:147], v[56:59]\n"
"v_mfma_f32_16x16x32_bf16 v[56:59], a[172:175], a[148:151], v[56:59]\n"
"v_mfma_f32_16x16x32_bf16 v[60:63], a[168:171], a[152:155], v[60:63]\n"
"global_load_dwordx4 a[168:171], v246, s[16:17] offset:-2048 sc1\n"
"v_mfma_f32_16x16x32_bf16 v[60:63], a[172:175], a[156:159], v[60:63]\n"
"s_waitcnt vmcnt(22)\n"
"v_mfma_f32_16x16x32_bf16 v[64:67], a[176:179], a[96:99], v[64:67]\n"
"v_mfma_f32_16x16x32_bf16 v[64:67], a[180:183], a[100:103], v[64:67]\n"
"global_load_dwordx4 a[172:175], v246, s[16:17] offset:-1024 sc1\n"
"v_mfma_f32_16x16x32_bf16 v[68:71], a[176:179], a[104:107], v[68:71]\n"
"s_mul_i32 s48, s49, -49152\n"
"s_add_u32 s48, s48, 131072\n"
"v_mfma_f32_16x16x32_bf16 v[68:71], a[180:183], a[108:111], v[68:71]\n"
"v_add_u32 v242, s48, v240\n"
"v_mfma_f32_16x16x32_bf16 v[72:75], a[176:179], a[112:115], v[72:75]\n"
"v_add_u32 v243, s48, v241\n"
"v_mfma_f32_16x16x32_bf16 v[72:75], a[180:183], a[116:119], v[72:75]\n"
"v_mfma_f32_16x16x32_bf16 v[76:79], a[176:179], a[120:123], v[76:79]\n"
"v_mfma_f32_16x16x32_bf16 v[76:79], a[180:183], a[124:127], v[76:79]\n"
"v_mfma_f32_16x16x32_bf16 v[80:83], a[176:179], a[128:131], v[80:83]\n"
"s_add_u32 m0, s57, 98304\n"
"v_add_u32 v238, 2097152, v234\n"
"v_mfma_f32_16x16x32_bf16 v[80:83], a[180:183], a[132:135], v[80:83]\n"
"global_load_lds_dwordx4 v238, s[40:41] offset:0 nt\n"
"v_mfma_f32_16x16x32_bf16 v[84:87], a[176:179], a[136:139], v[84:87]\n"
"v_mfma_f32_16x16x32_bf16 v[84:87], a[180:183], a[140:143], v[84:87]\n"
"v_mfma_f32_16x16x32_bf16 v[88:91], a[176:179], a[144:147], v[88:91]\n"
"v_mfma_f32_16x16x32_bf16 v[88:91], a[180:183], a[148:151], v[88:91]\n"
"v_mfma_f32_16x16x32_bf16 v[92:95], a[176:179], a[152:155], v[92:95]\n"
"global_load_dwordx4 a[176:179], v246, s[16:17] offset:0 sc1\n"
"v_mfma_f32_16x16x32_bf16 v[92:95], a[180:183], a[156:159], v[92:95]\n"
"s_waitcnt vmcnt(23)\n"
"v_mfma_f32_16x16x32_bf16 v[96:99], a[184:187], a[96:99], v[96:99]\n"
"v_mfma_f32_16x16x32_bf16 v[96:99], a[188:191], a[100:103], v[96:99]\n"
"global_load_dwordx4 a[180:183], v246, s[16:17] offset:1024 sc1\n"
"v_mfma_f32_16x16x32_bf16 v[100:103], a[184:187], a[104:107], v[100:103]\n"
"v_mfma_f32_16x16x32_bf16 v[100:103], a[188:191], a[108:111], v[100:103]\n"
"v_mfma_f32_16x16x32_bf16 v[104:107], a[184:187], a[112:115], v[104:107]\n"
"v_mfma_f32_16x16x32_bf16 v[104:107], a[188:191], a[116:119], v[104:107]\n"
"v_mfma_f32_16x16x32_bf16 v[108:111], a[184:187], a[120:123], v[108:111]\n"
"v_mfma_f32_16x16x32_bf16 v[108:111], a[188:191], a[124:127], v[108:111]\n"
"v_mfma_f32_16x16x32_bf16 v[112:115], a[184:187], a[128:131], v[112:115]\n"
"v_mfma_f32_16x16x32_bf16 v[112:115], a[188:191], a[132:135], v[112:115]\n"
"v_mfma_f32_16x16x32_bf16 v[116:119], a[184:187], a[136:139], v[116:119]\n"
"v_mfma_f32_16x16x32_bf16 v[116:119], a[188:191], a[140:143], v[116:119]\n"
"v_mfma_f32_16x16x32_bf16 v[120:123], a[184:187], a[144:147], v[120:123]\n"
"v_mfma_f32_16x16x32_bf16 v[120:123], a[188:191], a[148:151], v[120:123]\n"
"v_mfma_f32_16x16x32_bf16 v[124:127], a[184:187], a[152:155], v[124:127]\n"
"global_load_dwordx4 a[184:187], v246, s[16:17] offset:2048 sc1\n"
"v_mfma_f32_16x16x32_bf16 v[124:127], a[188:191], a[156:159], v[124:127]\n"
"s_waitcnt vmcnt(20)\n"
"v_mfma_f32_16x16x32_bf16 v[128:131], a[192:195], a[96:99], v[128:131]\n"
"v_mfma_f32_16x16x32_bf16 v[128:131], a[196:199], a[100:103], v[128:131]\n"
"global_load_dwordx4 a[188:191], v246, s[16:17] offset:3072 sc1\n"
"v_mfma_f32_16x16x32_bf16 v[132:135], a[192:195], a[104:107], v[132:135]\n"
"v_mfma_f32_16x16x32_bf16 v[132:135], a[196:199], a[108:111], v[132:135]\n"
"v_add_u32 v246, 0x2000, v246\n"
"v_mfma_f32_16x16x32_bf16 v[136:139], a[192:195], a[112:115], v[136:139]\n"
"v_mfma_f32_16x16x32_bf16 v[136:139], a[196:199], a[116:119], v[136:139]\n"
"v_mfma_f32_16x16x32_bf16 v[140:143], a[192:195], a[120:123], v[140:143]\n"
"v_mfma_f32_16x16x32_bf16 v[140:143], a[196:199], a[124:127], v[140:143]\n"
"v_mfma_f32_16x16x32_bf16 v[144:147], a[192:195], a[128:131], v[144:147]\n"
"v_mfma_f32_16x16x32_bf16 v[144:147], a[196:199], a[132:135], v[144:147]\n"
"v_mfma_f32_16x16x32_bf16 v[148:151], a[192:195], a[136:139], v[148:151]\n"
"v_mfma_f32_16x16x32_bf16 v[148:151], a[196:199], a[140:143], v[148:151]\n"
"v_mfma_f32_16x16x32_bf16 v[152:155], a[192:195], a[144:147], v[152:155]\n"
"v_mfma_f32_16x16x32_bf16 v[152:155], a[196:199], a[148:151], v[152:155]\n"
"v_mfma_f32_16x16x32_bf16 v[156:159], a[192:195], a[152:155], v[156:159]\n"
"global_load_dwordx4 a[192:195], v246, s[16:17] offset:-4096 sc1\n"
"v_mfma_f32_16x16x32_bf16 v[156:159], a[196:199], a[156:159], v[156:159]\n"
"s_waitcnt vmcnt(19)\n"
"v_mfma_f32_16x16x32_bf16 v[160:163], a[200:203], a[96:99], v[160:163]\n"
"v_mfma_f32_16x16x32_bf16 v[160:163], a[204:207], a[100:103], v[160:163]\n"
"global_load_dwordx4 a[196:199], v246, s[16:17] offset:-3072 sc1\n"
"v_mfma_f32_16x16x32_bf16 v[164:167], a[200:203], a[104:107], v[164:167]\n"
"v_mfma_f32_16x16x32_bf16 v[164:167], a[204:207], a[108:111], v[164:167]\n"
"v_mfma_f32_16x16x32_bf16 v[168:171], a[200:203], a[112:115], v[168:171]\n"
"v_mfma_f32_16x16x32_bf16 v[168:171], a[204:207], a[116:119], v[168:171]\n"
"v_mfma_f32_16x16x32_bf16 v[172:175], a[200:203], a[120:123], v[172:175]\n"
"v_mfma_f32_16x16x32_bf16 v[172:175], a[204:207], a[124:127], v[172:175]\n"
"v_mfma_f32_16x16x32_bf16 v[176:179], a[200:203], a[128:131], v[176:179]\n"
"v_mfma_f32_16x16x32_bf16 v[176:179], a[204:207], a[132:135], v[176:179]\n"
"v_mfma_f32_16x16x32_bf16 v[180:183], a[200:203], a[136:139], v[180:183]\n"
"v_mfma_f32_16x16x32_bf16 v[180:183], a[204:207], a[140:143], v[180:183]\n"
"v_mfma_f32_16x16x32_bf16 v[184:187], a[200:203], a[144:147], v[184:187]\n"
"v_mfma_f32_16x16x32_bf16 v[184:187], a[204:207], a[148:151], v[184:187]\n"
"v_mfma_f32_16x16x32_bf16 v[188:191], a[200:203], a[152:155], v[188:191]\n"
"global_load_dwordx4 a[200:203], v246, s[16:17] offset:-2048 sc1\n"
"v_mfma_f32_16x16x32_bf16 v[188:191], a[204:207], a[156:159], v[188:191]\n"
"s_waitcnt vmcnt(18)\n"
"v_mfma_f32_16x16x32_bf16 v[192:195], a[208:211], a[96:99], v[192:195]\n"
"v_mfma_f32_16x16x32_bf16 v[192:195], a[212:215], a[100:103], v[192:195]\n"
"global_load_dwordx4 a[204:207], v246, s[16:17] offset:-1024 sc1\n"
"v_mfma_f32_16x16x32_bf16 v[196:199], a[208:211], a[104:107], v[196:199]\n"
"v_mfma_f32_16x16x32_bf16 v[196:199], a[212:215], a[108:111], v[196:199]\n"
"v_mfma_f32_16x16x32_bf16 v[200:203], a[208:211], a[112:115], v[200:203]\n"
"v_mfma_f32_16x16x32_bf16 v[200:203], a[212:215], a[116:119], v[200:203]\n"
"v_mfma_f32_16x16x32_bf16 v[204:207], a[208:211], a[120:123], v[204:207]\n"
"v_mfma_f32_16x16x32_bf16 v[204:207], a[212:215], a[124:127], v[204:207]\n"
"v_mfma_f32_16x16x32_bf16 v[208:211], a[208:211], a[128:131], v[208:211]\n"
"v_mfma_f32_16x16x32_bf16 v[208:211], a[212:215], a[132:135], v[208:211]\n"
"v_mfma_f32_16x16x32_bf16 v[212:215], a[208:211], a[136:139], v[212:215]\n"
"v_mfma_f32_16x16x32_bf16 v[212:215], a[212:215], a[140:143], v[212:215]\n"
"v_mfma_f32_16x16x32_bf16 v[216:219], a[208:211], a[144:147], v[216:219]\n"
"v_mfma_f32_16x16x32_bf16 v[216:219], a[212:215], a[148:151], v[216:219]\n"
"s_barrier\n"
"v_mfma_f32_16x16x32_bf16 v[220:223], a[208:211], a[152:155], v[220:223]\n"
"global_load_dwordx4 a[208:211], v246, s[16:17] offset:0 sc1\n"
"v_mfma_f32_16x16x32_bf16 v[220:223], a[212:215], a[156:159], v[220:223]\n"
"s_waitcnt vmcnt(18)\n"
"v_mfma_f32_16x16x32_bf16 a[0:3], a[216:219], a[96:99], a[0:3]\n"
"ds_read_b128 a[96:99], v242 offset:0\n"
"v_mfma_f32_16x16x32_bf16 a[0:3], a[220:223], a[100:103], a[0:3]\n"
"ds_read_b128 a[100:103], v243 offset:0\n"
"global_load_dwordx4 a[212:215], v246, s[16:17] offset:1024 sc1\n"
"v_mfma_f32_16x16x32_bf16 a[4:7], a[216:219], a[104:107], a[4:7]\n"
"ds_read_b128 a[104:107], v242 offset:2048\n"
"v_mfma_f32_16x16x32_bf16 a[4:7], a[220:223], a[108:111], a[4:7]\n"
"ds_read_b128 a[108:111], v243 offset:2048\n"
"v_mfma_f32_16x16x32_bf16 a[8:11], a[216:219], a[112:115], a[8:11]\n"
"ds_read_b128 a[112:115], v242 offset:4096\n"
"v_mfma_f32_16x16x32_bf16 a[8:11], a[220:223], a[116:119], a[8:11]\n"
"ds_read_b128 a[116:119], v243 offset:4096\n"
"v_mfma_f32_16x16x32_bf16 a[12:15], a[216:219], a[120:123], a[12:15]\n"
"ds_read_b128 a[120:123], v242 offset:6144\n"
"v_mfma_f32_16x16x32_bf16 a[12:15], a[220:223], a[124:127], a[12:15]\n"
"ds_read_b128 a[124:127], v243 offset:6144\n"
"v_mfma_f32_16x16x32_bf16 a[16:19], a[216:219], a[128:131], a[16:19]\n"
"ds_read_b128 a[128:131], v242 offset:8192\n"
"v_mfma_f32_16x16x32_bf16 a[16:19], a[220:223], a[132:135], a[16:19]\n"
"ds_read_b128 a[132:135], v243 offset:8192\n"
"v_mfma_f32_16x16x32_bf16 a[20:23], a[216:219], a[136:139], a[20:23]\n"
"ds_read_b128 a[136:139], v242 offset:10240\n"
"v_mfma_f32_16x16x32_bf16 a[20:23], a[220:223], a[140:143], a[20:23]\n"
"ds_read_b128 a[140:143], v243 offset:10240\n"
"v_mfma_f32_16x16x32_bf16 a[24:27], a[216:219], a[144:147], a[24:27]\n"
"ds_read_b128 a[144:147], v242 offset:12288\n"
"v_mfma_f32_16x16x32_bf16 a[24:27], a[220:223], a[148:151], a[24:27]\n"
"ds_read_b128 a[148:151], v243 offset:12288\n"
"v_mfma_f32_16x16x32_bf16 a[28:31], a[216:219], a[152:155], a[28:31]\n"
"ds_read_b128 a[152:155], v242 offset:14336\n"
"global_load_dwordx4 a[216:219], v246, s[16:17] offset:2048 sc1\n"
"v_mfma_f32_16x16x32_bf16 a[28:31], a[220:223], a[156:159], a[28:31]\n"
"ds_read_b128 a[156:159], v243 offset:14336\n"
"s_waitcnt vmcnt(15) lgkmcnt(14)\n"
"v_mfma_f32_16x16x32_bf16 v[0:3], a[160:163], a[96:99], v[0:3]\n"
"v_mfma_f32_16x16x32_bf16 v[0:3], a[164:167], a[100:103], v[0:3]\n"
"global_load_dwordx4 a[220:223], v246, s[16:17] offset:3072 sc1\n"
"s_waitcnt lgkmcnt(12)\n"
"v_mfma_f32_16x16x32_bf16 v[4:7], a[160:163], a[104:107], v[4:7]\n"
"s_mul_i32 s45, s50, -131072\n"
"s_add_u32 s45, s45, 131072\n"
"s_add_u32 s45, s45, s42\n"
"v_mfma_f32_16x16x32_bf16 v[4:7], a[164:167], a[108:111], v[4:7]\n"
"v_add_u32 v246, 0x2000, v246\n"
"s_waitcnt lgkmcnt(10)\n"
"v_mfma_f32_16x16x32_bf16 v[8:11], a[160:163], a[112:115], v[8:11]\n"
"s_add_u32 m0, s45, 0\n"
"v_add_u32 v238, 0, v248\n"
"v_mfma_f32_16x16x32_bf16 v[8:11], a[164:167], a[116:119], v[8:11]\n"
"global_load_lds_dwordx4 v238, s[20:21] offset:0\n"
"s_waitcnt lgkmcnt(8)\n"
"v_mfma_f32_16x16x32_bf16 v[12:15], a[160:163], a[120:123], v[12:15]\n"
"v_mfma_f32_16x16x32_bf16 v[12:15], a[164:167], a[124:127], v[12:15]\n"
"s_waitcnt lgkmcnt(6)\n"
"v_mfma_f32_16x16x32_bf16 v[16:19], a[160:163], a[128:131], v[16:19]\n"
"v_mfma_f32_16x16x32_bf16 v[16:19], a[164:167], a[132:135], v[16:19]\n"
"v_add_u32 v238, s66, v238\n"
"s_waitcnt lgkmcnt(4)\n"
"v_mfma_f32_16x16x32_bf16 v[20:23], a[160:163], a[136:139], v[20:23]\n"
"global_load_lds_dwordx4 v238, s[20:21] offset:1024\n"
"v_mfma_f32_16x16x32_bf16 v[20:23], a[164:167], a[140:143], v[20:23]\n"
"s_waitcnt lgkmcnt(2)\n"
"v_mfma_f32_16x16x32_bf16 v[24:27], a[160:163], a[144:147], v[24:27]\n"
"v_mfma_f32_16x16x32_bf16 v[24:27], a[164:167], a[148:151], v[24:27]\n"
"s_waitcnt lgkmcnt(0)\n"
"v_mfma_f32_16x16x32_bf16 v[28:31], a[160:163], a[152:155], v[28:31]\n"
"global_load_dwordx4 a[160:163], v246, s[16:17] offset:-4096 sc1\n"
"v_add_u32 v238, s66, v238\n"
"v_mfma_f32_16x16x32_bf16 v[28:31], a[164:167], a[156:159], v[28:31]\n"
"global_load_lds_dwordx4 v238, s[20:21] offset:2048\n"
"s_waitcnt vmcnt(17)\n"
"v_mfma_f32_16x16x32_bf16 v[32:35], a[168:171], a[96:99], v[32:35]\n"
"global_load_dwordx4 a[164:167], v246, s[16:17] offset:-3072 sc1\n"
"v_mfma_f32_16x16x32_bf16 v[32:35], a[172:175], a[100:103], v[32:35]\n"
"v_mfma_f32_16x16x32_bf16 v[36:39], a[168:171], a[104:107], v[36:39]\n"
"v_mfma_f32_16x16x32_bf16 v[36:39], a[172:175], a[108:111], v[36:39]\n"
"v_add_u32 v238, s66, v238\n"
"global_load_lds_dwordx4 v238, s[20:21] offset:3072\n"
"v_mfma_f32_16x16x32_bf16 v[40:43], a[168:171], a[112:115], v[40:43]\n"
"v_mfma_f32_16x16x32_bf16 v[40:43], a[172:175], a[116:119], v[40:43]\n"
"v_mfma_f32_16x16x32_bf16 v[44:47], a[168:171], a[120:123], v[44:47]\n"
"v_mfma_f32_16x16x32_bf16 v[44:47], a[172:175], a[124:127], v[44:47]\n"
"v_mfma_f32_16x16x32_bf16 v[48:51], a[168:171], a[128:131], v[48:51]\n"
"v_mfma_f32_16x16x32_bf16 v[48:51], a[172:175], a[132:135], v[48:51]\n"
"v_mfma_f32_16x16x32_bf16 v[52:55], a[168:171], a[136:139], v[52:55]\n"
"v_mfma_f32_16x16x32_bf16 v[52:55], a[172:175], a[140:143], v[52:55]\n"
"v_mfma_f32_16x16x32_bf16 v[56:59], a[168:171], a[144:147], v[56:59]\n"
"v_mfma_f32_16x16x32_bf16 v[56:59], a[172:175], a[148:151], v[56:59]\n"
"v_mfma_f32_16x16x32_bf16 v[60:63], a[168:171], a[152:155], v[60:63]\n"
"global_load_dwordx4 a[168:171], v246, s[16:17] offset:-2048 sc1\n"
"v_mfma_f32_16x16x32_bf16 v[60:63], a[172:175], a[156:159], v[60:63]\n"
"s_waitcnt vmcnt(17)\n"
"v_mfma_f32_16x16x32_bf16 v[64:67], a[176:179], a[96:99], v[64:67]\n"
"global_load_dwordx4 a[172:175], v246, s[16:17] offset:-1024 sc1\n"
"v_mfma_f32_16x16x32_bf16 v[64:67], a[180:183], a[100:103], v[64:67]\n"
"v_mfma_f32_16x16x32_bf16 v[68:71], a[176:179], a[104:107], v[68:71]\n"
"s_mul_i32 s48, s50, -32768\n"
"s_add_u32 s48, s48, 147456\n"
"v_mfma_f32_16x16x32_bf16 v[68:71], a[180:183], a[108:111], v[68:71]\n"
"v_add_u32 v242, s48, v240\n"
"v_mfma_f32_16x16x32_bf16 v[72:75], a[176:179], a[112:115], v[72:75]\n"
"v_add_u32 v243, s48, v241\n"
"v_mfma_f32_16x16x32_bf16 v[72:75], a[180:183], a[116:119], v[72:75]\n"
"v_mfma_f32_16x16x32_bf16 v[76:79], a[176:179], a[120:123], v[76:79]\n"
"v_mfma_f32_16x16x32_bf16 v[76:79], a[180:183], a[124:127], v[76:79]\n"
"v_mfma_f32_16x16x32_bf16 v[80:83], a[176:179], a[128:131], v[80:83]\n"
"s_add_u32 m0, s57, 99328\n"
"v_add_u32 v238, 2105344, v234\n"
"v_mfma_f32_16x16x32_bf16 v[80:83], a[180:183], a[132:135], v[80:83]\n"
"global_load_lds_dwordx4 v238, s[40:41] offset:0 nt\n"
"v_mfma_f32_16x16x32_bf16 v[84:87], a[176:179], a[136:139], v[84:87]\n"
"v_mfma_f32_16x16x32_bf16 v[84:87], a[180:183], a[140:143], v[84:87]\n"
"v_mfma_f32_16x16x32_bf16 v[88:91], a[176:179], a[144:147], v[88:91]\n"
"v_mfma_f32_16x16x32_bf16 v[88:91], a[180:183], a[148:151], v[88:91]\n"
"v_mfma_f32_16x16x32_bf16 v[92:95], a[176:179], a[152:155], v[92:95]\n"
"global_load_dwordx4 a[176:179], v246, s[16:17] offset:0 sc1\n"
"v_mfma_f32_16x16x32_bf16 v[92:95], a[180:183], a[156:159], v[92:95]\n"
"s_waitcnt vmcnt(18)\n"
"v_mfma_f32_16x16x32_bf16 v[96:99], a[184:187], a[96:99], v[96:99]\n"
"global_load_dwordx4 a[180:183], v246, s[16:17] offset:1024 sc1\n"
"v_mfma_f32_16x16x32_bf16 v[96:99], a[188:191], a[100:103], v[96:99]\n"
"v_mfma_f32_16x16x32_bf16 v[100:103], a[184:187], a[104:107], v[100:103]\n"
"v_mfma_f32_16x16x32_bf16 v[100:103], a[188:191], a[108:111], v[100:103]\n"
"v_mfma_f32_16x16x32_bf16 v[104:107], a[184:187], a[112:115], v[104:107]\n"
"v_mfma_f32_16x16x32_bf16 v[104:107], a[188:191], a[116:119], v[104:107]\n"
"v_mfma_f32_16x16x32_bf16 v[108:111], a[184:187], a[120:123], v[108:111]\n"
"v_mfma_f32_16x16x32_bf16 v[108:111], a[188:191], a[124:127], v[108:111]\n"
"v_mfma_f32_16x16x32_bf16 v[112:115], a[184:187], a[128:131], v[112:115]\n"
"v_mfma_f32_16x16x32_bf16 v[112:115], a[188:191], a[132:135], v[112:115]\n"
"v_mfma_f32_16x16x32_bf16 v[116:119], a[184:187], a[136:139], v[116:119]\n"
"v_mfma_f32_16x16x32_bf16 v[116:119], a[188:191], a[140:143], v[116:119]\n"
"v_mfma_f32_16x16x32_bf16 v[120:123], a[184:187], a[144:147], v[120:123]\n"
"v_mfma_f32_16x16x32_bf16 v[120:123], a[188:191], a[148:151], v[120:123]\n"
"v_mfma_f32_16x16x32_bf16 v[124:127], a[184:187], a[152:155], v[124:127]\n"
"global_load_dwordx4 a[184:187], v246, s[16:17] offset:2048 sc1\n"
"v_mfma_f32_16x16x32_bf16 v[124:127], a[188:191], a[156:159], v[124:127]\n"
"s_waitcnt vmcnt(18)\n"
"v_mfma_f32_16x16x32_bf16 v[128:131], a[192:195], a[96:99], v[128:131]\n"
"global_load_dwordx4 a[188:191], v246, s[16:17] offset:3072 sc1\n"
"v_mfma_f32_16x16x32_bf16 v[128:131], a[196:199], a[100:103], v[128:131]\n"
"v_mfma_f32_16x16x32_bf16 v[132:135], a[192:195], a[104:107], v[132:135]\n"
"v_add_u32 v246, 0x2000, v246\n"
"v_mfma_f32_16x16x32_bf16 v[132:135], a[196:199], a[108:111], v[132:135]\n"
"v_mfma_f32_16x16x32_bf16 v[136:139], a[192:195], a[112:115], v[136:139]\n"
"v_mfma_f32_16x16x32_bf16 v[136:139], a[196:199], a[116:119], v[136:139]\n"
"v_mfma_f32_16x16x32_bf16 v[140:143], a[192:195], a[120:123], v[140:143]\n"
"v_mfma_f32_16x16x32_bf16 v[140:143], a[196:199], a[124:127], v[140:143]\n"
"v_mfma_f32_16x16x32_bf16 v[144:147], a[192:195], a[128:131], v[144:147]\n"
"v_mfma_f32_16x16x32_bf16 v[144:147], a[196:199], a[132:135], v[144:147]\n"
"v_mfma_f32_16x16x32_bf16 v[148:151], a[192:195], a[136:139], v[148:151]\n"
"v_mfma_f32_16x16x32_bf16 v[148:151], a[196:199], a[140:143], v[148:151]\n"
"v_mfma_f32_16x16x32_bf16 v[152:155], a[192:195], a[144:147], v[152:155]\n"
"v_mfma_f32_16x16x32_bf16 v[152:155], a[196:199], a[148:151], v[152:155]\n"
"v_mfma_f32_16x16x32_bf16 v[156:159], a[192:195], a[152:155], v[156:159]\n"
"global_load_dwordx4 a[192:195], v246, s[16:17] offset:-4096 sc1\n"
"v_mfma_f32_16x16x32_bf16 v[156:159], a[196:199], a[156:159], v[156:159]\n"
"s_waitcnt vmcnt(18)\n"
"v_mfma_f32_16x16x32_bf16 v[160:163], a[200:203], a[96:99], v[160:163]\n"
"global_load_dwordx4 a[196:199], v246, s[16:17] offset:-3072 sc1\n"
"v_mfma_f32_16x16x32_bf16 v[160:163], a[204:207], a[100:103], v[160:163]\n"
"v_mfma_f32_16x16x32_bf16 v[164:167], a[200:203], a[104:107], v[164:167]\n"
"v_mfma_f32_16x16x32_bf16 v[164:167], a[204:207], a[108:111], v[164:167]\n"
"v_mfma_f32_16x16x32_bf16 v[168:171], a[200:203], a[112:115], v[168:171]\n"
"v_mfma_f32_16x16x32_bf16 v[168:171], a[204:207], a[116:119], v[168:171]\n"
"v_mfma_f32_16x16x32_bf16 v[172:175], a[200:203], a[120:123], v[172:175]\n"
"v_mfma_f32_16x16x32_bf16 v[172:175], a[204:207], a[124:127], v[172:175]\n"
"v_mfma_f32_16x16x32_bf16 v[176:179], a[200:203], a[128:131], v[176:179]\n"
"v_mfma_f32_16x16x32_bf16 v[176:179], a[204:207], a[132:135], v[176:179]\n"
"v_mfma_f32_16x16x32_bf16 v[180:183], a[200:203], a[136:139], v[180:183]\n"
"v_mfma_f32_16x16x32_bf16 v[180:183], a[204:207], a[140:143], v[180:183]\n"
"v_mfma_f32_16x16x32_bf16 v[184:187], a[200:203], a[144:147], v[184:187]\n"
"v_mfma_f32_16x16x32_bf16 v[184:187], a[204:207], a[148:151], v[184:187]\n"
"v_mfma_f32_16x16x32_bf16 v[188:191], a[200:203], a[152:155], v[188:191]\n"
"global_load_dwordx4 a[200:203], v246, s[16:17] offset:-2048 sc1\n"
"v_mfma_f32_16x16x32_bf16 v[188:191], a[204:207], a[156:159], v[188:191]\n"
"s_waitcnt vmcnt(18)\n"
"v_mfma_f32_16x16x32_bf16 v[192:195], a[208:211], a[96:99], v[192:195]\n"
"global_load_dwordx4 a[204:207], v246, s[16:17] offset:-1024 sc1\n"
"v_mfma_f32_16x16x32_bf16 v[192:195], a[212:215], a[100:103], v[192:195]\n"
"v_mfma_f32_16x16x32_bf16 v[196:199], a[208:211], a[104:107], v[196:199]\n"
"v_mfma_f32_16x16x32_bf16 v[196:199], a[212:215], a[108:111], v[196:199]\n"
"v_mfma_f32_16x16x32_bf16 v[200:203], a[208:211], a[112:115], v[200:203]\n"
"v_mfma_f32_16x16x32_bf16 v[200:203], a[212:215], a[116:119], v[200:203]\n"
"v_mfma_f32_16x16x32_bf16 v[204:207], a[208:211], a[120:123], v[204:207]\n"
"v_mfma_f32_16x16x32_bf16 v[204:207], a[212:215], a[124:127], v[204:207]\n"
"v_mfma_f32_16x16x32_bf16 v[208:211], a[208:211], a[128:131], v[208:211]\n"
"v_mfma_f32_16x16x32_bf16 v[208:211], a[212:215], a[132:135], v[208:211]\n"
"v_mfma_f32_16x16x32_bf16 v[212:215], a[208:211], a[136:139], v[212:215]\n"
"v_mfma_f32_16x16x32_bf16 v[212:215], a[212:215], a[140:143], v[212:215]\n"
"v_mfma_f32_16x16x32_bf16 v[216:219], a[208:211], a[144:147], v[216:219]\n"
"v_mfma_f32_16x16x32_bf16 v[216:219], a[212:215], a[148:151], v[216:219]\n"
"s_barrier\n"
"v_mfma_f32_16x16x32_bf16 v[220:223], a[208:211], a[152:155], v[220:223]\n"
"global_load_dwordx4 a[208:211], v246, s[16:17] offset:0 sc1\n"
"v_mfma_f32_16x16x32_bf16 v[220:223], a[212:215], a[156:159], v[220:223]\n"
"s_waitcnt vmcnt(18)\n"
"v_mfma_f32_16x16x32_bf16 a[0:3], a[216:219], a[96:99], a[0:3]\n"
"global_load_dwordx4 a[212:215], v246, s[16:17] offset:1024 sc1\n"
"ds_read_b128 a[96:99], v242 offset:0\n"
"v_mfma_f32_16x16x32_bf16 a[0:3], a[220:223], a[100:103], a[0:3]\n"
"ds_read_b128 a[100:103], v243 offset:0\n"
".Lpg3_%=:\n"
"s_cbranch_execz .Lpg7_%=\n"
"v_mfma_f32_16x16x32_bf16 a[4:7], a[216:219], a[104:107], a[4:7]\n"
"ds_read_b128 a[104:107], v242 offset:2048\n"
"v_mfma_f32_16x16x32_bf16 a[4:7], a[220:223], a[108:111], a[4:7]\n"
"ds_read_b128 a[108:111], v243 offset:2048\n"
"v_mfma_f32_16x16x32_bf16 a[8:11], a[216:219], a[112:115], a[8:11]\n"
"ds_read_b128 a[112:115], v242 offset:4096\n"
"v_mfma_f32_16x16x32_bf16 a[8:11], a[220:223], a[116:119], a[8:11]\n"
"ds_read_b128 a[116:119], v243 offset:4096\n"
"v_mfma_f32_16x16x32_bf16 a[12:15], a[216:219], a[120:123], a[12:15]\n"
"ds_read_b128 a[120:123], v242 offset:6144\n"
"v_mfma_f32_16x16x32_bf16 a[12:15], a[220:223], a[124:127], a[12:15]\n"
"ds_read_b128 a[124:127], v243 offset:6144\n"
"v_mfma_f32_16x16x32_bf16 a[16:19], a[216:219], a[128:131], a[16:19]\n"
"ds_read_b128 a[128:131], v242 offset:8192\n"
"v_mfma_f32_16x16x32_bf16 a[16:19], a[220:223], a[132:135], a[16:19]\n"
"ds_read_b128 a[132:135], v243 offset:8192\n"
"v_mfma_f32_16x16x32_bf16 a[20:23], a[216:219], a[136:139], a[20:23]\n"
"ds_read_b128 a[136:139], v242 offset:10240\n"
"v_mfma_f32_16x16x32_bf16 a[20:23], a[220:223], a[140:143], a[20:23]\n"
"ds_read_b128 a[140:143], v243 offset:10240\n"
"v_mfma_f32_16x16x32_bf16 a[24:27], a[216:219], a[144:147], a[24:27]\n"
"ds_read_b128 a[144:147], v242 offset:12288\n"
"v_mfma_f32_16x16x32_bf16 a[24:27], a[220:223], a[148:151], a[24:27]\n"
"ds_read_b128 a[148:151], v243 offset:12288\n"
"v_mfma_f32_16x16x32_bf16 a[28:31], a[216:219], a[152:155], a[28:31]\n"
"global_load_dwordx4 a[216:219], v246, s[16:17] offset:2048 sc1\n"
"ds_read_b128 a[152:155], v242 offset:14336\n"
"v_mfma_f32_16x16x32_bf16 a[28:31], a[220:223], a[156:159], a[28:31]\n"
"ds_read_b128 a[156:159], v243 offset:14336\n"
"s_waitcnt vmcnt(15) lgkmcnt(14)\n"
"v_mfma_f32_16x16x32_bf16 v[0:3], a[160:163], a[96:99], v[0:3]\n"
"global_load_dwordx4 a[220:223], v246, s[16:17] offset:3072 sc1\n"
"v_mfma_f32_16x16x32_bf16 v[0:3], a[164:167], a[100:103], v[0:3]\n"
"s_waitcnt lgkmcnt(12)\n"
"v_mfma_f32_16x16x32_bf16 v[4:7], a[160:163], a[104:107], v[4:7]\n"
"v_add_u32 v246, 0x2000, v246\n"
"v_mfma_f32_16x16x32_bf16 v[4:7], a[164:167], a[108:111], v[4:7]\n"
"s_waitcnt lgkmcnt(10)\n"
"v_mfma_f32_16x16x32_bf16 v[8:11], a[160:163], a[112:115], v[8:11]\n"
"s_mul_i32 s45, s49, -131072\n"
"s_add_u32 s45, s45, 131072\n"
"s_add_u32 s45, s45, s42\n"
"v_mfma_f32_16x16x32_bf16 v[8:11], a[164:167], a[116:119], v[8:11]\n"
"s_waitcnt lgkmcnt(8)\n"
"v_mfma_f32_16x16x32_bf16 v[12:15], a[160:163], a[120:123], v[12:15]\n"
"s_add_u32 m0, s45, 0\n"
"v_add_u32 v238, 1024, v248\n"
"v_mfma_f32_16x16x32_bf16 v[12:15], a[164:167], a[124:127], v[12:15]\n"
"global_load_lds_dwordx4 v238, s[20:21] offset:0\n"
"s_waitcnt lgkmcnt(6)\n"
"v_mfma_f32_16x16x32_bf16 v[16:19], a[160:163], a[128:131], v[16:19]\n"
"v_mfma_f32_16x16x32_bf16 v[16:19], a[164:167], a[132:135], v[16:19]\n"
"s_waitcnt lgkmcnt(4)\n"
"v_mfma_f32_16x16x32_bf16 v[20:23], a[160:163], a[136:139], v[20:23]\n"
"v_mfma_f32_16x16x32_bf16 v[20:23], a[164:167], a[140:143], v[20:23]\n"
"v_add_u32 v238, s66, v238\n"
"s_waitcnt lgkmcnt(2)\n"
"v_mfma_f32_16x16x32_bf16 v[24:27], a[160:163], a[144:147], v[24:27]\n"
"global_load_lds_dwordx4 v238, s[20:21] offset:1024\n"
"v_mfma_f32_16x16x32_bf16 v[24:27], a[164:167], a[148:151], v[24:27]\n"
"s_waitcnt lgkmcnt(0)\n"
"v_mfma_f32_16x16x32_bf16 v[28:31], a[160:163], a[152:155], v[28:31]\n"
"global_load_dwordx4 a[160:163], v246, s[16:17] offset:-4096 sc1\n"
"v_mfma_f32_16x16x32_bf16 v[28:31], a[164:167], a[156:159], v[28:31]\n"
"s_waitcnt vmcnt(16)\n"
"v_mfma_f32_16x16x32_bf16 v[32:35], a[168:171], a[96:99], v[32:35]\n"
"global_load_dwordx4 a[164:167], v246, s[16:17] offset:-3072 sc1\n"
"v_add_u32 v238, s66, v238\n"
"v_mfma_f32_16x16x32_bf16 v[32:35], a[172:175], a[100:103], v[32:35]\n"
"global_load_lds_dwordx4 v238, s[20:21] offset:2048\n"
"v_mfma_f32_16x16x32_bf16 v[36:39], a[168:171], a[104:107], v[36:39]\n"
"v_mfma_f32_16x16x32_bf16 v[36:39], a[172:175], a[108:111], v[36:39]\n"
"v_mfma_f32_16x16x32_bf16 v[40:43], a[168:171], a[112:115], v[40:43]\n"
"s_add_u32 s64, s60, 16384\n"
"s_addc_u32 s65, s61, 0\n"
"v_mfma_f32_16x16x32_bf16 v[40:43], a[172:175], a[116:119], v[40:43]\n"
"v_add_u32 v238, s66, v238\n"
"global_load_lds_dwordx4 v238, s[20:21] offset:3072\n"
"v_mfma_f32_16x16x32_bf16 v[44:47], a[168:171], a[120:123], v[44:47]\n"
"v_mfma_f32_16x16x32_bf16 v[44:47], a[172:175], a[124:127], v[44:47]\n"
"v_mfma_f32_16x16x32_bf16 v[48:51], a[168:171], a[128:131], v[48:51]\n"
"v_mfma_f32_16x16x32_bf16 v[48:51], a[172:175], a[132:135], v[48:51]\n"
"v_mfma_f32_16x16x32_bf16 v[52:55], a[168:171], a[136:139], v[52:55]\n"
"v_mfma_f32_16x16x32_bf16 v[52:55], a[172:175], a[140:143], v[52:55]\n"
"v_mfma_f32_16x16x32_bf16 v[56:59], a[168:171], a[144:147], v[56:59]\n"
"v_mfma_f32_16x16x32_bf16 v[56:59], a[172:175], a[148:151], v[56:59]\n"
"v_mfma_f32_16x16x32_bf16 v[60:63], a[168:171], a[152:155], v[60:63]\n"
"global_load_dwordx4 a[168:171], v246, s[16:17] offset:-2048 sc1\n"
"v_mfma_f32_16x16x32_bf16 v[60:63], a[172:175], a[156:159], v[60:63]\n"
"s_waitcnt vmcnt(17)\n"
"v_mfma_f32_16x16x32_bf16 v[64:67], a[176:179], a[96:99], v[64:67]\n"
"global_load_dwordx4 a[172:175], v246, s[16:17] offset:-1024 sc1\n"
"v_mfma_f32_16x16x32_bf16 v[64:67], a[180:183], a[100:103], v[64:67]\n"
"v_mfma_f32_16x16x32_bf16 v[68:71], a[176:179], a[104:107], v[68:71]\n"
"s_mul_i32 s48, s49, -32768\n"
"s_add_u32 s48, s48, 147456\n"
"v_mfma_f32_16x16x32_bf16 v[68:71], a[180:183], a[108:111], v[68:71]\n"
"v_add_u32 v242, s48, v240\n"
"v_mfma_f32_16x16x32_bf16 v[72:75], a[176:179], a[112:115], v[72:75]\n"
"v_add_u32 v243, s48, v241\n"
"v_mfma_f32_16x16x32_bf16 v[72:75], a[180:183], a[116:119], v[72:75]\n"
"v_mfma_f32_16x16x32_bf16 v[76:79], a[176:179], a[120:123], v[76:79]\n"
"v_mfma_f32_16x16x32_bf16 v[76:79], a[180:183], a[124:127], v[76:79]\n"
"v_mfma_f32_16x16x32_bf16 v[80:83], a[176:179], a[128:131], v[80:83]\n"
"s_add_u32 m0, s57, 106496\n"
"v_add_u32 v238, 4194304, v234\n"
"v_mfma_f32_16x16x32_bf16 v[80:83], a[180:183], a[132:135], v[80:83]\n"
"global_load_lds_dwordx4 v238, s[40:41] offset:0 nt\n"
"v_mfma_f32_16x16x32_bf16 v[84:87], a[176:179], a[136:139], v[84:87]\n"
"v_mfma_f32_16x16x32_bf16 v[84:87], a[180:183], a[140:143], v[84:87]\n"
"v_mfma_f32_16x16x32_bf16 v[88:91], a[176:179], a[144:147], v[88:91]\n"
"v_mfma_f32_16x16x32_bf16 v[88:91], a[180:183], a[148:151], v[88:91]\n"
"v_mfma_f32_16x16x32_bf16 v[92:95], a[176:179], a[152:155], v[92:95]\n"
"global_load_dword a176, v250, s[64:65]\n"
"global_load_dwordx4 a[176:179], v246, s[16:17] offset:0 sc1\n"
"v_mfma_f32_16x16x32_bf16 v[92:95], a[180:183], a[156:159], v[92:95]\n"
"s_waitcnt vmcnt(19)\n"
"v_mfma_f32_16x16x32_bf16 v[96:99], a[184:187], a[96:99], v[96:99]\n"
"global_load_dwordx4 a[180:183], v246, s[16:17] offset:1024 sc1\n"
"v_mfma_f32_16x16x32_bf16 v[96:99], a[188:191], a[100:103], v[96:99]\n"
"v_mfma_f32_16x16x32_bf16 v[100:103], a[184:187], a[104:107], v[100:103]\n"
"v_mfma_f32_16x16x32_bf16 v[100:103], a[188:191], a[108:111], v[100:103]\n"
"v_mfma_f32_16x16x32_bf16 v[104:107], a[184:187], a[112:115], v[104:107]\n"
"v_mfma_f32_16x16x32_bf16 v[104:107], a[188:191], a[116:119], v[104:107]\n"
"v_mfma_f32_16x16x32_bf16 v[108:111], a[184:187], a[120:123], v[108:111]\n"
"v_mfma_f32_16x16x32_bf16 v[108:111], a[188:191], a[124:127], v[108:111]\n"
"v_mfma_f32_16x16x32_bf16 v[112:115], a[184:187], a[128:131], v[112:115]\n"
"v_mfma_f32_16x16x32_bf16 v[112:115], a[188:191], a[132:135], v[112:115]\n"
"v_mfma_f32_16x16x32_bf16 v[116:119], a[184:187], a[136:139], v[116:119]\n"
"v_mfma_f32_16x16x32_bf16 v[116:119], a[188:191], a[140:143], v[116:119]\n"
"v_mfma_f32_16x16x32_bf16 v[120:123], a[184:187], a[144:147], v[120:123]\n"
"v_mfma_f32_16x16x32_bf16 v[120:123], a[188:191], a[148:151], v[120:123]\n"
"v_mfma_f32_16x16x32_bf16 v[124:127], a[184:187], a[152:155], v[124:127]\n"
"global_load_dwordx4 a[184:187], v246, s[16:17] offset:2048 sc1\n"
"v_mfma_f32_16x16x32_bf16 v[124:127], a[188:191], a[156:159], v[124:127]\n"
"s_waitcnt vmcnt(19)\n"
"v_mfma_f32_16x16x32_bf16 v[128:131], a[192:195], a[96:99], v[128:131]\n"
"global_load_dwordx4 a[188:191], v246, s[16:17] offset:3072 sc1\n"
"v_mfma_f32_16x16x32_bf16 v[128:131], a[196:199], a[100:103], v[128:131]\n"
"v_mfma_f32_16x16x32_bf16 v[132:135], a[192:195], a[104:107], v[132:135]\n"
"v_add_u32 v246, 0x2000, v246\n"
"v_mfma_f32_16x16x32_bf16 v[132:135], a[196:199], a[108:111], v[132:135]\n"
"v_mfma_f32_16x16x32_bf16 v[136:139], a[192:195], a[112:115], v[136:139]\n"
"v_mfma_f32_16x16x32_bf16 v[136:139], a[196:199], a[116:119], v[136:139]\n"
"v_mfma_f32_16x16x32_bf16 v[140:143], a[192:195], a[120:123], v[140:143]\n"
"v_mfma_f32_16x16x32_bf16 v[140:143], a[196:199], a[124:127], v[140:143]\n"
"v_mfma_f32_16x16x32_bf16 v[144:147], a[192:195], a[128:131], v[144:147]\n"
"v_mfma_f32_16x16x32_bf16 v[144:147], a[196:199], a[132:135], v[144:147]\n"
"v_mfma_f32_16x16x32_bf16 v[148:151], a[192:195], a[136:139], v[148:151]\n"
"v_mfma_f32_16x16x32_bf16 v[148:151], a[196:199], a[140:143], v[148:151]\n"
"v_mfma_f32_16x16x32_bf16 v[152:155], a[192:195], a[144:147], v[152:155]\n"
"v_mfma_f32_16x16x32_bf16 v[152:155], a[196:199], a[148:151], v[152:155]\n"
"v_mfma_f32_16x16x32_bf16 v[156:159], a[192:195], a[152:155], v[156:159]\n"
"global_load_dwordx4 a[192:195], v246, s[16:17] offset:-4096 sc1\n"
"v_mfma_f32_16x16x32_bf16 v[156:159], a[196:199], a[156:159], v[156:159]\n"
"s_waitcnt vmcnt(19)\n"
"v_mfma_f32_16x16x32_bf16 v[160:163], a[200:203], a[96:99], v[160:163]\n"
"global_load_dwordx4 a[196:199], v246, s[16:17] offset:-3072 sc1\n"
"v_mfma_f32_16x16x32_bf16 v[160:163], a[204:207], a[100:103], v[160:163]\n"
"v_mfma_f32_16x16x32_bf16 v[164:167], a[200:203], a[104:107], v[164:167]\n"
"v_mfma_f32_16x16x32_bf16 v[164:167], a[204:207], a[108:111], v[164:167]\n"
"v_mfma_f32_16x16x32_bf16 v[168:171], a[200:203], a[112:115], v[168:171]\n"
"v_mfma_f32_16x16x32_bf16 v[168:171], a[204:207], a[116:119], v[168:171]\n"
"v_mfma_f32_16x16x32_bf16 v[172:175], a[200:203], a[120:123], v[172:175]\n"
"v_mfma_f32_16x16x32_bf16 v[172:175], a[204:207], a[124:127], v[172:175]\n"
"v_mfma_f32_16x16x32_bf16 v[176:179], a[200:203], a[128:131], v[176:179]\n"
"v_mfma_f32_16x16x32_bf16 v[176:179], a[204:207], a[132:135], v[176:179]\n"
"v_mfma_f32_16x16x32_bf16 v[180:183], a[200:203], a[136:139], v[180:183]\n"
"v_mfma_f32_16x16x32_bf16 v[180:183], a[204:207], a[140:143], v[180:183]\n"
"v_mfma_f32_16x16x32_bf16 v[184:187], a[200:203], a[144:147], v[184:187]\n"
"v_mfma_f32_16x16x32_bf16 v[184:187], a[204:207], a[148:151], v[184:187]\n"
"v_mfma_f32_16x16x32_bf16 v[188:191], a[200:203], a[152:155], v[188:191]\n"
"global_load_dwordx4 a[200:203], v246, s[16:17] offset:-2048 sc1\n"
"v_mfma_f32_16x16x32_bf16 v[188:191], a[204:207], a[156:159], v[188:191]\n"
"s_waitcnt vmcnt(19)\n"
"v_mfma_f32_16x16x32_bf16 v[192:195], a[208:211], a[96:99], v[192:195]\n"
"global_load_dwordx4 a[204:207], v246, s[16:17] offset:-1024 sc1\n"
"v_mfma_f32_16x16x32_bf16 v[192:195], a[212:215], a[100:103], v[192:195]\n"
"v_mfma_f32_16x16x32_bf16 v[196:199], a[208:211], a[104:107], v[196:199]\n"
"v_mfma_f32_16x16x32_bf16 v[196:199], a[212:215], a[108:111], v[196:199]\n"
"v_mfma_f32_16x16x32_bf16 v[200:203], a[208:211], a[112:115], v[200:203]\n"
"v_mfma_f32_16x16x32_bf16 v[200:203], a[212:215], a[116:119], v[200:203]\n"
"v_mfma_f32_16x16x32_bf16 v[204:207], a[208:211], a[120:123], v[204:207]\n"
"v_mfma_f32_16x16x32_bf16 v[204:207], a[212:215], a[124:127], v[204:207]\n"
"v_mfma_f32_16x16x32_bf16 v[208:211], a[208:211], a[128:131], v[208:211]\n"
"v_mfma_f32_16x16x32_bf16 v[208:211], a[212:215], a[132:135], v[208:211]\n"
"v_mfma_f32_16x16x32_bf16 v[212:215], a[208:211], a[136:139], v[212:215]\n"
"v_mfma_f32_16x16x32_bf16 v[212:215], a[212:215], a[140:143], v[212:215]\n"
"v_mfma_f32_16x16x32_bf16 v[216:219], a[208:211], a[144:147], v[216:219]\n"
"v_mfma_f32_16x16x32_bf16 v[216:219], a[212:215], a[148:151], v[216:219]\n"
"s_barrier\n"
"v_mfma_f32_16x16x32_bf16 v[220:223], a[208:211], a[152:155], v[220:223]\n"
"global_load_dwordx4 a[208:211], v246, s[16:17] offset:0 sc1\n"
"v_mfma_f32_16x16x32_bf16 v[220:223], a[212:215], a[156:159], v[220:223]\n"
"s_waitcnt vmcnt(19)\n"
"v_mfma_f32_16x16x32_bf16 a[0:3], a[216:219], a[96:99], a[0:3]\n"
"global_load_dwordx4 a[212:215], v246, s[16:17] offset:1024 sc1\n"
"ds_read_b128 a[96:99], v242 offset:0\n"
"v_mfma_f32_16x16x32_bf16 a[0:3], a[220:223], a[100:103], a[0:3]\n"
"ds_read_b128 a[100:103], v243 offset:0\n"
"v_mfma_f32_16x16x32_bf16 a[4:7], a[216:219], a[104:107], a[4:7]\n"
"ds_read_b128 a[104:107], v242 offset:2048\n"
"v_mfma_f32_16x16x32_bf16 a[4:7], a[220:223], a[108:111], a[4:7]\n"
"ds_read_b128 a[108:111], v243 offset:2048\n"
"v_mfma_f32_16x16x32_bf16 a[8:11], a[216:219], a[112:115], a[8:11]\n"
"ds_read_b128 a[112:115], v242 offset:4096\n"
"v_mfma_f32_16x16x32_bf16 a[8:11], a[220:223], a[116:119], a[8:11]\n"
"ds_read_b128 a[116:119], v243 offset:4096\n"
"v_mfma_f32_16x16x32_bf16 a[12:15], a[216:219], a[120:123], a[12:15]\n"
"ds_read_b128 a[120:123], v242 offset:6144\n"
"v_mfma_f32_16x16x32_bf16 a[12:15], a[220:223], a[124:127], a[12:15]\n"
"ds_read_b128 a[124:127], v243 offset:6144\n"
"v_mfma_f32_16x16x32_bf16 a[16:19], a[216:219], a[128:131], a[16:19]\n"
"ds_read_b128 a[128:131], v242 offset:8192\n"
"v_mfma_f32_16x16x32_bf16 a[16:19], a[220:223], a[132:135], a[16:19]\n"
"ds_read_b128 a[132:135], v243 offset:8192\n"
"v_mfma_f32_16x16x32_bf16 a[20:23], a[216:219], a[136:139], a[20:23]\n"
"ds_read_b128 a[136:139], v242 offset:10240\n"
"v_mfma_f32_16x16x32_bf16 a[20:23], a[220:223], a[140:143], a[20:23]\n"
"ds_read_b128 a[140:143], v243 offset:10240\n"
"v_mfma_f32_16x16x32_bf16 a[24:27], a[216:219], a[144:147], a[24:27]\n"
"ds_read_b128 a[144:147], v242 offset:12288\n"
"v_mfma_f32_16x16x32_bf16 a[24:27], a[220:223], a[148:151], a[24:27]\n"
"ds_read_b128 a[148:151], v243 offset:12288\n"
"v_mfma_f32_16x16x32_bf16 a[28:31], a[216:219], a[152:155], a[28:31]\n"
"global_load_dwordx4 a[216:219], v246, s[16:17] offset:2048 sc1\n"
"ds_read_b128 a[152:155], v242 offset:14336\n"
"v_mfma_f32_16x16x32_bf16 a[28:31], a[220:223], a[156:159], a[28:31]\n"
"ds_read_b128 a[156:159], v243 offset:14336\n"
"s_waitcnt vmcnt(17) lgkmcnt(14)\n"
"v_mfma_f32_16x16x32_bf16 v[0:3], a[160:163], a[96:99], v[0:3]\n"
"global_load_dwordx4 a[220:223], v246, s[16:17] offset:3072 sc1\n"
"v_mfma_f32_16x16x32_bf16 v[0:3], a[164:167], a[100:103], v[0:3]\n"
"s_waitcnt lgkmcnt(12)\n"
"v_mfma_f32_16x16x32_bf16 v[4:7], a[160:163], a[104:107], v[4:7]\n"
"v_add_u32 v246, 0x2000, v246\n"
"v_mfma_f32_16x16x32_bf16 v[4:7], a[164:167], a[108:111], v[4:7]\n"
"s_waitcnt lgkmcnt(10)\n"
"v_mfma_f32_16x16x32_bf16 v[8:11], a[160:163], a[112:115], v[8:11]\n"
"s_mul_i32 s45, s50, -114688\n"
"s_add_u32 s45, s45, 147456\n"
"s_add_u32 s45, s45, s42\n"
"v_mfma_f32_16x16x32_bf16 v[8:11], a[164:167], a[116:119], v[8:11]\n"
"s_waitcnt lgkmcnt(8)\n"
"v_mfma_f32_16x16x32_bf16 v[12:15], a[160:163], a[120:123], v[12:15]\n"
"s_add_u32 m0, s45, 0\n"
"v_add_u32 v238, 256, v248\n"
"v_mfma_f32_16x16x32_bf16 v[12:15], a[164:167], a[124:127], v[12:15]\n"
"global_load_lds_dwordx4 v238, s[20:21] offset:0\n"
"s_waitcnt lgkmcnt(6)\n"
"v_mfma_f32_16x16x32_bf16 v[16:19], a[160:163], a[128:131], v[16:19]\n"
"v_mfma_f32_16x16x32_bf16 v[16:19], a[164:167], a[132:135], v[16:19]\n"
"s_waitcnt lgkmcnt(4)\n"
"v_mfma_f32_16x16x32_bf16 v[20:23], a[160:163], a[136:139], v[20:23]\n"
"v_mfma_f32_16x16x32_bf16 v[20:23], a[164:167], a[140:143], v[20:23]\n"
"v_add_u32 v238, s66, v238\n"
"s_waitcnt lgkmcnt(2)\n"
"v_mfma_f32_16x16x32_bf16 v[24:27], a[160:163], a[144:147], v[24:27]\n"
"global_load_lds_dwordx4 v238, s[20:21] offset:1024\n"
"v_mfma_f32_16x16x32_bf16 v[24:27], a[164:167], a[148:151], v[24:27]\n"
"s_waitcnt lgkmcnt(0)\n"
"v_mfma_f32_16x16x32_bf16 v[28:31], a[160:163], a[152:155], v[28:31]\n"
"global_load_dwordx4 a[160:163], v246, s[16:17] offset:-4096 sc1\n"
"v_mfma_f32_16x16x32_bf16 v[28:31], a[164:167], a[156:159], v[28:31]\n"
"s_waitcnt vmcnt(17)\n"
"v_mfma_f32_16x16x32_bf16 v[32:35], a[168:171], a[96:99], v[32:35]\n"
"global_load_dwordx4 a[164:167], v246, s[16:17] offset:-3072 sc1\n"
"v_add_u32 v238, s66, v238\n"
"v_mfma_f32_16x16x32_bf16 v[32:35], a[172:175], a[100:103], v[32:35]\n"
"global_load_lds_dwordx4 v238, s[20:21] offset:2048\n"
"v_mfma_f32_16x16x32_bf16 v[36:39], a[168:171], a[104:107], v[36:39]\n"
"v_mfma_f32_16x16x32_bf16 v[36:39], a[172:175], a[108:111], v[36:39]\n"
"v_mfma_f32_16x16x32_bf16 v[40:43], a[168:171], a[112:115], v[40:43]\n"
"v_mfma_f32_16x16x32_bf16 v[40:43], a[172:175], a[116:119], v[40:43]\n"
"v_add_u32 v238, s66, v238\n"
"global_load_lds_dwordx4 v238, s[20:21] offset:3072\n"
"v_mfma_f32_16x16x32_bf16 v[44:47], a[168:171], a[120:123], v[44:47]\n"
"v_mfma_f32_16x16x32_bf16 v[44:47], a[172:175], a[124:127], v[44:47]\n"
"v_mfma_f32_16x16x32_bf16 v[48:51], a[168:171], a[128:131], v[48:51]\n"
"v_mfma_f32_16x16x32_bf16 v[48:51], a[172:175], a[132:135], v[48:51]\n"
"v_mfma_f32_16x16x32_bf16 v[52:55], a[168:171], a[136:139], v[52:55]\n"
"v_mfma_f32_16x16x32_bf16 v[52:55], a[172:175], a[140:143], v[52:55]\n"
"v_mfma_f32_16x16x32_bf16 v[56:59], a[168:171], a[144:147], v[56:59]\n"
"v_mfma_f32_16x16x32_bf16 v[56:59], a[172:175], a[148:151], v[56:59]\n"
"v_mfma_f32_16x16x32_bf16 v[60:63], a[168:171], a[152:155], v[60:63]\n"
"global_load_dwordx4 a[168:171], v246, s[16:17] offset:-2048 sc1\n"
"v_mfma_f32_16x16x32_bf16 v[60:63], a[172:175], a[156:159], v[60:63]\n"
"s_waitcnt vmcnt(17)\n"
"v_mfma_f32_16x16x32_bf16 v[64:67], a[176:179], a[96:99], v[64:67]\n"
"global_load_dwordx4 a[172:175], v246, s[16:17] offset:-1024 sc1\n"
"v_mfma_f32_16x16x32_bf16 v[64:67], a[180:183], a[100:103], v[64:67]\n"
"v_mfma_f32_16x16x32_bf16 v[68:71], a[176:179], a[104:107], v[68:71]\n"
"s_mul_i32 s48, s50, -131072\n"
"s_add_u32 s48, s48, 131072\n"
"v_mfma_f32_16x16x32_bf16 v[68:71], a[180:183], a[108:111], v[68:71]\n"
"v_add_u32 v242, s48, v240\n"
"v_mfma_f32_16x16x32_bf16 v[72:75], a[176:179], a[112:115], v[72:75]\n"
"v_add_u32 v243, s48, v241\n"
"v_mfma_f32_16x16x32_bf16 v[72:75], a[180:183], a[116:119], v[72:75]\n"
"v_mfma_f32_16x16x32_bf16 v[76:79], a[176:179], a[120:123], v[76:79]\n"
"v_mfma_f32_16x16x32_bf16 v[76:79], a[180:183], a[124:127], v[76:79]\n"
"v_mfma_f32_16x16x32_bf16 v[80:83], a[176:179], a[128:131], v[80:83]\n"
"s_add_u32 m0, s57, 107520\n"
"v_add_u32 v238, 4202496, v234\n"
"v_mfma_f32_16x16x32_bf16 v[80:83], a[180:183], a[132:135], v[80:83]\n"
"global_load_lds_dwordx4 v238, s[40:41] offset:0 nt\n"
"v_mfma_f32_16x16x32_bf16 v[84:87], a[176:179], a[136:139], v[84:87]\n"
"v_mfma_f32_16x16x32_bf16 v[84:87], a[180:183], a[140:143], v[84:87]\n"
"v_mfma_f32_16x16x32_bf16 v[88:91], a[176:179], a[144:147], v[88:91]\n"
"v_mfma_f32_16x16x32_bf16 v[88:91], a[180:183], a[148:151], v[88:91]\n"
"v_mfma_f32_16x16x32_bf16 v[92:95], a[176:179], a[152:155], v[92:95]\n"
"global_load_dwordx4 a[176:179], v246, s[16:17] offset:0 sc1\n"
"v_mfma_f32_16x16x32_bf16 v[92:95], a[180:183], a[156:159], v[92:95]\n"
"s_waitcnt vmcnt(18)\n"
"v_mfma_f32_16x16x32_bf16 v[96:99], a[184:187], a[96:99], v[96:99]\n"
"global_load_dwordx4 a[180:183], v246, s[16:17] offset:1024 sc1\n"
"v_mfma_f32_16x16x32_bf16 v[96:99], a[188:191], a[100:103], v[96:99]\n"
"v_mfma_f32_16x16x32_bf16 v[100:103], a[184:187], a[104:107], v[100:103]\n"
"v_mfma_f32_16x16x32_bf16 v[100:103], a[188:191], a[108:111], v[100:103]\n"
"v_mfma_f32_16x16x32_bf16 v[104:107], a[184:187], a[112:115], v[104:107]\n"
"v_mfma_f32_16x16x32_bf16 v[104:107], a[188:191], a[116:119], v[104:107]\n"
"v_mfma_f32_16x16x32_bf16 v[108:111], a[184:187], a[120:123], v[108:111]\n"
"v_mfma_f32_16x16x32_bf16 v[108:111], a[188:191], a[124:127], v[108:111]\n"
"v_mfma_f32_16x16x32_bf16 v[112:115], a[184:187], a[128:131], v[112:115]\n"
"v_mfma_f32_16x16x32_bf16 v[112:115], a[188:191], a[132:135], v[112:115]\n"
"v_mfma_f32_16x16x32_bf16 v[116:119], a[184:187], a[136:139], v[116:119]\n"
"v_mfma_f32_16x16x32_bf16 v[116:119], a[188:191], a[140:143], v[116:119]\n"
"v_mfma_f32_16x16x32_bf16 v[120:123], a[184:187], a[144:147], v[120:123]\n"
"v_mfma_f32_16x16x32_bf16 v[120:123], a[188:191], a[148:151], v[120:123]\n"
"v_mfma_f32_16x16x32_bf16 v[124:127], a[184:187], a[152:155], v[124:127]\n"
"global_load_dwordx4 a[184:187], v246, s[16:17] offset:2048 sc1\n"
"v_mfma_f32_16x16x32_bf16 v[124:127], a[188:191], a[156:159], v[124:127]\n"
"s_waitcnt vmcnt(18)\n"
"v_mfma_f32_16x16x32_bf16 v[128:131], a[192:195], a[96:99], v[128:131]\n"
"global_load_dwordx4 a[188:191], v246, s[16:17] offset:3072 sc1\n"
"v_mfma_f32_16x16x32_bf16 v[128:131], a[196:199], a[100:103], v[128:131]\n"
"v_mfma_f32_16x16x32_bf16 v[132:135], a[192:195], a[104:107], v[132:135]\n"
"v_add_u32 v246, 0x2000, v246\n"
"v_mfma_f32_16x16x32_bf16 v[132:135], a[196:199], a[108:111], v[132:135]\n"
"v_mfma_f32_16x16x32_bf16 v[136:139], a[192:195], a[112:115], v[136:139]\n"
"v_mfma_f32_16x16x32_bf16 v[136:139], a[196:199], a[116:119], v[136:139]\n"
"v_mfma_f32_16x16x32_bf16 v[140:143], a[192:195], a[120:123], v[140:143]\n"
"v_mfma_f32_16x16x32_bf16 v[140:143], a[196:199], a[124:127], v[140:143]\n"
"v_mfma_f32_16x16x32_bf16 v[144:147], a[192:195], a[128:131], v[144:147]\n"
"v_mfma_f32_16x16x32_bf16 v[144:147], a[196:199], a[132:135], v[144:147]\n"
"v_mfma_f32_16x16x32_bf16 v[148:151], a[192:195], a[136:139], v[148:151]\n"
"v_mfma_f32_16x16x32_bf16 v[148:151], a[196:199], a[140:143], v[148:151]\n"
"v_mfma_f32_16x16x32_bf16 v[152:155], a[192:195], a[144:147], v[152:155]\n"
"v_mfma_f32_16x16x32_bf16 v[152:155], a[196:199], a[148:151], v[152:155]\n"
"v_mfma_f32_16x16x32_bf16 v[156:159], a[192:195], a[152:155], v[156:159]\n"
"global_load_dwordx4 a[192:195], v246, s[16:17] offset:-4096 sc1\n"
"v_mfma_f32_16x16x32_bf16 v[156:159], a[196:199], a[156:159], v[156:159]\n"
"s_waitcnt vmcnt(18)\n"
"v_mfma_f32_16x16x32_bf16 v[160:163], a[200:203], a[96:99], v[160:163]\n"
"global_load_dwordx4 a[196:199], v246, s[16:17] offset:-3072 sc1\n"
"v_mfma_f32_16x16x32_bf16 v[160:163], a[204:207], a[100:103], v[160:163]\n"
"v_mfma_f32_16x16x32_bf16 v[164:167], a[200:203], a[104:107], v[164:167]\n"
"v_mfma_f32_16x16x32_bf16 v[164:167], a[204:207], a[108:111], v[164:167]\n"
"v_mfma_f32_16x16x32_bf16 v[168:171], a[200:203], a[112:115], v[168:171]\n"
"v_mfma_f32_16x16x32_bf16 v[168:171], a[204:207], a[116:119], v[168:171]\n"
"v_mfma_f32_16x16x32_bf16 v[172:175], a[200:203], a[120:123], v[172:175]\n"
"v_mfma_f32_16x16x32_bf16 v[172:175], a[204:207], a[124:127], v[172:175]\n"
"v_mfma_f32_16x16x32_bf16 v[176:179], a[200:203], a[128:131], v[176:179]\n"
"v_mfma_f32_16x16x32_bf16 v[176:179], a[204:207], a[132:135], v[176:179]\n"
"v_mfma_f32_16x16x32_bf16 v[180:183], a[200:203], a[136:139], v[180:183]\n"
"v_mfma_f32_16x16x32_bf16 v[180:183], a[204:207], a[140:143], v[180:183]\n"
"v_mfma_f32_16x16x32_bf16 v[184:187], a[200:203], a[144:147], v[184:187]\n"
"v_mfma_f32_16x16x32_bf16 v[184:187], a[204:207], a[148:151], v[184:187]\n"
"v_mfma_f32_16x16x32_bf16 v[188:191], a[200:203], a[152:155], v[188:191]\n"
"global_load_dwordx4 a[200:203], v246, s[16:17] offset:-2048 sc1\n"
"v_mfma_f32_16x16x32_bf16 v[188:191], a[204:207], a[156:159], v[188:191]\n"
"s_waitcnt vmcnt(18)\n"
"v_mfma_f32_16x16x32_bf16 v[192:195], a[208:211], a[96:99], v[192:195]\n"
"global_load_dwordx4 a[204:207], v246, s[16:17] offset:-1024 sc1\n"
"v_mfma_f32_16x16x32_bf16 v[192:195], a[212:215], a[100:103], v[192:195]\n"
"v_mfma_f32_16x16x32_bf16 v[196:199], a[208:211], a[104:107], v[196:199]\n"
"v_mfma_f32_16x16x32_bf16 v[196:199], a[212:215], a[108:111], v[196:199]\n"
"v_mfma_f32_16x16x32_bf16 v[200:203], a[208:211], a[112:115], v[200:203]\n"
"v_mfma_f32_16x16x32_bf16 v[200:203], a[212:215], a[116:119], v[200:203]\n"
"v_mfma_f32_16x16x32_bf16 v[204:207], a[208:211], a[120:123], v[204:207]\n"
"v_mfma_f32_16x16x32_bf16 v[204:207], a[212:215], a[124:127], v[204:207]\n"
"v_mfma_f32_16x16x32_bf16 v[208:211], a[208:211], a[128:131], v[208:211]\n"
"v_mfma_f32_16x16x32_bf16 v[208:211], a[212:215], a[132:135], v[208:211]\n"
"v_mfma_f32_16x16x32_bf16 v[212:215], a[208:211], a[136:139], v[212:215]\n"
"v_mfma_f32_16x16x32_bf16 v[212:215], a[212:215], a[140:143], v[212:215]\n"
"v_mfma_f32_16x16x32_bf16 v[216:219], a[208:211], a[144:147], v[216:219]\n"
"v_mfma_f32_16x16x32_bf16 v[216:219], a[212:215], a[148:151], v[216:219]\n"
"s_barrier\n"
"v_mfma_f32_16x16x32_bf16 v[220:223], a[208:211], a[152:155], v[220:223]\n"
"global_load_dwordx4 a[208:211], v246, s[16:17] offset:0 sc1\n"
"v_mfma_f32_16x16x32_bf16 v[220:223], a[212:215], a[156:159], v[220:223]\n"
"s_waitcnt vmcnt(18)\n"
"v_mfma_f32_16x16x32_bf16 a[0:3], a[216:219], a[96:99], a[0:3]\n"
"global_load_dwordx4 a[212:215], v246, s[16:17] offset:1024 sc1\n"
"ds_read_b128 a[96:99], v242 offset:0\n"
"v_mfma_f32_16x16x32_bf16 a[0:3], a[220:223], a[100:103], a[0:3]\n"
"ds_read_b128 a[100:103], v243 offset:0\n"
"v_mfma_f32_16x16x32_bf16 a[4:7], a[216:219], a[104:107], a[4:7]\n"
"ds_read_b128 a[104:107], v242 offset:2048\n"
"v_mfma_f32_16x16x32_bf16 a[4:7], a[220:223], a[108:111], a[4:7]\n"
"ds_read_b128 a[108:111], v243 offset:2048\n"
"v_mfma_f32_16x16x32_bf16 a[8:11], a[216:219], a[112:115], a[8:11]\n"
"ds_read_b128 a[112:115], v242 offset:4096\n"
"v_mfma_f32_16x16x32_bf16 a[8:11], a[220:223], a[116:119], a[8:11]\n"
"ds_read_b128 a[116:119], v243 offset:4096\n"
"v_mfma_f32_16x16x32_bf16 a[12:15], a[216:219], a[120:123], a[12:15]\n"
"ds_read_b128 a[120:123], v242 offset:6144\n"
"v_mfma_f32_16x16x32_bf16 a[12:15], a[220:223], a[124:127], a[12:15]\n"
"ds_read_b128 a[124:127], v243 offset:6144\n"
"v_mfma_f32_16x16x32_bf16 a[16:19], a[216:219], a[128:131], a[16:19]\n"
"ds_read_b128 a[128:131], v242 offset:8192\n"
"v_mfma_f32_16x16x32_bf16 a[16:19], a[220:223], a[132:135], a[16:19]\n"
"ds_read_b128 a[132:135], v243 offset:8192\n"
"v_mfma_f32_16x16x32_bf16 a[20:23], a[216:219], a[136:139], a[20:23]\n"
"ds_read_b128 a[136:139], v242 offset:10240\n"
"v_mfma_f32_16x16x32_bf16 a[20:23], a[220:223], a[140:143], a[20:23]\n"
"ds_read_b128 a[140:143], v243 offset:10240\n"
"v_mfma_f32_16x16x32_bf16 a[24:27], a[216:219], a[144:147], a[24:27]\n"
"ds_read_b128 a[144:147], v242 offset:12288\n"
"v_mfma_f32_16x16x32_bf16 a[24:27], a[220:223], a[148:151], a[24:27]\n"
"ds_read_b128 a[148:151], v243 offset:12288\n"
"v_mfma_f32_16x16x32_bf16 a[28:31], a[216:219], a[152:155], a[28:31]\n"
"global_load_dwordx4 a[216:219], v246, s[16:17] offset:2048 sc1\n"
"ds_read_b128 a[152:155], v242 offset:14336\n"
"v_mfma_f32_16x16x32_bf16 a[28:31], a[220:223], a[156:159], a[28:31]\n"
"ds_read_b128 a[156:159], v243 offset:14336\n"
"s_waitcnt vmcnt(16) lgkmcnt(14)\n"
"v_mfma_f32_16x16x32_bf16 v[0:3], a[160:163], a[96:99], v[0:3]\n"
"global_load_dwordx4 a[220:223], v246, s[16:17] offset:3072 sc1\n"
"v_mfma_f32_16x16x32_bf16 v[0:3], a[164:167], a[100:103], v[0:3]\n"
"s_waitcnt lgkmcnt(12)\n"
"v_mfma_f32_16x16x32_bf16 v[4:7], a[160:163], a[104:107], v[4:7]\n"
"v_add_u32 v246, 0x2000, v246\n"
"v_mfma_f32_16x16x32_bf16 v[4:7], a[164:167], a[108:111], v[4:7]\n"
"s_waitcnt lgkmcnt(10)\n"
"v_mfma_f32_16x16x32_bf16 v[8:11], a[160:163], a[112:115], v[8:11]\n"
"s_mul_i32 s45, s49, -114688\n"
"s_add_u32 s45, s45, 147456\n"
"s_add_u32 s45, s45, s42\n"
"v_mfma_f32_16x16x32_bf16 v[8:11], a[164:167], a[116:119], v[8:11]\n"
"s_waitcnt lgkmcnt(8)\n"
"v_mfma_f32_16x16x32_bf16 v[12:15], a[160:163], a[120:123], v[12:15]\n"
"s_add_u32 m0, s45, 0\n"
"v_add_u32 v238, 1280, v248\n"
"v_mfma_f32_16x16x32_bf16 v[12:15], a[164:167], a[124:127], v[12:15]\n"
"global_load_lds_dwordx4 v238, s[20:21] offset:0\n"
"s_waitcnt lgkmcnt(6)\n"
"v_mfma_f32_16x16x32_bf16 v[16:19], a[160:163], a[128:131], v[16:19]\n"
"v_mfma_f32_16x16x32_bf16 v[16:19], a[164:167], a[132:135], v[16:19]\n"
"s_waitcnt lgkmcnt(4)\n"
"v_mfma_f32_16x16x32_bf16 v[20:23], a[160:163], a[136:139], v[20:23]\n"
"v_mfma_f32_16x16x32_bf16 v[20:23], a[164:167], a[140:143], v[20:23]\n"
"v_add_u32 v238, s66, v238\n"
"s_waitcnt lgkmcnt(2)\n"
"v_mfma_f32_16x16x32_bf16 v[24:27], a[160:163], a[144:147], v[24:27]\n"
"global_load_lds_dwordx4 v238, s[20:21] offset:1024\n"
"v_mfma_f32_16x16x32_bf16 v[24:27], a[164:167], a[148:151], v[24:27]\n"
"s_waitcnt lgkmcnt(0)\n"
"v_mfma_f32_16x16x32_bf16 v[28:31], a[160:163], a[152:155], v[28:31]\n"
"global_load_dwordx4 a[160:163], v246, s[16:17] offset:-4096 sc1\n"
"v_mfma_f32_16x16x32_bf16 v[28:31], a[164:167], a[156:159], v[28:31]\n"
"s_waitcnt vmcnt(16)\n"
"v_mfma_f32_16x16x32_bf16 v[32:35], a[168:171], a[96:99], v[32:35]\n"
"global_load_dwordx4 a[164:167], v246, s[16:17] offset:-3072 sc1\n"
"v_add_u32 v238, s66, v238\n"
"v_mfma_f32_16x16x32_bf16 v[32:35], a[172:175], a[100:103], v[32:35]\n"
"global_load_lds_dwordx4 v238, s[20:21] offset:2048\n"
"v_mfma_f32_16x16x32_bf16 v[36:39], a[168:171], a[104:107], v[36:39]\n"
"v_mfma_f32_16x16x32_bf16 v[36:39], a[172:175], a[108:111], v[36:39]\n"
"v_mfma_f32_16x16x32_bf16 v[40:43], a[168:171], a[112:115], v[40:43]\n"
"v_mfma_f32_16x16x32_bf16 v[40:43], a[172:175], a[116:119], v[40:43]\n"
"v_add_u32 v238, s66, v238\n"
"global_load_lds_dwordx4 v238, s[20:21] offset:3072\n"
"v_mfma_f32_16x16x32_bf16 v[44:47], a[168:171], a[120:123], v[44:47]\n"
"v_mfma_f32_16x16x32_bf16 v[44:47], a[172:175], a[124:127], v[44:47]\n"
"v_mfma_f32_16x16x32_bf16 v[48:51], a[168:171], a[128:131], v[48:51]\n"
"v_mfma_f32_16x16x32_bf16 v[48:51], a[172:175], a[132:135], v[48:51]\n"
"v_mfma_f32_16x16x32_bf16 v[52:55], a[168:171], a[136:139], v[52:55]\n"
"v_mfma_f32_16x16x32_bf16 v[52:55], a[172:175], a[140:143], v[52:55]\n"
"v_mfma_f32_16x16x32_bf16 v[56:59], a[168:171], a[144:147], v[56:59]\n"
"v_mfma_f32_16x16x32_bf16 v[56:59], a[172:175], a[148:151], v[56:59]\n"
"v_mfma_f32_16x16x32_bf16 v[60:63], a[168:171], a[152:155], v[60:63]\n"
"global_load_dwordx4 a[168:171], v246, s[16:17] offset:-2048 sc1\n"
"v_mfma_f32_16x16x32_bf16 v[60:63], a[172:175], a[156:159], v[60:63]\n"
"s_waitcnt vmcnt(17)\n"
"v_mfma_f32_16x16x32_bf16 v[64:67], a[176:179], a[96:99], v[64:67]\n"
"global_load_dwordx4 a[172:175], v246, s[16:17] offset:-1024 sc1\n"
"v_mfma_f32_16x16x32_bf16 v[64:67], a[180:183], a[100:103], v[64:67]\n"
"v_mfma_f32_16x16x32_bf16 v[68:71], a[176:179], a[104:107], v[68:71]\n"
"s_mul_i32 s48, s49, -131072\n"
"s_add_u32 s48, s48, 131072\n"
"v_mfma_f32_16x16x32_bf16 v[68:71], a[180:183], a[108:111], v[68:71]\n"
"v_add_u32 v242, s48, v240\n"
"v_mfma_f32_16x16x32_bf16 v[72:75], a[176:179], a[112:115], v[72:75]\n"
"v_add_u32 v243, s48, v241\n"
"v_mfma_f32_16x16x32_bf16 v[72:75], a[180:183], a[116:119], v[72:75]\n"
"v_mfma_f32_16x16x32_bf16 v[76:79], a[176:179], a[120:123], v[76:79]\n"
"v_mfma_f32_16x16x32_bf16 v[76:79], a[180:183], a[124:127], v[76:79]\n"
"v_mfma_f32_16x16x32_bf16 v[80:83], a[176:179], a[128:131], v[80:83]\n"
"v_mfma_f32_16x16x32_bf16 v[80:83], a[180:183], a[132:135], v[80:83]\n"
"v_mfma_f32_16x16x32_bf16 v[84:87], a[176:179], a[136:139], v[84:87]\n"
"v_mfma_f32_16x16x32_bf16 v[84:87], a[180:183], a[140:143], v[84:87]\n"
"v_mfma_f32_16x16x32_bf16 v[88:91], a[176:179], a[144:147], v[88:91]\n"
"v_mfma_f32_16x16x32_bf16 v[88:91], a[180:183], a[148:151], v[88:91]\n"
"v_mfma_f32_16x16x32_bf16 v[92:95], a[176:179], a[152:155], v[92:95]\n"
"global_load_dwordx4 a[176:179], v246, s[16:17] offset:0 sc1\n"
"v_mfma_f32_16x16x32_bf16 v[92:95], a[180:183], a[156:159], v[92:95]\n"
"s_waitcnt vmcnt(17)\n"
"v_mfma_f32_16x16x32_bf16 v[96:99], a[184:187], a[96:99], v[96:99]\n"
"global_load_dwordx4 a[180:183], v246, s[16:17] offset:1024 sc1\n"
"v_mfma_f32_16x16x32_bf16 v[96:99], a[188:191], a[100:103], v[96:99]\n"
"v_mfma_f32_16x16x32_bf16 v[100:103], a[184:187], a[104:107], v[100:103]\n"
"v_mfma_f32_16x16x32_bf16 v[100:103], a[188:191], a[108:111], v[100:103]\n"
"v_mfma_f32_16x16x32_bf16 v[104:107], a[184:187], a[112:115], v[104:107]\n"
"v_mfma_f32_16x16x32_bf16 v[104:107], a[188:191], a[116:119], v[104:107]\n"
"v_mfma_f32_16x16x32_bf16 v[108:111], a[184:187], a[120:123], v[108:111]\n"
"v_mfma_f32_16x16x32_bf16 v[108:111], a[188:191], a[124:127], v[108:111]\n"
"v_mfma_f32_16x16x32_bf16 v[112:115], a[184:187], a[128:131], v[112:115]\n"
"v_mfma_f32_16x16x32_bf16 v[112:115], a[188:191], a[132:135], v[112:115]\n"
"v_mfma_f32_16x16x32_bf16 v[116:119], a[184:187], a[136:139], v[116:119]\n"
"v_mfma_f32_16x16x32_bf16 v[116:119], a[188:191], a[140:143], v[116:119]\n"
"v_mfma_f32_16x16x32_bf16 v[120:123], a[184:187], a[144:147], v[120:123]\n"
"v_mfma_f32_16x16x32_bf16 v[120:123], a[188:191], a[148:151], v[120:123]\n"
"v_mfma_f32_16x16x32_bf16 v[124:127], a[184:187], a[152:155], v[124:127]\n"
"global_load_dwordx4 a[184:187], v246, s[16:17] offset:2048 sc1\n"
"v_mfma_f32_16x16x32_bf16 v[124:127], a[188:191], a[156:159], v[124:127]\n"
"s_waitcnt vmcnt(17)\n"
"v_mfma_f32_16x16x32_bf16 v[128:131], a[192:195], a[96:99], v[128:131]\n"
"global_load_dwordx4 a[188:191], v246, s[16:17] offset:3072 sc1\n"
"v_mfma_f32_16x16x32_bf16 v[128:131], a[196:199], a[100:103], v[128:131]\n"
"v_mfma_f32_16x16x32_bf16 v[132:135], a[192:195], a[104:107], v[132:135]\n"
"v_add_u32 v246, 0x2000, v246\n"
"v_mfma_f32_16x16x32_bf16 v[132:135], a[196:199], a[108:111], v[132:135]\n"
"v_mfma_f32_16x16x32_bf16 v[136:139], a[192:195], a[112:115], v[136:139]\n"
"v_mfma_f32_16x16x32_bf16 v[136:139], a[196:199], a[116:119], v[136:139]\n"
"v_mfma_f32_16x16x32_bf16 v[140:143], a[192:195], a[120:123], v[140:143]\n"
"v_mfma_f32_16x16x32_bf16 v[140:143], a[196:199], a[124:127], v[140:143]\n"
"v_mfma_f32_16x16x32_bf16 v[144:147], a[192:195], a[128:131], v[144:147]\n"
"v_mfma_f32_16x16x32_bf16 v[144:147], a[196:199], a[132:135], v[144:147]\n"
"v_mfma_f32_16x16x32_bf16 v[148:151], a[192:195], a[136:139], v[148:151]\n"
"v_mfma_f32_16x16x32_bf16 v[148:151], a[196:199], a[140:143], v[148:151]\n"
"v_mfma_f32_16x16x32_bf16 v[152:155], a[192:195], a[144:147], v[152:155]\n"
"v_mfma_f32_16x16x32_bf16 v[152:155], a[196:199], a[148:151], v[152:155]\n"
"v_mfma_f32_16x16x32_bf16 v[156:159], a[192:195], a[152:155], v[156:159]\n"
"s_nop 0\n"
".Lpg4_%=:\n"
"s_cbranch_execz .Lpg8_%=\n"
"global_load_dwordx4 a[192:195], v246, s[16:17] offset:-4096 sc1\n"
"v_mfma_f32_16x16x32_bf16 v[156:159], a[196:199], a[156:159], v[156:159]\n"
"s_waitcnt vmcnt(17)\n"
"v_mfma_f32_16x16x32_bf16 v[160:163], a[200:203], a[96:99], v[160:163]\n"
"global_load_dwordx4 a[196:199], v246, s[16:17] offset:-3072 sc1\n"
"v_mfma_f32_16x16x32_bf16 v[160:163], a[204:207], a[100:103], v[160:163]\n"
"v_mfma_f32_16x16x32_bf16 v[164:167], a[200:203], a[104:107], v[164:167]\n"
"v_mfma_f32_16x16x32_bf16 v[164:167], a[204:207], a[108:111], v[164:167]\n"
"v_mfma_f32_16x16x32_bf16 v[168:171], a[200:203], a[112:115], v[168:171]\n"
"v_mfma_f32_16x16x32_bf16 v[168:171], a[204:207], a[116:119], v[168:171]\n"
"v_mfma_f32_16x16x32_bf16 v[172:175], a[200:203], a[120:123], v[172:175]\n"
"v_mfma_f32_16x16x32_bf16 v[172:175], a[204:207], a[124:127], v[172:175]\n"
"v_mfma_f32_16x16x32_bf16 v[176:179], a[200:203], a[128:131], v[176:179]\n"
"v_mfma_f32_16x16x32_bf16 v[176:179], a[204:207], a[132:135], v[176:179]\n"
"v_mfma_f32_16x16x32_bf16 v[180:183], a[200:203], a[136:139], v[180:183]\n"
"v_mfma_f32_16x16x32_bf16 v[180:183], a[204:207], a[140:143], v[180:183]\n"
"v_mfma_f32_16x16x32_bf16 v[184:187], a[200:203], a[144:147], v[184:187]\n"
"v_mfma_f32_16x16x32_bf16 v[184:187], a[204:207], a[148:151], v[184:187]\n"
"v_mfma_f32_16x16x32_bf16 v[188:191], a[200:203], a[152:155], v[188:191]\n"
"global_load_dwordx4 a[200:203], v246, s[16:17] offset:-2048 sc1\n"
"v_mfma_f32_16x16x32_bf16 v[188:191], a[204:207], a[156:159], v[188:191]\n"
"s_waitcnt vmcnt(17)\n"
"v_mfma_f32_16x16x32_bf16 v[192:195], a[208:211], a[96:99], v[192:195]\n"
"global_load_dwordx4 a[204:207], v246, s[16:17] offset:-1024 sc1\n"
"v_mfma_f32_16x16x32_bf16 v[192:195], a[212:215], a[100:103], v[192:195]\n"
"v_mfma_f32_16x16x32_bf16 v[196:199], a[208:211], a[104:107], v[196:199]\n"
"v_mfma_f32_16x16x32_bf16 v[196:199], a[212:215], a[108:111], v[196:199]\n"
"v_mfma_f32_16x16x32_bf16 v[200:203], a[208:211], a[112:115], v[200:203]\n"
"v_mfma_f32_16x16x32_bf16 v[200:203], a[212:215], a[116:119], v[200:203]\n"
"v_mfma_f32_16x16x32_bf16 v[204:207], a[208:211], a[120:123], v[204:207]\n"
"v_mfma_f32_16x16x32_bf16 v[204:207], a[212:215], a[124:127], v[204:207]\n"
"v_mfma_f32_16x16x32_bf16 v[208:211], a[208:211], a[128:131], v[208:211]\n"
"v_mfma_f32_16x16x32_bf16 v[208:211], a[212:215], a[132:135], v[208:211]\n"
"v_mfma_f32_16x16x32_bf16 v[212:215], a[208:211], a[136:139], v[212:215]\n"
"v_mfma_f32_16x16x32_bf16 v[212:215], a[212:215], a[140:143], v[212:215]\n"
"v_mfma_f32_16x16x32_bf16 v[216:219], a[208:211], a[144:147], v[216:219]\n"
"v_mfma_f32_16x16x32_bf16 v[216:219], a[212:215], a[148:151], v[216:219]\n"
"s_barrier\n"
"v_mfma_f32_16x16x32_bf16 v[220:223], a[208:211], a[152:155], v[220:223]\n"
"global_load_dwordx4 a[208:211], v246, s[16:17] offset:0 sc1\n"
"v_mfma_f32_16x16x32_bf16 v[220:223], a[212:215], a[156:159], v[220:223]\n"
"s_waitcnt vmcnt(17)\n"
"v_mfma_f32_16x16x32_bf16 a[0:3], a[216:219], a[96:99], a[0:3]\n"
"global_load_dwordx4 a[212:215], v246, s[16:17] offset:1024 sc1\n"
"ds_read_b128 a[96:99], v242 offset:0\n"
"v_mfma_f32_16x16x32_bf16 a[0:3], a[220:223], a[100:103], a[0:3]\n"
"ds_read_b128 a[100:103], v243 offset:0\n"
"v_mfma_f32_16x16x32_bf16 a[4:7], a[216:219], a[104:107], a[4:7]\n"
"ds_read_b128 a[104:107], v242 offset:2048\n"
"v_mfma_f32_16x16x32_bf16 a[4:7], a[220:223], a[108:111], a[4:7]\n"
"ds_read_b128 a[108:111], v243 offset:2048\n"
"v_mfma_f32_16x16x32_bf16 a[8:11], a[216:219], a[112:115], a[8:11]\n"
"ds_read_b128 a[112:115], v242 offset:4096\n"
"v_mfma_f32_16x16x32_bf16 a[8:11], a[220:223], a[116:119], a[8:11]\n"
"ds_read_b128 a[116:119], v243 offset:4096\n"
"v_mfma_f32_16x16x32_bf16 a[12:15], a[216:219], a[120:123], a[12:15]\n"
"ds_read_b128 a[120:123], v242 offset:6144\n"
"v_mfma_f32_16x16x32_bf16 a[12:15], a[220:223], a[124:127], a[12:15]\n"
"ds_read_b128 a[124:127], v243 offset:6144\n"
"v_mfma_f32_16x16x32_bf16 a[16:19], a[216:219], a[128:131], a[16:19]\n"
"ds_read_b128 a[128:131], v242 offset:8192\n"
"v_mfma_f32_16x16x32_bf16 a[16:19], a[220:223], a[132:135], a[16:19]\n"
"ds_read_b128 a[132:135], v243 offset:8192\n"
"v_mfma_f32_16x16x32_bf16 a[20:23], a[216:219], a[136:139], a[20:23]\n"
"ds_read_b128 a[136:139], v242 offset:10240\n"
"v_mfma_f32_16x16x32_bf16 a[20:23], a[220:223], a[140:143], a[20:23]\n"
"ds_read_b128 a[140:143], v243 offset:10240\n"
"v_mfma_f32_16x16x32_bf16 a[24:27], a[216:219], a[144:147], a[24:27]\n"
"ds_read_b128 a[144:147], v242 offset:12288\n"
"v_mfma_f32_16x16x32_bf16 a[24:27], a[220:223], a[148:151], a[24:27]\n"
"ds_read_b128 a[148:151], v243 offset:12288\n"
"v_mfma_f32_16x16x32_bf16 a[28:31], a[216:219], a[152:155], a[28:31]\n"
"global_load_dwordx4 a[216:219], v246, s[16:17] offset:2048 sc1\n"
"ds_read_b128 a[152:155], v242 offset:14336\n"
"v_mfma_f32_16x16x32_bf16 a[28:31], a[220:223], a[156:159], a[28:31]\n"
"ds_read_b128 a[156:159], v243 offset:14336\n"
"s_waitcnt vmcnt(15) lgkmcnt(14)\n"
"v_mfma_f32_16x16x32_bf16 v[0:3], a[160:163], a[96:99], v[0:3]\n"
"global_load_dwordx4 a[220:223], v246, s[16:17] offset:3072 sc1\n"
"v_mfma_f32_16x16x32_bf16 v[0:3], a[164:167], a[100:103], v[0:3]\n"
"s_waitcnt lgkmcnt(12)\n"
"v_mfma_f32_16x16x32_bf16 v[4:7], a[160:163], a[104:107], v[4:7]\n"
"v_add_u32 v246, 0x2000, v246\n"
"v_mfma_f32_16x16x32_bf16 v[4:7], a[164:167], a[108:111], v[4:7]\n"
"s_waitcnt lgkmcnt(10)\n"
"v_mfma_f32_16x16x32_bf16 v[8:11], a[160:163], a[112:115], v[8:11]\n"
"s_mul_i32 s45, s50, -65536\n"
"s_add_u32 s45, s45, 131072\n"
"s_add_u32 s45, s45, s42\n"
"v_mfma_f32_16x16x32_bf16 v[8:11], a[164:167], a[116:119], v[8:11]\n"
"s_waitcnt lgkmcnt(8)\n"
"v_mfma_f32_16x16x32_bf16 v[12:15], a[160:163], a[120:123], v[12:15]\n"
"s_add_u32 m0, s45, 0\n"
"v_add_u32 v238, 512, v248\n"
"v_mfma_f32_16x16x32_bf16 v[12:15], a[164:167], a[124:127], v[12:15]\n"
"global_load_lds_dwordx4 v238, s[20:21] offset:0\n"
"s_waitcnt lgkmcnt(6)\n"
"v_mfma_f32_16x16x32_bf16 v[16:19], a[160:163], a[128:131], v[16:19]\n"
"s_add_u32 s64, s60, 20480\n"
"s_addc_u32 s65, s61, 0\n"
"v_mfma_f32_16x16x32_bf16 v[16:19], a[164:167], a[132:135], v[16:19]\n"
"s_waitcnt lgkmcnt(4)\n"
"v_mfma_f32_16x16x32_bf16 v[20:23], a[160:163], a[136:139], v[20:23]\n"
"v_mfma_f32_16x16x32_bf16 v[20:23], a[164:167], a[140:143], v[20:23]\n"
"v_add_u32 v238, s66, v238\n"
"s_waitcnt lgkmcnt(2)\n"
"v_mfma_f32_16x16x32_bf16 v[24:27], a[160:163], a[144:147], v[24:27]\n"
"global_load_lds_dwordx4 v238, s[20:21] offset:1024\n"
"v_mfma_f32_16x16x32_bf16 v[24:27], a[164:167], a[148:151], v[24:27]\n"
"s_waitcnt lgkmcnt(0)\n"
"v_mfma_f32_16x16x32_bf16 v[28:31], a[160:163], a[152:155], v[28:31]\n"
"global_load_dwordx4 a[160:163], v246, s[16:17] offset:-4096 sc1\n"
"v_mfma_f32_16x16x32_bf16 v[28:31], a[164:167], a[156:159], v[28:31]\n"
"s_waitcnt vmcnt(15)\n"
"v_mfma_f32_16x16x32_bf16 v[32:35], a[168:171], a[96:99], v[32:35]\n"
"global_load_dwordx4 a[164:167], v246, s[16:17] offset:-3072 sc1\n"
"v_add_u32 v238, s66, v238\n"
"v_mfma_f32_16x16x32_bf16 v[32:35], a[172:175], a[100:103], v[32:35]\n"
"global_load_lds_dwordx4 v238, s[20:21] offset:2048\n"
"v_mfma_f32_16x16x32_bf16 v[36:39], a[168:171], a[104:107], v[36:39]\n"
"v_mfma_f32_16x16x32_bf16 v[36:39], a[172:175], a[108:111], v[36:39]\n"
"v_mfma_f32_16x16x32_bf16 v[40:43], a[168:171], a[112:115], v[40:43]\n"
"v_mfma_f32_16x16x32_bf16 v[40:43], a[172:175], a[116:119], v[40:43]\n"
"v_add_u32 v238, s66, v238\n"
"global_load_lds_dwordx4 v238, s[20:21] offset:3072\n"
"v_mfma_f32_16x16x32_bf16 v[44:47], a[168:171], a[120:123], v[44:47]\n"
"v_mfma_f32_16x16x32_bf16 v[44:47], a[172:175], a[124:127], v[44:47]\n"
"v_mfma_f32_16x16x32_bf16 v[48:51], a[168:171], a[128:131], v[48:51]\n"
"v_mfma_f32_16x16x32_bf16 v[48:51], a[172:175], a[132:135], v[48:51]\n"
"v_mfma_f32_16x16x32_bf16 v[52:55], a[168:171], a[136:139], v[52:55]\n"
"v_mfma_f32_16x16x32_bf16 v[52:55], a[172:175], a[140:143], v[52:55]\n"
"v_mfma_f32_16x16x32_bf16 v[56:59], a[168:171], a[144:147], v[56:59]\n"
"v_mfma_f32_16x16x32_bf16 v[56:59], a[172:175], a[148:151], v[56:59]\n"
"v_mfma_f32_16x16x32_bf16 v[60:63], a[168:171], a[152:155], v[60:63]\n"
"global_load_dword a168, v250, s[64:65]\n"
"global_load_dwordx4 a[168:171], v246, s[16:17] offset:-2048 sc1\n"
"v_mfma_f32_16x16x32_bf16 v[60:63], a[172:175], a[156:159], v[60:63]\n"
"s_waitcnt vmcnt(18)\n"
"v_mfma_f32_16x16x32_bf16 v[64:67], a[176:179], a[96:99], v[64:67]\n"
"global_load_dwordx4 a[172:175], v246, s[16:17] offset:-1024 sc1\n"
"v_mfma_f32_16x16x32_bf16 v[64:67], a[180:183], a[100:103], v[64:67]\n"
"v_mfma_f32_16x16x32_bf16 v[68:71], a[176:179], a[104:107], v[68:71]\n"
"s_mul_i32 s48, s50, -114688\n"
"s_add_u32 s48, s48, 147456\n"
"v_mfma_f32_16x16x32_bf16 v[68:71], a[180:183], a[108:111], v[68:71]\n"
"v_add_u32 v242, s48, v240\n"
"v_mfma_f32_16x16x32_bf16 v[72:75], a[176:179], a[112:115], v[72:75]\n"
"v_add_u32 v243, s48, v241\n"
"v_mfma_f32_16x16x32_bf16 v[72:75], a[180:183], a[116:119], v[72:75]\n"
"v_mfma_f32_16x16x32_bf16 v[76:79], a[176:179], a[120:123], v[76:79]\n"
"v_mfma_f32_16x16x32_bf16 v[76:79], a[180:183], a[124:127], v[76:79]\n"
"v_mfma_f32_16x16x32_bf16 v[80:83], a[176:179], a[128:131], v[80:83]\n"
"v_mfma_f32_16x16x32_bf16 v[80:83], a[180:183], a[132:135], v[80:83]\n"
"v_mfma_f32_16x16x32_bf16 v[84:87], a[176:179], a[136:139], v[84:87]\n"
"v_mfma_f32_16x16x32_bf16 v[84:87], a[180:183], a[140:143], v[84:87]\n"
"v_mfma_f32_16x16x32_bf16 v[88:91], a[176:179], a[144:147], v[88:91]\n"
"ds_read_b64_tr_b16 a[40:41], v224 offset:0\n"
"v_mfma_f32_16x16x32_bf16 v[88:91], a[180:183], a[148:151], v[88:91]\n"
"v_mfma_f32_16x16x32_bf16 v[92:95], a[176:179], a[152:155], v[92:95]\n"
"global_load_dwordx4 a[176:179], v246, s[16:17] offset:0 sc1\n"
"v_mfma_f32_16x16x32_bf16 v[92:95], a[180:183], a[156:159], v[92:95]\n"
"s_waitcnt vmcnt(18)\n"
"v_mfma_f32_16x16x32_bf16 v[96:99], a[184:187], a[96:99], v[96:99]\n"
"global_load_dwordx4 a[180:183], v246, s[16:17] offset:1024 sc1\n"
"v_mfma_f32_16x16x32_bf16 v[96:99], a[188:191], a[100:103], v[96:99]\n"
"ds_read_b64_tr_b16 a[42:43], v224 offset:1024\n"
"v_mfma_f32_16x16x32_bf16 v[100:103], a[184:187], a[104:107], v[100:103]\n"
"v_mfma_f32_16x16x32_bf16 v[100:103], a[188:191], a[108:111], v[100:103]\n"
"ds_read_b64_tr_b16 a[44:45], v224 offset:8\n"
"v_mfma_f32_16x16x32_bf16 v[104:107], a[184:187], a[112:115], v[104:107]\n"
"v_mfma_f32_16x16x32_bf16 v[104:107], a[188:191], a[116:119], v[104:107]\n"
"ds_read_b64_tr_b16 a[46:47], v224 offset:1032\n"
"v_mfma_f32_16x16x32_bf16 v[108:111], a[184:187], a[120:123], v[108:111]\n"
"v_mfma_f32_16x16x32_bf16 v[108:111], a[188:191], a[124:127], v[108:111]\n"
"ds_read_b64_tr_b16 a[48:49], v224 offset:8192\n"
"v_mfma_f32_16x16x32_bf16 v[112:115], a[184:187], a[128:131], v[112:115]\n"
"v_mfma_f32_16x16x32_bf16 v[112:115], a[188:191], a[132:135], v[112:115]\n"
"ds_read_b64_tr_b16 a[50:51], v224 offset:9216\n"
"v_mfma_f32_16x16x32_bf16 v[116:119], a[184:187], a[136:139], v[116:119]\n"
"v_mfma_f32_16x16x32_bf16 v[116:119], a[188:191], a[140:143], v[116:119]\n"
"ds_read_b64_tr_b16 a[52:53], v224 offset:8200\n"
"v_mfma_f32_16x16x32_bf16 v[120:123], a[184:187], a[144:147], v[120:123]\n"
"v_mfma_f32_16x16x32_bf16 v[120:123], a[188:191], a[148:151], v[120:123]\n"
"ds_read_b64_tr_b16 a[54:55], v224 offset:9224\n"
"v_mfma_f32_16x16x32_bf16 v[124:127], a[184:187], a[152:155], v[124:127]\n"
"global_load_dwordx4 a[184:187], v246, s[16:17] offset:2048 sc1\n"
"v_mfma_f32_16x16x32_bf16 v[124:127], a[188:191], a[156:159], v[124:127]\n"
"s_waitcnt vmcnt(18)\n"
"v_mfma_f32_16x16x32_bf16 v[128:131], a[192:195], a[96:99], v[128:131]\n"
"global_load_dwordx4 a[188:191], v246, s[16:17] offset:3072 sc1\n"
"v_mfma_f32_16x16x32_bf16 v[128:131], a[196:199], a[100:103], v[128:131]\n"
"v_mfma_f32_16x16x32_bf16 v[132:135], a[192:195], a[104:107], v[132:135]\n"
"v_add_u32 v246, 0x2000, v246\n"
"v_mfma_f32_16x16x32_bf16 v[132:135], a[196:199], a[108:111], v[132:135]\n"
"v_mfma_f32_16x16x32_bf16 v[136:139], a[192:195], a[112:115], v[136:139]\n"
"v_mfma_f32_16x16x32_bf16 v[136:139], a[196:199], a[116:119], v[136:139]\n"
"v_mfma_f32_16x16x32_bf16 v[140:143], a[192:195], a[120:123], v[140:143]\n"
"v_mfma_f32_16x16x32_bf16 v[140:143], a[196:199], a[124:127], v[140:143]\n"
"v_mfma_f32_16x16x32_bf16 v[144:147], a[192:195], a[128:131], v[144:147]\n"
"v_mfma_f32_16x16x32_bf16 v[144:147], a[196:199], a[132:135], v[144:147]\n"
"v_mfma_f32_16x16x32_bf16 v[148:151], a[192:195], a[136:139], v[148:151]\n"
"v_mfma_f32_16x16x32_bf16 v[148:151], a[196:199], a[140:143], v[148:151]\n"
"v_mfma_f32_16x16x32_bf16 v[152:155], a[192:195], a[144:147], v[152:155]\n"
"v_mfma_f32_16x16x32_bf16 v[152:155], a[196:199], a[148:151], v[152:155]\n"
"v_mfma_f32_16x16x32_bf16 v[156:159], a[192:195], a[152:155], v[156:159]\n"
"global_load_dwordx4 a[192:195], v246, s[16:17] offset:-4096 sc1\n"
"v_mfma_f32_16x16x32_bf16 v[156:159], a[196:199], a[156:159], v[156:159]\n"
"s_waitcnt vmcnt(18)\n"
"v_mfma_f32_16x16x32_bf16 v[160:163], a[200:203], a[96:99], v[160:163]\n"
"global_load_dwordx4 a[196:199], v246, s[16:17] offset:-3072 sc1\n"
"v_mfma_f32_16x16x32_bf16 v[160:163], a[204:207], a[100:103], v[160:163]\n"
"s_waitcnt lgkmcnt(6)\n"
"global_store_dwordx4 v226, a[40:43], s[12:13] offset:0 sc1\n"
"v_mfma_f32_16x16x32_bf16 v[164:167], a[200:203], a[104:107], v[164:167]\n"
"v_mfma_f32_16x16x32_bf16 v[164:167], a[204:207], a[108:111], v[164:167]\n"
"v_mfma_f32_16x16x32_bf16 v[168:171], a[200:203], a[112:115], v[168:171]\n"
"v_mfma_f32_16x16x32_bf16 v[168:171], a[204:207], a[116:119], v[168:171]\n"
"v_mfma_f32_16x16x32_bf16 v[172:175], a[200:203], a[120:123], v[172:175]\n"
"v_mfma_f32_16x16x32_bf16 v[172:175], a[204:207], a[124:127], v[172:175]\n"
"v_mfma_f32_16x16x32_bf16 v[176:179], a[200:203], a[128:131], v[176:179]\n"
"v_mfma_f32_16x16x32_bf16 v[176:179], a[204:207], a[132:135], v[176:179]\n"
"v_mfma_f32_16x16x32_bf16 v[180:183], a[200:203], a[136:139], v[180:183]\n"
"v_mfma_f32_16x16x32_bf16 v[180:183], a[204:207], a[140:143], v[180:183]\n"
"v_mfma_f32_16x16x32_bf16 v[184:187], a[200:203], a[144:147], v[184:187]\n"
"v_mfma_f32_16x16x32_bf16 v[184:187], a[204:207], a[148:151], v[184:187]\n"
"v_mfma_f32_16x16x32_bf16 v[188:191], a[200:203], a[152:155], v[188:191]\n"
"global_load_dwordx4 a[200:203], v246, s[16:17] offset:-2048 sc1\n"
"v_mfma_f32_16x16x32_bf16 v[188:191], a[204:207], a[156:159], v[188:191]\n"
"s_waitcnt vmcnt(19)\n"
"v_mfma_f32_16x16x32_bf16 v[192:195], a[208:211], a[96:99], v[192:195]\n"
"global_load_dwordx4 a[204:207], v246, s[16:17] offset:-1024 sc1\n"
"v_mfma_f32_16x16x32_bf16 v[192:195], a[212:215], a[100:103], v[192:195]\n"
"v_mfma_f32_16x16x32_bf16 v[196:199], a[208:211], a[104:107], v[196:199]\n"
"v_mfma_f32_16x16x32_bf16 v[196:199], a[212:215], a[108:111], v[196:199]\n"
"v_mfma_f32_16x16x32_bf16 v[200:203], a[208:211], a[112:115], v[200:203]\n"
"v_mfma_f32_16x16x32_bf16 v[200:203], a[212:215], a[116:119], v[200:203]\n"
"v_mfma_f32_16x16x32_bf16 v[204:207], a[208:211], a[120:123], v[204:207]\n"
"v_mfma_f32_16x16x32_bf16 v[204:207], a[212:215], a[124:127], v[204:207]\n"
"v_mfma_f32_16x16x32_bf16 v[208:211], a[208:211], a[128:131], v[208:211]\n"
"v_mfma_f32_16x16x32_bf16 v[208:211], a[212:215], a[132:135], v[208:211]\n"
"v_mfma_f32_16x16x32_bf16 v[212:215], a[208:211], a[136:139], v[212:215]\n"
"v_mfma_f32_16x16x32_bf16 v[212:215], a[212:215], a[140:143], v[212:215]\n"
"v_mfma_f32_16x16x32_bf16 v[216:219], a[208:211], a[144:147], v[216:219]\n"
"v_mfma_f32_16x16x32_bf16 v[216:219], a[212:215], a[148:151], v[216:219]\n"
"s_barrier\n"
"v_mfma_f32_16x16x32_bf16 v[220:223], a[208:211], a[152:155], v[220:223]\n"
"global_load_dwordx4 a[208:211], v246, s[16:17] offset:0 sc1\n"
"v_mfma_f32_16x16x32_bf16 v[220:223], a[212:215], a[156:159], v[220:223]\n"
"s_waitcnt vmcnt(19)\n"
"v_mfma_f32_16x16x32_bf16 a[0:3], a[216:219], a[96:99], a[0:3]\n"
"global_load_dwordx4 a[212:215], v246, s[16:17] offset:1024 sc1\n"
"ds_read_b128 a[96:99], v242 offset:0\n"
"v_mfma_f32_16x16x32_bf16 a[0:3], a[220:223], a[100:103], a[0:3]\n"
"ds_read_b128 a[100:103], v243 offset:0\n"
"v_mfma_f32_16x16x32_bf16 a[4:7], a[216:219], a[104:107], a[4:7]\n"
"ds_read_b128 a[104:107], v242 offset:2048\n"
"v_mfma_f32_16x16x32_bf16 a[4:7], a[220:223], a[108:111], a[4:7]\n"
"ds_read_b128 a[108:111], v243 offset:2048\n"
"v_mfma_f32_16x16x32_bf16 a[8:11], a[216:219], a[112:115], a[8:11]\n"
"ds_read_b128 a[112:115], v242 offset:4096\n"
"v_mfma_f32_16x16x32_bf16 a[8:11], a[220:223], a[116:119], a[8:11]\n"
"ds_read_b128 a[116:119], v243 offset:4096\n"
"v_mfma_f32_16x16x32_bf16 a[12:15], a[216:219], a[120:123], a[12:15]\n"
"ds_read_b128 a[120:123], v242 offset:6144\n"
"v_mfma_f32_16x16x32_bf16 a[12:15], a[220:223], a[124:127], a[12:15]\n"
"ds_read_b128 a[124:127], v243 offset:6144\n"
"v_mfma_f32_16x16x32_bf16 a[16:19], a[216:219], a[128:131], a[16:19]\n"
"ds_read_b128 a[128:131], v242 offset:8192\n"
"v_mfma_f32_16x16x32_bf16 a[16:19], a[220:223], a[132:135], a[16:19]\n"
"ds_read_b128 a[132:135], v243 offset:8192\n"
"v_mfma_f32_16x16x32_bf16 a[20:23], a[216:219], a[136:139], a[20:23]\n"
"ds_read_b128 a[136:139], v242 offset:10240\n"
"v_mfma_f32_16x16x32_bf16 a[20:23], a[220:223], a[140:143], a[20:23]\n"
"ds_read_b128 a[140:143], v243 offset:10240\n"
"v_mfma_f32_16x16x32_bf16 a[24:27], a[216:219], a[144:147], a[24:27]\n"
"ds_read_b128 a[144:147], v242 offset:12288\n"
"v_mfma_f32_16x16x32_bf16 a[24:27], a[220:223], a[148:151], a[24:27]\n"
"ds_read_b128 a[148:151], v243 offset:12288\n"
"v_mfma_f32_16x16x32_bf16 a[28:31], a[216:219], a[152:155], a[28:31]\n"
"global_load_dwordx4 a[216:219], v246, s[16:17] offset:2048 sc1\n"
"ds_read_b128 a[152:155], v242 offset:14336\n"
"v_mfma_f32_16x16x32_bf16 a[28:31], a[220:223], a[156:159], a[28:31]\n"
"ds_read_b128 a[156:159], v243 offset:14336\n"
"s_waitcnt vmcnt(17) lgkmcnt(14)\n"
"v_mfma_f32_16x16x32_bf16 v[0:3], a[160:163], a[96:99], v[0:3]\n"
"global_load_dwordx4 a[220:223], v246, s[16:17] offset:3072 sc1\n"
"v_mfma_f32_16x16x32_bf16 v[0:3], a[164:167], a[100:103], v[0:3]\n"
"s_waitcnt lgkmcnt(12)\n"
"v_mfma_f32_16x16x32_bf16 v[4:7], a[160:163], a[104:107], v[4:7]\n"
"v_add_u32 v246, 0x2000, v246\n"
"v_mfma_f32_16x16x32_bf16 v[4:7], a[164:167], a[108:111], v[4:7]\n"
"s_waitcnt lgkmcnt(10)\n"
"v_mfma_f32_16x16x32_bf16 v[8:11], a[160:163], a[112:115], v[8:11]\n"
"s_mul_i32 s45, s49, -65536\n"
"s_add_u32 s45, s45, 131072\n"
"s_add_u32 s45, s45, s42\n"
"v_mfma_f32_16x16x32_bf16 v[8:11], a[164:167], a[116:119], v[8:11]\n"
"s_waitcnt lgkmcnt(8)\n"
"v_mfma_f32_16x16x32_bf16 v[12:15], a[160:163], a[120:123], v[12:15]\n"
"s_add_u32 m0, s45, 0\n"
"v_add_u32 v238, 1536, v248\n"
"v_mfma_f32_16x16x32_bf16 v[12:15], a[164:167], a[124:127], v[12:15]\n"
"global_load_lds_dwordx4 v238, s[20:21] offset:0\n"
"s_waitcnt lgkmcnt(6)\n"
"v_mfma_f32_16x16x32_bf16 v[16:19], a[160:163], a[128:131], v[16:19]\n"
"global_store_dwordx4 v226, a[44:47], s[12:13] offset:2048 sc1\n"
"v_mfma_f32_16x16x32_bf16 v[16:19], a[164:167], a[132:135], v[16:19]\n"
"s_waitcnt lgkmcnt(4)\n"
"v_mfma_f32_16x16x32_bf16 v[20:23], a[160:163], a[136:139], v[20:23]\n"
"v_mfma_f32_16x16x32_bf16 v[20:23], a[164:167], a[140:143], v[20:23]\n"
"v_add_u32 v238, s66, v238\n"
"s_waitcnt lgkmcnt(2)\n"
"v_mfma_f32_16x16x32_bf16 v[24:27], a[160:163], a[144:147], v[24:27]\n"
"global_load_lds_dwordx4 v238, s[20:21] offset:1024\n"
"v_mfma_f32_16x16x32_bf16 v[24:27], a[164:167], a[148:151], v[24:27]\n"
"s_waitcnt lgkmcnt(0)\n"
"v_mfma_f32_16x16x32_bf16 v[28:31], a[160:163], a[152:155], v[28:31]\n"
"global_load_dwordx4 a[160:163], v246, s[16:17] offset:-4096 sc1\n"
"v_mfma_f32_16x16x32_bf16 v[28:31], a[164:167], a[156:159], v[28:31]\n"
"s_waitcnt vmcnt(17)\n"
"v_mfma_f32_16x16x32_bf16 v[32:35], a[168:171], a[96:99], v[32:35]\n"
"global_load_dwordx4 a[164:167], v246, s[16:17] offset:-3072 sc1\n"
"v_add_u32 v238, s66, v238\n"
"v_mfma_f32_16x16x32_bf16 v[32:35], a[172:175], a[100:103], v[32:35]\n"
"global_load_lds_dwordx4 v238, s[20:21] offset:2048\n"
"v_mfma_f32_16x16x32_bf16 v[36:39], a[168:171], a[104:107], v[36:39]\n"
"v_mfma_f32_16x16x32_bf16 v[36:39], a[172:175], a[108:111], v[36:39]\n"
"v_mfma_f32_16x16x32_bf16 v[40:43], a[168:171], a[112:115], v[40:43]\n"
"v_mfma_f32_16x16x32_bf16 v[40:43], a[172:175], a[116:119], v[40:43]\n"
"v_add_u32 v238, s66, v238\n"
"global_load_lds_dwordx4 v238, s[20:21] offset:3072\n"
"v_mfma_f32_16x16x32_bf16 v[44:47], a[168:171], a[120:123], v[44:47]\n"
"v_mfma_f32_16x16x32_bf16 v[44:47], a[172:175], a[124:127], v[44:47]\n"
"v_mfma_f32_16x16x32_bf16 v[48:51], a[168:171], a[128:131], v[48:51]\n"
"v_mfma_f32_16x16x32_bf16 v[48:51], a[172:175], a[132:135], v[48:51]\n"
"v_mfma_f32_16x16x32_bf16 v[52:55], a[168:171], a[136:139], v[52:55]\n"
"v_mfma_f32_16x16x32_bf16 v[52:55], a[172:175], a[140:143], v[52:55]\n"
"v_mfma_f32_16x16x32_bf16 v[56:59], a[168:171], a[144:147], v[56:59]\n"
"v_mfma_f32_16x16x32_bf16 v[56:59], a[172:175], a[148:151], v[56:59]\n"
"v_mfma_f32_16x16x32_bf16 v[60:63], a[168:171], a[152:155], v[60:63]\n"
"global_load_dwordx4 a[168:171], v246, s[16:17] offset:-2048 sc1\n"
"v_mfma_f32_16x16x32_bf16 v[60:63], a[172:175], a[156:159], v[60:63]\n"
"s_waitcnt vmcnt(19)\n"
"v_mfma_f32_16x16x32_bf16 v[64:67], a[176:179], a[96:99], v[64:67]\n"
"global_load_dwordx4 a[172:175], v246, s[16:17] offset:-1024 sc1\n"
"v_mfma_f32_16x16x32_bf16 v[64:67], a[180:183], a[100:103], v[64:67]\n"
"v_mfma_f32_16x16x32_bf16 v[68:71], a[176:179], a[104:107], v[68:71]\n"
"s_mul_i32 s48, s49, -114688\n"
"s_add_u32 s48, s48, 147456\n"
"v_mfma_f32_16x16x32_bf16 v[68:71], a[180:183], a[108:111], v[68:71]\n"
"v_add_u32 v242, s48, v240\n"
"v_mfma_f32_16x16x32_bf16 v[72:75], a[176:179], a[112:115], v[72:75]\n"
"v_add_u32 v243, s48, v241\n"
"v_mfma_f32_16x16x32_bf16 v[72:75], a[180:183], a[116:119], v[72:75]\n"
"v_mfma_f32_16x16x32_bf16 v[76:79], a[176:179], a[120:123], v[76:79]\n"
"v_mfma_f32_16x16x32_bf16 v[76:79], a[180:183], a[124:127], v[76:79]\n"
"v_mfma_f32_16x16x32_bf16 v[80:83], a[176:179], a[128:131], v[80:83]\n"
"ds_read_b128 a[224:227], v244 offset:16384\n"
"v_mfma_f32_16x16x32_bf16 v[80:83], a[180:183], a[132:135], v[80:83]\n"
"v_mfma_f32_16x16x32_bf16 v[84:87], a[176:179], a[136:139], v[84:87]\n"
"ds_read_b128 a[228:231], v244 offset:18432\n"
"v_mfma_f32_16x16x32_bf16 v[84:87], a[180:183], a[140:143], v[84:87]\n"
"v_mfma_f32_16x16x32_bf16 v[88:91], a[176:179], a[144:147], v[88:91]\n"
"ds_read_b128 a[232:235], v244 offset:20480\n"
"v_mfma_f32_16x16x32_bf16 v[88:91], a[180:183], a[148:151], v[88:91]\n"
"v_mfma_f32_16x16x32_bf16 v[92:95], a[176:179], a[152:155], v[92:95]\n"
"global_load_dwordx4 a[176:179], v246, s[16:17] offset:0 sc1\n"
"v_mfma_f32_16x16x32_bf16 v[92:95], a[180:183], a[156:159], v[92:95]\n"
"s_waitcnt vmcnt(19)\n"
"v_mfma_f32_16x16x32_bf16 v[96:99], a[184:187], a[96:99], v[96:99]\n"
"global_load_dwordx4 a[180:183], v246, s[16:17] offset:1024 sc1\n"
"v_mfma_f32_16x16x32_bf16 v[96:99], a[188:191], a[100:103], v[96:99]\n"
"global_store_dwordx4 v227, a[48:51], s[12:13] offset:0 sc1\n"
"v_mfma_f32_16x16x32_bf16 v[100:103], a[184:187], a[104:107], v[100:103]\n"
"ds_read_b128 a[236:239], v244 offset:22528\n"
"v_mfma_f32_16x16x32_bf16 v[100:103], a[188:191], a[108:111], v[100:103]\n"
"v_mfma_f32_16x16x32_bf16 v[104:107], a[184:187], a[112:115], v[104:107]\n"
"ds_read_b128 a[240:243], v244 offset:24576\n"
"v_mfma_f32_16x16x32_bf16 v[104:107], a[188:191], a[116:119], v[104:107]\n"
"v_mfma_f32_16x16x32_bf16 v[108:111], a[184:187], a[120:123], v[108:111]\n"
"ds_read_b128 a[244:247], v244 offset:26624\n"
"v_mfma_f32_16x16x32_bf16 v[108:111], a[188:191], a[124:127], v[108:111]\n"
"v_mfma_f32_16x16x32_bf16 v[112:115], a[184:187], a[128:131], v[112:115]\n"
"ds_read_b128 a[248:251], v244 offset:28672\n"
"v_mfma_f32_16x16x32_bf16 v[112:115], a[188:191], a[132:135], v[112:115]\n"
"v_mfma_f32_16x16x32_bf16 v[116:119], a[184:187], a[136:139], v[116:119]\n"
"ds_read_b128 a[252:255], v244 offset:30720\n"
"v_mfma_f32_16x16x32_bf16 v[116:119], a[188:191], a[140:143], v[116:119]\n"
"v_mfma_f32_16x16x32_bf16 v[120:123], a[184:187], a[144:147], v[120:123]\n"
"v_mfma_f32_16x16x32_bf16 v[120:123], a[188:191], a[148:151], v[120:123]\n"
"v_mfma_f32_16x16x32_bf16 v[124:127], a[184:187], a[152:155], v[124:127]\n"
"global_load_dwordx4 a[184:187], v246, s[16:17] offset:2048 sc1\n"
"v_mfma_f32_16x16x32_bf16 v[124:127], a[188:191], a[156:159], v[124:127]\n"
"s_waitcnt vmcnt(20)\n"
"v_mfma_f32_16x16x32_bf16 v[128:131], a[192:195], a[96:99], v[128:131]\n"
"global_load_dwordx4 a[188:191], v246, s[16:17] offset:3072 sc1\n"
"v_mfma_f32_16x16x32_bf16 v[128:131], a[196:199], a[100:103], v[128:131]\n"
"v_mfma_f32_16x16x32_bf16 v[132:135], a[192:195], a[104:107], v[132:135]\n"
"v_add_u32 v246, 0x2000, v246\n"
"v_mfma_f32_16x16x32_bf16 v[132:135], a[196:199], a[108:111], v[132:135]\n"
"v_mfma_f32_16x16x32_bf16 v[136:139], a[192:195], a[112:115], v[136:139]\n"
"v_mfma_f32_16x16x32_bf16 v[136:139], a[196:199], a[116:119], v[136:139]\n"
"v_mfma_f32_16x16x32_bf16 v[140:143], a[192:195], a[120:123], v[140:143]\n"
"v_mfma_f32_16x16x32_bf16 v[140:143], a[196:199], a[124:127], v[140:143]\n"
"v_mfma_f32_16x16x32_bf16 v[144:147], a[192:195], a[128:131], v[144:147]\n"
"v_mfma_f32_16x16x32_bf16 v[144:147], a[196:199], a[132:135], v[144:147]\n"
"v_mfma_f32_16x16x32_bf16 v[148:151], a[192:195], a[136:139], v[148:151]\n"
"v_mfma_f32_16x16x32_bf16 v[148:151], a[196:199], a[140:143], v[148:151]\n"
"v_mfma_f32_16x16x32_bf16 v[152:155], a[192:195], a[144:147], v[152:155]\n"
"v_mfma_f32_16x16x32_bf16 v[152:155], a[196:199], a[148:151], v[152:155]\n"
"v_mfma_f32_16x16x32_bf16 v[156:159], a[192:195], a[152:155], v[156:159]\n"
"global_load_dwordx4 a[192:195], v246, s[16:17] offset:-4096 sc1\n"
"v_mfma_f32_16x16x32_bf16 v[156:159], a[196:199], a[156:159], v[156:159]\n"
"s_waitcnt vmcnt(19)\n"
"v_mfma_f32_16x16x32_bf16 v[160:163], a[200:203], a[96:99], v[160:163]\n"
"global_load_dwordx4 a[196:199], v246, s[16:17] offset:-3072 sc1\n"
"v_mfma_f32_16x16x32_bf16 v[160:163], a[204:207], a[100:103], v[160:163]\n"
"v_mfma_f32_16x16x32_bf16 v[164:167], a[200:203], a[104:107], v[164:167]\n"
"v_mfma_f32_16x16x32_bf16 v[164:167], a[204:207], a[108:111], v[164:167]\n"
"v_mfma_f32_16x16x32_bf16 v[168:171], a[200:203], a[112:115], v[168:171]\n"
"v_mfma_f32_16x16x32_bf16 v[168:171], a[204:207], a[116:119], v[168:171]\n"
"v_mfma_f32_16x16x32_bf16 v[172:175], a[200:203], a[120:123], v[172:175]\n"
"v_mfma_f32_16x16x32_bf16 v[172:175], a[204:207], a[124:127], v[172:175]\n"
"v_mfma_f32_16x16x32_bf16 v[176:179], a[200:203], a[128:131], v[176:179]\n"
"v_mfma_f32_16x16x32_bf16 v[176:179], a[204:207], a[132:135], v[176:179]\n"
"v_mfma_f32_16x16x32_bf16 v[180:183], a[200:203], a[136:139], v[180:183]\n"
"v_mfma_f32_16x16x32_bf16 v[180:183], a[204:207], a[140:143], v[180:183]\n"
"v_mfma_f32_16x16x32_bf16 v[184:187], a[200:203], a[144:147], v[184:187]\n"
"v_mfma_f32_16x16x32_bf16 v[184:187], a[204:207], a[148:151], v[184:187]\n"
"v_mfma_f32_16x16x32_bf16 v[188:191], a[200:203], a[152:155], v[188:191]\n"
"global_load_dwordx4 a[200:203], v246, s[16:17] offset:-2048 sc1\n"
"v_mfma_f32_16x16x32_bf16 v[188:191], a[204:207], a[156:159], v[188:191]\n"
"s_waitcnt vmcnt(19)\n"
"v_mfma_f32_16x16x32_bf16 v[192:195], a[208:211], a[96:99], v[192:195]\n"
"global_load_dwordx4 a[204:207], v246, s[16:17] offset:-1024 sc1\n"
"v_mfma_f32_16x16x32_bf16 v[192:195], a[212:215], a[100:103], v[192:195]\n"
"global_store_dwordx4 v227, a[52:55], s[12:13] offset:2048 sc1\n"
"v_mfma_f32_16x16x32_bf16 v[196:199], a[208:211], a[104:107], v[196:199]\n"
"v_mfma_f32_16x16x32_bf16 v[196:199], a[212:215], a[108:111], v[196:199]\n"
"v_mfma_f32_16x16x32_bf16 v[200:203], a[208:211], a[112:115], v[200:203]\n"
"v_mfma_f32_16x16x32_bf16 v[200:203], a[212:215], a[116:119], v[200:203]\n"
"v_mfma_f32_16x16x32_bf16 v[204:207], a[208:211], a[120:123], v[204:207]\n"
"v_mfma_f32_16x16x32_bf16 v[204:207], a[212:215], a[124:127], v[204:207]\n"
"v_mfma_f32_16x16x32_bf16 v[208:211], a[208:211], a[128:131], v[208:211]\n"
"v_mfma_f32_16x16x32_bf16 v[208:211], a[212:215], a[132:135], v[208:211]\n"
"v_mfma_f32_16x16x32_bf16 v[212:215], a[208:211], a[136:139], v[212:215]\n"
"v_mfma_f32_16x16x32_bf16 v[212:215], a[212:215], a[140:143], v[212:215]\n"
"v_mfma_f32_16x16x32_bf16 v[216:219], a[208:211], a[144:147], v[216:219]\n"
"v_mfma_f32_16x16x32_bf16 v[216:219], a[212:215], a[148:151], v[216:219]\n"
"s_barrier\n"
"v_mfma_f32_16x16x32_bf16 v[220:223], a[208:211], a[152:155], v[220:223]\n"
"global_load_dwordx4 a[208:211], v246, s[16:17] offset:0 sc1\n"
"v_mfma_f32_16x16x32_bf16 v[220:223], a[212:215], a[156:159], v[220:223]\n"
"s_waitcnt vmcnt(20)\n"
"v_mfma_f32_16x16x32_bf16 a[0:3], a[216:219], a[96:99], a[0:3]\n"
"global_load_dwordx4 a[212:215], v246, s[16:17] offset:1024 sc1\n"
"ds_read_b128 a[96:99], v242 offset:0\n"
"v_mfma_f32_16x16x32_bf16 a[0:3], a[220:223], a[100:103], a[0:3]\n"
"ds_read_b128 a[100:103], v243 offset:0\n"
"v_mfma_f32_16x16x32_bf16 a[4:7], a[216:219], a[104:107], a[4:7]\n"
"ds_read_b128 a[104:107], v242 offset:2048\n"
"v_mfma_f32_16x16x32_bf16 a[4:7], a[220:223], a[108:111], a[4:7]\n"
"ds_read_b128 a[108:111], v243 offset:2048\n"
"v_mfma_f32_16x16x32_bf16 a[8:11], a[216:219], a[112:115], a[8:11]\n"
"ds_read_b128 a[112:115], v242 offset:4096\n"
"v_mfma_f32_16x16x32_bf16 a[8:11], a[220:223], a[116:119], a[8:11]\n"
"ds_read_b128 a[116:119], v243 offset:4096\n"
"v_mfma_f32_16x16x32_bf16 a[12:15], a[216:219], a[120:123], a[12:15]\n"
"ds_read_b128 a[120:123], v242 offset:6144\n"
"v_mfma_f32_16x16x32_bf16 a[12:15], a[220:223], a[124:127], a[12:15]\n"
"ds_read_b128 a[124:127], v243 offset:6144\n"
"v_mfma_f32_16x16x32_bf16 a[16:19], a[216:219], a[128:131], a[16:19]\n"
"ds_read_b128 a[128:131], v242 offset:8192\n"
"v_mfma_f32_16x16x32_bf16 a[16:19], a[220:223], a[132:135], a[16:19]\n"
"ds_read_b128 a[132:135], v243 offset:8192\n"
"v_mfma_f32_16x16x32_bf16 a[20:23], a[216:219], a[136:139], a[20:23]\n"
"ds_read_b128 a[136:139], v242 offset:10240\n"
"v_mfma_f32_16x16x32_bf16 a[20:23], a[220:223], a[140:143], a[20:23]\n"
"ds_read_b128 a[140:143], v243 offset:10240\n"
"v_mfma_f32_16x16x32_bf16 a[24:27], a[216:219], a[144:147], a[24:27]\n"
"ds_read_b128 a[144:147], v242 offset:12288\n"
"v_mfma_f32_16x16x32_bf16 a[24:27], a[220:223], a[148:151], a[24:27]\n"
"ds_read_b128 a[148:151], v243 offset:12288\n"
"v_mfma_f32_16x16x32_bf16 a[28:31], a[216:219], a[152:155], a[28:31]\n"
"global_load_dwordx4 a[216:219], v246, s[16:17] offset:2048 sc1\n"
"ds_read_b128 a[152:155], v242 offset:14336\n"
"v_mfma_f32_16x16x32_bf16 a[28:31], a[220:223], a[156:159], a[28:31]\n"
"ds_read_b128 a[156:159], v243 offset:14336\n"
"s_waitcnt vmcnt(17) lgkmcnt(14)\n"
"v_mfma_f32_16x16x32_bf16 v[0:3], a[160:163], a[96:99], v[0:3]\n"
"global_load_dwordx4 a[220:223], v246, s[16:17] offset:3072 sc1\n"
"v_mfma_f32_16x16x32_bf16 v[0:3], a[164:167], a[100:103], v[0:3]\n"
"s_waitcnt lgkmcnt(12)\n"
"v_mfma_f32_16x16x32_bf16 v[4:7], a[160:163], a[104:107], v[4:7]\n"
"v_add_u32 v246, 0x2000, v246\n"
"v_mfma_f32_16x16x32_bf16 v[4:7], a[164:167], a[108:111], v[4:7]\n"
"s_waitcnt lgkmcnt(10)\n"
"v_mfma_f32_16x16x32_bf16 v[8:11], a[160:163], a[112:115], v[8:11]\n"
"s_mul_i32 s45, s50, -49152\n"
"s_add_u32 s45, s45, 147456\n"
"s_add_u32 s45, s45, s42\n"
"v_mfma_f32_16x16x32_bf16 v[8:11], a[164:167], a[116:119], v[8:11]\n"
"s_waitcnt lgkmcnt(8)\n"
"v_mfma_f32_16x16x32_bf16 v[12:15], a[160:163], a[120:123], v[12:15]\n"
"s_add_u32 m0, s45, 0\n"
"v_add_u32 v238, 768, v248\n"
"v_mfma_f32_16x16x32_bf16 v[12:15], a[164:167], a[124:127], v[12:15]\n"
"global_load_lds_dwordx4 v238, s[20:21] offset:0\n"
"s_waitcnt lgkmcnt(6)\n"
"v_mfma_f32_16x16x32_bf16 v[16:19], a[160:163], a[128:131], v[16:19]\n"
"v_mfma_f32_16x16x32_bf16 v[16:19], a[164:167], a[132:135], v[16:19]\n"
"s_waitcnt lgkmcnt(4)\n"
"v_mfma_f32_16x16x32_bf16 v[20:23], a[160:163], a[136:139], v[20:23]\n"
"v_mfma_f32_16x16x32_bf16 v[20:23], a[164:167], a[140:143], v[20:23]\n"
"v_add_u32 v238, s66, v238\n"
"s_waitcnt lgkmcnt(2)\n"
"v_mfma_f32_16x16x32_bf16 v[24:27], a[160:163], a[144:147], v[24:27]\n"
"global_load_lds_dwordx4 v238, s[20:21] offset:1024\n"
"v_mfma_f32_16x16x32_bf16 v[24:27], a[164:167], a[148:151], v[24:27]\n"
"s_waitcnt lgkmcnt(0)\n"
"v_mfma_f32_16x16x32_bf16 v[28:31], a[160:163], a[152:155], v[28:31]\n"
"global_load_dwordx4 a[160:163], v246, s[16:17] offset:-4096 sc1\n"
"v_mfma_f32_16x16x32_bf16 v[28:31], a[164:167], a[156:159], v[28:31]\n"
"s_waitcnt vmcnt(17)\n"
"v_mfma_f32_16x16x32_bf16 v[32:35], a[168:171], a[96:99], v[32:35]\n"
"global_load_dwordx4 a[164:167], v246, s[16:17] offset:-3072 sc1\n"
"v_add_u32 v238, s66, v238\n"
"v_mfma_f32_16x16x32_bf16 v[32:35], a[172:175], a[100:103], v[32:35]\n"
"global_load_lds_dwordx4 v238, s[20:21] offset:2048\n"
"v_mfma_f32_16x16x32_bf16 v[36:39], a[168:171], a[104:107], v[36:39]\n"
"v_mfma_f32_16x16x32_bf16 v[36:39], a[172:175], a[108:111], v[36:39]\n"
"v_mfma_f32_16x16x32_bf16 v[40:43], a[168:171], a[112:115], v[40:43]\n"
"v_mfma_f32_16x16x32_bf16 v[40:43], a[172:175], a[116:119], v[40:43]\n"
"v_add_u32 v238, s66, v238\n"
"global_load_lds_dwordx4 v238, s[20:21] offset:3072\n"
"v_mfma_f32_16x16x32_bf16 v[44:47], a[168:171], a[120:123], v[44:47]\n"
"s_barrier\n"
"v_mfma_f32_16x16x32_bf16 v[44:47], a[172:175], a[124:127], v[44:47]\n"
"v_mfma_f32_16x16x32_bf16 v[48:51], a[168:171], a[128:131], v[48:51]\n"
"v_mfma_f32_16x16x32_bf16 v[48:51], a[172:175], a[132:135], v[48:51]\n"
"v_mfma_f32_16x16x32_bf16 v[52:55], a[168:171], a[136:139], v[52:55]\n"
"v_mfma_f32_16x16x32_bf16 v[52:55], a[172:175], a[140:143], v[52:55]\n"
"v_mfma_f32_16x16x32_bf16 v[56:59], a[168:171], a[144:147], v[56:59]\n"
"v_mfma_f32_16x16x32_bf16 v[56:59], a[172:175], a[148:151], v[56:59]\n"
"v_mfma_f32_16x16x32_bf16 v[60:63], a[168:171], a[152:155], v[60:63]\n"
".Lpg5_%=:\n"
"s_cbranch_execz .Lpg9_%=\n"
"global_load_dwordx4 a[168:171], v246, s[16:17] offset:-2048 sc1\n"
"v_mfma_f32_16x16x32_bf16 v[60:63], a[172:175], a[156:159], v[60:63]\n"
"s_waitcnt vmcnt(19)\n"
"v_mfma_f32_16x16x32_bf16 v[64:67], a[176:179], a[96:99], v[64:67]\n"
"global_load_dwordx4 a[172:175], v246, s[16:17] offset:-1024 sc1\n"
"v_mfma_f32_16x16x32_bf16 v[64:67], a[180:183], a[100:103], v[64:67]\n"
"v_mfma_f32_16x16x32_bf16 v[68:71], a[176:179], a[104:107], v[68:71]\n"
"s_mul_i32 s48, s50, -65536\n"
"s_add_u32 s48, s48, 131072\n"
"v_mfma_f32_16x16x32_bf16 v[68:71], a[180:183], a[108:111], v[68:71]\n"
"v_add_u32 v242, s48, v240\n"
"v_mfma_f32_16x16x32_bf16 v[72:75], a[176:179], a[112:115], v[72:75]\n"
"v_add_u32 v243, s48, v241\n"
"v_mfma_f32_16x16x32_bf16 v[72:75], a[180:183], a[116:119], v[72:75]\n"
"v_mfma_f32_16x16x32_bf16 v[76:79], a[176:179], a[120:123], v[76:79]\n"
"v_mfma_f32_16x16x32_bf16 v[76:79], a[180:183], a[124:127], v[76:79]\n"
"v_mfma_f32_16x16x32_bf16 v[80:83], a[176:179], a[128:131], v[80:83]\n"
"v_mfma_f32_16x16x32_bf16 v[80:83], a[180:183], a[132:135], v[80:83]\n"
"v_mfma_f32_16x16x32_bf16 v[84:87], a[176:179], a[136:139], v[84:87]\n"
"v_mfma_f32_16x16x32_bf16 v[84:87], a[180:183], a[140:143], v[84:87]\n"
"v_mfma_f32_16x16x32_bf16 v[88:91], a[176:179], a[144:147], v[88:91]\n"
"v_mfma_f32_16x16x32_bf16 v[88:91], a[180:183], a[148:151], v[88:91]\n"
"v_mfma_f32_16x16x32_bf16 v[92:95], a[176:179], a[152:155], v[92:95]\n"
"global_load_dwordx4 a[176:179], v246, s[16:17] offset:0 sc1\n"
"v_mfma_f32_16x16x32_bf16 v[92:95], a[180:183], a[156:159], v[92:95]\n"
"s_waitcnt vmcnt(18)\n"
"v_mfma_f32_16x16x32_bf16 v[96:99], a[184:187], a[96:99], v[96:99]\n"
"global_load_dwordx4 a[180:183], v246, s[16:17] offset:1024 sc1\n"
"v_mfma_f32_16x16x32_bf16 v[96:99], a[188:191], a[100:103], v[96:99]\n"
"v_mfma_f32_16x16x32_bf16 v[100:103], a[184:187], a[104:107], v[100:103]\n"
"v_mfma_f32_16x16x32_bf16 v[100:103], a[188:191], a[108:111], v[100:103]\n"
"v_mfma_f32_16x16x32_bf16 v[104:107], a[184:187], a[112:115], v[104:107]\n"
"v_mfma_f32_16x16x32_bf16 v[104:107], a[188:191], a[116:119], v[104:107]\n"
"v_mfma_f32_16x16x32_bf16 v[108:111], a[184:187], a[120:123], v[108:111]\n"
"v_mfma_f32_16x16x32_bf16 v[108:111], a[188:191], a[124:127], v[108:111]\n"
"v_mfma_f32_16x16x32_bf16 v[112:115], a[184:187], a[128:131], v[112:115]\n"
"v_mfma_f32_16x16x32_bf16 v[112:115], a[188:191], a[132:135], v[112:115]\n"
"v_mfma_f32_16x16x32_bf16 v[116:119], a[184:187], a[136:139], v[116:119]\n"
"v_mfma_f32_16x16x32_bf16 v[116:119], a[188:191], a[140:143], v[116:119]\n"
"v_mfma_f32_16x16x32_bf16 v[120:123], a[184:187], a[144:147], v[120:123]\n"
"v_mfma_f32_16x16x32_bf16 v[120:123], a[188:191], a[148:151], v[120:123]\n"
"v_mfma_f32_16x16x32_bf16 v[124:127], a[184:187], a[152:155], v[124:127]\n"
"global_load_dwordx4 a[184:187], v246, s[16:17] offset:2048 sc1\n"
"v_mfma_f32_16x16x32_bf16 v[124:127], a[188:191], a[156:159], v[124:127]\n"
"s_waitcnt vmcnt(18)\n"
"v_mfma_f32_16x16x32_bf16 v[128:131], a[192:195], a[96:99], v[128:131]\n"
"global_load_dwordx4 a[188:191], v246, s[16:17] offset:3072 sc1\n"
"v_mfma_f32_16x16x32_bf16 v[128:131], a[196:199], a[100:103], v[128:131]\n"
"v_mfma_f32_16x16x32_bf16 v[132:135], a[192:195], a[104:107], v[132:135]\n"
"v_add_u32 v246, 0x2000, v246\n"
"v_mfma_f32_16x16x32_bf16 v[132:135], a[196:199], a[108:111], v[132:135]\n"
"v_mfma_f32_16x16x32_bf16 v[136:139], a[192:195], a[112:115], v[136:139]\n"
"v_mfma_f32_16x16x32_bf16 v[136:139], a[196:199], a[116:119], v[136:139]\n"
"v_mfma_f32_16x16x32_bf16 v[140:143], a[192:195], a[120:123], v[140:143]\n"
"v_mfma_f32_16x16x32_bf16 v[140:143], a[196:199], a[124:127], v[140:143]\n"
"v_mfma_f32_16x16x32_bf16 v[144:147], a[192:195], a[128:131], v[144:147]\n"
"v_mfma_f32_16x16x32_bf16 v[144:147], a[196:199], a[132:135], v[144:147]\n"
"v_mfma_f32_16x16x32_bf16 v[148:151], a[192:195], a[136:139], v[148:151]\n"
"v_mfma_f32_16x16x32_bf16 v[148:151], a[196:199], a[140:143], v[148:151]\n"
"v_mfma_f32_16x16x32_bf16 v[152:155], a[192:195], a[144:147], v[152:155]\n"
"v_mfma_f32_16x16x32_bf16 v[152:155], a[196:199], a[148:151], v[152:155]\n"
"v_mfma_f32_16x16x32_bf16 v[156:159], a[192:195], a[152:155], v[156:159]\n"
"global_load_dwordx4 a[192:195], v246, s[16:17] offset:-4096 sc1\n"
"v_mfma_f32_16x16x32_bf16 v[156:159], a[196:199], a[156:159], v[156:159]\n"
"s_waitcnt vmcnt(18)\n"
"v_mfma_f32_16x16x32_bf16 v[160:163], a[200:203], a[96:99], v[160:163]\n"
"global_load_dwordx4 a[196:199], v246, s[16:17] offset:-3072 sc1\n"
"v_mfma_f32_16x16x32_bf16 v[160:163], a[204:207], a[100:103], v[160:163]\n"
"v_mfma_f32_16x16x32_bf16 v[164:167], a[200:203], a[104:107], v[164:167]\n"
"s_mul_i32 s45, s49, -49152\n"
"s_add_u32 s45, s45, 147456\n"
"s_add_u32 s45, s45, s42\n"
"v_mfma_f32_16x16x32_bf16 v[164:167], a[204:207], a[108:111], v[164:167]\n"
"s_add_u32 m0, s45, 0\n"
"v_add_u32 v238, 1792, v248\n"
"v_mfma_f32_16x16x32_bf16 v[168:171], a[200:203], a[112:115], v[168:171]\n"
"global_load_lds_dwordx4 v238, s[20:21] offset:0\n"
"v_mfma_f32_16x16x32_bf16 v[168:171], a[204:207], a[116:119], v[168:171]\n"
"s_add_u32 s64, s60, 24576\n"
"s_addc_u32 s65, s61, 0\n"
"v_mfma_f32_16x16x32_bf16 v[172:175], a[200:203], a[120:123], v[172:175]\n"
"v_mfma_f32_16x16x32_bf16 v[172:175], a[204:207], a[124:127], v[172:175]\n"
"v_mfma_f32_16x16x32_bf16 v[176:179], a[200:203], a[128:131], v[176:179]\n"
"v_add_u32 v238, s66, v238\n"
"global_load_lds_dwordx4 v238, s[20:21] offset:1024\n"
"v_mfma_f32_16x16x32_bf16 v[176:179], a[204:207], a[132:135], v[176:179]\n"
"v_mfma_f32_16x16x32_bf16 v[180:183], a[200:203], a[136:139], v[180:183]\n"
"v_mfma_f32_16x16x32_bf16 v[180:183], a[204:207], a[140:143], v[180:183]\n"
"v_mfma_f32_16x16x32_bf16 v[184:187], a[200:203], a[144:147], v[184:187]\n"
"v_add_u32 v238, s66, v238\n"
"global_load_lds_dwordx4 v238, s[20:21] offset:2048\n"
"v_mfma_f32_16x16x32_bf16 v[184:187], a[204:207], a[148:151], v[184:187]\n"
"v_mfma_f32_16x16x32_bf16 v[188:191], a[200:203], a[152:155], v[188:191]\n"
"global_load_dwordx4 a[200:203], v246, s[16:17] offset:-2048 sc1\n"
"v_mfma_f32_16x16x32_bf16 v[188:191], a[204:207], a[156:159], v[188:191]\n"
"s_waitcnt vmcnt(20)\n"
"v_mfma_f32_16x16x32_bf16 v[192:195], a[208:211], a[96:99], v[192:195]\n"
"global_load_dwordx4 a[204:207], v246, s[16:17] offset:-1024 sc1\n"
"v_add_u32 v238, s66, v238\n"
"v_mfma_f32_16x16x32_bf16 v[192:195], a[212:215], a[100:103], v[192:195]\n"
"global_load_lds_dwordx4 v238, s[20:21] offset:3072\n"
"v_mfma_f32_16x16x32_bf16 v[196:199], a[208:211], a[104:107], v[196:199]\n"
"v_mfma_f32_16x16x32_bf16 v[196:199], a[212:215], a[108:111], v[196:199]\n"
"v_mfma_f32_16x16x32_bf16 v[200:203], a[208:211], a[112:115], v[200:203]\n"
"v_mfma_f32_16x16x32_bf16 v[200:203], a[212:215], a[116:119], v[200:203]\n"
"v_mfma_f32_16x16x32_bf16 v[204:207], a[208:211], a[120:123], v[204:207]\n"
"v_mfma_f32_16x16x32_bf16 v[204:207], a[212:215], a[124:127], v[204:207]\n"
"v_mfma_f32_16x16x32_bf16 v[208:211], a[208:211], a[128:131], v[208:211]\n"
"v_mfma_f32_16x16x32_bf16 v[208:211], a[212:215], a[132:135], v[208:211]\n"
"v_mfma_f32_16x16x32_bf16 v[212:215], a[208:211], a[136:139], v[212:215]\n"
"v_mfma_f32_16x16x32_bf16 v[212:215], a[212:215], a[140:143], v[212:215]\n"
"v_mfma_f32_16x16x32_bf16 v[216:219], a[208:211], a[144:147], v[216:219]\n"
"v_mfma_f32_16x16x32_bf16 v[216:219], a[212:215], a[148:151], v[216:219]\n"
"s_barrier\n"
"s_mov_b64 exec, 1\n"
"global_atomic_add v250, v252, s[38:39] offset:4 sc1\n"
"s_mov_b64 exec, -1\n"
"v_mfma_f32_16x16x32_bf16 v[220:223], a[208:211], a[152:155], v[220:223]\n"
"global_load_dword a208, v250, s[64:65]\n"
"global_load_dwordx4 a[208:211], v246, s[16:17] offset:0 sc1\n"
"v_mfma_f32_16x16x32_bf16 v[220:223], a[212:215], a[156:159], v[220:223]\n"
"s_waitcnt vmcnt(23)\n"
"v_mfma_f32_16x16x32_bf16 a[0:3], a[216:219], a[96:99], a[0:3]\n"
"global_load_dwordx4 a[212:215], v246, s[16:17] offset:1024 sc1\n"
"ds_read_b128 a[96:99], v242 offset:0\n"
"v_mfma_f32_16x16x32_bf16 a[0:3], a[220:223], a[100:103], a[0:3]\n"
"ds_read_b128 a[100:103], v243 offset:0\n"
"v_mfma_f32_16x16x32_bf16 a[4:7], a[216:219], a[104:107], a[4:7]\n"
"ds_read_b128 a[104:107], v242 offset:2048\n"
"v_mfma_f32_16x16x32_bf16 a[4:7], a[220:223], a[108:111], a[4:7]\n"
"ds_read_b128 a[108:111], v243 offset:2048\n"
"v_mfma_f32_16x16x32_bf16 a[8:11], a[216:219], a[112:115], a[8:11]\n"
"ds_read_b128 a[112:115], v242 offset:4096\n"
"v_mfma_f32_16x16x32_bf16 a[8:11], a[220:223], a[116:119], a[8:11]\n"
"ds_read_b128 a[116:119], v243 offset:4096\n"
"v_mfma_f32_16x16x32_bf16 a[12:15], a[216:219], a[120:123], a[12:15]\n"
"ds_read_b128 a[120:123], v242 offset:6144\n"
"v_mfma_f32_16x16x32_bf16 a[12:15], a[220:223], a[124:127], a[12:15]\n"
"ds_read_b128 a[124:127], v243 offset:6144\n"
"v_mfma_f32_16x16x32_bf16 a[16:19], a[216:219], a[128:131], a[16:19]\n"
"ds_read_b128 a[128:131], v242 offset:8192\n"
"v_mfma_f32_16x16x32_bf16 a[16:19], a[220:223], a[132:135], a[16:19]\n"
"ds_read_b128 a[132:135], v243 offset:8192\n"
"v_mfma_f32_16x16x32_bf16 a[20:23], a[216:219], a[136:139], a[20:23]\n"
"ds_read_b128 a[136:139], v242 offset:10240\n"
"v_mfma_f32_16x16x32_bf16 a[20:23], a[220:223], a[140:143], a[20:23]\n"
"ds_read_b128 a[140:143], v243 offset:10240\n"
"v_mfma_f32_16x16x32_bf16 a[24:27], a[216:219], a[144:147], a[24:27]\n"
"ds_read_b128 a[144:147], v242 offset:12288\n"
"v_mfma_f32_16x16x32_bf16 a[24:27], a[220:223], a[148:151], a[24:27]\n"
"ds_read_b128 a[148:151], v243 offset:12288\n"
"v_mfma_f32_16x16x32_bf16 a[28:31], a[216:219], a[152:155], a[28:31]\n"
"global_load_dwordx4 a[216:219], v246, s[16:17] offset:2048 sc1\n"
"ds_read_b128 a[152:155], v242 offset:14336\n"
"v_mfma_f32_16x16x32_bf16 a[28:31], a[220:223], a[156:159], a[28:31]\n"
"ds_read_b128 a[156:159], v243 offset:14336\n"
"s_waitcnt vmcnt(21) lgkmcnt(14)\n"
"v_mfma_f32_16x16x32_bf16 v[128:131], a[160:163], a[96:99], v[128:131]\n"
"global_load_dwordx4 a[220:223], v246, s[16:17] offset:3072 sc1\n"
"v_mfma_f32_16x16x32_bf16 v[128:131], a[164:167], a[100:103], v[128:131]\n"
"s_waitcnt lgkmcnt(12)\n"
"v_mfma_f32_16x16x32_bf16 v[132:135], a[160:163], a[104:107], v[132:135]\n"
"v_add_u32 v246, 0x2000, v246\n"
"v_mfma_f32_16x16x32_bf16 v[132:135], a[164:167], a[108:111], v[132:135]\n"
"s_waitcnt lgkmcnt(10)\n"
"v_mfma_f32_16x16x32_bf16 v[136:139], a[160:163], a[112:115], v[136:139]\n"
"v_mfma_f32_16x16x32_bf16 v[136:139], a[164:167], a[116:119], v[136:139]\n"
"s_waitcnt lgkmcnt(8)\n"
"v_mfma_f32_16x16x32_bf16 v[140:143], a[160:163], a[120:123], v[140:143]\n"
"v_mfma_f32_16x16x32_bf16 v[140:143], a[164:167], a[124:127], v[140:143]\n"
"s_waitcnt lgkmcnt(6)\n"
"v_mfma_f32_16x16x32_bf16 v[144:147], a[160:163], a[128:131], v[144:147]\n"
"v_mfma_f32_16x16x32_bf16 v[144:147], a[164:167], a[132:135], v[144:147]\n"
"s_waitcnt lgkmcnt(4)\n"
"v_mfma_f32_16x16x32_bf16 v[148:151], a[160:163], a[136:139], v[148:151]\n"
"v_mfma_f32_16x16x32_bf16 v[148:151], a[164:167], a[140:143], v[148:151]\n"
"s_waitcnt lgkmcnt(2)\n"
"v_mfma_f32_16x16x32_bf16 v[152:155], a[160:163], a[144:147], v[152:155]\n"
"v_mfma_f32_16x16x32_bf16 v[152:155], a[164:167], a[148:151], v[152:155]\n"
"s_waitcnt lgkmcnt(0)\n"
"v_mfma_f32_16x16x32_bf16 v[156:159], a[160:163], a[152:155], v[156:159]\n"
"global_load_dwordx4 a[160:163], v246, s[16:17] offset:-4096 sc1\n"
"v_mfma_f32_16x16x32_bf16 v[156:159], a[164:167], a[156:159], v[156:159]\n"
"s_waitcnt vmcnt(19)\n"
"v_mfma_f32_16x16x32_bf16 v[160:163], a[168:171], a[96:99], v[160:163]\n"
"global_load_dwordx4 a[164:167], v246, s[16:17] offset:-3072 sc1\n"
"v_mfma_f32_16x16x32_bf16 v[160:163], a[172:175], a[100:103], v[160:163]\n"
"v_mfma_f32_16x16x32_bf16 v[164:167], a[168:171], a[104:107], v[164:167]\n"
"v_mfma_f32_16x16x32_bf16 v[164:167], a[172:175], a[108:111], v[164:167]\n"
"v_mfma_f32_16x16x32_bf16 v[168:171], a[168:171], a[112:115], v[168:171]\n"
"v_mfma_f32_16x16x32_bf16 v[168:171], a[172:175], a[116:119], v[168:171]\n"
"v_mfma_f32_16x16x32_bf16 v[172:175], a[168:171], a[120:123], v[172:175]\n"
"v_mfma_f32_16x16x32_bf16 v[172:175], a[172:175], a[124:127], v[172:175]\n"
"v_mfma_f32_16x16x32_bf16 v[176:179], a[168:171], a[128:131], v[176:179]\n"
"v_mfma_f32_16x16x32_bf16 v[176:179], a[172:175], a[132:135], v[176:179]\n"
"v_mfma_f32_16x16x32_bf16 v[180:183], a[168:171], a[136:139], v[180:183]\n"
"v_mfma_f32_16x16x32_bf16 v[180:183], a[172:175], a[140:143], v[180:183]\n"
"v_mfma_f32_16x16x32_bf16 v[184:187], a[168:171], a[144:147], v[184:187]\n"
"v_mfma_f32_16x16x32_bf16 v[184:187], a[172:175], a[148:151], v[184:187]\n"
"v_mfma_f32_16x16x32_bf16 v[188:191], a[168:171], a[152:155], v[188:191]\n"
"global_load_dwordx4 a[168:171], v246, s[16:17] offset:-2048 sc1\n"
"v_mfma_f32_16x16x32_bf16 v[188:191], a[172:175], a[156:159], v[188:191]\n"
"s_waitcnt vmcnt(19)\n"
"v_mfma_f32_16x16x32_bf16 v[192:195], a[176:179], a[96:99], v[192:195]\n"
"global_load_dwordx4 a[172:175], v246, s[16:17] offset:-1024 sc1\n"
"v_mfma_f32_16x16x32_bf16 v[192:195], a[180:183], a[100:103], v[192:195]\n"
"v_mfma_f32_16x16x32_bf16 v[196:199], a[176:179], a[104:107], v[196:199]\n"
"s_mul_i32 s48, s49, -65536\n"
"s_add_u32 s48, s48, 131072\n"
"v_mfma_f32_16x16x32_bf16 v[196:199], a[180:183], a[108:111], v[196:199]\n"
"v_add_u32 v242, s48, v240\n"
"v_mfma_f32_16x16x32_bf16 v[200:203], a[176:179], a[112:115], v[200:203]\n"
"v_add_u32 v243, s48, v241\n"
"v_mfma_f32_16x16x32_bf16 v[200:203], a[180:183], a[116:119], v[200:203]\n"
"v_mfma_f32_16x16x32_bf16 v[204:207], a[176:179], a[120:123], v[204:207]\n"
"v_mfma_f32_16x16x32_bf16 v[204:207], a[180:183], a[124:127], v[204:207]\n"
"v_mfma_f32_16x16x32_bf16 v[208:211], a[176:179], a[128:131], v[208:211]\n"
"v_mfma_f32_16x16x32_bf16 v[208:211], a[180:183], a[132:135], v[208:211]\n"
"v_mfma_f32_16x16x32_bf16 v[212:215], a[176:179], a[136:139], v[212:215]\n"
"v_mfma_f32_16x16x32_bf16 v[212:215], a[180:183], a[140:143], v[212:215]\n"
"v_mfma_f32_16x16x32_bf16 v[216:219], a[176:179], a[144:147], v[216:219]\n"
"v_mfma_f32_16x16x32_bf16 v[216:219], a[180:183], a[148:151], v[216:219]\n"
"s_barrier\n"
"v_mfma_f32_16x16x32_bf16 v[220:223], a[176:179], a[152:155], v[220:223]\n"
"global_load_dwordx4 a[176:179], v246, s[16:17] offset:0 sc1\n"
"v_mfma_f32_16x16x32_bf16 v[220:223], a[180:183], a[156:159], v[220:223]\n"
"s_waitcnt vmcnt(19)\n"
"v_mfma_f32_16x16x32_bf16 a[0:3], a[184:187], a[96:99], a[0:3]\n"
"global_load_dwordx4 a[180:183], v246, s[16:17] offset:1024 sc1\n"
"ds_read_b128 a[96:99], v242 offset:0\n"
"v_mfma_f32_16x16x32_bf16 a[0:3], a[188:191], a[100:103], a[0:3]\n"
"ds_read_b128 a[100:103], v243 offset:0\n"
"v_mfma_f32_16x16x32_bf16 a[4:7], a[184:187], a[104:107], a[4:7]\n"
"ds_read_b128 a[104:107], v242 offset:2048\n"
"v_mfma_f32_16x16x32_bf16 a[4:7], a[188:191], a[108:111], a[4:7]\n"
"ds_read_b128 a[108:111], v243 offset:2048\n"
"v_mfma_f32_16x16x32_bf16 a[8:11], a[184:187], a[112:115], a[8:11]\n"
"ds_read_b128 a[112:115], v242 offset:4096\n"
"v_mfma_f32_16x16x32_bf16 a[8:11], a[188:191], a[116:119], a[8:11]\n"
"ds_read_b128 a[116:119], v243 offset:4096\n"
"v_mfma_f32_16x16x32_bf16 a[12:15], a[184:187], a[120:123], a[12:15]\n"
"ds_read_b128 a[120:123], v242 offset:6144\n"
"v_mfma_f32_16x16x32_bf16 a[12:15], a[188:191], a[124:127], a[12:15]\n"
"ds_read_b128 a[124:127], v243 offset:6144\n"
"v_mfma_f32_16x16x32_bf16 a[16:19], a[184:187], a[128:131], a[16:19]\n"
"ds_read_b128 a[128:131], v242 offset:8192\n"
"v_mfma_f32_16x16x32_bf16 a[16:19], a[188:191], a[132:135], a[16:19]\n"
"ds_read_b128 a[132:135], v243 offset:8192\n"
"v_mfma_f32_16x16x32_bf16 a[20:23], a[184:187], a[136:139], a[20:23]\n"
"ds_read_b128 a[136:139], v242 offset:10240\n"
"v_mfma_f32_16x16x32_bf16 a[20:23], a[188:191], a[140:143], a[20:23]\n"
"ds_read_b128 a[140:143], v243 offset:10240\n"
"v_mfma_f32_16x16x32_bf16 a[24:27], a[184:187], a[144:147], a[24:27]\n"
"ds_read_b128 a[144:147], v242 offset:12288\n"
"v_mfma_f32_16x16x32_bf16 a[24:27], a[188:191], a[148:151], a[24:27]\n"
"ds_read_b128 a[148:151], v243 offset:12288\n"
"v_mfma_f32_16x16x32_bf16 a[28:31], a[184:187], a[152:155], a[28:31]\n"
"global_load_dwordx4 a[184:187], v246, s[16:17] offset:2048 sc1\n"
"ds_read_b128 a[152:155], v242 offset:14336\n"
"v_mfma_f32_16x16x32_bf16 a[28:31], a[188:191], a[156:159], a[28:31]\n"
"ds_read_b128 a[156:159], v243 offset:14336\n"
"s_waitcnt vmcnt(19) lgkmcnt(14)\n"
"v_mfma_f32_16x16x32_bf16 v[128:131], a[192:195], a[96:99], v[128:131]\n"
"global_load_dwordx4 a[188:191], v246, s[16:17] offset:3072 sc1\n"
"v_mfma_f32_16x16x32_bf16 v[128:131], a[196:199], a[100:103], v[128:131]\n"
"s_waitcnt lgkmcnt(12)\n"
"v_mfma_f32_16x16x32_bf16 v[132:135], a[192:195], a[104:107], v[132:135]\n"
"v_add_u32 v246, 0x2000, v246\n"
"v_mfma_f32_16x16x32_bf16 v[132:135], a[196:199], a[108:111], v[132:135]\n"
"s_waitcnt lgkmcnt(10)\n"
"v_mfma_f32_16x16x32_bf16 v[136:139], a[192:195], a[112:115], v[136:139]\n"
"v_mfma_f32_16x16x32_bf16 v[136:139], a[196:199], a[116:119], v[136:139]\n"
"s_waitcnt lgkmcnt(8)\n"
"v_mfma_f32_16x16x32_bf16 v[140:143], a[192:195], a[120:123], v[140:143]\n"
"v_mfma_f32_16x16x32_bf16 v[140:143], a[196:199], a[124:127], v[140:143]\n"
"s_waitcnt lgkmcnt(6)\n"
"v_mfma_f32_16x16x32_bf16 v[144:147], a[192:195], a[128:131], v[144:147]\n"
"v_mfma_f32_16x16x32_bf16 v[144:147], a[196:199], a[132:135], v[144:147]\n"
"s_waitcnt lgkmcnt(4)\n"
"v_mfma_f32_16x16x32_bf16 v[148:151], a[192:195], a[136:139], v[148:151]\n"
"v_mfma_f32_16x16x32_bf16 v[148:151], a[196:199], a[140:143], v[148:151]\n"
"s_waitcnt lgkmcnt(2)\n"
"v_mfma_f32_16x16x32_bf16 v[152:155], a[192:195], a[144:147], v[152:155]\n"
"v_mfma_f32_16x16x32_bf16 v[152:155], a[196:199], a[148:151], v[152:155]\n"
"s_waitcnt lgkmcnt(0)\n"
"v_mfma_f32_16x16x32_bf16 v[156:159], a[192:195], a[152:155], v[156:159]\n"
"global_load_dwordx4 a[192:195], v246, s[16:17] offset:-4096 sc1\n"
"v_mfma_f32_16x16x32_bf16 v[156:159], a[196:199], a[156:159], v[156:159]\n"
"s_waitcnt vmcnt(16)\n"
"v_mfma_f32_16x16x32_bf16 v[160:163], a[200:203], a[96:99], v[160:163]\n"
"global_load_dwordx4 a[196:199], v246, s[16:17] offset:-3072 sc1\n"
"v_mfma_f32_16x16x32_bf16 v[160:163], a[204:207], a[100:103], v[160:163]\n"
"v_mfma_f32_16x16x32_bf16 v[164:167], a[200:203], a[104:107], v[164:167]\n"
"v_mfma_f32_16x16x32_bf16 v[164:167], a[204:207], a[108:111], v[164:167]\n"
"v_mfma_f32_16x16x32_bf16 v[168:171], a[200:203], a[112:115], v[168:171]\n"
"v_mfma_f32_16x16x32_bf16 v[168:171], a[204:207], a[116:119], v[168:171]\n"
"v_mfma_f32_16x16x32_bf16 v[172:175], a[200:203], a[120:123], v[172:175]\n"
"v_mfma_f32_16x16x32_bf16 v[172:175], a[204:207], a[124:127], v[172:175]\n"
"v_mfma_f32_16x16x32_bf16 v[176:179], a[200:203], a[128:131], v[176:179]\n"
"v_mfma_f32_16x16x32_bf16 v[176:179], a[204:207], a[132:135], v[176:179]\n"
"v_mfma_f32_16x16x32_bf16 v[180:183], a[200:203], a[136:139], v[180:183]\n"
"v_mfma_f32_16x16x32_bf16 v[180:183], a[204:207], a[140:143], v[180:183]\n"
"v_mfma_f32_16x16x32_bf16 v[184:187], a[200:203], a[144:147], v[184:187]\n"
"v_mfma_f32_16x16x32_bf16 v[184:187], a[204:207], a[148:151], v[184:187]\n"
"v_mfma_f32_16x16x32_bf16 v[188:191], a[200:203], a[152:155], v[188:191]\n"
"global_load_dwordx4 a[200:203], v246, s[16:17] offset:-2048 sc1\n"
"v_mfma_f32_16x16x32_bf16 v[188:191], a[204:207], a[156:159], v[188:191]\n"
"s_waitcnt vmcnt(13)\n"
"v_mfma_f32_16x16x32_bf16 v[192:195], a[208:211], a[96:99], v[192:195]\n"
"global_load_dwordx4 a[204:207], v246, s[16:17] offset:-1024 sc1\n"
"v_mfma_f32_16x16x32_bf16 v[192:195], a[212:215], a[100:103], v[192:195]\n"
"v_mfma_f32_16x16x32_bf16 v[196:199], a[208:211], a[104:107], v[196:199]\n"
"s_mul_i32 s48, s50, -49152\n"
"s_add_u32 s48, s48, 147456\n"
"v_mfma_f32_16x16x32_bf16 v[196:199], a[212:215], a[108:111], v[196:199]\n"
"v_add_u32 v242, s48, v240\n"
"v_mfma_f32_16x16x32_bf16 v[200:203], a[208:211], a[112:115], v[200:203]\n"
"v_add_u32 v243, s48, v241\n"
"v_mfma_f32_16x16x32_bf16 v[200:203], a[212:215], a[116:119], v[200:203]\n"
"global_load_dword v235, v228, s[36:37] offset:4 sc1\n"
"v_mfma_f32_16x16x32_bf16 v[204:207], a[208:211], a[120:123], v[204:207]\n"
"v_mfma_f32_16x16x32_bf16 v[204:207], a[212:215], a[124:127], v[204:207]\n"
"v_mfma_f32_16x16x32_bf16 v[208:211], a[208:211], a[128:131], v[208:211]\n"
"v_mfma_f32_16x16x32_bf16 v[208:211], a[212:215], a[132:135], v[208:211]\n"
"v_mfma_f32_16x16x32_bf16 v[212:215], a[208:211], a[136:139], v[212:215]\n"
"v_mfma_f32_16x16x32_bf16 v[212:215], a[212:215], a[140:143], v[212:215]\n"
"v_mfma_f32_16x16x32_bf16 v[216:219], a[208:211], a[144:147], v[216:219]\n"
"v_mfma_f32_16x16x32_bf16 v[216:219], a[212:215], a[148:151], v[216:219]\n"
"s_barrier\n"
"v_mfma_f32_16x16x32_bf16 v[220:223], a[208:211], a[152:155], v[220:223]\n"
"global_load_dwordx4 a[208:211], v246, s[16:17] offset:0 sc1\n"
"v_mfma_f32_16x16x32_bf16 v[220:223], a[212:215], a[156:159], v[220:223]\n"
"s_waitcnt vmcnt(14)\n"
"v_mfma_f32_16x16x32_bf16 a[0:3], a[216:219], a[96:99], a[0:3]\n"
"global_load_dwordx4 a[212:215], v246, s[16:17] offset:1024 sc1\n"
"ds_read_b128 a[96:99], v242 offset:0\n"
"v_mfma_f32_16x16x32_bf16 a[0:3], a[220:223], a[100:103], a[0:3]\n"
"ds_read_b128 a[100:103], v243 offset:0\n"
"v_mfma_f32_16x16x32_bf16 a[4:7], a[216:219], a[104:107], a[4:7]\n"
"ds_read_b128 a[104:107], v242 offset:2048\n"
"v_mfma_f32_16x16x32_bf16 a[4:7], a[220:223], a[108:111], a[4:7]\n"
"ds_read_b128 a[108:111], v243 offset:2048\n"
"v_mfma_f32_16x16x32_bf16 a[8:11], a[216:219], a[112:115], a[8:11]\n"
"ds_read_b128 a[112:115], v242 offset:4096\n"
"v_mfma_f32_16x16x32_bf16 a[8:11], a[220:223], a[116:119], a[8:11]\n"
"ds_read_b128 a[116:119], v243 offset:4096\n"
"v_mfma_f32_16x16x32_bf16 a[12:15], a[216:219], a[120:123], a[12:15]\n"
"ds_read_b128 a[120:123], v242 offset:6144\n"
"v_mfma_f32_16x16x32_bf16 a[12:15], a[220:223], a[124:127], a[12:15]\n"
"ds_read_b128 a[124:127], v243 offset:6144\n"
"v_mfma_f32_16x16x32_bf16 a[16:19], a[216:219], a[128:131], a[16:19]\n"
"ds_read_b128 a[128:131], v242 offset:8192\n"
"v_mfma_f32_16x16x32_bf16 a[16:19], a[220:223], a[132:135], a[16:19]\n"
"ds_read_b128 a[132:135], v243 offset:8192\n"
"v_mfma_f32_16x16x32_bf16 a[20:23], a[216:219], a[136:139], a[20:23]\n"
"ds_read_b128 a[136:139], v242 offset:10240\n"
"v_mfma_f32_16x16x32_bf16 a[20:23], a[220:223], a[140:143], a[20:23]\n"
"ds_read_b128 a[140:143], v243 offset:10240\n"
"v_mfma_f32_16x16x32_bf16 a[24:27], a[216:219], a[144:147], a[24:27]\n"
"ds_read_b128 a[144:147], v242 offset:12288\n"
"v_mfma_f32_16x16x32_bf16 a[24:27], a[220:223], a[148:151], a[24:27]\n"
"ds_read_b128 a[148:151], v243 offset:12288\n"
"v_mfma_f32_16x16x32_bf16 a[28:31], a[216:219], a[152:155], a[28:31]\n"
"global_load_dwordx4 a[216:219], v246, s[16:17] offset:2048 sc1\n"
"ds_read_b128 a[152:155], v242 offset:14336\n"
"v_mfma_f32_16x16x32_bf16 a[28:31], a[220:223], a[156:159], a[28:31]\n"
"ds_read_b128 a[156:159], v243 offset:14336\n"
"s_waitcnt vmcnt(14) lgkmcnt(14)\n"
"v_mfma_f32_16x16x32_bf16 v[128:131], a[160:163], a[96:99], v[128:131]\n"
"global_load_dwordx4 a[220:223], v246, s[16:17] offset:3072 sc1\n"
"v_mfma_f32_16x16x32_bf16 v[128:131], a[164:167], a[100:103], v[128:131]\n"
"s_waitcnt lgkmcnt(12)\n"
"v_mfma_f32_16x16x32_bf16 v[132:135], a[160:163], a[104:107], v[132:135]\n"
"v_add_u32 v246, 0x2000, v246\n"
"v_mfma_f32_16x16x32_bf16 v[132:135], a[164:167], a[108:111], v[132:135]\n"
"s_waitcnt lgkmcnt(10)\n"
"v_mfma_f32_16x16x32_bf16 v[136:139], a[160:163], a[112:115], v[136:139]\n"
"v_mfma_f32_16x16x32_bf16 v[136:139], a[164:167], a[116:119], v[136:139]\n"
"s_waitcnt lgkmcnt(8)\n"
"v_mfma_f32_16x16x32_bf16 v[140:143], a[160:163], a[120:123], v[140:143]\n"
"v_mfma_f32_16x16x32_bf16 v[140:143], a[164:167], a[124:127], v[140:143]\n"
"s_waitcnt lgkmcnt(6)\n"
"v_mfma_f32_16x16x32_bf16 v[144:147], a[160:163], a[128:131], v[144:147]\n"
"v_mfma_f32_16x16x32_bf16 v[144:147], a[164:167], a[132:135], v[144:147]\n"
"s_waitcnt lgkmcnt(4)\n"
"v_mfma_f32_16x16x32_bf16 v[148:151], a[160:163], a[136:139], v[148:151]\n"
"v_mfma_f32_16x16x32_bf16 v[148:151], a[164:167], a[140:143], v[148:151]\n"
"s_waitcnt lgkmcnt(2)\n"
"v_mfma_f32_16x16x32_bf16 v[152:155], a[160:163], a[144:147], v[152:155]\n"
"v_mfma_f32_16x16x32_bf16 v[152:155], a[164:167], a[148:151], v[152:155]\n"
"s_waitcnt lgkmcnt(0)\n"
"v_mfma_f32_16x16x32_bf16 v[156:159], a[160:163], a[152:155], v[156:159]\n"
"global_load_dwordx4 a[160:163], v246, s[16:17] offset:-4096 sc1\n"
"v_mfma_f32_16x16x32_bf16 v[156:159], a[164:167], a[156:159], v[156:159]\n"
"s_waitcnt vmcnt(14)\n"
"v_mfma_f32_16x16x32_bf16 v[160:163], a[168:171], a[96:99], v[160:163]\n"
"global_load_dwordx4 a[164:167], v246, s[16:17] offset:-3072 sc1\n"
"v_mfma_f32_16x16x32_bf16 v[160:163], a[172:175], a[100:103], v[160:163]\n"
"v_mfma_f32_16x16x32_bf16 v[164:167], a[168:171], a[104:107], v[164:167]\n"
"v_mfma_f32_16x16x32_bf16 v[164:167], a[172:175], a[108:111], v[164:167]\n"
"v_mfma_f32_16x16x32_bf16 v[168:171], a[168:171], a[112:115], v[168:171]\n"
"v_mfma_f32_16x16x32_bf16 v[168:171], a[172:175], a[116:119], v[168:171]\n"
"v_mfma_f32_16x16x32_bf16 v[172:175], a[168:171], a[120:123], v[172:175]\n"
"v_mfma_f32_16x16x32_bf16 v[172:175], a[172:175], a[124:127], v[172:175]\n"
"v_mfma_f32_16x16x32_bf16 v[176:179], a[168:171], a[128:131], v[176:179]\n"
"v_mfma_f32_16x16x32_bf16 v[176:179], a[172:175], a[132:135], v[176:179]\n"
"v_mfma_f32_16x16x32_bf16 v[180:183], a[168:171], a[136:139], v[180:183]\n"
"v_mfma_f32_16x16x32_bf16 v[180:183], a[172:175], a[140:143], v[180:183]\n"
"v_mfma_f32_16x16x32_bf16 v[184:187], a[168:171], a[144:147], v[184:187]\n"
"v_mfma_f32_16x16x32_bf16 v[184:187], a[172:175], a[148:151], v[184:187]\n"
"v_mfma_f32_16x16x32_bf16 v[188:191], a[168:171], a[152:155], v[188:191]\n"
"global_load_dwordx4 a[168:171], v246, s[16:17] offset:-2048 sc1\n"
"v_mfma_f32_16x16x32_bf16 v[188:191], a[172:175], a[156:159], v[188:191]\n"
"s_waitcnt vmcnt(14)\n"
"v_mfma_f32_16x16x32_bf16 v[192:195], a[176:179], a[96:99], v[192:195]\n"
"global_load_dwordx4 a[172:175], v246, s[16:17] offset:-1024 sc1\n"
"v_mfma_f32_16x16x32_bf16 v[192:195], a[180:183], a[100:103], v[192:195]\n"
"v_mfma_f32_16x16x32_bf16 v[196:199], a[176:179], a[104:107], v[196:199]\n"
"s_mul_i32 s48, s49, -49152\n"
"s_add_u32 s48, s48, 147456\n"
"v_mfma_f32_16x16x32_bf16 v[196:199], a[180:183], a[108:111], v[196:199]\n"
"v_add_u32 v242, s48, v240\n"
"v_mfma_f32_16x16x32_bf16 v[200:203], a[176:179], a[112:115], v[200:203]\n"
"v_add_u32 v243, s48, v241\n"
"v_mfma_f32_16x16x32_bf16 v[200:203], a[180:183], a[116:119], v[200:203]\n"
"v_mfma_f32_16x16x32_bf16 v[204:207], a[176:179], a[120:123], v[204:207]\n"
"v_mfma_f32_16x16x32_bf16 v[204:207], a[180:183], a[124:127], v[204:207]\n"
"v_mfma_f32_16x16x32_bf16 v[208:211], a[176:179], a[128:131], v[208:211]\n"
"v_mfma_f32_16x16x32_bf16 v[208:211], a[180:183], a[132:135], v[208:211]\n"
"v_mfma_f32_16x16x32_bf16 v[212:215], a[176:179], a[136:139], v[212:215]\n"
"v_mfma_f32_16x16x32_bf16 v[212:215], a[180:183], a[140:143], v[212:215]\n"
"v_mfma_f32_16x16x32_bf16 v[216:219], a[176:179], a[144:147], v[216:219]\n"
"v_mfma_f32_16x16x32_bf16 v[216:219], a[180:183], a[148:151], v[216:219]\n"
"s_barrier\n"
"v_mfma_f32_16x16x32_bf16 v[220:223], a[176:179], a[152:155], v[220:223]\n"
"global_load_dwordx4 a[176:179], v246, s[16:17] offset:0 sc1\n"
"v_mfma_f32_16x16x32_bf16 v[220:223], a[180:183], a[156:159], v[220:223]\n"
"s_waitcnt vmcnt(14)\n"
"v_mfma_f32_16x16x32_bf16 a[0:3], a[184:187], a[96:99], a[0:3]\n"
"global_load_dwordx4 a[180:183], v246, s[16:17] offset:1024 sc1\n"
"ds_read_b128 a[96:99], v242 offset:0\n"
"v_mfma_f32_16x16x32_bf16 a[0:3], a[188:191], a[100:103], a[0:3]\n"
"ds_read_b128 a[100:103], v243 offset:0\n"
"v_mfma_f32_16x16x32_bf16 a[4:7], a[184:187], a[104:107], a[4:7]\n"
"ds_read_b128 a[104:107], v242 offset:2048\n"
"v_mfma_f32_16x16x32_bf16 a[4:7], a[188:191], a[108:111], a[4:7]\n"
"ds_read_b128 a[108:111], v243 offset:2048\n"
"v_mfma_f32_16x16x32_bf16 a[8:11], a[184:187], a[112:115], a[8:11]\n"
"ds_read_b128 a[112:115], v242 offset:4096\n"
"v_mfma_f32_16x16x32_bf16 a[8:11], a[188:191], a[116:119], a[8:11]\n"
"ds_read_b128 a[116:119], v243 offset:4096\n"
"v_mfma_f32_16x16x32_bf16 a[12:15], a[184:187], a[120:123], a[12:15]\n"
"ds_read_b128 a[120:123], v242 offset:6144\n"
"v_mfma_f32_16x16x32_bf16 a[12:15], a[188:191], a[124:127], a[12:15]\n"
"ds_read_b128 a[124:127], v243 offset:6144\n"
"v_mfma_f32_16x16x32_bf16 a[16:19], a[184:187], a[128:131], a[16:19]\n"
"ds_read_b128 a[128:131], v242 offset:8192\n"
"v_mfma_f32_16x16x32_bf16 a[16:19], a[188:191], a[132:135], a[16:19]\n"
"ds_read_b128 a[132:135], v243 offset:8192\n"
"v_mfma_f32_16x16x32_bf16 a[20:23], a[184:187], a[136:139], a[20:23]\n"
"ds_read_b128 a[136:139], v242 offset:10240\n"
"v_mfma_f32_16x16x32_bf16 a[20:23], a[188:191], a[140:143], a[20:23]\n"
"ds_read_b128 a[140:143], v243 offset:10240\n"
"v_mfma_f32_16x16x32_bf16 a[24:27], a[184:187], a[144:147], a[24:27]\n"
"ds_read_b128 a[144:147], v242 offset:12288\n"
"v_mfma_f32_16x16x32_bf16 a[24:27], a[188:191], a[148:151], a[24:27]\n"
"ds_read_b128 a[148:151], v243 offset:12288\n"
"v_mfma_f32_16x16x32_bf16 a[28:31], a[184:187], a[152:155], a[28:31]\n"
"global_load_dwordx4 a[184:187], v246, s[16:17] offset:2048 sc1\n"
"ds_read_b128 a[152:155], v242 offset:14336\n"
"v_mfma_f32_16x16x32_bf16 a[28:31], a[188:191], a[156:159], a[28:31]\n"
"ds_read_b128 a[156:159], v243 offset:14336\n"
"s_waitcnt vmcnt(14) lgkmcnt(14)\n"
"v_mfma_f32_16x16x32_bf16 v[128:131], a[192:195], a[96:99], v[128:131]\n"
"global_load_dwordx4 a[188:191], v246, s[16:17] offset:3072 sc1\n"
"v_mfma_f32_16x16x32_bf16 v[128:131], a[196:199], a[100:103], v[128:131]\n"
"s_waitcnt lgkmcnt(12)\n"
"v_mfma_f32_16x16x32_bf16 v[132:135], a[192:195], a[104:107], v[132:135]\n"
"v_add_u32 v246, 0x2000, v246\n"
"v_mfma_f32_16x16x32_bf16 v[132:135], a[196:199], a[108:111], v[132:135]\n"
"s_waitcnt lgkmcnt(10)\n"
"v_mfma_f32_16x16x32_bf16 v[136:139], a[192:195], a[112:115], v[136:139]\n"
"v_mfma_f32_16x16x32_bf16 v[136:139], a[196:199], a[116:119], v[136:139]\n"
"s_waitcnt lgkmcnt(8)\n"
"v_mfma_f32_16x16x32_bf16 v[140:143], a[192:195], a[120:123], v[140:143]\n"
"v_mfma_f32_16x16x32_bf16 v[140:143], a[196:199], a[124:127], v[140:143]\n"
"s_waitcnt lgkmcnt(6)\n"
"v_mfma_f32_16x16x32_bf16 v[144:147], a[192:195], a[128:131], v[144:147]\n"
"v_mfma_f32_16x16x32_bf16 v[144:147], a[196:199], a[132:135], v[144:147]\n"
"s_waitcnt lgkmcnt(4)\n"
"v_mfma_f32_16x16x32_bf16 v[148:151], a[192:195], a[136:139], v[148:151]\n"
"v_mfma_f32_16x16x32_bf16 v[148:151], a[196:199], a[140:143], v[148:151]\n"
"s_waitcnt lgkmcnt(2)\n"
"v_mfma_f32_16x16x32_bf16 v[152:155], a[192:195], a[144:147], v[152:155]\n"
"v_mfma_f32_16x16x32_bf16 v[152:155], a[196:199], a[148:151], v[152:155]\n"
"s_waitcnt lgkmcnt(0)\n"
"v_mfma_f32_16x16x32_bf16 v[156:159], a[192:195], a[152:155], v[156:159]\n"
"global_load_dwordx4 a[192:195], v246, s[16:17] offset:-4096 sc1\n"
"v_mfma_f32_16x16x32_bf16 v[156:159], a[196:199], a[156:159], v[156:159]\n"
"s_waitcnt vmcnt(14)\n"
"v_mfma_f32_16x16x32_bf16 v[160:163], a[200:203], a[96:99], v[160:163]\n"
"global_load_dwordx4 a[196:199], v246, s[16:17] offset:-3072 sc1\n"
"v_mfma_f32_16x16x32_bf16 v[160:163], a[204:207], a[100:103], v[160:163]\n"
"ds_read_b128 a[32:35], v244 offset:0\n"
"v_mfma_f32_16x16x32_bf16 v[164:167], a[200:203], a[104:107], v[164:167]\n"
"ds_read_b128 a[36:39], v244 offset:2048\n"
"v_mfma_f32_16x16x32_bf16 v[164:167], a[204:207], a[108:111], v[164:167]\n"
"ds_read_b128 a[40:43], v244 offset:4096\n"
"v_mfma_f32_16x16x32_bf16 v[168:171], a[200:203], a[112:115], v[168:171]\n"
"s_waitcnt vmcnt(14)\n"
"v_cmp_ne_u32 vcc, v235, v231\n"
"s_nop 0\n"
"s_cmp_eq_u64 vcc, -1\n"
"s_cbranch_scc0 .Lwslow1_%=\n"
".Lwback1_%=:\n"
"v_mfma_f32_16x16x32_bf16 v[168:171], a[204:207], a[116:119], v[168:171]\n"
"ds_read_b128 a[44:47], v244 offset:6144\n"
"v_mfma_f32_16x16x32_bf16 v[172:175], a[200:203], a[120:123], v[172:175]\n"
"ds_read_b128 a[48:51], v244 offset:8192\n"
"v_mfma_f32_16x16x32_bf16 v[172:175], a[204:207], a[124:127], v[172:175]\n"
"ds_read_b128 a[52:55], v244 offset:10240\n"
"v_mfma_f32_16x16x32_bf16 v[176:179], a[200:203], a[128:131], v[176:179]\n"
"ds_read_b128 a[56:59], v244 offset:12288\n"
"v_mfma_f32_16x16x32_bf16 v[176:179], a[204:207], a[132:135], v[176:179]\n"
"ds_read_b128 a[60:63], v244 offset:14336\n"
"v_mfma_f32_16x16x32_bf16 v[180:183], a[200:203], a[136:139], v[180:183]\n"
"ds_read_b128 a[64:67], v245 offset:0\n"
"v_mfma_f32_16x16x32_bf16 v[180:183], a[204:207], a[140:143], v[180:183]\n"
"ds_read_b128 a[68:71], v245 offset:2048\n"
"v_mfma_f32_16x16x32_bf16 v[184:187], a[200:203], a[144:147], v[184:187]\n"
"ds_read_b128 a[72:75], v245 offset:4096\n"
"v_mfma_f32_16x16x32_bf16 v[184:187], a[204:207], a[148:151], v[184:187]\n"
"ds_read_b128 a[76:79], v245 offset:6144\n"
"v_mfma_f32_16x16x32_bf16 v[188:191], a[200:203], a[152:155], v[188:191]\n"
"global_load_dwordx4 a[200:203], v246, s[16:17] offset:-2048 sc1\n"
"v_mfma_f32_16x16x32_bf16 v[188:191], a[204:207], a[156:159], v[188:191]\n"
"s_waitcnt vmcnt(13)\n"
"v_mfma_f32_16x16x32_bf16 v[192:195], a[208:211], a[96:99], v[192:195]\n"
"global_load_dwordx4 a[204:207], v246, s[16:17] offset:-1024 sc1\n"
"v_mfma_f32_16x16x32_bf16 v[192:195], a[212:215], a[100:103], v[192:195]\n"
"ds_read_b128 a[80:83], v245 offset:8192\n"
"v_mfma_f32_16x16x32_bf16 v[196:199], a[208:211], a[104:107], v[196:199]\n"
"s_mul_i32 s48, s50, -65536\n"
"s_add_u32 s48, s48, 131072\n"
"v_mfma_f32_16x16x32_bf16 v[196:199], a[212:215], a[108:111], v[196:199]\n"
"v_add_u32 v242, s48, v240\n"
"v_mfma_f32_16x16x32_bf16 v[200:203], a[208:211], a[112:115], v[200:203]\n"
"v_add_u32 v243, s48, v241\n"
"v_mfma_f32_16x16x32_bf16 v[200:203], a[212:215], a[116:119], v[200:203]\n"
"ds_read_b128 a[84:87], v245 offset:10240\n"
"s_nop 0\n"
".Lpg6_%=:\n"
"s_cbranch_execz .Lpg10_%=\n"
"v_mfma_f32_16x16x32_bf16 v[204:207], a[208:211], a[120:123], v[204:207]\n"
"ds_read_b128 a[88:91], v245 offset:12288\n"
"v_mfma_f32_16x16x32_bf16 v[204:207], a[212:215], a[124:127], v[204:207]\n"
"ds_read_b128 a[92:95], v245 offset:14336\n"
"v_mfma_f32_16x16x32_bf16 v[208:211], a[208:211], a[128:131], v[208:211]\n"
"v_mfma_f32_16x16x32_bf16 v[208:211], a[212:215], a[132:135], v[208:211]\n"
"v_mfma_f32_16x16x32_bf16 v[212:215], a[208:211], a[136:139], v[212:215]\n"
"v_mfma_f32_16x16x32_bf16 v[212:215], a[212:215], a[140:143], v[212:215]\n"
"v_mfma_f32_16x16x32_bf16 v[216:219], a[208:211], a[144:147], v[216:219]\n"
"v_mfma_f32_16x16x32_bf16 v[216:219], a[212:215], a[148:151], v[216:219]\n"
"v_mfma_f32_16x16x32_bf16 v[220:223], a[208:211], a[152:155], v[220:223]\n"
"global_load_dwordx4 a[208:211], v246, s[16:17] offset:0 sc1\n"
"v_mfma_f32_16x16x32_bf16 v[220:223], a[212:215], a[156:159], v[220:223]\n"
"s_waitcnt vmcnt(13)\n"
"v_mfma_f32_16x16x32_bf16 a[0:3], a[216:219], a[96:99], a[0:3]\n"
"global_load_dwordx4 a[212:215], v246, s[16:17] offset:1024 sc1\n"
"ds_read_b128 a[96:99], v242 offset:0\n"
"v_mfma_f32_16x16x32_bf16 a[0:3], a[220:223], a[100:103], a[0:3]\n"
"ds_read_b128 a[100:103], v243 offset:0\n"
"v_mfma_f32_16x16x32_bf16 a[4:7], a[216:219], a[104:107], a[4:7]\n"
"ds_read_b128 a[104:107], v242 offset:2048\n"
"v_mfma_f32_16x16x32_bf16 a[4:7], a[220:223], a[108:111], a[4:7]\n"
"ds_read_b128 a[108:111], v243 offset:2048\n"
"v_mfma_f32_16x16x32_bf16 a[8:11], a[216:219], a[112:115], a[8:11]\n"
"ds_read_b128 a[112:115], v242 offset:4096\n"
"ds_read_b128 v[232:235], v245 offset:16384\n"
"v_mfma_f32_16x16x32_bf16 a[8:11], a[220:223], a[116:119], a[8:11]\n"
"ds_read_b128 a[116:119], v243 offset:4096\n"
"v_mfma_f32_16x16x32_bf16 a[12:15], a[216:219], a[120:123], a[12:15]\n"
"ds_read_b128 a[120:123], v242 offset:6144\n"
"v_mfma_f32_16x16x32_bf16 a[12:15], a[220:223], a[124:127], a[12:15]\n"
"ds_read_b128 a[124:127], v243 offset:6144\n"
"v_mfma_f32_16x16x32_bf16 a[16:19], a[216:219], a[128:131], a[16:19]\n"
"ds_read_b128 a[128:131], v242 offset:8192\n"
"v_mfma_f32_16x16x32_bf16 a[16:19], a[220:223], a[132:135], a[16:19]\n"
"ds_read_b128 a[132:135], v243 offset:8192\n"
"v_mfma_f32_16x16x32_bf16 a[20:23], a[216:219], a[136:139], a[20:23]\n"
"ds_read_b128 a[136:139], v242 offset:10240\n"
"v_mfma_f32_16x16x32_bf16 a[20:23], a[220:223], a[140:143], a[20:23]\n"
"ds_read_b128 a[140:143], v243 offset:10240\n"
"v_mfma_f32_16x16x32_bf16 a[24:27], a[216:219], a[144:147], a[24:27]\n"
"ds_read_b128 a[144:147], v242 offset:12288\n"
"v_mfma_f32_16x16x32_bf16 a[24:27], a[220:223], a[148:151], a[24:27]\n"
"ds_read_b128 a[148:151], v243 offset:12288\n"
"v_mfma_f32_16x16x32_bf16 a[28:31], a[216:219], a[152:155], a[28:31]\n"
"global_load_dwordx4 a[216:219], v246, s[16:17] offset:2048 sc1\n"
"ds_read_b128 a[152:155], v242 offset:14336\n"
"v_mfma_f32_16x16x32_bf16 a[28:31], a[220:223], a[156:159], a[28:31]\n"
"ds_read_b128 a[156:159], v243 offset:14336\n"
"s_waitcnt vmcnt(13) lgkmcnt(15)\n"
"v_mfma_f32_16x16x32_bf16 v[0:3], a[160:163], a[96:99], v[0:3]\n"
"global_load_dwordx4 a[220:223], v246, s[16:17] offset:3072 sc1\n"
"v_mfma_f32_16x16x32_bf16 v[0:3], a[164:167], a[100:103], v[0:3]\n"
"s_waitcnt lgkmcnt(13)\n"
"v_mfma_f32_16x16x32_bf16 v[4:7], a[160:163], a[104:107], v[4:7]\n"
"v_add_u32 v246, 0x2000, v246\n"
"v_mfma_f32_16x16x32_bf16 v[224:227], v[252:255], a[224:227], 0\n"
"v_mfma_f32_16x16x32_bf16 v[4:7], a[164:167], a[108:111], v[4:7]\n"
"s_waitcnt lgkmcnt(10)\n"
"v_mfma_f32_16x16x32_bf16 v[8:11], a[160:163], a[112:115], v[8:11]\n"
"s_add_u32 s64, s60, 28672\n"
"s_addc_u32 s65, s61, 0\n"
"v_mfma_f32_16x16x32_bf16 v[8:11], a[164:167], a[116:119], v[8:11]\n"
"s_waitcnt lgkmcnt(8)\n"
"v_mfma_f32_16x16x32_bf16 v[12:15], a[160:163], a[120:123], v[12:15]\n"
"v_mfma_f32_16x16x32_bf16 v[12:15], a[164:167], a[124:127], v[12:15]\n"
"v_mul_f32 v224, v128, v224\n"
"s_waitcnt lgkmcnt(6)\n"
"v_mfma_f32_16x16x32_bf16 v[16:19], a[160:163], a[128:131], v[16:19]\n"
"v_mul_f32 v225, v129, v225\n"
"v_mfma_f32_16x16x32_bf16 v[228:231], v[250:253], a[224:227], 0\n"
"v_mfma_f32_16x16x32_bf16 v[16:19], a[164:167], a[132:135], v[16:19]\n"
"v_mul_f32 v226, v130, v226\n"
"s_waitcnt lgkmcnt(4)\n"
"v_mfma_f32_16x16x32_bf16 v[20:23], a[160:163], a[136:139], v[20:23]\n"
"v_mul_f32 v227, v131, v227\n"
"v_mfma_f32_16x16x32_bf16 v[20:23], a[164:167], a[140:143], v[20:23]\n"
"v_cvt_pk_bf16_f32 v224, v224, v225\n"
"s_waitcnt lgkmcnt(2)\n"
"v_mfma_f32_16x16x32_bf16 v[24:27], a[160:163], a[144:147], v[24:27]\n"
"v_cvt_pk_bf16_f32 v225, v226, v227\n"
"ds_write_b64 v244, v[224:225] offset:16384\n"
"v_mfma_f32_16x16x32_bf16 v[24:27], a[164:167], a[148:151], v[24:27]\n"
"v_mul_f32 v228, v160, v228\n"
"s_waitcnt lgkmcnt(1)\n"
"v_mfma_f32_16x16x32_bf16 v[28:31], a[160:163], a[152:155], v[28:31]\n"
"global_load_dwordx4 a[160:163], v246, s[16:17] offset:-4096 sc1\n"
"v_mul_f32 v229, v161, v229\n"
"v_mfma_f32_16x16x32_bf16 v[28:31], a[164:167], a[156:159], v[28:31]\n"
"v_mfma_f32_16x16x32_bf16 v[224:227], v[252:255], v[232:235], 0\n"
"s_waitcnt vmcnt(13)\n"
"v_mfma_f32_16x16x32_bf16 v[32:35], a[168:171], a[96:99], v[32:35]\n"
"global_load_dwordx4 a[164:167], v246, s[16:17] offset:-3072 sc1\n"
"v_mul_f32 v230, v162, v230\n"
"v_mfma_f32_16x16x32_bf16 v[32:35], a[172:175], a[100:103], v[32:35]\n"
"v_mul_f32 v231, v163, v231\n"
"v_cvt_pk_bf16_f32 v228, v228, v229\n"
"v_mfma_f32_16x16x32_bf16 v[36:39], a[168:171], a[104:107], v[36:39]\n"
"v_cvt_pk_bf16_f32 v229, v230, v231\n"
"v_mfma_f32_16x16x32_bf16 v[36:39], a[172:175], a[108:111], v[36:39]\n"
"ds_write_b64 v244, v[228:229] offset:16392\n"
"v_mfma_f32_16x16x32_bf16 v[40:43], a[168:171], a[112:115], v[40:43]\n"
"v_mul_f32 v224, v192, v224\n"
"v_mul_f32 v225, v193, v225\n"
"v_mfma_f32_16x16x32_bf16 v[40:43], a[172:175], a[116:119], v[40:43]\n"
"v_mfma_f32_16x16x32_bf16 v[228:231], v[250:253], v[232:235], 0\n"
"v_mfma_f32_16x16x32_bf16 v[44:47], a[168:171], a[120:123], v[44:47]\n"
"ds_read_b128 v[232:235], v245 offset:18432\n"
"v_mul_f32 v226, v194, v226\n"
"v_mfma_f32_16x16x32_bf16 v[44:47], a[172:175], a[124:127], v[44:47]\n"
"v_mul_f32 v227, v195, v227\n"
"v_mfma_f32_16x16x32_bf16 v[48:51], a[168:171], a[128:131], v[48:51]\n"
"v_cvt_pk_bf16_f32 v224, v224, v225\n"
"v_mfma_f32_16x16x32_bf16 v[48:51], a[172:175], a[132:135], v[48:51]\n"
"v_cvt_pk_bf16_f32 v225, v226, v227\n"
"ds_write_b64 v245, v[224:225] offset:16384\n"
"v_mfma_f32_16x16x32_bf16 v[52:55], a[168:171], a[136:139], v[52:55]\n"
"v_accvgpr_read_b32 v249, a0\n"
"v_mfma_f32_16x16x32_bf16 v[52:55], a[172:175], a[140:143], v[52:55]\n"
"v_mul_f32_e64 v228, v249, v228\n"
"v_mfma_f32_16x16x32_bf16 v[224:227], v[252:255], a[228:231], 0\n"
"v_mfma_f32_16x16x32_bf16 v[56:59], a[168:171], a[144:147], v[56:59]\n"
"v_accvgpr_read_b32 v249, a1\n"
"v_mul_f32_e64 v229, v249, v229\n"
"v_mfma_f32_16x16x32_bf16 v[56:59], a[172:175], a[148:151], v[56:59]\n"
"v_accvgpr_read_b32 v249, a2\n"
"v_mul_f32_e64 v230, v249, v230\n"
"v_mfma_f32_16x16x32_bf16 v[60:63], a[168:171], a[152:155], v[60:63]\n"
"global_load_dword a168, v250, s[64:65]\n"
"global_load_dwordx4 a[168:171], v246, s[16:17] offset:-2048 sc1\n"
"v_accvgpr_read_b32 v249, a3\n"
"v_mfma_f32_16x16x32_bf16 v[60:63], a[172:175], a[156:159], v[60:63]\n"
"v_mul_f32_e64 v231, v249, v231\n"
"s_waitcnt vmcnt(14)\n"
"v_mfma_f32_16x16x32_bf16 v[64:67], a[176:179], a[96:99], v[64:67]\n"
"global_load_dwordx4 a[172:175], v246, s[16:17] offset:-1024 sc1\n"
"v_cvt_pk_bf16_f32 v228, v228, v229\n"
"v_mfma_f32_16x16x32_bf16 v[64:67], a[180:183], a[100:103], v[64:67]\n"
"v_cvt_pk_bf16_f32 v229, v230, v231\n"
"ds_write_b64 v245, v[228:229] offset:16392\n"
"v_mfma_f32_16x16x32_bf16 v[68:71], a[176:179], a[104:107], v[68:71]\n"
"s_mul_i32 s48, s49, -65536\n"
"s_add_u32 s48, s48, 131072\n"
"v_mfma_f32_16x16x32_bf16 v[68:71], a[180:183], a[108:111], v[68:71]\n"
"v_add_u32 v242, s48, v240\n"
"v_mul_f32 v224, v132, v224\n"
"v_mfma_f32_16x16x32_bf16 v[72:75], a[176:179], a[112:115], v[72:75]\n"
"v_add_u32 v243, s48, v241\n"
"v_mul_f32 v225, v133, v225\n"
"v_mfma_f32_16x16x32_bf16 v[72:75], a[180:183], a[116:119], v[72:75]\n"
"v_mfma_f32_16x16x32_bf16 v[228:231], v[250:253], a[228:231], 0\n"
"v_mul_f32 v226, v134, v226\n"
"v_mfma_f32_16x16x32_bf16 v[76:79], a[176:179], a[120:123], v[76:79]\n"
"v_mul_f32 v227, v135, v227\n"
"v_cvt_pk_bf16_f32 v224, v224, v225\n"
"v_mfma_f32_16x16x32_bf16 v[76:79], a[180:183], a[124:127], v[76:79]\n"
"v_cvt_pk_bf16_f32 v225, v226, v227\n"
"ds_write_b64 v244, v[224:225] offset:18432\n"
"v_mfma_f32_16x16x32_bf16 v[80:83], a[176:179], a[128:131], v[80:83]\n"
"v_mul_f32 v228, v164, v228\n"
"v_mul_f32 v229, v165, v229\n"
"v_mfma_f32_16x16x32_bf16 v[80:83], a[180:183], a[132:135], v[80:83]\n"
"s_waitcnt lgkmcnt(3)\n"
"v_mfma_f32_16x16x32_bf16 v[224:227], v[252:255], v[232:235], 0\n"
"v_mul_f32 v230, v166, v230\n"
"v_mfma_f32_16x16x32_bf16 v[84:87], a[176:179], a[136:139], v[84:87]\n"
"v_mul_f32 v231, v167, v231\n"
"v_mfma_f32_16x16x32_bf16 v[84:87], a[180:183], a[140:143], v[84:87]\n"
"v_cvt_pk_bf16_f32 v228, v228, v229\n"
"v_mfma_f32_16x16x32_bf16 v[88:91], a[176:179], a[144:147], v[88:91]\n"
"v_cvt_pk_bf16_f32 v229, v230, v231\n"
"v_mfma_f32_16x16x32_bf16 v[88:91], a[180:183], a[148:151], v[88:91]\n"
"ds_write_b64 v244, v[228:229] offset:18440\n"
"v_mfma_f32_16x16x32_bf16 v[92:95], a[176:179], a[152:155], v[92:95]\n"
"global_load_dwordx4 a[176:179], v246, s[16:17] offset:0 sc1\n"
"v_mul_f32 v224, v196, v224\n"
"v_mfma_f32_16x16x32_bf16 v[92:95], a[180:183], a[156:159], v[92:95]\n"
"v_mul_f32 v225, v197, v225\n"
"s_waitcnt vmcnt(14)\n"
"v_mfma_f32_16x16x32_bf16 v[96:99], a[184:187], a[96:99], v[96:99]\n"
"global_load_dwordx4 a[180:183], v246, s[16:17] offset:1024 sc1\n"
"ds_read_b128 a[96:99], v242 offset:0\n"
"v_mfma_f32_16x16x32_bf16 v[96:99], a[188:191], a[100:103], v[96:99]\n"
"ds_read_b128 a[100:103], v243 offset:0\n"
"v_mfma_f32_16x16x32_bf16 v[228:231], v[250:253], v[232:235], 0\n"
"v_mfma_f32_16x16x32_bf16 v[100:103], a[184:187], a[104:107], v[100:103]\n"
"ds_read_b128 a[104:107], v242 offset:2048\n"
"ds_read_b128 v[232:235], v245 offset:20480\n"
"v_mfma_f32_16x16x32_bf16 v[100:103], a[188:191], a[108:111], v[100:103]\n"
"ds_read_b128 a[108:111], v243 offset:2048\n"
"v_mul_f32 v226, v198, v226\n"
"v_mfma_f32_16x16x32_bf16 v[104:107], a[184:187], a[112:115], v[104:107]\n"
"ds_read_b128 a[112:115], v242 offset:4096\n"
"v_mul_f32 v227, v199, v227\n"
"v_mfma_f32_16x16x32_bf16 v[104:107], a[188:191], a[116:119], v[104:107]\n"
"ds_read_b128 a[116:119], v243 offset:4096\n"
"v_cvt_pk_bf16_f32 v224, v224, v225\n"
"v_mfma_f32_16x16x32_bf16 v[108:111], a[184:187], a[120:123], v[108:111]\n"
"ds_read_b128 a[120:123], v242 offset:6144\n"
"v_cvt_pk_bf16_f32 v225, v226, v227\n"
"v_mfma_f32_16x16x32_bf16 v[108:111], a[188:191], a[124:127], v[108:111]\n"
"ds_read_b128 a[124:127], v243 offset:6144\n"
"ds_write_b64 v245, v[224:225] offset:18432\n"
"v_mfma_f32_16x16x32_bf16 v[112:115], a[184:187], a[128:131], v[112:115]\n"
"ds_read_b128 a[128:131], v242 offset:8192\n"
"v_accvgpr_read_b32 v249, a4\n"
"v_mfma_f32_16x16x32_bf16 v[112:115], a[188:191], a[132:135], v[112:115]\n"
"ds_read_b128 a[132:135], v243 offset:8192\n"
"v_mul_f32_e64 v228, v249, v228\n"
"v_mfma_f32_16x16x32_bf16 v[116:119], a[184:187], a[136:139], v[116:119]\n"
"ds_read_b128 a[136:139], v242 offset:10240\n"
"v_mfma_f32_16x16x32_bf16 v[224:227], v[252:255], a[232:235], 0\n"
"v_mfma_f32_16x16x32_bf16 v[116:119], a[188:191], a[140:143], v[116:119]\n"
"ds_read_b128 a[140:143], v243 offset:10240\n"
"v_accvgpr_read_b32 v249, a5\n"
"v_mfma_f32_16x16x32_bf16 v[120:123], a[184:187], a[144:147], v[120:123]\n"
"ds_read_b128 a[144:147], v242 offset:12288\n"
"v_mul_f32_e64 v229, v249, v229\n"
"v_mfma_f32_16x16x32_bf16 v[120:123], a[188:191], a[148:151], v[120:123]\n"
"ds_read_b128 a[148:151], v243 offset:12288\n"
"v_accvgpr_read_b32 v249, a6\n"
"v_mfma_f32_16x16x32_bf16 v[124:127], a[184:187], a[152:155], v[124:127]\n"
"global_load_dwordx4 a[184:187], v246, s[16:17] offset:2048 sc1\n"
"ds_read_b128 a[152:155], v242 offset:14336\n"
"v_mfma_f32_16x16x32_bf16 v[124:127], a[188:191], a[156:159], v[124:127]\n"
"ds_read_b128 a[156:159], v243 offset:14336\n"
"s_waitcnt vmcnt(14) lgkmcnt(15)\n"
"v_mfma_f32_16x16x32_bf16 v[0:3], a[192:195], a[96:99], v[0:3]\n"
"global_load_dwordx4 a[188:191], v246, s[16:17] offset:3072 sc1\n"
"v_mul_f32_e64 v230, v249, v230\n"
"v_mfma_f32_16x16x32_bf16 v[0:3], a[196:199], a[100:103], v[0:3]\n"
"v_accvgpr_read_b32 v249, a7\n"
"s_waitcnt lgkmcnt(13)\n"
"v_mfma_f32_16x16x32_bf16 v[4:7], a[192:195], a[104:107], v[4:7]\n"
"v_add_u32 v246, 0x2000, v246\n"
"v_mul_f32_e64 v231, v249, v231\n"
"v_mfma_f32_16x16x32_bf16 v[4:7], a[196:199], a[108:111], v[4:7]\n"
"v_cvt_pk_bf16_f32 v228, v228, v229\n"
"s_waitcnt lgkmcnt(11)\n"
"v_mfma_f32_16x16x32_bf16 v[8:11], a[192:195], a[112:115], v[8:11]\n"
"v_cvt_pk_bf16_f32 v229, v230, v231\n"
"ds_write_b64 v245, v[228:229] offset:18440\n"
"v_mfma_f32_16x16x32_bf16 v[8:11], a[196:199], a[116:119], v[8:11]\n"
"v_mul_f32 v224, v136, v224\n"
"s_waitcnt lgkmcnt(10)\n"
"v_mfma_f32_16x16x32_bf16 v[12:15], a[192:195], a[120:123], v[12:15]\n"
"v_mul_f32 v225, v137, v225\n"
"v_mfma_f32_16x16x32_bf16 v[228:231], v[250:253], a[232:235], 0\n"
"v_mfma_f32_16x16x32_bf16 v[12:15], a[196:199], a[124:127], v[12:15]\n"
"v_mul_f32 v226, v138, v226\n"
"s_waitcnt lgkmcnt(7)\n"
"v_mfma_f32_16x16x32_bf16 v[16:19], a[192:195], a[128:131], v[16:19]\n"
"v_mul_f32 v227, v139, v227\n"
"v_cvt_pk_bf16_f32 v224, v224, v225\n"
"v_mfma_f32_16x16x32_bf16 v[16:19], a[196:199], a[132:135], v[16:19]\n"
"v_cvt_pk_bf16_f32 v225, v226, v227\n"
"s_waitcnt lgkmcnt(5)\n"
"v_mfma_f32_16x16x32_bf16 v[20:23], a[192:195], a[136:139], v[20:23]\n"
"ds_write_b64 v244, v[224:225] offset:20480\n"
"v_mul_f32 v228, v168, v228\n"
"v_mfma_f32_16x16x32_bf16 v[20:23], a[196:199], a[140:143], v[20:23]\n"
"v_mul_f32 v229, v169, v229\n"
"s_waitcnt lgkmcnt(4)\n"
"v_mfma_f32_16x16x32_bf16 v[24:27], a[192:195], a[144:147], v[24:27]\n"
"v_mfma_f32_16x16x32_bf16 v[224:227], v[252:255], v[232:235], 0\n"
"v_mul_f32 v230, v170, v230\n"
"v_mfma_f32_16x16x32_bf16 v[24:27], a[196:199], a[148:151], v[24:27]\n"
"v_mul_f32 v231, v171, v231\n"
"s_waitcnt lgkmcnt(2)\n"
"v_mfma_f32_16x16x32_bf16 v[28:31], a[192:195], a[152:155], v[28:31]\n"
"global_load_dwordx4 a[192:195], v246, s[16:17] offset:-4096 sc1\n"
"v_cvt_pk_bf16_f32 v228, v228, v229\n"
"v_mfma_f32_16x16x32_bf16 v[28:31], a[196:199], a[156:159], v[28:31]\n"
"v_cvt_pk_bf16_f32 v229, v230, v231\n"
"s_waitcnt vmcnt(14)\n"
"v_mfma_f32_16x16x32_bf16 v[32:35], a[200:203], a[96:99], v[32:35]\n"
"global_load_dwordx4 a[196:199], v246, s[16:17] offset:-3072 sc1\n"
"ds_write_b64 v244, v[228:229] offset:20488\n"
"v_mfma_f32_16x16x32_bf16 v[32:35], a[204:207], a[100:103], v[32:35]\n"
"v_mul_f32 v224, v200, v224\n"
"v_mul_f32 v225, v201, v225\n"
"v_mfma_f32_16x16x32_bf16 v[36:39], a[200:203], a[104:107], v[36:39]\n"
"v_mfma_f32_16x16x32_bf16 v[228:231], v[250:253], v[232:235], 0\n"
"ds_read_b128 v[232:235], v245 offset:22528\n"
"v_mfma_f32_16x16x32_bf16 v[36:39], a[204:207], a[108:111], v[36:39]\n"
"v_mul_f32 v226, v202, v226\n"
"v_mul_f32 v227, v203, v227\n"
"v_mfma_f32_16x16x32_bf16 v[40:43], a[200:203], a[112:115], v[40:43]\n"
"v_cvt_pk_bf16_f32 v224, v224, v225\n"
"v_cvt_pk_bf16_f32 v225, v226, v227\n"
"v_mfma_f32_16x16x32_bf16 v[40:43], a[204:207], a[116:119], v[40:43]\n"
"ds_write_b64 v245, v[224:225] offset:20480\n"
"v_accvgpr_read_b32 v249, a8\n"
"v_mfma_f32_16x16x32_bf16 v[44:47], a[200:203], a[120:123], v[44:47]\n"
"v_mul_f32_e64 v228, v249, v228\n"
"v_mfma_f32_16x16x32_bf16 v[224:227], v[252:255], a[236:239], 0\n"
"v_mfma_f32_16x16x32_bf16 v[44:47], a[204:207], a[124:127], v[44:47]\n"
"v_accvgpr_read_b32 v249, a9\n"
"v_mul_f32_e64 v229, v249, v229\n"
"v_mfma_f32_16x16x32_bf16 v[48:51], a[200:203], a[128:131], v[48:51]\n"
"v_accvgpr_read_b32 v249, a10\n"
"v_mul_f32_e64 v230, v249, v230\n"
"v_mfma_f32_16x16x32_bf16 v[48:51], a[204:207], a[132:135], v[48:51]\n"
"v_accvgpr_read_b32 v249, a11\n"
"v_mul_f32_e64 v231, v249, v231\n"
"v_mfma_f32_16x16x32_bf16 v[52:55], a[200:203], a[136:139], v[52:55]\n"
"v_cvt_pk_bf16_f32 v228, v228, v229\n"
"v_cvt_pk_bf16_f32 v229, v230, v231\n"
"v_mfma_f32_16x16x32_bf16 v[52:55], a[204:207], a[140:143], v[52:55]\n"
"ds_write_b64 v245, v[228:229] offset:20488\n"
"v_mul_f32 v224, v140, v224\n"
"v_mfma_f32_16x16x32_bf16 v[56:59], a[200:203], a[144:147], v[56:59]\n"
"v_mul_f32 v225, v141, v225\n"
"v_mfma_f32_16x16x32_bf16 v[228:231], v[250:253], a[236:239], 0\n"
"v_mfma_f32_16x16x32_bf16 v[56:59], a[204:207], a[148:151], v[56:59]\n"
"v_mul_f32 v226, v142, v226\n"
"v_mul_f32 v227, v143, v227\n"
"v_mfma_f32_16x16x32_bf16 v[60:63], a[200:203], a[152:155], v[60:63]\n"
"global_load_dwordx4 a[200:203], v246, s[16:17] offset:-2048 sc1\n"
"v_cvt_pk_bf16_f32 v224, v224, v225\n"
"v_mfma_f32_16x16x32_bf16 v[60:63], a[204:207], a[156:159], v[60:63]\n"
"v_cvt_pk_bf16_f32 v225, v226, v227\n"
"s_waitcnt vmcnt(14)\n"
"v_mfma_f32_16x16x32_bf16 v[64:67], a[208:211], a[96:99], v[64:67]\n"
"global_load_dwordx4 a[204:207], v246, s[16:17] offset:-1024 sc1\n"
"ds_write_b64 v244, v[224:225] offset:22528\n"
"v_mfma_f32_16x16x32_bf16 v[64:67], a[212:215], a[100:103], v[64:67]\n"
"v_mul_f32 v228, v172, v228\n"
"v_mul_f32 v229, v173, v229\n"
"v_mfma_f32_16x16x32_bf16 v[68:71], a[208:211], a[104:107], v[68:71]\n"
"s_mul_i32 s48, s50, -49152\n"
"s_add_u32 s48, s48, 147456\n"
"v_mfma_f32_16x16x32_bf16 v[68:71], a[212:215], a[108:111], v[68:71]\n"
"v_add_u32 v242, s48, v240\n"
"s_waitcnt lgkmcnt(3)\n"
"v_mfma_f32_16x16x32_bf16 v[224:227], v[252:255], v[232:235], 0\n"
"v_mfma_f32_16x16x32_bf16 v[72:75], a[208:211], a[112:115], v[72:75]\n"
"v_add_u32 v243, s48, v241\n"
"v_mul_f32 v230, v174, v230\n"
"v_mfma_f32_16x16x32_bf16 v[72:75], a[212:215], a[116:119], v[72:75]\n"
"v_mul_f32 v231, v175, v231\n"
"v_cvt_pk_bf16_f32 v228, v228, v229\n"
"v_mfma_f32_16x16x32_bf16 v[76:79], a[208:211], a[120:123], v[76:79]\n"
"v_cvt_pk_bf16_f32 v229, v230, v231\n"
"ds_write_b64 v244, v[228:229] offset:22536\n"
"v_mfma_f32_16x16x32_bf16 v[76:79], a[212:215], a[124:127], v[76:79]\n"
"v_mul_f32 v224, v204, v224\n"
"v_mul_f32 v225, v205, v225\n"
"v_mfma_f32_16x16x32_bf16 v[80:83], a[208:211], a[128:131], v[80:83]\n"
"v_mfma_f32_16x16x32_bf16 v[228:231], v[250:253], v[232:235], 0\n"
"ds_read_b128 v[232:235], v245 offset:24576\n"
"v_mfma_f32_16x16x32_bf16 v[80:83], a[212:215], a[132:135], v[80:83]\n"
"v_mul_f32 v226, v206, v226\n"
"v_mul_f32 v227, v207, v227\n"
"v_mfma_f32_16x16x32_bf16 v[84:87], a[208:211], a[136:139], v[84:87]\n"
"v_cvt_pk_bf16_f32 v224, v224, v225\n"
"v_cvt_pk_bf16_f32 v225, v226, v227\n"
"v_mfma_f32_16x16x32_bf16 v[84:87], a[212:215], a[140:143], v[84:87]\n"
"ds_write_b64 v245, v[224:225] offset:22528\n"
"v_accvgpr_read_b32 v249, a12\n"
"v_mfma_f32_16x16x32_bf16 v[88:91], a[208:211], a[144:147], v[88:91]\n"
"v_mul_f32_e64 v228, v249, v228\n"
"v_mfma_f32_16x16x32_bf16 v[224:227], v[252:255], a[240:243], 0\n"
"v_mfma_f32_16x16x32_bf16 v[88:91], a[212:215], a[148:151], v[88:91]\n"
"v_accvgpr_read_b32 v249, a13\n"
"v_mul_f32_e64 v229, v249, v229\n"
"v_mfma_f32_16x16x32_bf16 v[92:95], a[208:211], a[152:155], v[92:95]\n"
"global_load_dwordx4 a[208:211], v246, s[16:17] offset:0 sc1\n"
"v_accvgpr_read_b32 v249, a14\n"
"v_mfma_f32_16x16x32_bf16 v[92:95], a[212:215], a[156:159], v[92:95]\n"
"v_mul_f32_e64 v230, v249, v230\n"
"s_waitcnt vmcnt(14)\n"
"v_mfma_f32_16x16x32_bf16 v[96:99], a[216:219], a[96:99], v[96:99]\n"
"global_load_dwordx4 a[212:215], v246, s[16:17] offset:1024 sc1\n"
"ds_read_b128 a[96:99], v242 offset:0\n"
"v_mfma_f32_16x16x32_bf16 v[96:99], a[220:223], a[100:103], v[96:99]\n"
"ds_read_b128 a[100:103], v243 offset:0\n"
"v_accvgpr_read_b32 v249, a15\n"
"v_mfma_f32_16x16x32_bf16 v[100:103], a[216:219], a[104:107], v[100:103]\n"
"ds_read_b128 a[104:107], v242 offset:2048\n"
"v_mul_f32_e64 v231, v249, v231\n"
"v_mfma_f32_16x16x32_bf16 v[100:103], a[220:223], a[108:111], v[100:103]\n"
"ds_read_b128 a[108:111], v243 offset:2048\n"
"v_cvt_pk_bf16_f32 v228, v228, v229\n"
"v_mfma_f32_16x16x32_bf16 v[104:107], a[216:219], a[112:115], v[104:107]\n"
"ds_read_b128 a[112:115], v242 offset:4096\n"
"v_cvt_pk_bf16_f32 v229, v230, v231\n"
"v_mfma_f32_16x16x32_bf16 v[104:107], a[220:223], a[116:119], v[104:107]\n"
"ds_read_b128 a[116:119], v243 offset:4096\n"
"ds_write_b64 v245, v[228:229] offset:22536\n"
"v_mfma_f32_16x16x32_bf16 v[108:111], a[216:219], a[120:123], v[108:111]\n"
"ds_read_b128 a[120:123], v242 offset:6144\n"
"v_mul_f32 v224, v144, v224\n"
"v_mfma_f32_16x16x32_bf16 v[108:111], a[220:223], a[124:127], v[108:111]\n"
"ds_read_b128 a[124:127], v243 offset:6144\n"
"v_mul_f32 v225, v145, v225\n"
"v_mfma_f32_16x16x32_bf16 v[112:115], a[216:219], a[128:131], v[112:115]\n"
"ds_read_b128 a[128:131], v242 offset:8192\n"
"v_mfma_f32_16x16x32_bf16 v[228:231], v[250:253], a[240:243], 0\n"
"v_mfma_f32_16x16x32_bf16 v[112:115], a[220:223], a[132:135], v[112:115]\n"
"ds_read_b128 a[132:135], v243 offset:8192\n"
"v_mul_f32 v226, v146, v226\n"
"v_mfma_f32_16x16x32_bf16 v[116:119], a[216:219], a[136:139], v[116:119]\n"
"ds_read_b128 a[136:139], v242 offset:10240\n"
"v_mul_f32 v227, v147, v227\n"
"v_mfma_f32_16x16x32_bf16 v[116:119], a[220:223], a[140:143], v[116:119]\n"
"ds_read_b128 a[140:143], v243 offset:10240\n"
"v_cvt_pk_bf16_f32 v224, v224, v225\n"
"v_mfma_f32_16x16x32_bf16 v[120:123], a[216:219], a[144:147], v[120:123]\n"
"ds_read_b128 a[144:147], v242 offset:12288\n"
"v_cvt_pk_bf16_f32 v225, v226, v227\n"
"v_mfma_f32_16x16x32_bf16 v[120:123], a[220:223], a[148:151], v[120:123]\n"
"ds_read_b128 a[148:151], v243 offset:12288\n"
"ds_write_b64 v244, v[224:225] offset:24576\n"
"v_mfma_f32_16x16x32_bf16 v[124:127], a[216:219], a[152:155], v[124:127]\n"
"global_load_dwordx4 a[216:219], v246, s[16:17] offset:2048 sc1\n"
"ds_read_b128 a[152:155], v242 offset:14336\n"
"v_mfma_f32_16x16x32_bf16 v[124:127], a[220:223], a[156:159], v[124:127]\n"
"ds_read_b128 a[156:159], v243 offset:14336\n"
"s_waitcnt vmcnt(14) lgkmcnt(15)\n"
"v_mfma_f32_16x16x32_bf16 v[0:3], a[160:163], a[96:99], v[0:3]\n"
"global_load_dwordx4 a[220:223], v246, s[16:17] offset:3072 sc1\n"
"v_mul_f32 v228, v176, v228\n"
"v_mfma_f32_16x16x32_bf16 v[0:3], a[164:167], a[100:103], v[0:3]\n"
"v_mul_f32 v229, v177, v229\n"
"s_waitcnt lgkmcnt(14)\n"
"v_mfma_f32_16x16x32_bf16 v[4:7], a[160:163], a[104:107], v[4:7]\n"
"v_add_u32 v246, 0x2000, v246\n"
"v_mfma_f32_16x16x32_bf16 v[224:227], v[252:255], v[232:235], 0\n"
"v_mfma_f32_16x16x32_bf16 v[4:7], a[164:167], a[108:111], v[4:7]\n"
"v_mul_f32 v230, v178, v230\n"
"s_waitcnt lgkmcnt(12)\n"
"v_mfma_f32_16x16x32_bf16 v[8:11], a[160:163], a[112:115], v[8:11]\n"
"v_mul_f32 v231, v179, v231\n"
"v_cvt_pk_bf16_f32 v228, v228, v229\n"
"v_mfma_f32_16x16x32_bf16 v[8:11], a[164:167], a[116:119], v[8:11]\n"
"v_cvt_pk_bf16_f32 v229, v230, v231\n"
"s_waitcnt lgkmcnt(9)\n"
"v_mfma_f32_16x16x32_bf16 v[12:15], a[160:163], a[120:123], v[12:15]\n"
"ds_write_b64 v244, v[228:229] offset:24584\n"
"v_mul_f32 v224, v208, v224\n"
"v_mfma_f32_16x16x32_bf16 v[12:15], a[164:167], a[124:127], v[12:15]\n"
"v_mul_f32 v225, v209, v225\n"
"s_waitcnt lgkmcnt(8)\n"
"v_mfma_f32_16x16x32_bf16 v[16:19], a[160:163], a[128:131], v[16:19]\n"
"v_mfma_f32_16x16x32_bf16 v[228:231], v[250:253], v[232:235], 0\n"
"ds_read_b128 v[232:235], v245 offset:26624\n"
"v_mfma_f32_16x16x32_bf16 v[16:19], a[164:167], a[132:135], v[16:19]\n"
"v_mul_f32 v226, v210, v226\n"
"s_waitcnt lgkmcnt(7)\n"
"v_mfma_f32_16x16x32_bf16 v[20:23], a[160:163], a[136:139], v[20:23]\n"
"v_mul_f32 v227, v211, v227\n"
"v_cvt_pk_bf16_f32 v224, v224, v225\n"
"v_mfma_f32_16x16x32_bf16 v[20:23], a[164:167], a[140:143], v[20:23]\n"
"v_cvt_pk_bf16_f32 v225, v226, v227\n"
"s_waitcnt lgkmcnt(5)\n"
"v_mfma_f32_16x16x32_bf16 v[24:27], a[160:163], a[144:147], v[24:27]\n"
"ds_write_b64 v245, v[224:225] offset:24576\n"
"v_accvgpr_read_b32 v249, a16\n"
"v_mfma_f32_16x16x32_bf16 v[24:27], a[164:167], a[148:151], v[24:27]\n"
"v_mul_f32_e64 v228, v249, v228\n"
"s_waitcnt lgkmcnt(3)\n"
"v_mfma_f32_16x16x32_bf16 v[28:31], a[160:163], a[152:155], v[28:31]\n"
"global_load_dwordx4 a[160:163], v246, s[16:17] offset:-4096 sc1\n"
"v_mfma_f32_16x16x32_bf16 v[224:227], v[252:255], a[244:247], 0\n"
"v_mfma_f32_16x16x32_bf16 v[28:31], a[164:167], a[156:159], v[28:31]\n"
"v_accvgpr_read_b32 v249, a17\n"
"s_waitcnt vmcnt(13)\n"
"v_mfma_f32_16x16x32_bf16 v[32:35], a[168:171], a[96:99], v[32:35]\n"
"global_load_dwordx4 a[164:167], v246, s[16:17] offset:-3072 sc1\n"
"v_mul_f32_e64 v229, v249, v229\n"
"v_mfma_f32_16x16x32_bf16 v[32:35], a[172:175], a[100:103], v[32:35]\n"
"v_accvgpr_read_b32 v249, a18\n"
"v_mul_f32_e64 v230, v249, v230\n"
"v_mfma_f32_16x16x32_bf16 v[36:39], a[168:171], a[104:107], v[36:39]\n"
"v_accvgpr_read_b32 v249, a19\n"
"v_mul_f32_e64 v231, v249, v231\n"
"v_mfma_f32_16x16x32_bf16 v[36:39], a[172:175], a[108:111], v[36:39]\n"
"v_cvt_pk_bf16_f32 v228, v228, v229\n"
"v_cvt_pk_bf16_f32 v229, v230, v231\n"
"v_mfma_f32_16x16x32_bf16 v[40:43], a[168:171], a[112:115], v[40:43]\n"
"ds_write_b64 v245, v[228:229] offset:24584\n"
"v_mul_f32 v224, v148, v224\n"
"v_mfma_f32_16x16x32_bf16 v[40:43], a[172:175], a[116:119], v[40:43]\n"
"v_mul_f32 v225, v149, v225\n"
"v_mfma_f32_16x16x32_bf16 v[228:231], v[250:253], a[244:247], 0\n"
"v_mfma_f32_16x16x32_bf16 v[44:47], a[168:171], a[120:123], v[44:47]\n"
"v_mul_f32 v226, v150, v226\n"
"v_mul_f32 v227, v151, v227\n"
"v_mfma_f32_16x16x32_bf16 v[44:47], a[172:175], a[124:127], v[44:47]\n"
"v_cvt_pk_bf16_f32 v224, v224, v225\n"
"v_cvt_pk_bf16_f32 v225, v226, v227\n"
"v_mfma_f32_16x16x32_bf16 v[48:51], a[168:171], a[128:131], v[48:51]\n"
"ds_write_b64 v244, v[224:225] offset:26624\n"
"v_mul_f32 v228, v180, v228\n"
"v_mfma_f32_16x16x32_bf16 v[48:51], a[172:175], a[132:135], v[48:51]\n"
"v_mul_f32 v229, v181, v229\n"
"s_waitcnt lgkmcnt(3)\n"
"v_mfma_f32_16x16x32_bf16 v[224:227], v[252:255], v[232:235], 0\n"
"v_mfma_f32_16x16x32_bf16 v[52:55], a[168:171], a[136:139], v[52:55]\n"
"v_mul_f32 v230, v182, v230\n"
"v_mul_f32 v231, v183, v231\n"
"v_mfma_f32_16x16x32_bf16 v[52:55], a[172:175], a[140:143], v[52:55]\n"
"v_cvt_pk_bf16_f32 v228, v228, v229\n"
"v_cvt_pk_bf16_f32 v229, v230, v231\n"
"v_mfma_f32_16x16x32_bf16 v[56:59], a[168:171], a[144:147], v[56:59]\n"
"ds_write_b64 v244, v[228:229] offset:26632\n"
"v_mul_f32 v224, v212, v224\n"
"v_mfma_f32_16x16x32_bf16 v[56:59], a[172:175], a[148:151], v[56:59]\n"
"v_mul_f32 v225, v213, v225\n"
"v_mfma_f32_16x16x32_bf16 v[228:231], v[250:253], v[232:235], 0\n"
"v_mfma_f32_16x16x32_bf16 v[60:63], a[168:171], a[152:155], v[60:63]\n"
"global_load_dwordx4 a[168:171], v246, s[16:17] offset:-2048 sc1\n"
"ds_read_b128 v[232:235], v245 offset:28672\n"
"v_mfma_f32_16x16x32_bf16 v[60:63], a[172:175], a[156:159], v[60:63]\n"
"v_mul_f32 v226, v214, v226\n"
"s_waitcnt vmcnt(13)\n"
"v_mfma_f32_16x16x32_bf16 v[64:67], a[176:179], a[96:99], v[64:67]\n"
"global_load_dwordx4 a[172:175], v246, s[16:17] offset:-1024 sc1\n"
"v_mul_f32 v227, v215, v227\n"
"v_mfma_f32_16x16x32_bf16 v[64:67], a[180:183], a[100:103], v[64:67]\n"
"v_cvt_pk_bf16_f32 v224, v224, v225\n"
"v_cvt_pk_bf16_f32 v225, v226, v227\n"
"v_mfma_f32_16x16x32_bf16 v[68:71], a[176:179], a[104:107], v[68:71]\n"
"s_mul_i32 s48, s49, -49152\n"
"s_add_u32 s48, s48, 147456\n"
"v_mfma_f32_16x16x32_bf16 v[68:71], a[180:183], a[108:111], v[68:71]\n"
"v_add_u32 v242, s48, v240\n"
"ds_write_b64 v245, v[224:225] offset:26624\n"
"v_mfma_f32_16x16x32_bf16 v[72:75], a[176:179], a[112:115], v[72:75]\n"
"v_add_u32 v243, s48, v241\n"
"v_accvgpr_read_b32 v249, a20\n"
"v_mfma_f32_16x16x32_bf16 v[72:75], a[180:183], a[116:119], v[72:75]\n"
"v_mul_f32_e64 v228, v249, v228\n"
"v_mfma_f32_16x16x32_bf16 v[224:227], v[252:255], a[248:251], 0\n"
"v_mfma_f32_16x16x32_bf16 v[76:79], a[176:179], a[120:123], v[76:79]\n"
"v_accvgpr_read_b32 v249, a21\n"
"v_mul_f32_e64 v229, v249, v229\n"
"v_mfma_f32_16x16x32_bf16 v[76:79], a[180:183], a[124:127], v[76:79]\n"
"v_accvgpr_read_b32 v249, a22\n"
"v_mul_f32_e64 v230, v249, v230\n"
"v_mfma_f32_16x16x32_bf16 v[80:83], a[176:179], a[128:131], v[80:83]\n"
"v_accvgpr_read_b32 v249, a23\n"
"v_mul_f32_e64 v231, v249, v231\n"
"v_mfma_f32_16x16x32_bf16 v[80:83], a[180:183], a[132:135], v[80:83]\n"
"v_cvt_pk_bf16_f32 v228, v228, v229\n"
"v_cvt_pk_bf16_f32 v229, v230, v231\n"
"v_mfma_f32_16x16x32_bf16 v[84:87], a[176:179], a[136:139], v[84:87]\n"
"ds_write_b64 v245, v[228:229] offset:26632\n"
"v_mul_f32 v224, v152, v224\n"
"v_mfma_f32_16x16x32_bf16 v[84:87], a[180:183], a[140:143], v[84:87]\n"
"v_mul_f32 v225, v153, v225\n"
"v_mfma_f32_16x16x32_bf16 v[228:231], v[250:253], a[248:251], 0\n"
"v_mfma_f32_16x16x32_bf16 v[88:91], a[176:179], a[144:147], v[88:91]\n"
"v_mul_f32 v226, v154, v226\n"
"v_mul_f32 v227, v155, v227\n"
"v_mfma_f32_16x16x32_bf16 v[88:91], a[180:183], a[148:151], v[88:91]\n"
"v_cvt_pk_bf16_f32 v224, v224, v225\n"
"v_cvt_pk_bf16_f32 v225, v226, v227\n"
"v_mfma_f32_16x16x32_bf16 v[92:95], a[176:179], a[152:155], v[92:95]\n"
"global_load_dwordx4 a[176:179], v246, s[16:17] offset:0 sc1\n"
"ds_write_b64 v244, v[224:225] offset:28672\n"
"v_mfma_f32_16x16x32_bf16 v[92:95], a[180:183], a[156:159], v[92:95]\n"
"v_mul_f32 v228, v184, v228\n"
"s_waitcnt vmcnt(13)\n"
"v_mfma_f32_16x16x32_bf16 v[96:99], a[184:187], a[96:99], v[96:99]\n"
"global_load_dwordx4 a[180:183], v246, s[16:17] offset:1024 sc1\n"
"ds_read_b128 a[96:99], v242 offset:0\n"
"v_mfma_f32_16x16x32_bf16 v[96:99], a[188:191], a[100:103], v[96:99]\n"
"ds_read_b128 a[100:103], v243 offset:0\n"
"v_mul_f32 v229, v185, v229\n"
"v_mfma_f32_16x16x32_bf16 v[100:103], a[184:187], a[104:107], v[100:103]\n"
"s_nop 0\n"
".Lpg7_%=:\n"
"s_cbranch_execz .Lpg11_%=\n"
"ds_read_b128 a[104:107], v242 offset:2048\n"
"s_waitcnt lgkmcnt(6)\n"
"v_mfma_f32_16x16x32_bf16 v[224:227], v[252:255], v[232:235], 0\n"
"v_mfma_f32_16x16x32_bf16 v[100:103], a[188:191], a[108:111], v[100:103]\n"
"ds_read_b128 a[108:111], v243 offset:2048\n"
"v_mul_f32 v230, v186, v230\n"
"v_mfma_f32_16x16x32_bf16 v[104:107], a[184:187], a[112:115], v[104:107]\n"
"ds_read_b128 a[112:115], v242 offset:4096\n"
"v_mul_f32 v231, v187, v231\n"
"v_mfma_f32_16x16x32_bf16 v[104:107], a[188:191], a[116:119], v[104:107]\n"
"ds_read_b128 a[116:119], v243 offset:4096\n"
"v_cvt_pk_bf16_f32 v228, v228, v229\n"
"v_mfma_f32_16x16x32_bf16 v[108:111], a[184:187], a[120:123], v[108:111]\n"
"ds_read_b128 a[120:123], v242 offset:6144\n"
"v_cvt_pk_bf16_f32 v229, v230, v231\n"
"v_mfma_f32_16x16x32_bf16 v[108:111], a[188:191], a[124:127], v[108:111]\n"
"ds_read_b128 a[124:127], v243 offset:6144\n"
"ds_write_b64 v244, v[228:229] offset:28680\n"
"v_mfma_f32_16x16x32_bf16 v[112:115], a[184:187], a[128:131], v[112:115]\n"
"ds_read_b128 a[128:131], v242 offset:8192\n"
"v_mul_f32 v224, v216, v224\n"
"v_mfma_f32_16x16x32_bf16 v[112:115], a[188:191], a[132:135], v[112:115]\n"
"ds_read_b128 a[132:135], v243 offset:8192\n"
"v_mul_f32 v225, v217, v225\n"
"v_mfma_f32_16x16x32_bf16 v[116:119], a[184:187], a[136:139], v[116:119]\n"
"ds_read_b128 a[136:139], v242 offset:10240\n"
"v_mfma_f32_16x16x32_bf16 v[228:231], v[250:253], v[232:235], 0\n"
"v_mfma_f32_16x16x32_bf16 v[116:119], a[188:191], a[140:143], v[116:119]\n"
"ds_read_b128 a[140:143], v243 offset:10240\n"
"ds_read_b128 v[232:235], v245 offset:30720\n"
"v_mfma_f32_16x16x32_bf16 v[120:123], a[184:187], a[144:147], v[120:123]\n"
"ds_read_b128 a[144:147], v242 offset:12288\n"
"v_mul_f32 v226, v218, v226\n"
"v_mfma_f32_16x16x32_bf16 v[120:123], a[188:191], a[148:151], v[120:123]\n"
"ds_read_b128 a[148:151], v243 offset:12288\n"
"v_mul_f32 v227, v219, v227\n"
"v_mfma_f32_16x16x32_bf16 v[124:127], a[184:187], a[152:155], v[124:127]\n"
"global_load_dwordx4 a[184:187], v246, s[16:17] offset:2048 sc1\n"
"ds_read_b128 a[152:155], v242 offset:14336\n"
"v_mfma_f32_16x16x32_bf16 v[124:127], a[188:191], a[156:159], v[124:127]\n"
"ds_read_b128 a[156:159], v243 offset:14336\n"
"s_waitcnt vmcnt(13) lgkmcnt(15)\n"
"v_mfma_f32_16x16x32_bf16 v[0:3], a[192:195], a[96:99], v[0:3]\n"
"global_load_dwordx4 a[188:191], v246, s[16:17] offset:3072 sc1\n"
"v_cvt_pk_bf16_f32 v224, v224, v225\n"
"v_mfma_f32_16x16x32_bf16 v[0:3], a[196:199], a[100:103], v[0:3]\n"
"v_cvt_pk_bf16_f32 v225, v226, v227\n"
"s_waitcnt lgkmcnt(14)\n"
"v_mfma_f32_16x16x32_bf16 v[4:7], a[192:195], a[104:107], v[4:7]\n"
"v_add_u32 v246, 0x2000, v246\n"
"ds_write_b64 v245, v[224:225] offset:28672\n"
"v_mfma_f32_16x16x32_bf16 v[4:7], a[196:199], a[108:111], v[4:7]\n"
"v_accvgpr_read_b32 v249, a24\n"
"s_waitcnt lgkmcnt(13)\n"
"v_mfma_f32_16x16x32_bf16 v[8:11], a[192:195], a[112:115], v[8:11]\n"
"v_mul_f32_e64 v228, v249, v228\n"
"v_mfma_f32_16x16x32_bf16 v[224:227], v[252:255], a[252:255], 0\n"
"v_mfma_f32_16x16x32_bf16 v[8:11], a[196:199], a[116:119], v[8:11]\n"
"v_accvgpr_read_b32 v249, a25\n"
"s_waitcnt lgkmcnt(11)\n"
"v_mfma_f32_16x16x32_bf16 v[12:15], a[192:195], a[120:123], v[12:15]\n"
"v_mul_f32_e64 v229, v249, v229\n"
"v_accvgpr_read_b32 v249, a26\n"
"v_mfma_f32_16x16x32_bf16 v[12:15], a[196:199], a[124:127], v[12:15]\n"
"v_mul_f32_e64 v230, v249, v230\n"
"s_waitcnt lgkmcnt(8)\n"
"v_mfma_f32_16x16x32_bf16 v[16:19], a[192:195], a[128:131], v[16:19]\n"
"v_accvgpr_read_b32 v249, a27\n"
"v_mul_f32_e64 v231, v249, v231\n"
"v_mfma_f32_16x16x32_bf16 v[16:19], a[196:199], a[132:135], v[16:19]\n"
"v_cvt_pk_bf16_f32 v228, v228, v229\n"
"s_waitcnt lgkmcnt(6)\n"
"v_mfma_f32_16x16x32_bf16 v[20:23], a[192:195], a[136:139], v[20:23]\n"
"v_cvt_pk_bf16_f32 v229, v230, v231\n"
"ds_write_b64 v245, v[228:229] offset:28680\n"
"v_mfma_f32_16x16x32_bf16 v[20:23], a[196:199], a[140:143], v[20:23]\n"
"v_mul_f32 v224, v156, v224\n"
"s_waitcnt lgkmcnt(4)\n"
"v_mfma_f32_16x16x32_bf16 v[24:27], a[192:195], a[144:147], v[24:27]\n"
"v_mul_f32 v225, v157, v225\n"
"v_mfma_f32_16x16x32_bf16 v[228:231], v[250:253], a[252:255], 0\n"
"v_mfma_f32_16x16x32_bf16 v[24:27], a[196:199], a[148:151], v[24:27]\n"
"v_mul_f32 v226, v158, v226\n"
"s_waitcnt lgkmcnt(2)\n"
"v_mfma_f32_16x16x32_bf16 v[28:31], a[192:195], a[152:155], v[28:31]\n"
"global_load_dwordx4 a[192:195], v246, s[16:17] offset:-4096 sc1\n"
"v_mul_f32 v227, v159, v227\n"
"v_mfma_f32_16x16x32_bf16 v[28:31], a[196:199], a[156:159], v[28:31]\n"
"v_cvt_pk_bf16_f32 v224, v224, v225\n"
"s_waitcnt vmcnt(13)\n"
"v_mfma_f32_16x16x32_bf16 v[32:35], a[200:203], a[96:99], v[32:35]\n"
"global_load_dwordx4 a[196:199], v246, s[16:17] offset:-3072 sc1\n"
"v_cvt_pk_bf16_f32 v225, v226, v227\n"
"v_mfma_f32_16x16x32_bf16 v[32:35], a[204:207], a[100:103], v[32:35]\n"
"ds_write_b64 v244, v[224:225] offset:30720\n"
"v_mul_f32 v228, v188, v228\n"
"v_mfma_f32_16x16x32_bf16 v[36:39], a[200:203], a[104:107], v[36:39]\n"
"v_mul_f32 v229, v189, v229\n"
"v_mfma_f32_16x16x32_bf16 v[224:227], v[252:255], v[232:235], 0\n"
"v_mfma_f32_16x16x32_bf16 v[36:39], a[204:207], a[108:111], v[36:39]\n"
"v_mul_f32 v230, v190, v230\n"
"v_mul_f32 v231, v191, v231\n"
"v_mfma_f32_16x16x32_bf16 v[40:43], a[200:203], a[112:115], v[40:43]\n"
"v_cvt_pk_bf16_f32 v228, v228, v229\n"
"v_cvt_pk_bf16_f32 v229, v230, v231\n"
"v_mfma_f32_16x16x32_bf16 v[40:43], a[204:207], a[116:119], v[40:43]\n"
"ds_write_b64 v244, v[228:229] offset:30728\n"
"v_mul_f32 v224, v220, v224\n"
"v_mfma_f32_16x16x32_bf16 v[44:47], a[200:203], a[120:123], v[44:47]\n"
"v_mul_f32 v225, v221, v225\n"
"v_mfma_f32_16x16x32_bf16 v[228:231], v[250:253], v[232:235], 0\n"
"v_mfma_f32_16x16x32_bf16 v[44:47], a[204:207], a[124:127], v[44:47]\n"
"v_mul_f32 v226, v222, v226\n"
"v_mul_f32 v227, v223, v227\n"
"v_mfma_f32_16x16x32_bf16 v[48:51], a[200:203], a[128:131], v[48:51]\n"
"v_cvt_pk_bf16_f32 v224, v224, v225\n"
"v_cvt_pk_bf16_f32 v225, v226, v227\n"
"v_mfma_f32_16x16x32_bf16 v[48:51], a[204:207], a[132:135], v[48:51]\n"
"ds_write_b64 v245, v[224:225] offset:30720\n"
"v_accvgpr_read_b32 v249, a28\n"
"v_mfma_f32_16x16x32_bf16 v[52:55], a[200:203], a[136:139], v[52:55]\n"
"v_mul_f32_e64 v228, v249, v228\n"
"v_accvgpr_read_b32 v249, a29\n"
"v_mfma_f32_16x16x32_bf16 v[52:55], a[204:207], a[140:143], v[52:55]\n"
"v_mul_f32_e64 v229, v249, v229\n"
"v_accvgpr_read_b32 v249, a30\n"
"v_mfma_f32_16x16x32_bf16 v[56:59], a[200:203], a[144:147], v[56:59]\n"
"v_mul_f32_e64 v230, v249, v230\n"
"v_accvgpr_read_b32 v249, a31\n"
"v_mfma_f32_16x16x32_bf16 v[56:59], a[204:207], a[148:151], v[56:59]\n"
"v_mul_f32_e64 v231, v249, v231\n"
"v_cvt_pk_bf16_f32 v228, v228, v229\n"
"v_mfma_f32_16x16x32_bf16 v[60:63], a[200:203], a[152:155], v[60:63]\n"
"global_load_dwordx4 a[200:203], v246, s[16:17] offset:-2048 sc1\n"
"v_cvt_pk_bf16_f32 v229, v230, v231\n"
"v_mfma_f32_16x16x32_bf16 v[60:63], a[204:207], a[156:159], v[60:63]\n"
"ds_write_b64 v245, v[228:229] offset:30728\n"
"s_waitcnt vmcnt(13)\n"
"v_mfma_f32_16x16x32_bf16 v[64:67], a[208:211], a[96:99], v[64:67]\n"
"global_load_dwordx4 a[204:207], v246, s[16:17] offset:-1024 sc1\n"
"v_mfma_f32_16x16x32_bf16 v[64:67], a[212:215], a[100:103], v[64:67]\n"
"s_add_u32 s64, s60, 32768\n"
"s_addc_u32 s65, s61, 0\n"
"v_mfma_f32_16x16x32_bf16 v[68:71], a[208:211], a[104:107], v[68:71]\n"
"v_add_u32 v242, 0x10000, v240\n"
"v_mfma_f32_16x16x32_bf16 v[68:71], a[212:215], a[108:111], v[68:71]\n"
"v_add_u32 v243, 0x10000, v241\n"
"v_mfma_f32_16x16x32_bf16 v[72:75], a[208:211], a[112:115], v[72:75]\n"
"v_mfma_f32_16x16x32_bf16 v[72:75], a[212:215], a[116:119], v[72:75]\n"
"v_mfma_f32_16x16x32_bf16 v[76:79], a[208:211], a[120:123], v[76:79]\n"
"v_mfma_f32_16x16x32_bf16 v[76:79], a[212:215], a[124:127], v[76:79]\n"
"v_mfma_f32_16x16x32_bf16 v[80:83], a[208:211], a[128:131], v[80:83]\n"
"v_mfma_f32_16x16x32_bf16 v[80:83], a[212:215], a[132:135], v[80:83]\n"
"v_mfma_f32_16x16x32_bf16 v[84:87], a[208:211], a[136:139], v[84:87]\n"
"v_mfma_f32_16x16x32_bf16 v[84:87], a[212:215], a[140:143], v[84:87]\n"
"v_mfma_f32_16x16x32_bf16 v[88:91], a[208:211], a[144:147], v[88:91]\n"
"v_mfma_f32_16x16x32_bf16 v[88:91], a[212:215], a[148:151], v[88:91]\n"
"s_waitcnt lgkmcnt(0)\n"
"s_barrier\n"
"v_mfma_f32_16x16x32_bf16 v[92:95], a[208:211], a[152:155], v[92:95]\n"
"global_load_dword a208, v250, s[64:65]\n"
"global_load_dwordx4 a[208:211], v246, s[16:17] offset:0 sc1\n"
"v_mfma_f32_16x16x32_bf16 v[92:95], a[212:215], a[156:159], v[92:95]\n"
"s_waitcnt vmcnt(14)\n"
"v_mfma_f32_16x16x32_bf16 v[96:99], a[216:219], a[96:99], v[96:99]\n"
"global_load_dwordx4 a[212:215], v246, s[16:17] offset:1024 sc1\n"
"ds_read_b128 a[96:99], v240 offset:16384\n"
"v_mfma_f32_16x16x32_bf16 v[96:99], a[220:223], a[100:103], v[96:99]\n"
"ds_read_b128 a[100:103], v241 offset:16384\n"
"v_mfma_f32_16x16x32_bf16 v[100:103], a[216:219], a[104:107], v[100:103]\n"
"ds_read_b128 a[104:107], v240 offset:18432\n"
"v_mfma_f32_16x16x32_bf16 v[100:103], a[220:223], a[108:111], v[100:103]\n"
"ds_read_b128 a[108:111], v241 offset:18432\n"
"v_mfma_f32_16x16x32_bf16 v[104:107], a[216:219], a[112:115], v[104:107]\n"
"ds_read_b128 a[112:115], v240 offset:20480\n"
"v_mfma_f32_16x16x32_bf16 v[104:107], a[220:223], a[116:119], v[104:107]\n"
"ds_read_b128 a[116:119], v241 offset:20480\n"
"v_mfma_f32_16x16x32_bf16 v[108:111], a[216:219], a[120:123], v[108:111]\n"
"ds_read_b128 a[120:123], v240 offset:22528\n"
"v_mfma_f32_16x16x32_bf16 v[108:111], a[220:223], a[124:127], v[108:111]\n"
"ds_read_b128 a[124:127], v241 offset:22528\n"
"v_mfma_f32_16x16x32_bf16 v[112:115], a[216:219], a[128:131], v[112:115]\n"
"ds_read_b128 a[128:131], v240 offset:24576\n"
"v_mfma_f32_16x16x32_bf16 v[112:115], a[220:223], a[132:135], v[112:115]\n"
"ds_read_b128 a[132:135], v241 offset:24576\n"
"v_mfma_f32_16x16x32_bf16 v[116:119], a[216:219], a[136:139], v[116:119]\n"
"ds_read_b128 a[136:139], v240 offset:26624\n"
"v_mfma_f32_16x16x32_bf16 v[116:119], a[220:223], a[140:143], v[116:119]\n"
"ds_read_b128 a[140:143], v241 offset:26624\n"
"v_mfma_f32_16x16x32_bf16 v[120:123], a[216:219], a[144:147], v[120:123]\n"
"ds_read_b128 a[144:147], v240 offset:28672\n"
"v_mfma_f32_16x16x32_bf16 v[120:123], a[220:223], a[148:151], v[120:123]\n"
"ds_read_b128 a[148:151], v241 offset:28672\n"
"v_mfma_f32_16x16x32_bf16 v[124:127], a[216:219], a[152:155], v[124:127]\n"
"global_load_dwordx4 a[216:219], v246, s[16:17] offset:2048 sc1\n"
"ds_read_b128 a[152:155], v240 offset:30720\n"
"v_mfma_f32_16x16x32_bf16 v[124:127], a[220:223], a[156:159], v[124:127]\n"
"ds_read_b128 a[156:159], v241 offset:30720\n"
"s_waitcnt vmcnt(14) lgkmcnt(14)\n"
"v_mfma_f32_16x16x32_bf16 v[128:131], a[160:163], a[96:99], v[128:131]\n"
"global_load_dwordx4 a[220:223], v246, s[16:17] offset:3072 sc1\n"
"v_mfma_f32_16x16x32_bf16 v[128:131], a[164:167], a[100:103], v[128:131]\n"
"s_waitcnt lgkmcnt(12)\n"
"v_mfma_f32_16x16x32_bf16 v[132:135], a[160:163], a[104:107], v[132:135]\n"
"v_add_u32 v246, 0x2000, v246\n"
"v_mfma_f32_16x16x32_bf16 v[232:235], v[252:255], a[32:35], 0\n"
"v_mfma_f32_16x16x32_bf16 v[132:135], a[164:167], a[108:111], v[132:135]\n"
"s_waitcnt lgkmcnt(10)\n"
"v_mfma_f32_16x16x32_bf16 v[136:139], a[160:163], a[112:115], v[136:139]\n"
"v_mfma_f32_16x16x32_bf16 v[136:139], a[164:167], a[116:119], v[136:139]\n"
"s_waitcnt lgkmcnt(8)\n"
"v_mfma_f32_16x16x32_bf16 v[140:143], a[160:163], a[120:123], v[140:143]\n"
"ds_read_b128 v[224:227], v247 offset:16384\n"
"v_mfma_f32_16x16x32_bf16 v[140:143], a[164:167], a[124:127], v[140:143]\n"
"v_mul_f32 v232, v0, v232\n"
"s_waitcnt lgkmcnt(7)\n"
"v_mfma_f32_16x16x32_bf16 v[144:147], a[160:163], a[128:131], v[144:147]\n"
"ds_read_b128 v[228:231], v247 offset:17408\n"
"v_mul_f32 v233, v1, v233\n"
"v_mfma_f32_16x16x32_bf16 v[144:147], a[164:167], a[132:135], v[144:147]\n"
"v_mfma_f32_16x16x32_bf16 v[236:239], v[252:255], a[36:39], 0\n"
"s_waitcnt lgkmcnt(6)\n"
"v_mfma_f32_16x16x32_bf16 v[148:151], a[160:163], a[136:139], v[148:151]\n"
"v_mul_f32 v234, v2, v234\n"
"v_mfma_f32_16x16x32_bf16 v[148:151], a[164:167], a[140:143], v[148:151]\n"
"v_mul_f32 v235, v3, v235\n"
"s_waitcnt lgkmcnt(4)\n"
"v_mfma_f32_16x16x32_bf16 v[152:155], a[160:163], a[144:147], v[152:155]\n"
"v_cvt_pk_bf16_f32 v232, v232, v233\n"
"v_mfma_f32_16x16x32_bf16 v[152:155], a[164:167], a[148:151], v[152:155]\n"
"v_cvt_pk_bf16_f32 v233, v234, v235\n"
"s_waitcnt lgkmcnt(2)\n"
"v_mfma_f32_16x16x32_bf16 v[156:159], a[160:163], a[152:155], v[156:159]\n"
"global_load_dwordx4 a[160:163], v246, s[16:17] offset:-4096 sc1\n"
"ds_write_b64 v244, v[232:233] offset:0\n"
"v_mfma_f32_16x16x32_bf16 v[156:159], a[164:167], a[156:159], v[156:159]\n"
"s_waitcnt vmcnt(14)\n"
"v_mfma_f32_16x16x32_bf16 v[160:163], a[168:171], a[96:99], v[160:163]\n"
"global_load_dwordx4 a[164:167], v246, s[16:17] offset:-3072 sc1\n"
"v_mul_f32 v236, v4, v236\n"
"v_mfma_f32_16x16x32_bf16 v[160:163], a[172:175], a[100:103], v[160:163]\n"
"v_mul_f32 v237, v5, v237\n"
"v_mfma_f32_16x16x32_bf16 v[164:167], a[168:171], a[104:107], v[164:167]\n"
"v_add_u32 v249, 128, v248\n"
"v_mfma_f32_16x16x32_bf16 v[232:235], v[252:255], a[40:43], 0\n"
"v_mfma_f32_16x16x32_bf16 v[164:167], a[172:175], a[108:111], v[164:167]\n"
"s_waitcnt lgkmcnt(2)\n"
"global_store_dwordx4 v249, v[224:227], s[24:25] sc1\n"
"v_mfma_f32_16x16x32_bf16 v[168:171], a[168:171], a[112:115], v[168:171]\n"
"v_mul_f32 v238, v6, v238\n"
"v_mul_f32 v239, v7, v239\n"
"v_mfma_f32_16x16x32_bf16 v[168:171], a[172:175], a[116:119], v[168:171]\n"
"v_cvt_pk_bf16_f32 v236, v236, v237\n"
"v_mfma_f32_16x16x32_bf16 v[172:175], a[168:171], a[120:123], v[172:175]\n"
"ds_read_b128 v[224:227], v247 offset:18432\n"
"v_cvt_pk_bf16_f32 v237, v238, v239\n"
"v_mfma_f32_16x16x32_bf16 v[172:175], a[172:175], a[124:127], v[172:175]\n"
"ds_write_b64 v244, v[236:237] offset:2048\n"
"v_mfma_f32_16x16x32_bf16 v[176:179], a[168:171], a[128:131], v[176:179]\n"
"v_mfma_f32_16x16x32_bf16 v[176:179], a[172:175], a[132:135], v[176:179]\n"
"v_mul_f32 v232, v8, v232\n"
"v_mfma_f32_16x16x32_bf16 v[180:183], a[168:171], a[136:139], v[180:183]\n"
"v_mul_f32 v233, v9, v233\n"
"v_mfma_f32_16x16x32_bf16 v[180:183], a[172:175], a[140:143], v[180:183]\n"
"v_mfma_f32_16x16x32_bf16 v[236:239], v[252:255], a[44:47], 0\n"
"v_mfma_f32_16x16x32_bf16 v[184:187], a[168:171], a[144:147], v[184:187]\n"
"v_mul_f32 v234, v10, v234\n"
"v_mfma_f32_16x16x32_bf16 v[184:187], a[172:175], a[148:151], v[184:187]\n"
"v_mul_f32 v235, v11, v235\n"
"v_mfma_f32_16x16x32_bf16 v[188:191], a[168:171], a[152:155], v[188:191]\n"
"global_load_dwordx4 a[168:171], v246, s[16:17] offset:-2048 sc1\n"
"v_cvt_pk_bf16_f32 v232, v232, v233\n"
"v_mfma_f32_16x16x32_bf16 v[188:191], a[172:175], a[156:159], v[188:191]\n"
"v_cvt_pk_bf16_f32 v233, v234, v235\n"
"s_waitcnt vmcnt(15)\n"
"v_mfma_f32_16x16x32_bf16 v[192:195], a[176:179], a[96:99], v[192:195]\n"
"global_load_dwordx4 a[172:175], v246, s[16:17] offset:-1024 sc1\n"
"ds_write_b64 v244, v[232:233] offset:4096\n"
"v_mfma_f32_16x16x32_bf16 v[192:195], a[180:183], a[100:103], v[192:195]\n"
"v_mfma_f32_16x16x32_bf16 v[196:199], a[176:179], a[104:107], v[196:199]\n"
"v_mul_f32 v236, v12, v236\n"
"v_mfma_f32_16x16x32_bf16 v[196:199], a[180:183], a[108:111], v[196:199]\n"
"v_mul_f32 v237, v13, v237\n"
"v_mfma_f32_16x16x32_bf16 v[200:203], a[176:179], a[112:115], v[200:203]\n"
"v_mfma_f32_16x16x32_bf16 v[232:235], v[252:255], a[48:51], 0\n"
"v_mfma_f32_16x16x32_bf16 v[200:203], a[180:183], a[116:119], v[200:203]\n"
"v_mul_f32 v238, v14, v238\n"
"v_mfma_f32_16x16x32_bf16 v[204:207], a[176:179], a[120:123], v[204:207]\n"
"v_mul_f32 v239, v15, v239\n"
"v_mfma_f32_16x16x32_bf16 v[204:207], a[180:183], a[124:127], v[204:207]\n"
"v_cvt_pk_bf16_f32 v236, v236, v237\n"
"v_mfma_f32_16x16x32_bf16 v[208:211], a[176:179], a[128:131], v[208:211]\n"
"v_cvt_pk_bf16_f32 v237, v238, v239\n"
"v_mfma_f32_16x16x32_bf16 v[208:211], a[180:183], a[132:135], v[208:211]\n"
"ds_write_b64 v244, v[236:237] offset:6144\n"
"v_mfma_f32_16x16x32_bf16 v[212:215], a[176:179], a[136:139], v[212:215]\n"
"v_mfma_f32_16x16x32_bf16 v[212:215], a[180:183], a[140:143], v[212:215]\n"
"v_mul_f32 v232, v16, v232\n"
"v_mfma_f32_16x16x32_bf16 v[216:219], a[176:179], a[144:147], v[216:219]\n"
"v_mul_f32 v233, v17, v233\n"
"v_mfma_f32_16x16x32_bf16 v[216:219], a[180:183], a[148:151], v[216:219]\n"
"v_mfma_f32_16x16x32_bf16 v[236:239], v[252:255], a[52:55], 0\n"
"v_mfma_f32_16x16x32_bf16 v[220:223], a[176:179], a[152:155], v[220:223]\n"
"global_load_dwordx4 a[176:179], v246, s[16:17] offset:0 sc1\n"
"v_mul_f32 v234, v18, v234\n"
"v_mfma_f32_16x16x32_bf16 v[220:223], a[180:183], a[156:159], v[220:223]\n"
"v_mul_f32 v235, v19, v235\n"
"s_waitcnt vmcnt(15)\n"
"v_mfma_f32_16x16x32_bf16 a[0:3], a[184:187], a[96:99], a[0:3]\n"
"global_load_dwordx4 a[180:183], v246, s[16:17] offset:1024 sc1\n"
"ds_read_b128 a[96:99], v240 offset:49152\n"
"v_mfma_f32_16x16x32_bf16 a[0:3], a[188:191], a[100:103], a[0:3]\n"
"ds_read_b128 a[100:103], v241 offset:49152\n"
"v_cvt_pk_bf16_f32 v232, v232, v233\n"
"v_mfma_f32_16x16x32_bf16 a[4:7], a[184:187], a[104:107], a[4:7]\n"
"ds_read_b128 a[104:107], v240 offset:51200\n"
"v_cvt_pk_bf16_f32 v233, v234, v235\n"
"v_mfma_f32_16x16x32_bf16 a[4:7], a[188:191], a[108:111], a[4:7]\n"
"ds_read_b128 a[108:111], v241 offset:51200\n"
"ds_write_b64 v244, v[232:233] offset:8192\n"
"v_mfma_f32_16x16x32_bf16 a[8:11], a[184:187], a[112:115], a[8:11]\n"
"ds_read_b128 a[112:115], v240 offset:53248\n"
"v_mul_f32 v236, v20, v236\n"
"v_mfma_f32_16x16x32_bf16 a[8:11], a[188:191], a[116:119], a[8:11]\n"
"ds_read_b128 a[116:119], v241 offset:53248\n"
"v_mul_f32 v237, v21, v237\n"
"v_mfma_f32_16x16x32_bf16 a[12:15], a[184:187], a[120:123], a[12:15]\n"
"ds_read_b128 a[120:123], v240 offset:55296\n"
"v_mfma_f32_16x16x32_bf16 v[232:235], v[252:255], a[56:59], 0\n"
"v_mfma_f32_16x16x32_bf16 a[12:15], a[188:191], a[124:127], a[12:15]\n"
"ds_read_b128 a[124:127], v241 offset:55296\n"
"v_mul_f32 v238, v22, v238\n"
"v_mfma_f32_16x16x32_bf16 a[16:19], a[184:187], a[128:131], a[16:19]\n"
"ds_read_b128 a[128:131], v240 offset:57344\n"
"v_mul_f32 v239, v23, v239\n"
"v_mfma_f32_16x16x32_bf16 a[16:19], a[188:191], a[132:135], a[16:19]\n"
"ds_read_b128 a[132:135], v241 offset:57344\n"
"v_cvt_pk_bf16_f32 v236, v236, v237\n"
"v_mfma_f32_16x16x32_bf16 a[20:23], a[184:187], a[136:139], a[20:23]\n"
"ds_read_b128 a[136:139], v240 offset:59392\n"
"v_cvt_pk_bf16_f32 v237, v238, v239\n"
"v_mfma_f32_16x16x32_bf16 a[20:23], a[188:191], a[140:143], a[20:23]\n"
"ds_read_b128 a[140:143], v241 offset:59392\n"
"ds_write_b64 v244, v[236:237] offset:10240\n"
"v_mfma_f32_16x16x32_bf16 a[24:27], a[184:187], a[144:147], a[24:27]\n"
"ds_read_b128 a[144:147], v240 offset:61440\n"
"v_mfma_f32_16x16x32_bf16 a[24:27], a[188:191], a[148:151], a[24:27]\n"
"ds_read_b128 a[148:151], v241 offset:61440\n"
"v_mul_f32 v232, v24, v232\n"
"v_mfma_f32_16x16x32_bf16 a[28:31], a[184:187], a[152:155], a[28:31]\n"
"global_load_dwordx4 a[184:187], v246, s[16:17] offset:2048 sc1\n"
"ds_read_b128 a[152:155], v240 offset:63488\n"
"v_mfma_f32_16x16x32_bf16 a[28:31], a[188:191], a[156:159], a[28:31]\n"
"ds_read_b128 a[156:159], v241 offset:63488\n"
"s_waitcnt vmcnt(15) lgkmcnt(15)\n"
"v_mfma_f32_16x16x32_bf16 v[128:131], a[192:195], a[96:99], v[128:131]\n"
"global_load_dwordx4 a[188:191], v246, s[16:17] offset:3072 sc1\n"
"v_mul_f32 v233, v25, v233\n"
"v_mfma_f32_16x16x32_bf16 v[128:131], a[196:199], a[100:103], v[128:131]\n"
"v_mfma_f32_16x16x32_bf16 v[236:239], v[252:255], a[60:63], 0\n"
"s_waitcnt lgkmcnt(14)\n"
"v_mfma_f32_16x16x32_bf16 v[132:135], a[192:195], a[104:107], v[132:135]\n"
"v_add_u32 v246, 0x2000, v246\n"
"v_mul_f32 v234, v26, v234\n"
"v_mfma_f32_16x16x32_bf16 v[132:135], a[196:199], a[108:111], v[132:135]\n"
"v_mul_f32 v235, v27, v235\n"
"s_waitcnt lgkmcnt(11)\n"
"v_mfma_f32_16x16x32_bf16 v[136:139], a[192:195], a[112:115], v[136:139]\n"
"v_add_u32 v249, 16512, v248\n"
"v_cvt_pk_bf16_f32 v232, v232, v233\n"
"v_mfma_f32_16x16x32_bf16 v[136:139], a[196:199], a[116:119], v[136:139]\n"
"v_cvt_pk_bf16_f32 v233, v234, v235\n"
"s_waitcnt lgkmcnt(9)\n"
"v_mfma_f32_16x16x32_bf16 v[140:143], a[192:195], a[120:123], v[140:143]\n"
"global_store_dwordx4 v249, v[228:231], s[24:25] sc1\n"
"v_mfma_f32_16x16x32_bf16 v[140:143], a[196:199], a[124:127], v[140:143]\n"
"ds_write_b64 v244, v[232:233] offset:12288\n"
"s_waitcnt lgkmcnt(8)\n"
"v_mfma_f32_16x16x32_bf16 v[144:147], a[192:195], a[128:131], v[144:147]\n"
"v_mul_f32 v236, v28, v236\n"
"v_mul_f32 v237, v29, v237\n"
"v_mfma_f32_16x16x32_bf16 v[144:147], a[196:199], a[132:135], v[144:147]\n"
"v_mfma_f32_16x16x32_bf16 v[232:235], v[250:253], a[32:35], 0\n"
"s_waitcnt lgkmcnt(6)\n"
"v_mfma_f32_16x16x32_bf16 v[148:151], a[192:195], a[136:139], v[148:151]\n"
"ds_read_b128 v[228:231], v247 offset:19456\n"
"v_mul_f32 v238, v30, v238\n"
"v_mfma_f32_16x16x32_bf16 v[148:151], a[196:199], a[140:143], v[148:151]\n"
"v_mul_f32 v239, v31, v239\n"
"s_waitcnt lgkmcnt(4)\n"
"v_mfma_f32_16x16x32_bf16 v[152:155], a[192:195], a[144:147], v[152:155]\n"
"v_cvt_pk_bf16_f32 v236, v236, v237\n"
"v_cvt_pk_bf16_f32 v237, v238, v239\n"
"v_mfma_f32_16x16x32_bf16 v[152:155], a[196:199], a[148:151], v[152:155]\n"
"ds_write_b64 v244, v[236:237] offset:14336\n"
"s_waitcnt lgkmcnt(3)\n"
"v_mfma_f32_16x16x32_bf16 v[156:159], a[192:195], a[152:155], v[156:159]\n"
"global_load_dwordx4 a[192:195], v246, s[16:17] offset:-4096 sc1\n"
"v_mfma_f32_16x16x32_bf16 v[156:159], a[196:199], a[156:159], v[156:159]\n"
"v_mul_f32 v232, v32, v232\n"
"s_waitcnt vmcnt(16)\n"
"v_mfma_f32_16x16x32_bf16 v[160:163], a[200:203], a[96:99], v[160:163]\n"
"global_load_dwordx4 a[196:199], v246, s[16:17] offset:-3072 sc1\n"
"v_mul_f32 v233, v33, v233\n"
"v_mfma_f32_16x16x32_bf16 v[160:163], a[204:207], a[100:103], v[160:163]\n"
"v_mfma_f32_16x16x32_bf16 v[236:239], v[250:253], a[36:39], 0\n"
"v_mfma_f32_16x16x32_bf16 v[164:167], a[200:203], a[104:107], v[164:167]\n"
"v_mul_f32 v234, v34, v234\n"
"v_mfma_f32_16x16x32_bf16 v[164:167], a[204:207], a[108:111], v[164:167]\n"
"v_mul_f32 v235, v35, v235\n"
"v_mfma_f32_16x16x32_bf16 v[168:171], a[200:203], a[112:115], v[168:171]\n"
"v_add_u32 v249, 32896, v248\n"
"v_cvt_pk_bf16_f32 v232, v232, v233\n"
"v_mfma_f32_16x16x32_bf16 v[168:171], a[204:207], a[116:119], v[168:171]\n"
"global_store_dwordx4 v249, v[224:227], s[24:25] sc1\n"
"v_mfma_f32_16x16x32_bf16 v[172:175], a[200:203], a[120:123], v[172:175]\n"
"v_cvt_pk_bf16_f32 v233, v234, v235\n"
"ds_write_b64 v244, v[232:233] offset:8\n"
"v_mfma_f32_16x16x32_bf16 v[172:175], a[204:207], a[124:127], v[172:175]\n"
"v_mfma_f32_16x16x32_bf16 v[176:179], a[200:203], a[128:131], v[176:179]\n"
"v_mul_f32 v236, v36, v236\n"
"v_mfma_f32_16x16x32_bf16 v[176:179], a[204:207], a[132:135], v[176:179]\n"
"v_mul_f32 v237, v37, v237\n"
"v_mfma_f32_16x16x32_bf16 v[180:183], a[200:203], a[136:139], v[180:183]\n"
"v_mfma_f32_16x16x32_bf16 v[232:235], v[250:253], a[40:43], 0\n"
"v_mfma_f32_16x16x32_bf16 v[180:183], a[204:207], a[140:143], v[180:183]\n"
"v_mul_f32 v238, v38, v238\n"
"v_mfma_f32_16x16x32_bf16 v[184:187], a[200:203], a[144:147], v[184:187]\n"
"v_mul_f32 v239, v39, v239\n"
"v_mfma_f32_16x16x32_bf16 v[184:187], a[204:207], a[148:151], v[184:187]\n"
"v_cvt_pk_bf16_f32 v236, v236, v237\n"
"v_mfma_f32_16x16x32_bf16 v[188:191], a[200:203], a[152:155], v[188:191]\n"
"global_load_dwordx4 a[200:203], v246, s[16:17] offset:-2048 sc1\n"
"v_cvt_pk_bf16_f32 v237, v238, v239\n"
"v_mfma_f32_16x16x32_bf16 v[188:191], a[204:207], a[156:159], v[188:191]\n"
"ds_write_b64 v244, v[236:237] offset:2056\n"
"s_waitcnt vmcnt(16)\n"
"v_mfma_f32_16x16x32_bf16 v[192:195], a[208:211], a[96:99], v[192:195]\n"
"global_load_dwordx4 a[204:207], v246, s[16:17] offset:-1024 sc1\n"
"v_mfma_f32_16x16x32_bf16 v[192:195], a[212:215], a[100:103], v[192:195]\n"
"v_mul_f32 v232, v40, v232\n"
"v_mfma_f32_16x16x32_bf16 v[196:199], a[208:211], a[104:107], v[196:199]\n"
"v_mul_f32 v233, v41, v233\n"
"v_mfma_f32_16x16x32_bf16 v[196:199], a[212:215], a[108:111], v[196:199]\n"
"v_mfma_f32_16x16x32_bf16 v[236:239], v[250:253], a[44:47], 0\n"
"v_mfma_f32_16x16x32_bf16 v[200:203], a[208:211], a[112:115], v[200:203]\n"
"v_mul_f32 v234, v42, v234\n"
"v_mfma_f32_16x16x32_bf16 v[200:203], a[212:215], a[116:119], v[200:203]\n"
"v_mul_f32 v235, v43, v235\n"
"v_mfma_f32_16x16x32_bf16 v[204:207], a[208:211], a[120:123], v[204:207]\n"
"v_cvt_pk_bf16_f32 v232, v232, v233\n"
"v_mfma_f32_16x16x32_bf16 v[204:207], a[212:215], a[124:127], v[204:207]\n"
"v_cvt_pk_bf16_f32 v233, v234, v235\n"
"v_mfma_f32_16x16x32_bf16 v[208:211], a[208:211], a[128:131], v[208:211]\n"
"ds_write_b64 v244, v[232:233] offset:4104\n"
"v_mfma_f32_16x16x32_bf16 v[208:211], a[212:215], a[132:135], v[208:211]\n"
"v_mfma_f32_16x16x32_bf16 v[212:215], a[208:211], a[136:139], v[212:215]\n"
"v_mul_f32 v236, v44, v236\n"
"v_mfma_f32_16x16x32_bf16 v[212:215], a[212:215], a[140:143], v[212:215]\n"
"v_mul_f32 v237, v45, v237\n"
"v_mfma_f32_16x16x32_bf16 v[216:219], a[208:211], a[144:147], v[216:219]\n"
"v_mfma_f32_16x16x32_bf16 v[232:235], v[250:253], a[48:51], 0\n"
"v_mfma_f32_16x16x32_bf16 v[216:219], a[212:215], a[148:151], v[216:219]\n"
"v_mul_f32 v238, v46, v238\n"
"v_mfma_f32_16x16x32_bf16 v[220:223], a[208:211], a[152:155], v[220:223]\n"
"global_load_dwordx4 a[208:211], v246, s[16:17] offset:0 sc1\n"
"v_mul_f32 v239, v47, v239\n"
"v_mfma_f32_16x16x32_bf16 v[220:223], a[212:215], a[156:159], v[220:223]\n"
"v_cvt_pk_bf16_f32 v236, v236, v237\n"
"s_waitcnt vmcnt(16)\n"
"v_mfma_f32_16x16x32_bf16 a[0:3], a[216:219], a[96:99], a[0:3]\n"
"global_load_dwordx4 a[212:215], v246, s[16:17] offset:1024 sc1\n"
"ds_read_b128 a[96:99], v242 offset:16384\n"
"v_mfma_f32_16x16x32_bf16 a[0:3], a[220:223], a[100:103], a[0:3]\n"
"ds_read_b128 a[100:103], v243 offset:16384\n"
"v_cvt_pk_bf16_f32 v237, v238, v239\n"
"v_mfma_f32_16x16x32_bf16 a[4:7], a[216:219], a[104:107], a[4:7]\n"
"ds_read_b128 a[104:107], v242 offset:18432\n"
"ds_write_b64 v244, v[236:237] offset:6152\n"
"v_mfma_f32_16x16x32_bf16 a[4:7], a[220:223], a[108:111], a[4:7]\n"
"ds_read_b128 a[108:111], v243 offset:18432\n"
"v_mul_f32 v232, v48, v232\n"
"v_mfma_f32_16x16x32_bf16 a[8:11], a[216:219], a[112:115], a[8:11]\n"
"ds_read_b128 a[112:115], v242 offset:20480\n"
"v_mul_f32 v233, v49, v233\n"
"v_mfma_f32_16x16x32_bf16 a[8:11], a[220:223], a[116:119], a[8:11]\n"
"ds_read_b128 a[116:119], v243 offset:20480\n"
"v_mfma_f32_16x16x32_bf16 v[236:239], v[250:253], a[52:55], 0\n"
"v_mfma_f32_16x16x32_bf16 a[12:15], a[216:219], a[120:123], a[12:15]\n"
"ds_read_b128 a[120:123], v242 offset:22528\n"
"v_mul_f32 v234, v50, v234\n"
"v_mfma_f32_16x16x32_bf16 a[12:15], a[220:223], a[124:127], a[12:15]\n"
"ds_read_b128 a[124:127], v243 offset:22528\n"
"v_mul_f32 v235, v51, v235\n"
"v_mfma_f32_16x16x32_bf16 a[16:19], a[216:219], a[128:131], a[16:19]\n"
"ds_read_b128 a[128:131], v242 offset:24576\n"
"v_cvt_pk_bf16_f32 v232, v232, v233\n"
"v_mfma_f32_16x16x32_bf16 a[16:19], a[220:223], a[132:135], a[16:19]\n"
"ds_read_b128 a[132:135], v243 offset:24576\n"
"v_cvt_pk_bf16_f32 v233, v234, v235\n"
"v_mfma_f32_16x16x32_bf16 a[20:23], a[216:219], a[136:139], a[20:23]\n"
"ds_read_b128 a[136:139], v242 offset:26624\n"
"ds_write_b64 v244, v[232:233] offset:8200\n"
"v_mfma_f32_16x16x32_bf16 a[20:23], a[220:223], a[140:143], a[20:23]\n"
"ds_read_b128 a[140:143], v243 offset:26624\n"
"v_mfma_f32_16x16x32_bf16 a[24:27], a[216:219], a[144:147], a[24:27]\n"
"ds_read_b128 a[144:147], v242 offset:28672\n"
"v_mul_f32 v236, v52, v236\n"
"v_mfma_f32_16x16x32_bf16 a[24:27], a[220:223], a[148:151], a[24:27]\n"
"ds_read_b128 a[148:151], v243 offset:28672\n"
"v_mul_f32 v237, v53, v237\n"
"v_mfma_f32_16x16x32_bf16 a[28:31], a[216:219], a[152:155], a[28:31]\n"
"global_load_dwordx4 a[216:219], v246, s[16:17] offset:2048 sc1\n"
"ds_read_b128 a[152:155], v242 offset:30720\n"
"v_mfma_f32_16x16x32_bf16 a[28:31], a[220:223], a[156:159], a[28:31]\n"
"ds_read_b128 a[156:159], v243 offset:30720\n"
"s_waitcnt vmcnt(16) lgkmcnt(15)\n"
"v_mfma_f32_16x16x32_bf16 v[128:131], a[160:163], a[96:99], v[128:131]\n"
"global_load_dwordx4 a[220:223], v246, s[16:17] offset:3072 sc1\n"
"v_mfma_f32_16x16x32_bf16 v[232:235], v[250:253], a[56:59], 0\n"
"v_mfma_f32_16x16x32_bf16 v[128:131], a[164:167], a[100:103], v[128:131]\n"
"v_mul_f32 v238, v54, v238\n"
"s_waitcnt lgkmcnt(13)\n"
"v_mfma_f32_16x16x32_bf16 v[132:135], a[160:163], a[104:107], v[132:135]\n"
"v_add_u32 v246, 0x2000, v246\n"
"v_mul_f32 v239, v55, v239\n"
"v_mfma_f32_16x16x32_bf16 v[132:135], a[164:167], a[108:111], v[132:135]\n"
"v_cvt_pk_bf16_f32 v236, v236, v237\n"
"s_waitcnt lgkmcnt(11)\n"
"v_mfma_f32_16x16x32_bf16 v[136:139], a[160:163], a[112:115], v[136:139]\n"
"v_add_u32 v249, 49280, v248\n"
"v_cvt_pk_bf16_f32 v237, v238, v239\n"
"v_mfma_f32_16x16x32_bf16 v[136:139], a[164:167], a[116:119], v[136:139]\n"
"ds_write_b64 v244, v[236:237] offset:10248\n"
"s_waitcnt lgkmcnt(10)\n"
"v_mfma_f32_16x16x32_bf16 v[140:143], a[160:163], a[120:123], v[140:143]\n"
"global_store_dwordx4 v249, v[228:231], s[24:25] sc1\n"
"v_mfma_f32_16x16x32_bf16 v[140:143], a[164:167], a[124:127], v[140:143]\n"
"v_mul_f32 v232, v56, v232\n"
"s_waitcnt lgkmcnt(8)\n"
"v_mfma_f32_16x16x32_bf16 v[144:147], a[160:163], a[128:131], v[144:147]\n"
"v_mul_f32 v233, v57, v233\n"
"v_mfma_f32_16x16x32_bf16 v[236:239], v[250:253], a[60:63], 0\n"
"v_mfma_f32_16x16x32_bf16 v[144:147], a[164:167], a[132:135], v[144:147]\n"
"v_mul_f32 v234, v58, v234\n"
"s_waitcnt lgkmcnt(5)\n"
"v_mfma_f32_16x16x32_bf16 v[148:151], a[160:163], a[136:139], v[148:151]\n"
"ds_read_b128 v[224:227], v247 offset:49152\n"
"v_mul_f32 v235, v59, v235\n"
"v_mfma_f32_16x16x32_bf16 v[148:151], a[164:167], a[140:143], v[148:151]\n"
"v_cvt_pk_bf16_f32 v232, v232, v233\n"
"s_waitcnt lgkmcnt(4)\n"
"v_mfma_f32_16x16x32_bf16 v[152:155], a[160:163], a[144:147], v[152:155]\n"
"ds_read_b128 v[228:231], v247 offset:50176\n"
"v_cvt_pk_bf16_f32 v233, v234, v235\n"
"v_mfma_f32_16x16x32_bf16 v[152:155], a[164:167], a[148:151], v[152:155]\n"
"ds_write_b64 v244, v[232:233] offset:12296\n"
"s_waitcnt lgkmcnt(4)\n"
"v_mfma_f32_16x16x32_bf16 v[156:159], a[160:163], a[152:155], v[156:159]\n"
"global_load_dwordx4 a[160:163], v246, s[16:17] offset:-4096 sc1\n"
"v_mul_f32 v236, v60, v236\n"
"v_mfma_f32_16x16x32_bf16 v[156:159], a[164:167], a[156:159], v[156:159]\n"
"v_mul_f32 v237, v61, v237\n"
"s_waitcnt vmcnt(16)\n"
"v_mfma_f32_16x16x32_bf16 v[160:163], a[168:171], a[96:99], v[160:163]\n"
"global_load_dwordx4 a[164:167], v246, s[16:17] offset:-3072 sc1\n"
"v_mfma_f32_16x16x32_bf16 v[232:235], v[252:255], a[64:67], 0\n"
"v_mfma_f32_16x16x32_bf16 v[160:163], a[172:175], a[100:103], v[160:163]\n"
"v_mul_f32 v238, v62, v238\n"
"v_mfma_f32_16x16x32_bf16 v[164:167], a[168:171], a[104:107], v[164:167]\n"
"v_mul_f32 v239, v63, v239\n"
"v_mfma_f32_16x16x32_bf16 v[164:167], a[172:175], a[108:111], v[164:167]\n"
"v_cvt_pk_bf16_f32 v236, v236, v237\n"
"v_mfma_f32_16x16x32_bf16 v[168:171], a[168:171], a[112:115], v[168:171]\n"
"v_cvt_pk_bf16_f32 v237, v238, v239\n"
"v_mfma_f32_16x16x32_bf16 v[168:171], a[172:175], a[116:119], v[168:171]\n"
".Lpg8_%=:\n"
"s_cbranch_execz .Lpg12_%=\n"
"ds_write_b64 v244, v[236:237] offset:14344\n"
"v_mfma_f32_16x16x32_bf16 v[172:175], a[168:171], a[120:123], v[172:175]\n"
"v_add_u32 v249, 384, v248\n"
"v_mfma_f32_16x16x32_bf16 v[172:175], a[172:175], a[124:127], v[172:175]\n"
"s_waitcnt lgkmcnt(3)\n"
"global_store_dwordx4 v249, v[224:227], s[24:25] sc1\n"
"v_mfma_f32_16x16x32_bf16 v[176:179], a[168:171], a[128:131], v[176:179]\n"
"v_mul_f32 v232, v64, v232\n"
"v_mul_f32 v233, v65, v233\n"
"v_mfma_f32_16x16x32_bf16 v[176:179], a[172:175], a[132:135], v[176:179]\n"
"v_mfma_f32_16x16x32_bf16 v[236:239], v[252:255], a[68:71], 0\n"
"v_mfma_f32_16x16x32_bf16 v[180:183], a[168:171], a[136:139], v[180:183]\n"
"ds_read_b128 v[224:227], v247 offset:51200\n"
"v_mul_f32 v234, v66, v234\n"
"v_mfma_f32_16x16x32_bf16 v[180:183], a[172:175], a[140:143], v[180:183]\n"
"v_mul_f32 v235, v67, v235\n"
"v_mfma_f32_16x16x32_bf16 v[184:187], a[168:171], a[144:147], v[184:187]\n"
"v_cvt_pk_bf16_f32 v232, v232, v233\n"
"v_mfma_f32_16x16x32_bf16 v[184:187], a[172:175], a[148:151], v[184:187]\n"
"v_cvt_pk_bf16_f32 v233, v234, v235\n"
"v_mfma_f32_16x16x32_bf16 v[188:191], a[168:171], a[152:155], v[188:191]\n"
"global_load_dwordx4 a[168:171], v246, s[16:17] offset:-2048 sc1\n"
"ds_write_b64 v245, v[232:233] offset:0\n"
"v_mfma_f32_16x16x32_bf16 v[188:191], a[172:175], a[156:159], v[188:191]\n"
"s_waitcnt vmcnt(17)\n"
"v_mfma_f32_16x16x32_bf16 v[192:195], a[176:179], a[96:99], v[192:195]\n"
"global_load_dwordx4 a[172:175], v246, s[16:17] offset:-1024 sc1\n"
"v_mul_f32 v236, v68, v236\n"
"v_mfma_f32_16x16x32_bf16 v[192:195], a[180:183], a[100:103], v[192:195]\n"
"v_mul_f32 v237, v69, v237\n"
"v_mfma_f32_16x16x32_bf16 v[196:199], a[176:179], a[104:107], v[196:199]\n"
"v_mfma_f32_16x16x32_bf16 v[232:235], v[252:255], a[72:75], 0\n"
"v_mfma_f32_16x16x32_bf16 v[196:199], a[180:183], a[108:111], v[196:199]\n"
"v_mul_f32 v238, v70, v238\n"
"v_mfma_f32_16x16x32_bf16 v[200:203], a[176:179], a[112:115], v[200:203]\n"
"v_mul_f32 v239, v71, v239\n"
"v_mfma_f32_16x16x32_bf16 v[200:203], a[180:183], a[116:119], v[200:203]\n"
"v_cvt_pk_bf16_f32 v236, v236, v237\n"
"v_mfma_f32_16x16x32_bf16 v[204:207], a[176:179], a[120:123], v[204:207]\n"
"v_cvt_pk_bf16_f32 v237, v238, v239\n"
"v_mfma_f32_16x16x32_bf16 v[204:207], a[180:183], a[124:127], v[204:207]\n"
"ds_write_b64 v245, v[236:237] offset:2048\n"
"v_mfma_f32_16x16x32_bf16 v[208:211], a[176:179], a[128:131], v[208:211]\n"
"v_mfma_f32_16x16x32_bf16 v[208:211], a[180:183], a[132:135], v[208:211]\n"
"v_mul_f32 v232, v72, v232\n"
"v_mfma_f32_16x16x32_bf16 v[212:215], a[176:179], a[136:139], v[212:215]\n"
"v_mul_f32 v233, v73, v233\n"
"v_mfma_f32_16x16x32_bf16 v[212:215], a[180:183], a[140:143], v[212:215]\n"
"v_mfma_f32_16x16x32_bf16 v[236:239], v[252:255], a[76:79], 0\n"
"v_mfma_f32_16x16x32_bf16 v[216:219], a[176:179], a[144:147], v[216:219]\n"
"v_mul_f32 v234, v74, v234\n"
"v_mfma_f32_16x16x32_bf16 v[216:219], a[180:183], a[148:151], v[216:219]\n"
"v_mul_f32 v235, v75, v235\n"
"v_mfma_f32_16x16x32_bf16 v[220:223], a[176:179], a[152:155], v[220:223]\n"
"global_load_dwordx4 a[176:179], v246, s[16:17] offset:0 sc1\n"
"v_cvt_pk_bf16_f32 v232, v232, v233\n"
"v_mfma_f32_16x16x32_bf16 v[220:223], a[180:183], a[156:159], v[220:223]\n"
"v_cvt_pk_bf16_f32 v233, v234, v235\n"
"s_waitcnt vmcnt(17)\n"
"v_mfma_f32_16x16x32_bf16 a[0:3], a[184:187], a[96:99], a[0:3]\n"
"global_load_dwordx4 a[180:183], v246, s[16:17] offset:1024 sc1\n"
"ds_read_b128 a[96:99], v242 offset:49152\n"
"v_mfma_f32_16x16x32_bf16 a[0:3], a[188:191], a[100:103], a[0:3]\n"
"ds_read_b128 a[100:103], v243 offset:49152\n"
"ds_write_b64 v245, v[232:233] offset:4096\n"
"v_mfma_f32_16x16x32_bf16 a[4:7], a[184:187], a[104:107], a[4:7]\n"
"ds_read_b128 a[104:107], v242 offset:51200\n"
"v_mul_f32 v236, v76, v236\n"
"v_mfma_f32_16x16x32_bf16 a[4:7], a[188:191], a[108:111], a[4:7]\n"
"ds_read_b128 a[108:111], v243 offset:51200\n"
"v_mul_f32 v237, v77, v237\n"
"v_mfma_f32_16x16x32_bf16 a[8:11], a[184:187], a[112:115], a[8:11]\n"
"ds_read_b128 a[112:115], v242 offset:53248\n"
"v_mfma_f32_16x16x32_bf16 v[232:235], v[252:255], a[80:83], 0\n"
"v_mfma_f32_16x16x32_bf16 a[8:11], a[188:191], a[116:119], a[8:11]\n"
"ds_read_b128 a[116:119], v243 offset:53248\n"
"v_mul_f32 v238, v78, v238\n"
"v_mfma_f32_16x16x32_bf16 a[12:15], a[184:187], a[120:123], a[12:15]\n"
"ds_read_b128 a[120:123], v242 offset:55296\n"
"v_mul_f32 v239, v79, v239\n"
"v_mfma_f32_16x16x32_bf16 a[12:15], a[188:191], a[124:127], a[12:15]\n"
"ds_read_b128 a[124:127], v243 offset:55296\n"
"v_cvt_pk_bf16_f32 v236, v236, v237\n"
"v_mfma_f32_16x16x32_bf16 a[16:19], a[184:187], a[128:131], a[16:19]\n"
"ds_read_b128 a[128:131], v242 offset:57344\n"
"v_cvt_pk_bf16_f32 v237, v238, v239\n"
"v_mfma_f32_16x16x32_bf16 a[16:19], a[188:191], a[132:135], a[16:19]\n"
"ds_read_b128 a[132:135], v243 offset:57344\n"
"ds_write_b64 v245, v[236:237] offset:6144\n"
"v_mfma_f32_16x16x32_bf16 a[20:23], a[184:187], a[136:139], a[20:23]\n"
"ds_read_b128 a[136:139], v242 offset:59392\n"
"v_mfma_f32_16x16x32_bf16 a[20:23], a[188:191], a[140:143], a[20:23]\n"
"ds_read_b128 a[140:143], v243 offset:59392\n"
"v_mul_f32 v232, v80, v232\n"
"v_mfma_f32_16x16x32_bf16 a[24:27], a[184:187], a[144:147], a[24:27]\n"
"ds_read_b128 a[144:147], v242 offset:61440\n"
"v_mul_f32 v233, v81, v233\n"
"v_mfma_f32_16x16x32_bf16 a[24:27], a[188:191], a[148:151], a[24:27]\n"
"ds_read_b128 a[148:151], v243 offset:61440\n"
"v_mfma_f32_16x16x32_bf16 v[236:239], v[252:255], a[84:87], 0\n"
"v_mfma_f32_16x16x32_bf16 a[28:31], a[184:187], a[152:155], a[28:31]\n"
"global_load_dwordx4 a[184:187], v246, s[16:17] offset:2048 sc1\n"
"ds_read_b128 a[152:155], v242 offset:63488\n"
"v_mfma_f32_16x16x32_bf16 a[28:31], a[188:191], a[156:159], a[28:31]\n"
"ds_read_b128 a[156:159], v243 offset:63488\n"
"s_waitcnt vmcnt(16) lgkmcnt(15)\n"
"v_mfma_f32_16x16x32_bf16 v[128:131], a[192:195], a[96:99], v[128:131]\n"
"global_load_dwordx4 a[188:191], v246, s[16:17] offset:3072 sc1\n"
"v_mul_f32 v234, v82, v234\n"
"v_mfma_f32_16x16x32_bf16 v[128:131], a[196:199], a[100:103], v[128:131]\n"
"v_mul_f32 v235, v83, v235\n"
"s_waitcnt lgkmcnt(13)\n"
"v_mfma_f32_16x16x32_bf16 v[132:135], a[192:195], a[104:107], v[132:135]\n"
"v_add_u32 v246, 0x2000, v246\n"
"v_cvt_pk_bf16_f32 v232, v232, v233\n"
"v_mfma_f32_16x16x32_bf16 v[132:135], a[196:199], a[108:111], v[132:135]\n"
"v_cvt_pk_bf16_f32 v233, v234, v235\n"
"s_waitcnt lgkmcnt(11)\n"
"v_mfma_f32_16x16x32_bf16 v[136:139], a[192:195], a[112:115], v[136:139]\n"
"v_add_u32 v249, 16768, v248\n"
"ds_write_b64 v245, v[232:233] offset:8192\n"
"v_mfma_f32_16x16x32_bf16 v[136:139], a[196:199], a[116:119], v[136:139]\n"
"v_mul_f32 v236, v84, v236\n"
"s_waitcnt lgkmcnt(10)\n"
"v_mfma_f32_16x16x32_bf16 v[140:143], a[192:195], a[120:123], v[140:143]\n"
"global_store_dwordx4 v249, v[228:231], s[24:25] sc1\n"
"v_mfma_f32_16x16x32_bf16 v[140:143], a[196:199], a[124:127], v[140:143]\n"
"v_mul_f32 v237, v85, v237\n"
"s_waitcnt lgkmcnt(8)\n"
"v_mfma_f32_16x16x32_bf16 v[144:147], a[192:195], a[128:131], v[144:147]\n"
"v_mfma_f32_16x16x32_bf16 v[232:235], v[252:255], a[88:91], 0\n"
"v_mul_f32 v238, v86, v238\n"
"v_mfma_f32_16x16x32_bf16 v[144:147], a[196:199], a[132:135], v[144:147]\n"
"v_mul_f32 v239, v87, v239\n"
"s_waitcnt lgkmcnt(5)\n"
"v_mfma_f32_16x16x32_bf16 v[148:151], a[192:195], a[136:139], v[148:151]\n"
"ds_read_b128 v[228:231], v247 offset:52224\n"
"v_cvt_pk_bf16_f32 v236, v236, v237\n"
"v_mfma_f32_16x16x32_bf16 v[148:151], a[196:199], a[140:143], v[148:151]\n"
"v_cvt_pk_bf16_f32 v237, v238, v239\n"
"s_waitcnt lgkmcnt(4)\n"
"v_mfma_f32_16x16x32_bf16 v[152:155], a[192:195], a[144:147], v[152:155]\n"
"ds_write_b64 v245, v[236:237] offset:10240\n"
"v_mfma_f32_16x16x32_bf16 v[152:155], a[196:199], a[148:151], v[152:155]\n"
"v_mul_f32 v232, v88, v232\n"
"s_waitcnt lgkmcnt(3)\n"
"v_mfma_f32_16x16x32_bf16 v[156:159], a[192:195], a[152:155], v[156:159]\n"
"global_load_dwordx4 a[192:195], v246, s[16:17] offset:-4096 sc1\n"
"v_mul_f32 v233, v89, v233\n"
"v_mfma_f32_16x16x32_bf16 v[156:159], a[196:199], a[156:159], v[156:159]\n"
"v_mfma_f32_16x16x32_bf16 v[236:239], v[252:255], a[92:95], 0\n"
"s_waitcnt vmcnt(16)\n"
"v_mfma_f32_16x16x32_bf16 v[160:163], a[200:203], a[96:99], v[160:163]\n"
"global_load_dwordx4 a[196:199], v246, s[16:17] offset:-3072 sc1\n"
"v_mul_f32 v234, v90, v234\n"
"v_mfma_f32_16x16x32_bf16 v[160:163], a[204:207], a[100:103], v[160:163]\n"
"v_mul_f32 v235, v91, v235\n"
"v_mfma_f32_16x16x32_bf16 v[164:167], a[200:203], a[104:107], v[164:167]\n"
"v_cvt_pk_bf16_f32 v232, v232, v233\n"
"v_mfma_f32_16x16x32_bf16 v[164:167], a[204:207], a[108:111], v[164:167]\n"
"v_cvt_pk_bf16_f32 v233, v234, v235\n"
"v_mfma_f32_16x16x32_bf16 v[168:171], a[200:203], a[112:115], v[168:171]\n"
"ds_write_b64 v245, v[232:233] offset:12288\n"
"v_mfma_f32_16x16x32_bf16 v[168:171], a[204:207], a[116:119], v[168:171]\n"
"s_add_u32 s64, s60, 36864\n"
"s_addc_u32 s65, s61, 0\n"
"v_mfma_f32_16x16x32_bf16 v[172:175], a[200:203], a[120:123], v[172:175]\n"
"v_mul_f32 v236, v92, v236\n"
"v_mfma_f32_16x16x32_bf16 v[172:175], a[204:207], a[124:127], v[172:175]\n"
"v_mul_f32 v237, v93, v237\n"
"v_mfma_f32_16x16x32_bf16 v[176:179], a[200:203], a[128:131], v[176:179]\n"
"v_add_u32 v249, 33152, v248\n"
"v_mfma_f32_16x16x32_bf16 v[232:235], v[250:253], a[64:67], 0\n"
"v_mfma_f32_16x16x32_bf16 v[176:179], a[204:207], a[132:135], v[176:179]\n"
"global_store_dwordx4 v249, v[224:227], s[24:25] sc1\n"
"v_mfma_f32_16x16x32_bf16 v[180:183], a[200:203], a[136:139], v[180:183]\n"
"v_mul_f32 v238, v94, v238\n"
"v_mul_f32 v239, v95, v239\n"
"v_mfma_f32_16x16x32_bf16 v[180:183], a[204:207], a[140:143], v[180:183]\n"
"v_cvt_pk_bf16_f32 v236, v236, v237\n"
"v_mfma_f32_16x16x32_bf16 v[184:187], a[200:203], a[144:147], v[184:187]\n"
"v_cvt_pk_bf16_f32 v237, v238, v239\n"
"v_mfma_f32_16x16x32_bf16 v[184:187], a[204:207], a[148:151], v[184:187]\n"
"ds_write_b64 v245, v[236:237] offset:14336\n"
"v_mfma_f32_16x16x32_bf16 v[188:191], a[200:203], a[152:155], v[188:191]\n"
"global_load_dword a200, v250, s[64:65]\n"
"global_load_dwordx4 a[200:203], v246, s[16:17] offset:-2048 sc1\n"
"v_mfma_f32_16x16x32_bf16 v[188:191], a[204:207], a[156:159], v[188:191]\n"
"v_mul_f32 v232, v96, v232\n"
"s_waitcnt vmcnt(18)\n"
"v_mfma_f32_16x16x32_bf16 v[192:195], a[208:211], a[96:99], v[192:195]\n"
"global_load_dwordx4 a[204:207], v246, s[16:17] offset:-1024 sc1\n"
"v_mul_f32 v233, v97, v233\n"
"v_mfma_f32_16x16x32_bf16 v[192:195], a[212:215], a[100:103], v[192:195]\n"
"v_mfma_f32_16x16x32_bf16 v[236:239], v[250:253], a[68:71], 0\n"
"v_mfma_f32_16x16x32_bf16 v[196:199], a[208:211], a[104:107], v[196:199]\n"
"v_mul_f32 v234, v98, v234\n"
"v_mfma_f32_16x16x32_bf16 v[196:199], a[212:215], a[108:111], v[196:199]\n"
"v_mul_f32 v235, v99, v235\n"
"v_mfma_f32_16x16x32_bf16 v[200:203], a[208:211], a[112:115], v[200:203]\n"
"v_cvt_pk_bf16_f32 v232, v232, v233\n"
"v_mfma_f32_16x16x32_bf16 v[200:203], a[212:215], a[116:119], v[200:203]\n"
"v_cvt_pk_bf16_f32 v233, v234, v235\n"
"v_mfma_f32_16x16x32_bf16 v[204:207], a[208:211], a[120:123], v[204:207]\n"
"ds_write_b64 v245, v[232:233] offset:8\n"
"v_mfma_f32_16x16x32_bf16 v[204:207], a[212:215], a[124:127], v[204:207]\n"
"v_mfma_f32_16x16x32_bf16 v[208:211], a[208:211], a[128:131], v[208:211]\n"
"v_mul_f32 v236, v100, v236\n"
"v_mfma_f32_16x16x32_bf16 v[208:211], a[212:215], a[132:135], v[208:211]\n"
"v_mul_f32 v237, v101, v237\n"
"v_mfma_f32_16x16x32_bf16 v[212:215], a[208:211], a[136:139], v[212:215]\n"
"v_mfma_f32_16x16x32_bf16 v[232:235], v[250:253], a[72:75], 0\n"
"v_mfma_f32_16x16x32_bf16 v[212:215], a[212:215], a[140:143], v[212:215]\n"
"v_mul_f32 v238, v102, v238\n"
"v_mfma_f32_16x16x32_bf16 v[216:219], a[208:211], a[144:147], v[216:219]\n"
"v_mul_f32 v239, v103, v239\n"
"v_mfma_f32_16x16x32_bf16 v[216:219], a[212:215], a[148:151], v[216:219]\n"
"v_cvt_pk_bf16_f32 v236, v236, v237\n"
"v_mfma_f32_16x16x32_bf16 v[220:223], a[208:211], a[152:155], v[220:223]\n"
"global_load_dwordx4 a[208:211], v246, s[16:17] offset:0 sc1\n"
"v_cvt_pk_bf16_f32 v237, v238, v239\n"
"v_mfma_f32_16x16x32_bf16 v[220:223], a[212:215], a[156:159], v[220:223]\n"
"ds_write_b64 v245, v[236:237] offset:2056\n"
"s_waitcnt vmcnt(18)\n"
"v_mfma_f32_16x16x32_bf16 a[0:3], a[216:219], a[96:99], a[0:3]\n"
"global_load_dwordx4 a[212:215], v246, s[16:17] offset:1024 sc1\n"
"ds_read_b128 a[96:99], v240 offset:16384\n"
"v_mfma_f32_16x16x32_bf16 a[0:3], a[220:223], a[100:103], a[0:3]\n"
"ds_read_b128 a[100:103], v241 offset:16384\n"
"v_mul_f32 v232, v104, v232\n"
"v_mfma_f32_16x16x32_bf16 a[4:7], a[216:219], a[104:107], a[4:7]\n"
"ds_read_b128 a[104:107], v240 offset:18432\n"
"v_mul_f32 v233, v105, v233\n"
"v_mfma_f32_16x16x32_bf16 a[4:7], a[220:223], a[108:111], a[4:7]\n"
"ds_read_b128 a[108:111], v241 offset:18432\n"
"v_mfma_f32_16x16x32_bf16 v[236:239], v[250:253], a[76:79], 0\n"
"v_mfma_f32_16x16x32_bf16 a[8:11], a[216:219], a[112:115], a[8:11]\n"
"ds_read_b128 a[112:115], v240 offset:20480\n"
"v_mul_f32 v234, v106, v234\n"
"v_mfma_f32_16x16x32_bf16 a[8:11], a[220:223], a[116:119], a[8:11]\n"
"ds_read_b128 a[116:119], v241 offset:20480\n"
"v_mul_f32 v235, v107, v235\n"
"v_mfma_f32_16x16x32_bf16 a[12:15], a[216:219], a[120:123], a[12:15]\n"
"ds_read_b128 a[120:123], v240 offset:22528\n"
"v_cvt_pk_bf16_f32 v232, v232, v233\n"
"v_mfma_f32_16x16x32_bf16 a[12:15], a[220:223], a[124:127], a[12:15]\n"
"ds_read_b128 a[124:127], v241 offset:22528\n"
"v_cvt_pk_bf16_f32 v233, v234, v235\n"
"v_mfma_f32_16x16x32_bf16 a[16:19], a[216:219], a[128:131], a[16:19]\n"
"ds_read_b128 a[128:131], v240 offset:24576\n"
"ds_write_b64 v245, v[232:233] offset:4104\n"
"v_mfma_f32_16x16x32_bf16 a[16:19], a[220:223], a[132:135], a[16:19]\n"
"ds_read_b128 a[132:135], v241 offset:24576\n"
"v_mfma_f32_16x16x32_bf16 a[20:23], a[216:219], a[136:139], a[20:23]\n"
"ds_read_b128 a[136:139], v240 offset:26624\n"
"v_mul_f32 v236, v108, v236\n"
"v_mfma_f32_16x16x32_bf16 a[20:23], a[220:223], a[140:143], a[20:23]\n"
"ds_read_b128 a[140:143], v241 offset:26624\n"
"v_mul_f32 v237, v109, v237\n"
"v_mfma_f32_16x16x32_bf16 a[24:27], a[216:219], a[144:147], a[24:27]\n"
"ds_read_b128 a[144:147], v240 offset:28672\n"
"v_mfma_f32_16x16x32_bf16 v[232:235], v[250:253], a[80:83], 0\n"
"v_mfma_f32_16x16x32_bf16 a[24:27], a[220:223], a[148:151], a[24:27]\n"
"ds_read_b128 a[148:151], v241 offset:28672\n"
"v_mul_f32 v238, v110, v238\n"
"v_mfma_f32_16x16x32_bf16 a[28:31], a[216:219], a[152:155], a[28:31]\n"
"global_load_dwordx4 a[216:219], v246, s[16:17] offset:2048 sc1\n"
"ds_read_b128 a[152:155], v240 offset:30720\n"
"v_mfma_f32_16x16x32_bf16 a[28:31], a[220:223], a[156:159], a[28:31]\n"
"ds_read_b128 a[156:159], v241 offset:30720\n"
"s_waitcnt vmcnt(17) lgkmcnt(15)\n"
"v_mfma_f32_16x16x32_bf16 v[0:3], a[160:163], a[96:99], v[0:3]\n"
"global_load_dwordx4 a[220:223], v246, s[16:17] offset:3072 sc1\n"
"v_mul_f32 v239, v111, v239\n"
"v_mfma_f32_16x16x32_bf16 v[0:3], a[164:167], a[100:103], v[0:3]\n"
"v_cvt_pk_bf16_f32 v236, v236, v237\n"
"s_waitcnt lgkmcnt(13)\n"
"v_mfma_f32_16x16x32_bf16 v[4:7], a[160:163], a[104:107], v[4:7]\n"
"v_add_u32 v246, 0x2000, v246\n"
"v_cvt_pk_bf16_f32 v237, v238, v239\n"
"v_mfma_f32_16x16x32_bf16 v[4:7], a[164:167], a[108:111], v[4:7]\n"
"ds_write_b64 v245, v[236:237] offset:6152\n"
"s_waitcnt lgkmcnt(12)\n"
"v_mfma_f32_16x16x32_bf16 v[8:11], a[160:163], a[112:115], v[8:11]\n"
"v_add_u32 v249, 49536, v248\n"
"v_mul_f32 v232, v112, v232\n"
"v_mfma_f32_16x16x32_bf16 v[8:11], a[164:167], a[116:119], v[8:11]\n"
"v_mul_f32 v233, v113, v233\n"
"s_waitcnt lgkmcnt(10)\n"
"v_mfma_f32_16x16x32_bf16 v[12:15], a[160:163], a[120:123], v[12:15]\n"
"global_store_dwordx4 v249, v[228:231], s[24:25] sc1\n"
"v_mfma_f32_16x16x32_bf16 v[12:15], a[164:167], a[124:127], v[12:15]\n"
"v_mfma_f32_16x16x32_bf16 v[236:239], v[250:253], a[84:87], 0\n"
"s_waitcnt lgkmcnt(7)\n"
"v_mfma_f32_16x16x32_bf16 v[16:19], a[160:163], a[128:131], v[16:19]\n"
"v_xor_b32 v247, 0x10000, v247\n"
"v_mul_f32 v234, v114, v234\n"
"v_mfma_f32_16x16x32_bf16 v[16:19], a[164:167], a[132:135], v[16:19]\n"
"v_mul_f32 v235, v115, v235\n"
"s_waitcnt lgkmcnt(5)\n"
"v_mfma_f32_16x16x32_bf16 v[20:23], a[160:163], a[136:139], v[20:23]\n"
"v_cvt_pk_bf16_f32 v232, v232, v233\n"
"v_cvt_pk_bf16_f32 v233, v234, v235\n"
"v_mfma_f32_16x16x32_bf16 v[20:23], a[164:167], a[140:143], v[20:23]\n"
"ds_write_b64 v245, v[232:233] offset:8200\n"
"s_waitcnt lgkmcnt(4)\n"
"v_mfma_f32_16x16x32_bf16 v[24:27], a[160:163], a[144:147], v[24:27]\n"
"v_mul_f32 v236, v116, v236\n"
"v_mfma_f32_16x16x32_bf16 v[24:27], a[164:167], a[148:151], v[24:27]\n"
"v_mul_f32 v237, v117, v237\n"
"s_waitcnt lgkmcnt(2)\n"
"v_mfma_f32_16x16x32_bf16 v[28:31], a[160:163], a[152:155], v[28:31]\n"
"global_load_dwordx4 a[160:163], v246, s[16:17] offset:-4096 sc1\n"
"v_mfma_f32_16x16x32_bf16 v[232:235], v[250:253], a[88:91], 0\n"
"v_mfma_f32_16x16x32_bf16 v[28:31], a[164:167], a[156:159], v[28:31]\n"
"v_mul_f32 v238, v118, v238\n"
"s_waitcnt vmcnt(17)\n"
"v_mfma_f32_16x16x32_bf16 v[32:35], a[168:171], a[96:99], v[32:35]\n"
"global_load_dwordx4 a[164:167], v246, s[16:17] offset:-3072 sc1\n"
"v_mul_f32 v239, v119, v239\n"
"v_mfma_f32_16x16x32_bf16 v[32:35], a[172:175], a[100:103], v[32:35]\n"
"ds_read_b128 v[224:227], v247 offset:16384\n"
"v_cvt_pk_bf16_f32 v236, v236, v237\n"
"v_mfma_f32_16x16x32_bf16 v[36:39], a[168:171], a[104:107], v[36:39]\n"
"v_cvt_pk_bf16_f32 v237, v238, v239\n"
"v_mfma_f32_16x16x32_bf16 v[36:39], a[172:175], a[108:111], v[36:39]\n"
"ds_read_b128 v[228:231], v247 offset:17408\n"
"ds_write_b64 v245, v[236:237] offset:10248\n"
"v_mfma_f32_16x16x32_bf16 v[40:43], a[168:171], a[112:115], v[40:43]\n"
"v_mfma_f32_16x16x32_bf16 v[40:43], a[172:175], a[116:119], v[40:43]\n"
"v_mul_f32 v232, v120, v232\n"
"v_mfma_f32_16x16x32_bf16 v[44:47], a[168:171], a[120:123], v[44:47]\n"
"v_mul_f32 v233, v121, v233\n"
"v_mfma_f32_16x16x32_bf16 v[44:47], a[172:175], a[124:127], v[44:47]\n"
"v_mfma_f32_16x16x32_bf16 v[236:239], v[250:253], a[92:95], 0\n"
"v_mfma_f32_16x16x32_bf16 v[48:51], a[168:171], a[128:131], v[48:51]\n"
"v_mul_f32 v234, v122, v234\n"
"v_mfma_f32_16x16x32_bf16 v[48:51], a[172:175], a[132:135], v[48:51]\n"
"v_mul_f32 v235, v123, v235\n"
"v_mfma_f32_16x16x32_bf16 v[52:55], a[168:171], a[136:139], v[52:55]\n"
"v_add_u32 v249, 640, v248\n"
"v_cvt_pk_bf16_f32 v232, v232, v233\n"
"v_mfma_f32_16x16x32_bf16 v[52:55], a[172:175], a[140:143], v[52:55]\n"
"s_waitcnt lgkmcnt(2)\n"
"global_store_dwordx4 v249, v[224:227], s[24:25] sc1\n"
"v_mfma_f32_16x16x32_bf16 v[56:59], a[168:171], a[144:147], v[56:59]\n"
"v_cvt_pk_bf16_f32 v233, v234, v235\n"
"ds_write_b64 v245, v[232:233] offset:12296\n"
"v_mfma_f32_16x16x32_bf16 v[56:59], a[172:175], a[148:151], v[56:59]\n"
"v_mfma_f32_16x16x32_bf16 v[60:63], a[168:171], a[152:155], v[60:63]\n"
"global_load_dwordx4 a[168:171], v246, s[16:17] offset:-2048 sc1\n"
"v_mul_f32 v236, v124, v236\n"
"v_mfma_f32_16x16x32_bf16 v[60:63], a[172:175], a[156:159], v[60:63]\n"
"v_mul_f32 v237, v125, v237\n"
"s_waitcnt vmcnt(18)\n"
"v_mfma_f32_16x16x32_bf16 v[64:67], a[176:179], a[96:99], v[64:67]\n"
"global_load_dwordx4 a[172:175], v246, s[16:17] offset:-1024 sc1\n"
"v_mul_f32 v238, v126, v238\n"
"v_mfma_f32_16x16x32_bf16 v[64:67], a[180:183], a[100:103], v[64:67]\n"
"ds_read_b128 v[224:227], v247 offset:18432\n"
"v_mul_f32 v239, v127, v239\n"
"v_mfma_f32_16x16x32_bf16 v[68:71], a[176:179], a[104:107], v[68:71]\n"
"v_mfma_f32_16x16x32_bf16 v[68:71], a[180:183], a[108:111], v[68:71]\n"
"v_cvt_pk_bf16_f32 v236, v236, v237\n"
"v_mfma_f32_16x16x32_bf16 v[72:75], a[176:179], a[112:115], v[72:75]\n"
"v_cvt_pk_bf16_f32 v237, v238, v239\n"
"v_mfma_f32_16x16x32_bf16 v[72:75], a[180:183], a[116:119], v[72:75]\n"
"ds_write_b64 v245, v[236:237] offset:14344\n"
"v_mfma_f32_16x16x32_bf16 v[76:79], a[176:179], a[120:123], v[76:79]\n"
"v_mfma_f32_16x16x32_bf16 v[76:79], a[180:183], a[124:127], v[76:79]\n"
"v_mfma_f32_16x16x32_bf16 v[80:83], a[176:179], a[128:131], v[80:83]\n"
"v_mfma_f32_16x16x32_bf16 v[80:83], a[180:183], a[132:135], v[80:83]\n"
"v_mfma_f32_16x16x32_bf16 v[84:87], a[176:179], a[136:139], v[84:87]\n"
"v_mfma_f32_16x16x32_bf16 v[84:87], a[180:183], a[140:143], v[84:87]\n"
"v_mfma_f32_16x16x32_bf16 v[88:91], a[176:179], a[144:147], v[88:91]\n"
"v_mfma_f32_16x16x32_bf16 v[88:91], a[180:183], a[148:151], v[88:91]\n"
"v_mfma_f32_16x16x32_bf16 v[92:95], a[176:179], a[152:155], v[92:95]\n"
"global_load_dwordx4 a[176:179], v246, s[16:17] offset:0 sc1\n"
"v_mfma_f32_16x16x32_bf16 v[92:95], a[180:183], a[156:159], v[92:95]\n"
"s_waitcnt vmcnt(18)\n"
"v_mfma_f32_16x16x32_bf16 v[96:99], a[184:187], a[96:99], v[96:99]\n"
"global_load_dwordx4 a[180:183], v246, s[16:17] offset:1024 sc1\n"
"ds_read_b128 a[96:99], v240 offset:49152\n"
"v_mfma_f32_16x16x32_bf16 v[96:99], a[188:191], a[100:103], v[96:99]\n"
"ds_read_b128 a[100:103], v241 offset:49152\n"
"v_mfma_f32_16x16x32_bf16 v[100:103], a[184:187], a[104:107], v[100:103]\n"
"ds_read_b128 a[104:107], v240 offset:51200\n"
"v_mfma_f32_16x16x32_bf16 v[100:103], a[188:191], a[108:111], v[100:103]\n"
"ds_read_b128 a[108:111], v241 offset:51200\n"
"v_mfma_f32_16x16x32_bf16 v[104:107], a[184:187], a[112:115], v[104:107]\n"
"ds_read_b128 a[112:115], v240 offset:53248\n"
"v_mfma_f32_16x16x32_bf16 v[104:107], a[188:191], a[116:119], v[104:107]\n"
"ds_read_b128 a[116:119], v241 offset:53248\n"
"v_mfma_f32_16x16x32_bf16 v[108:111], a[184:187], a[120:123], v[108:111]\n"
"ds_read_b128 a[120:123], v240 offset:55296\n"
"v_mfma_f32_16x16x32_bf16 v[108:111], a[188:191], a[124:127], v[108:111]\n"
"ds_read_b128 a[124:127], v241 offset:55296\n"
"v_mfma_f32_16x16x32_bf16 v[112:115], a[184:187], a[128:131], v[112:115]\n"
"ds_read_b128 a[128:131], v240 offset:57344\n"
"v_mfma_f32_16x16x32_bf16 v[112:115], a[188:191], a[132:135], v[112:115]\n"
"ds_read_b128 a[132:135], v241 offset:57344\n"
"v_mfma_f32_16x16x32_bf16 v[116:119], a[184:187], a[136:139], v[116:119]\n"
"ds_read_b128 a[136:139], v240 offset:59392\n"
"v_mfma_f32_16x16x32_bf16 v[116:119], a[188:191], a[140:143], v[116:119]\n"
"ds_read_b128 a[140:143], v241 offset:59392\n"
"v_mfma_f32_16x16x32_bf16 v[120:123], a[184:187], a[144:147], v[120:123]\n"
"ds_read_b128 a[144:147], v240 offset:61440\n"
"v_mfma_f32_16x16x32_bf16 v[120:123], a[188:191], a[148:151], v[120:123]\n"
"ds_read_b128 a[148:151], v241 offset:61440\n"
"v_mfma_f32_16x16x32_bf16 v[124:127], a[184:187], a[152:155], v[124:127]\n"
"global_load_dwordx4 a[184:187], v246, s[16:17] offset:2048 sc1\n"
"ds_read_b128 a[152:155], v240 offset:63488\n"
"v_mfma_f32_16x16x32_bf16 v[124:127], a[188:191], a[156:159], v[124:127]\n"
"ds_read_b128 a[156:159], v241 offset:63488\n"
"s_waitcnt vmcnt(17) lgkmcnt(14)\n"
"v_mfma_f32_16x16x32_bf16 v[0:3], a[192:195], a[96:99], v[0:3]\n"
"global_load_dwordx4 a[188:191], v246, s[16:17] offset:3072 sc1\n"
"v_mfma_f32_16x16x32_bf16 v[0:3], a[196:199], a[100:103], v[0:3]\n"
"s_waitcnt lgkmcnt(12)\n"
"v_mfma_f32_16x16x32_bf16 v[4:7], a[192:195], a[104:107], v[4:7]\n"
"v_add_u32 v246, 0x2000, v246\n"
"v_mfma_f32_16x16x32_bf16 v[4:7], a[196:199], a[108:111], v[4:7]\n"
"s_waitcnt lgkmcnt(10)\n"
"v_mfma_f32_16x16x32_bf16 v[8:11], a[192:195], a[112:115], v[8:11]\n"
"v_add_u32 v249, 17024, v248\n"
"v_mfma_f32_16x16x32_bf16 v[8:11], a[196:199], a[116:119], v[8:11]\n"
"s_waitcnt lgkmcnt(8)\n"
"v_mfma_f32_16x16x32_bf16 v[12:15], a[192:195], a[120:123], v[12:15]\n"
"global_store_dwordx4 v249, v[228:231], s[24:25] sc1\n"
"v_mfma_f32_16x16x32_bf16 v[12:15], a[196:199], a[124:127], v[12:15]\n"
"s_waitcnt lgkmcnt(6)\n"
"v_mfma_f32_16x16x32_bf16 v[16:19], a[192:195], a[128:131], v[16:19]\n"
"v_mfma_f32_16x16x32_bf16 v[16:19], a[196:199], a[132:135], v[16:19]\n"
"s_waitcnt lgkmcnt(4)\n"
"v_mfma_f32_16x16x32_bf16 v[20:23], a[192:195], a[136:139], v[20:23]\n"
"ds_read_b128 v[228:231], v247 offset:19456\n"
"v_mfma_f32_16x16x32_bf16 v[20:23], a[196:199], a[140:143], v[20:23]\n"
"s_waitcnt lgkmcnt(3)\n"
"v_mfma_f32_16x16x32_bf16 v[24:27], a[192:195], a[144:147], v[24:27]\n"
"s_add_u32 s18, s18, 2048\n"
"s_addc_u32 s19, s19, 0\n"
"v_mfma_f32_16x16x32_bf16 v[24:27], a[196:199], a[148:151], v[24:27]\n"
"v_lshrrev_b32 v237, 4, v247\n"
"s_waitcnt lgkmcnt(1)\n"
"v_mfma_f32_16x16x32_bf16 v[28:31], a[192:195], a[152:155], v[28:31]\n"
"global_load_dwordx4 a[192:195], v246, s[16:17] offset:-4096 sc1\n"
"v_and_b32 v237, 0x30, v237\n"
"v_mfma_f32_16x16x32_bf16 v[28:31], a[196:199], a[156:159], v[28:31]\n"
"global_load_dwordx2 v[236:237], v237, s[18:19] offset:128\n"
"s_waitcnt vmcnt(17)\n"
"v_mfma_f32_16x16x32_bf16 v[32:35], a[200:203], a[96:99], v[32:35]\n"
"global_load_dwordx4 a[196:199], v246, s[16:17] offset:-3072 sc1\n"
"v_mfma_f32_16x16x32_bf16 v[32:35], a[204:207], a[100:103], v[32:35]\n"
"v_mfma_f32_16x16x32_bf16 v[36:39], a[200:203], a[104:107], v[36:39]\n"
"v_mfma_f32_16x16x32_bf16 v[36:39], a[204:207], a[108:111], v[36:39]\n"
"v_mfma_f32_16x16x32_bf16 v[40:43], a[200:203], a[112:115], v[40:43]\n"
"v_mfma_f32_16x16x32_bf16 v[40:43], a[204:207], a[116:119], v[40:43]\n"
"v_mfma_f32_16x16x32_bf16 v[44:47], a[200:203], a[120:123], v[44:47]\n"
"v_mfma_f32_16x16x32_bf16 v[44:47], a[204:207], a[124:127], v[44:47]\n"
"v_mfma_f32_16x16x32_bf16 v[48:51], a[200:203], a[128:131], v[48:51]\n"
"v_mfma_f32_16x16x32_bf16 v[48:51], a[204:207], a[132:135], v[48:51]\n"
"v_mfma_f32_16x16x32_bf16 v[52:55], a[200:203], a[136:139], v[52:55]\n"
"v_mfma_f32_16x16x32_bf16 v[52:55], a[204:207], a[140:143], v[52:55]\n"
"v_mfma_f32_16x16x32_bf16 v[56:59], a[200:203], a[144:147], v[56:59]\n"
"v_add_u32 v249, 33408, v248\n"
"v_mfma_f32_16x16x32_bf16 v[56:59], a[204:207], a[148:151], v[56:59]\n"
"global_store_dwordx4 v249, v[224:227], s[24:25] sc1\n"
"v_mfma_f32_16x16x32_bf16 v[60:63], a[200:203], a[152:155], v[60:63]\n"
"global_load_dwordx4 a[200:203], v246, s[16:17] offset:-2048 sc1\n"
"v_mfma_f32_16x16x32_bf16 v[60:63], a[204:207], a[156:159], v[60:63]\n"
"s_waitcnt vmcnt(18)\n"
"v_mfma_f32_16x16x32_bf16 v[64:67], a[208:211], a[96:99], v[64:67]\n"
"global_load_dwordx4 a[204:207], v246, s[16:17] offset:-1024 sc1\n"
"v_mfma_f32_16x16x32_bf16 v[64:67], a[212:215], a[100:103], v[64:67]\n"
"v_mfma_f32_16x16x32_bf16 v[68:71], a[208:211], a[104:107], v[68:71]\n"
"v_mfma_f32_16x16x32_bf16 v[68:71], a[212:215], a[108:111], v[68:71]\n"
"v_mfma_f32_16x16x32_bf16 v[72:75], a[208:211], a[112:115], v[72:75]\n"
"v_mfma_f32_16x16x32_bf16 v[72:75], a[212:215], a[116:119], v[72:75]\n"
"v_mfma_f32_16x16x32_bf16 v[76:79], a[208:211], a[120:123], v[76:79]\n"
"v_mfma_f32_16x16x32_bf16 v[76:79], a[212:215], a[124:127], v[76:79]\n"
"v_mfma_f32_16x16x32_bf16 v[80:83], a[208:211], a[128:131], v[80:83]\n"
"v_mfma_f32_16x16x32_bf16 v[80:83], a[212:215], a[132:135], v[80:83]\n"
"v_mfma_f32_16x16x32_bf16 v[84:87], a[208:211], a[136:139], v[84:87]\n"
"v_mfma_f32_16x16x32_bf16 v[84:87], a[212:215], a[140:143], v[84:87]\n"
"v_mfma_f32_16x16x32_bf16 v[88:91], a[208:211], a[144:147], v[88:91]\n"
"v_mfma_f32_16x16x32_bf16 v[88:91], a[212:215], a[148:151], v[88:91]\n"
"v_mfma_f32_16x16x32_bf16 v[92:95], a[208:211], a[152:155], v[92:95]\n"
"global_load_dwordx4 a[208:211], v246, s[16:17] offset:0 sc1\n"
"v_mfma_f32_16x16x32_bf16 v[92:95], a[212:215], a[156:159], v[92:95]\n"
"s_waitcnt vmcnt(18)\n"
"v_mfma_f32_16x16x32_bf16 v[96:99], a[216:219], a[96:99], v[96:99]\n"
"global_load_dwordx4 a[212:215], v246, s[16:17] offset:1024 sc1\n"
"ds_read_b128 a[96:99], v242 offset:16384\n"
"v_mfma_f32_16x16x32_bf16 v[96:99], a[220:223], a[100:103], v[96:99]\n"
"ds_read_b128 a[100:103], v243 offset:16384\n"
"v_mfma_f32_16x16x32_bf16 v[100:103], a[216:219], a[104:107], v[100:103]\n"
"ds_read_b128 a[104:107], v242 offset:18432\n"
"v_mfma_f32_16x16x32_bf16 v[100:103], a[220:223], a[108:111], v[100:103]\n"
"ds_read_b128 a[108:111], v243 offset:18432\n"
"v_mfma_f32_16x16x32_bf16 v[104:107], a[216:219], a[112:115], v[104:107]\n"
"ds_read_b128 a[112:115], v242 offset:20480\n"
"v_mfma_f32_16x16x32_bf16 v[104:107], a[220:223], a[116:119], v[104:107]\n"
"ds_read_b128 a[116:119], v243 offset:20480\n"
"v_mfma_f32_16x16x32_bf16 v[108:111], a[216:219], a[120:123], v[108:111]\n"
"ds_read_b128 a[120:123], v242 offset:22528\n"
"v_mfma_f32_16x16x32_bf16 v[108:111], a[220:223], a[124:127], v[108:111]\n"
"ds_read_b128 a[124:127], v243 offset:22528\n"
"v_mfma_f32_16x16x32_bf16 v[112:115], a[216:219], a[128:131], v[112:115]\n"
"ds_read_b128 a[128:131], v242 offset:24576\n"
"v_mfma_f32_16x16x32_bf16 v[112:115], a[220:223], a[132:135], v[112:115]\n"
"ds_read_b128 a[132:135], v243 offset:24576\n"
"v_mfma_f32_16x16x32_bf16 v[116:119], a[216:219], a[136:139], v[116:119]\n"
"ds_read_b128 a[136:139], v242 offset:26624\n"
"v_mfma_f32_16x16x32_bf16 v[116:119], a[220:223], a[140:143], v[116:119]\n"
"ds_read_b128 a[140:143], v243 offset:26624\n"
"v_mfma_f32_16x16x32_bf16 v[120:123], a[216:219], a[144:147], v[120:123]\n"
"ds_read_b128 a[144:147], v242 offset:28672\n"
"v_mfma_f32_16x16x32_bf16 v[120:123], a[220:223], a[148:151], v[120:123]\n"
"ds_read_b128 a[148:151], v243 offset:28672\n"
"v_mfma_f32_16x16x32_bf16 v[124:127], a[216:219], a[152:155], v[124:127]\n"
"global_load_dwordx4 a[216:219], v246, s[16:17] offset:2048 sc1\n"
"ds_read_b128 a[152:155], v242 offset:30720\n"
"v_mfma_f32_16x16x32_bf16 v[124:127], a[220:223], a[156:159], v[124:127]\n"
"ds_read_b128 a[156:159], v243 offset:30720\n"
"s_waitcnt vmcnt(17) lgkmcnt(14)\n"
"v_mfma_f32_16x16x32_bf16 v[0:3], a[160:163], a[96:99], v[0:3]\n"
"global_load_dwordx4 a[220:223], v246, s[16:17] offset:3072 sc1\n"
"v_mfma_f32_16x16x32_bf16 v[0:3], a[164:167], a[100:103], v[0:3]\n"
"s_waitcnt lgkmcnt(12)\n"
"v_mfma_f32_16x16x32_bf16 v[4:7], a[160:163], a[104:107], v[4:7]\n"
"v_add_u32 v246, 0x2000, v246\n"
"v_mfma_f32_16x16x32_bf16 v[4:7], a[164:167], a[108:111], v[4:7]\n"
"s_waitcnt lgkmcnt(10)\n"
"v_mfma_f32_16x16x32_bf16 v[8:11], a[160:163], a[112:115], v[8:11]\n"
"v_add_u32 v249, 49792, v248\n"
"v_mfma_f32_16x16x32_bf16 v[8:11], a[164:167], a[116:119], v[8:11]\n"
"s_waitcnt lgkmcnt(8)\n"
"v_mfma_f32_16x16x32_bf16 v[12:15], a[160:163], a[120:123], v[12:15]\n"
"global_store_dwordx4 v249, v[228:231], s[24:25] sc1\n"
"v_mfma_f32_16x16x32_bf16 v[12:15], a[164:167], a[124:127], v[12:15]\n"
"s_waitcnt lgkmcnt(6)\n"
"v_mfma_f32_16x16x32_bf16 v[16:19], a[160:163], a[128:131], v[16:19]\n"
"v_mfma_f32_16x16x32_bf16 v[16:19], a[164:167], a[132:135], v[16:19]\n"
"s_waitcnt lgkmcnt(4)\n"
"v_mfma_f32_16x16x32_bf16 v[20:23], a[160:163], a[136:139], v[20:23]\n"
"v_mfma_f32_16x16x32_bf16 v[20:23], a[164:167], a[140:143], v[20:23]\n"
"s_waitcnt lgkmcnt(2)\n"
"v_mfma_f32_16x16x32_bf16 v[24:27], a[160:163], a[144:147], v[24:27]\n"
"ds_read_b128 v[224:227], v247 offset:49152\n"
"v_mfma_f32_16x16x32_bf16 v[24:27], a[164:167], a[148:151], v[24:27]\n"
"s_waitcnt lgkmcnt(1)\n"
"v_mfma_f32_16x16x32_bf16 v[28:31], a[160:163], a[152:155], v[28:31]\n"
"global_load_dwordx4 a[160:163], v246, s[16:17] offset:-4096 sc1\n"
"v_mfma_f32_16x16x32_bf16 v[28:31], a[164:167], a[156:159], v[28:31]\n"
"s_waitcnt vmcnt(17)\n"
"v_mfma_f32_16x16x32_bf16 v[32:35], a[168:171], a[96:99], v[32:35]\n"
"global_load_dwordx4 a[164:167], v246, s[16:17] offset:-3072 sc1\n"
"v_mfma_f32_16x16x32_bf16 v[32:35], a[172:175], a[100:103], v[32:35]\n"
"ds_read_b128 v[228:231], v247 offset:50176\n"
"v_mfma_f32_16x16x32_bf16 v[36:39], a[168:171], a[104:107], v[36:39]\n"
"v_mfma_f32_16x16x32_bf16 v[36:39], a[172:175], a[108:111], v[36:39]\n"
"v_mfma_f32_16x16x32_bf16 v[40:43], a[168:171], a[112:115], v[40:43]\n"
"v_mfma_f32_16x16x32_bf16 v[40:43], a[172:175], a[116:119], v[40:43]\n"
"v_mfma_f32_16x16x32_bf16 v[44:47], a[168:171], a[120:123], v[44:47]\n"
"v_mfma_f32_16x16x32_bf16 v[44:47], a[172:175], a[124:127], v[44:47]\n"
"v_add_u32 v249, 896, v248\n"
"v_mfma_f32_16x16x32_bf16 v[48:51], a[168:171], a[128:131], v[48:51]\n"
"s_waitcnt lgkmcnt(1)\n"
"global_store_dwordx4 v249, v[224:227], s[24:25] sc1\n"
".Lpg9_%=:\n"
"s_cbranch_execz .Lpg13_%=\n"
"v_mfma_f32_16x16x32_bf16 v[48:51], a[172:175], a[132:135], v[48:51]\n"
"v_mfma_f32_16x16x32_bf16 v[52:55], a[168:171], a[136:139], v[52:55]\n"
"v_mfma_f32_16x16x32_bf16 v[52:55], a[172:175], a[140:143], v[52:55]\n"
"ds_read_b128 v[224:227], v247 offset:51200\n"
"v_mfma_f32_16x16x32_bf16 v[56:59], a[168:171], a[144:147], v[56:59]\n"
"v_mfma_f32_16x16x32_bf16 v[56:59], a[172:175], a[148:151], v[56:59]\n"
"v_mfma_f32_16x16x32_bf16 v[60:63], a[168:171], a[152:155], v[60:63]\n"
"global_load_dwordx4 a[168:171], v246, s[16:17] offset:-2048 sc1\n"
"v_mfma_f32_16x16x32_bf16 v[60:63], a[172:175], a[156:159], v[60:63]\n"
"s_waitcnt vmcnt(18)\n"
"v_mfma_f32_16x16x32_bf16 v[64:67], a[176:179], a[96:99], v[64:67]\n"
"global_load_dwordx4 a[172:175], v246, s[16:17] offset:-1024 sc1\n"
"v_mfma_f32_16x16x32_bf16 v[64:67], a[180:183], a[100:103], v[64:67]\n"
"v_mfma_f32_16x16x32_bf16 v[68:71], a[176:179], a[104:107], v[68:71]\n"
"v_mfma_f32_16x16x32_bf16 v[68:71], a[180:183], a[108:111], v[68:71]\n"
"v_mfma_f32_16x16x32_bf16 v[72:75], a[176:179], a[112:115], v[72:75]\n"
"s_mov_b64 exec, 1\n"
"global_atomic_add v250, v252, s[30:31] offset:0 sc1\n"
"s_mov_b64 exec, -1\n"
"v_mfma_f32_16x16x32_bf16 v[72:75], a[180:183], a[116:119], v[72:75]\n"
"v_mfma_f32_16x16x32_bf16 v[76:79], a[176:179], a[120:123], v[76:79]\n"
"v_mfma_f32_16x16x32_bf16 v[76:79], a[180:183], a[124:127], v[76:79]\n"
"v_mfma_f32_16x16x32_bf16 v[80:83], a[176:179], a[128:131], v[80:83]\n"
"v_mfma_f32_16x16x32_bf16 v[80:83], a[180:183], a[132:135], v[80:83]\n"
"v_mfma_f32_16x16x32_bf16 v[84:87], a[176:179], a[136:139], v[84:87]\n"
"v_mfma_f32_16x16x32_bf16 v[84:87], a[180:183], a[140:143], v[84:87]\n"
"v_mfma_f32_16x16x32_bf16 v[88:91], a[176:179], a[144:147], v[88:91]\n"
"v_mfma_f32_16x16x32_bf16 v[88:91], a[180:183], a[148:151], v[88:91]\n"
"v_mfma_f32_16x16x32_bf16 v[92:95], a[176:179], a[152:155], v[92:95]\n"
"global_load_dwordx4 a[176:179], v246, s[16:17] offset:0 sc1\n"
"v_mfma_f32_16x16x32_bf16 v[92:95], a[180:183], a[156:159], v[92:95]\n"
"s_waitcnt vmcnt(19)\n"
"v_mfma_f32_16x16x32_bf16 v[96:99], a[184:187], a[96:99], v[96:99]\n"
"global_load_dwordx4 a[180:183], v246, s[16:17] offset:1024 sc1\n"
"ds_read_b128 a[96:99], v242 offset:49152\n"
"v_mfma_f32_16x16x32_bf16 v[96:99], a[188:191], a[100:103], v[96:99]\n"
"ds_read_b128 a[100:103], v243 offset:49152\n"
"v_mfma_f32_16x16x32_bf16 v[100:103], a[184:187], a[104:107], v[100:103]\n"
"ds_read_b128 a[104:107], v242 offset:51200\n"
"v_mfma_f32_16x16x32_bf16 v[100:103], a[188:191], a[108:111], v[100:103]\n"
"ds_read_b128 a[108:111], v243 offset:51200\n"
"v_mfma_f32_16x16x32_bf16 v[104:107], a[184:187], a[112:115], v[104:107]\n"
"ds_read_b128 a[112:115], v242 offset:53248\n"
"v_mfma_f32_16x16x32_bf16 v[104:107], a[188:191], a[116:119], v[104:107]\n"
"ds_read_b128 a[116:119], v243 offset:53248\n"
"v_mfma_f32_16x16x32_bf16 v[108:111], a[184:187], a[120:123], v[108:111]\n"
"ds_read_b128 a[120:123], v242 offset:55296\n"
"v_mfma_f32_16x16x32_bf16 v[108:111], a[188:191], a[124:127], v[108:111]\n"
"ds_read_b128 a[124:127], v243 offset:55296\n"
"v_mfma_f32_16x16x32_bf16 v[112:115], a[184:187], a[128:131], v[112:115]\n"
"ds_read_b128 a[128:131], v242 offset:57344\n"
"v_mfma_f32_16x16x32_bf16 v[112:115], a[188:191], a[132:135], v[112:115]\n"
"ds_read_b128 a[132:135], v243 offset:57344\n"
"v_mfma_f32_16x16x32_bf16 v[116:119], a[184:187], a[136:139], v[116:119]\n"
"ds_read_b128 a[136:139], v242 offset:59392\n"
"v_mfma_f32_16x16x32_bf16 v[116:119], a[188:191], a[140:143], v[116:119]\n"
"ds_read_b128 a[140:143], v243 offset:59392\n"
"v_mfma_f32_16x16x32_bf16 v[120:123], a[184:187], a[144:147], v[120:123]\n"
"ds_read_b128 a[144:147], v242 offset:61440\n"
"v_mfma_f32_16x16x32_bf16 v[120:123], a[188:191], a[148:151], v[120:123]\n"
"ds_read_b128 a[148:151], v243 offset:61440\n"
"v_mfma_f32_16x16x32_bf16 v[124:127], a[184:187], a[152:155], v[124:127]\n"
"global_load_dwordx4 a[184:187], v246, s[16:17] offset:2048 sc1\n"
"ds_read_b128 a[152:155], v242 offset:63488\n"
"v_mfma_f32_16x16x32_bf16 v[124:127], a[188:191], a[156:159], v[124:127]\n"
"ds_read_b128 a[156:159], v243 offset:63488\n"
"s_waitcnt vmcnt(17) lgkmcnt(14)\n"
"v_mfma_f32_16x16x32_bf16 v[0:3], a[192:195], a[96:99], v[0:3]\n"
"global_load_dwordx4 a[188:191], v246, s[16:17] offset:3072 sc1\n"
"v_and_b32 v235, s67, v237\n"
"v_mfma_f32_16x16x32_bf16 v[0:3], a[196:199], a[100:103], v[0:3]\n"
"v_lshlrev_b32 v234, 16, v237\n"
"s_waitcnt lgkmcnt(12)\n"
"v_mfma_f32_16x16x32_bf16 v[4:7], a[192:195], a[104:107], v[4:7]\n"
"v_add_u32 v246, 0x2000, v246\n"
"v_and_b32 v233, s67, v236\n"
"v_mfma_f32_16x16x32_bf16 v[4:7], a[196:199], a[108:111], v[4:7]\n"
"v_lshlrev_b32 v232, 16, v236\n"
"s_waitcnt lgkmcnt(10)\n"
"v_mfma_f32_16x16x32_bf16 v[8:11], a[192:195], a[112:115], v[8:11]\n"
"v_add_u32 v249, 17280, v248\n"
"v_lshrrev_b32 v237, 4, v247\n"
"v_mfma_f32_16x16x32_bf16 v[8:11], a[196:199], a[116:119], v[8:11]\n"
"v_and_b32 v237, 0x30, v237\n"
"s_waitcnt lgkmcnt(8)\n"
"v_mfma_f32_16x16x32_bf16 v[12:15], a[192:195], a[120:123], v[12:15]\n"
"global_store_dwordx4 v249, v[228:231], s[24:25] sc1\n"
"v_mfma_f32_16x16x32_bf16 v[12:15], a[196:199], a[124:127], v[12:15]\n"
"global_load_dwordx2 v[236:237], v237, s[18:19] offset:136\n"
"s_waitcnt lgkmcnt(6)\n"
"v_mfma_f32_16x16x32_bf16 v[16:19], a[192:195], a[128:131], v[16:19]\n"
"v_add_f32 v128, v232, v128\n"
"v_mfma_f32_16x16x32_bf16 v[16:19], a[196:199], a[132:135], v[16:19]\n"
"s_waitcnt lgkmcnt(4)\n"
"v_mfma_f32_16x16x32_bf16 v[20:23], a[192:195], a[136:139], v[20:23]\n"
"ds_read_b128 v[228:231], v247 offset:52224\n"
"v_mfma_f32_16x16x32_bf16 v[20:23], a[196:199], a[140:143], v[20:23]\n"
"s_waitcnt lgkmcnt(3)\n"
"v_mfma_f32_16x16x32_bf16 v[24:27], a[192:195], a[144:147], v[24:27]\n"
"v_add_f32 v129, v233, v129\n"
"v_mfma_f32_16x16x32_bf16 v[24:27], a[196:199], a[148:151], v[24:27]\n"
"s_waitcnt lgkmcnt(1)\n"
"v_mfma_f32_16x16x32_bf16 v[28:31], a[192:195], a[152:155], v[28:31]\n"
"global_load_dwordx4 a[192:195], v246, s[16:17] offset:-4096 sc1\n"
"v_mfma_f32_16x16x32_bf16 v[28:31], a[196:199], a[156:159], v[28:31]\n"
"s_waitcnt vmcnt(18)\n"
"v_mfma_f32_16x16x32_bf16 v[32:35], a[200:203], a[96:99], v[32:35]\n"
"global_load_dwordx4 a[196:199], v246, s[16:17] offset:-3072 sc1\n"
"v_mfma_f32_16x16x32_bf16 v[32:35], a[204:207], a[100:103], v[32:35]\n"
"v_add_u32 v249, 33664, v248\n"
"v_mfma_f32_16x16x32_bf16 v[36:39], a[200:203], a[104:107], v[36:39]\n"
"global_store_dwordx4 v249, v[224:227], s[24:25] sc1\n"
"v_mfma_f32_16x16x32_bf16 v[36:39], a[204:207], a[108:111], v[36:39]\n"
"v_add_f32 v130, v234, v130\n"
"v_mfma_f32_16x16x32_bf16 v[40:43], a[200:203], a[112:115], v[40:43]\n"
"v_add_f32 v131, v235, v131\n"
"v_mfma_f32_16x16x32_bf16 v[40:43], a[204:207], a[116:119], v[40:43]\n"
"v_add_f32 v132, v232, v132\n"
"v_mfma_f32_16x16x32_bf16 v[44:47], a[200:203], a[120:123], v[44:47]\n"
"v_add_f32 v133, v233, v133\n"
"v_mfma_f32_16x16x32_bf16 v[44:47], a[204:207], a[124:127], v[44:47]\n"
"v_add_f32 v134, v234, v134\n"
"v_mfma_f32_16x16x32_bf16 v[48:51], a[200:203], a[128:131], v[48:51]\n"
"v_add_f32 v135, v235, v135\n"
"v_mfma_f32_16x16x32_bf16 v[48:51], a[204:207], a[132:135], v[48:51]\n"
"v_add_f32 v136, v232, v136\n"
"v_mfma_f32_16x16x32_bf16 v[52:55], a[200:203], a[136:139], v[52:55]\n"
"v_add_f32 v137, v233, v137\n"
"v_mfma_f32_16x16x32_bf16 v[52:55], a[204:207], a[140:143], v[52:55]\n"
"v_add_f32 v138, v234, v138\n"
"v_mfma_f32_16x16x32_bf16 v[56:59], a[200:203], a[144:147], v[56:59]\n"
"v_add_f32 v139, v235, v139\n"
"v_mfma_f32_16x16x32_bf16 v[56:59], a[204:207], a[148:151], v[56:59]\n"
"v_add_f32 v140, v232, v140\n"
"v_mfma_f32_16x16x32_bf16 v[60:63], a[200:203], a[152:155], v[60:63]\n"
"global_load_dwordx4 a[200:203], v246, s[16:17] offset:-2048 sc1\n"
"v_mfma_f32_16x16x32_bf16 v[60:63], a[204:207], a[156:159], v[60:63]\n"
"s_waitcnt vmcnt(19)\n"
"v_mfma_f32_16x16x32_bf16 v[64:67], a[208:211], a[96:99], v[64:67]\n"
"global_load_dwordx4 a[204:207], v246, s[16:17] offset:-1024 sc1\n"
"v_mfma_f32_16x16x32_bf16 v[64:67], a[212:215], a[100:103], v[64:67]\n"
"v_add_f32 v141, v233, v141\n"
"v_mfma_f32_16x16x32_bf16 v[68:71], a[208:211], a[104:107], v[68:71]\n"
"v_add_f32 v142, v234, v142\n"
"v_mfma_f32_16x16x32_bf16 v[68:71], a[212:215], a[108:111], v[68:71]\n"
"v_add_f32 v143, v235, v143\n"
"v_mfma_f32_16x16x32_bf16 v[72:75], a[208:211], a[112:115], v[72:75]\n"
"v_add_f32 v144, v232, v144\n"
"v_mfma_f32_16x16x32_bf16 v[72:75], a[212:215], a[116:119], v[72:75]\n"
"v_add_f32 v145, v233, v145\n"
"v_mfma_f32_16x16x32_bf16 v[76:79], a[208:211], a[120:123], v[76:79]\n"
"v_add_f32 v146, v234, v146\n"
"v_mfma_f32_16x16x32_bf16 v[76:79], a[212:215], a[124:127], v[76:79]\n"
"v_add_f32 v147, v235, v147\n"
"v_mfma_f32_16x16x32_bf16 v[80:83], a[208:211], a[128:131], v[80:83]\n"
"v_add_f32 v148, v232, v148\n"
"v_mfma_f32_16x16x32_bf16 v[80:83], a[212:215], a[132:135], v[80:83]\n"
"v_add_f32 v149, v233, v149\n"
"v_mfma_f32_16x16x32_bf16 v[84:87], a[208:211], a[136:139], v[84:87]\n"
"v_add_f32 v150, v234, v150\n"
"v_mfma_f32_16x16x32_bf16 v[84:87], a[212:215], a[140:143], v[84:87]\n"
"v_add_u32 v249, 50048, v248\n"
"v_mfma_f32_16x16x32_bf16 v[88:91], a[208:211], a[144:147], v[88:91]\n"
"s_waitcnt lgkmcnt(0)\n"
"global_store_dwordx4 v249, v[228:231], s[24:25] sc1\n"
"v_mfma_f32_16x16x32_bf16 v[88:91], a[212:215], a[148:151], v[88:91]\n"
"v_xor_b32 v247, 0x10000, v247\n"
"s_barrier\n"
"v_mfma_f32_16x16x32_bf16 v[92:95], a[208:211], a[152:155], v[92:95]\n"
"global_load_dwordx4 a[208:211], v246, s[16:17] offset:0 sc1\n"
"v_mfma_f32_16x16x32_bf16 v[92:95], a[212:215], a[156:159], v[92:95]\n"
"s_waitcnt vmcnt(20)\n"
"v_mfma_f32_16x16x32_bf16 v[96:99], a[216:219], a[96:99], v[96:99]\n"
"global_load_dwordx4 a[212:215], v246, s[16:17] offset:1024 sc1\n"
"ds_read_b128 a[96:99], v240 offset:0\n"
"v_mfma_f32_16x16x32_bf16 v[96:99], a[220:223], a[100:103], v[96:99]\n"
"ds_read_b128 a[100:103], v241 offset:0\n"
"v_mfma_f32_16x16x32_bf16 v[100:103], a[216:219], a[104:107], v[100:103]\n"
"ds_read_b128 a[104:107], v240 offset:2048\n"
"v_mfma_f32_16x16x32_bf16 v[100:103], a[220:223], a[108:111], v[100:103]\n"
"ds_read_b128 a[108:111], v241 offset:2048\n"
"v_mfma_f32_16x16x32_bf16 v[104:107], a[216:219], a[112:115], v[104:107]\n"
"ds_read_b128 a[112:115], v240 offset:4096\n"
"v_mfma_f32_16x16x32_bf16 v[104:107], a[220:223], a[116:119], v[104:107]\n"
"ds_read_b128 a[116:119], v241 offset:4096\n"
"v_mfma_f32_16x16x32_bf16 v[108:111], a[216:219], a[120:123], v[108:111]\n"
"ds_read_b128 a[120:123], v240 offset:6144\n"
"v_mfma_f32_16x16x32_bf16 v[108:111], a[220:223], a[124:127], v[108:111]\n"
"ds_read_b128 a[124:127], v241 offset:6144\n"
"v_mfma_f32_16x16x32_bf16 v[112:115], a[216:219], a[128:131], v[112:115]\n"
"ds_read_b128 a[128:131], v240 offset:8192\n"
"v_mfma_f32_16x16x32_bf16 v[112:115], a[220:223], a[132:135], v[112:115]\n"
"ds_read_b128 a[132:135], v241 offset:8192\n"
"v_mfma_f32_16x16x32_bf16 v[116:119], a[216:219], a[136:139], v[116:119]\n"
"ds_read_b128 a[136:139], v240 offset:10240\n"
"v_mfma_f32_16x16x32_bf16 v[116:119], a[220:223], a[140:143], v[116:119]\n"
"ds_read_b128 a[140:143], v241 offset:10240\n"
"v_mfma_f32_16x16x32_bf16 v[120:123], a[216:219], a[144:147], v[120:123]\n"
"ds_read_b128 a[144:147], v240 offset:12288\n"
"v_mfma_f32_16x16x32_bf16 v[120:123], a[220:223], a[148:151], v[120:123]\n"
"ds_read_b128 a[148:151], v241 offset:12288\n"
"v_mfma_f32_16x16x32_bf16 v[124:127], a[216:219], a[152:155], v[124:127]\n"
"global_load_dwordx4 a[216:219], v246, s[16:17] offset:2048 sc1\n"
"ds_read_b128 a[152:155], v240 offset:14336\n"
"v_mfma_f32_16x16x32_bf16 v[124:127], a[220:223], a[156:159], v[124:127]\n"
"ds_read_b128 a[156:159], v241 offset:14336\n"
"s_waitcnt vmcnt(19) lgkmcnt(14)\n"
"v_mfma_f32_16x16x32_bf16 v[0:3], a[160:163], a[96:99], v[0:3]\n"
"global_load_dwordx4 a[220:223], v246, s[16:17] offset:3072 sc1\n"
"v_mfma_f32_16x16x32_bf16 v[0:3], a[164:167], a[100:103], v[0:3]\n"
"s_waitcnt lgkmcnt(12)\n"
"v_mfma_f32_16x16x32_bf16 v[4:7], a[160:163], a[104:107], v[4:7]\n"
"v_add_u32 v246, 0x2000, v246\n"
"v_mfma_f32_16x16x32_bf16 v[4:7], a[164:167], a[108:111], v[4:7]\n"
"s_waitcnt lgkmcnt(10)\n"
"v_mfma_f32_16x16x32_bf16 v[8:11], a[160:163], a[112:115], v[8:11]\n"
"global_load_dword v239, v250, s[34:35] sc1\n"
"v_mfma_f32_16x16x32_bf16 v[8:11], a[164:167], a[116:119], v[8:11]\n"
"s_waitcnt lgkmcnt(8)\n"
"v_mfma_f32_16x16x32_bf16 v[12:15], a[160:163], a[120:123], v[12:15]\n"
"v_add_f32 v151, v235, v151\n"
"v_mfma_f32_16x16x32_bf16 v[12:15], a[164:167], a[124:127], v[12:15]\n"
"s_waitcnt lgkmcnt(6)\n"
"v_mfma_f32_16x16x32_bf16 v[16:19], a[160:163], a[128:131], v[16:19]\n"
"ds_read_b128 v[224:227], v247 offset:0\n"
"v_mfma_f32_16x16x32_bf16 v[16:19], a[164:167], a[132:135], v[16:19]\n"
"s_waitcnt lgkmcnt(5)\n"
"v_mfma_f32_16x16x32_bf16 v[20:23], a[160:163], a[136:139], v[20:23]\n"
"ds_read_b128 v[228:231], v247 offset:1024\n"
"v_mfma_f32_16x16x32_bf16 v[20:23], a[164:167], a[140:143], v[20:23]\n"
"s_waitcnt lgkmcnt(4)\n"
"v_mfma_f32_16x16x32_bf16 v[24:27], a[160:163], a[144:147], v[24:27]\n"
"v_add_f32 v152, v232, v152\n"
"v_mfma_f32_16x16x32_bf16 v[24:27], a[164:167], a[148:151], v[24:27]\n"
"s_waitcnt lgkmcnt(2)\n"
"v_mfma_f32_16x16x32_bf16 v[28:31], a[160:163], a[152:155], v[28:31]\n"
"global_load_dwordx4 a[160:163], v246, s[16:17] offset:-4096 sc1\n"
"v_mfma_f32_16x16x32_bf16 v[28:31], a[164:167], a[156:159], v[28:31]\n"
"s_waitcnt vmcnt(19)\n"
"v_mfma_f32_16x16x32_bf16 v[32:35], a[168:171], a[96:99], v[32:35]\n"
"global_load_dwordx4 a[164:167], v246, s[16:17] offset:-3072 sc1\n"
"v_mfma_f32_16x16x32_bf16 v[32:35], a[172:175], a[100:103], v[32:35]\n"
"v_add_f32 v153, v233, v153\n"
"v_mfma_f32_16x16x32_bf16 v[36:39], a[168:171], a[104:107], v[36:39]\n"
"v_add_f32 v154, v234, v154\n"
"v_mfma_f32_16x16x32_bf16 v[36:39], a[172:175], a[108:111], v[36:39]\n"
"v_add_f32 v155, v235, v155\n"
"v_mfma_f32_16x16x32_bf16 v[40:43], a[168:171], a[112:115], v[40:43]\n"
"v_add_u32 v249, 0, v248\n"
"v_mfma_f32_16x16x32_bf16 v[40:43], a[172:175], a[116:119], v[40:43]\n"
"s_waitcnt lgkmcnt(1)\n"
"global_store_dwordx4 v249, v[224:227], s[24:25] sc1\n"
"v_mfma_f32_16x16x32_bf16 v[44:47], a[168:171], a[120:123], v[44:47]\n"
"v_add_f32 v156, v232, v156\n"
"v_mfma_f32_16x16x32_bf16 v[44:47], a[172:175], a[124:127], v[44:47]\n"
"v_add_f32 v157, v233, v157\n"
"v_mfma_f32_16x16x32_bf16 v[48:51], a[168:171], a[128:131], v[48:51]\n"
"ds_read_b128 v[224:227], v247 offset:2048\n"
"v_mfma_f32_16x16x32_bf16 v[48:51], a[172:175], a[132:135], v[48:51]\n"
"v_add_f32 v158, v234, v158\n"
"v_mfma_f32_16x16x32_bf16 v[52:55], a[168:171], a[136:139], v[52:55]\n"
"v_add_f32 v159, v235, v159\n"
"v_mfma_f32_16x16x32_bf16 v[52:55], a[172:175], a[140:143], v[52:55]\n"
"s_add_u32 s64, s60, 40960\n"
"s_addc_u32 s65, s61, 0\n"
"v_mfma_f32_16x16x32_bf16 v[56:59], a[168:171], a[144:147], v[56:59]\n"
"v_mfma_f32_16x16x32_bf16 v[56:59], a[172:175], a[148:151], v[56:59]\n"
"v_mfma_f32_16x16x32_bf16 v[60:63], a[168:171], a[152:155], v[60:63]\n"
"global_load_dword a168, v250, s[64:65]\n"
"global_load_dwordx4 a[168:171], v246, s[16:17] offset:-2048 sc1\n"
"v_mfma_f32_16x16x32_bf16 v[60:63], a[172:175], a[156:159], v[60:63]\n"
"s_waitcnt vmcnt(20)\n"
"v_mfma_f32_16x16x32_bf16 v[64:67], a[176:179], a[96:99], v[64:67]\n"
"global_load_dwordx4 a[172:175], v246, s[16:17] offset:-1024 sc1\n"
"v_mfma_f32_16x16x32_bf16 v[64:67], a[180:183], a[100:103], v[64:67]\n"
"v_mfma_f32_16x16x32_bf16 v[68:71], a[176:179], a[104:107], v[68:71]\n"
"v_mfma_f32_16x16x32_bf16 v[68:71], a[180:183], a[108:111], v[68:71]\n"
"v_mfma_f32_16x16x32_bf16 v[72:75], a[176:179], a[112:115], v[72:75]\n"
"v_mfma_f32_16x16x32_bf16 v[72:75], a[180:183], a[116:119], v[72:75]\n"
"v_mfma_f32_16x16x32_bf16 v[76:79], a[176:179], a[120:123], v[76:79]\n"
"v_mfma_f32_16x16x32_bf16 v[76:79], a[180:183], a[124:127], v[76:79]\n"
"v_mfma_f32_16x16x32_bf16 v[80:83], a[176:179], a[128:131], v[80:83]\n"
"v_mfma_f32_16x16x32_bf16 v[80:83], a[180:183], a[132:135], v[80:83]\n"
"v_mfma_f32_16x16x32_bf16 v[84:87], a[176:179], a[136:139], v[84:87]\n"
"v_mfma_f32_16x16x32_bf16 v[84:87], a[180:183], a[140:143], v[84:87]\n"
"v_mfma_f32_16x16x32_bf16 v[88:91], a[176:179], a[144:147], v[88:91]\n"
"v_mfma_f32_16x16x32_bf16 v[88:91], a[180:183], a[148:151], v[88:91]\n"
"v_mfma_f32_16x16x32_bf16 v[92:95], a[176:179], a[152:155], v[92:95]\n"
"global_load_dwordx4 a[176:179], v246, s[16:17] offset:0 sc1\n"
"v_mfma_f32_16x16x32_bf16 v[92:95], a[180:183], a[156:159], v[92:95]\n"
"s_waitcnt vmcnt(20)\n"
"v_mfma_f32_16x16x32_bf16 v[96:99], a[184:187], a[96:99], v[96:99]\n"
"global_load_dwordx4 a[180:183], v246, s[16:17] offset:1024 sc1\n"
"v_mfma_f32_16x16x32_bf16 v[96:99], a[188:191], a[100:103], v[96:99]\n"
"v_mfma_f32_16x16x32_bf16 v[100:103], a[184:187], a[104:107], v[100:103]\n"
"v_mfma_f32_16x16x32_bf16 v[100:103], a[188:191], a[108:111], v[100:103]\n"
"v_mfma_f32_16x16x32_bf16 v[104:107], a[184:187], a[112:115], v[104:107]\n"
"v_mfma_f32_16x16x32_bf16 v[104:107], a[188:191], a[116:119], v[104:107]\n"
"v_mfma_f32_16x16x32_bf16 v[108:111], a[184:187], a[120:123], v[108:111]\n"
"v_mfma_f32_16x16x32_bf16 v[108:111], a[188:191], a[124:127], v[108:111]\n"
"v_mfma_f32_16x16x32_bf16 v[112:115], a[184:187], a[128:131], v[112:115]\n"
"s_waitcnt vmcnt(19)\n"
"v_and_b32 v235, s67, v237\n"
"v_lshlrev_b32 v234, 16, v237\n"
"v_mfma_f32_16x16x32_bf16 v[112:115], a[188:191], a[132:135], v[112:115]\n"
"v_and_b32 v233, s67, v236\n"
"v_lshlrev_b32 v232, 16, v236\n"
"v_mfma_f32_16x16x32_bf16 v[116:119], a[184:187], a[136:139], v[116:119]\n"
"v_lshrrev_b32 v237, 4, v247\n"
"v_mfma_f32_16x16x32_bf16 v[116:119], a[188:191], a[140:143], v[116:119]\n"
"v_and_b32 v237, 0x30, v237\n"
"v_mfma_f32_16x16x32_bf16 v[120:123], a[184:187], a[144:147], v[120:123]\n"
"v_add_u32 v249, 16384, v248\n"
"global_load_dwordx2 v[236:237], v237, s[18:19] offset:192\n"
"v_mfma_f32_16x16x32_bf16 v[120:123], a[188:191], a[148:151], v[120:123]\n"
"s_waitcnt lgkmcnt(1)\n"
"global_store_dwordx4 v249, v[228:231], s[24:25] sc1\n"
"v_mfma_f32_16x16x32_bf16 v[124:127], a[184:187], a[152:155], v[124:127]\n"
"global_load_dwordx4 a[184:187], v246, s[16:17] offset:2048 sc1\n"
"v_mfma_f32_16x16x32_bf16 v[124:127], a[188:191], a[156:159], v[124:127]\n"
"s_waitcnt vmcnt(20)\n"
"v_mfma_f32_16x16x32_bf16 v[128:131], a[192:195], a[96:99], v[128:131]\n"
"global_load_dwordx4 a[188:191], v246, s[16:17] offset:3072 sc1\n"
"v_mfma_f32_16x16x32_bf16 v[128:131], a[196:199], a[100:103], v[128:131]\n"
"ds_read_b128 v[228:231], v247 offset:3072\n"
"v_mfma_f32_16x16x32_bf16 v[132:135], a[192:195], a[104:107], v[132:135]\n"
"v_add_u32 v246, 0x2000, v246\n"
"v_mfma_f32_16x16x32_bf16 v[132:135], a[196:199], a[108:111], v[132:135]\n"
"v_add_f32 v160, v232, v160\n"
"v_mfma_f32_16x16x32_bf16 v[136:139], a[192:195], a[112:115], v[136:139]\n"
"s_waitcnt vmcnt(12)\n"
"v_readfirstlane_b32 s46, v239\n"
"v_mfma_f32_16x16x32_bf16 v[136:139], a[196:199], a[116:119], v[136:139]\n"
"v_add_f32 v161, v233, v161\n"
"v_mfma_f32_16x16x32_bf16 v[140:143], a[192:195], a[120:123], v[140:143]\n"
"v_add_f32 v162, v234, v162\n"
"v_mfma_f32_16x16x32_bf16 v[140:143], a[196:199], a[124:127], v[140:143]\n"
"s_sub_u32 s46, s46, s44\n"
"v_mfma_f32_16x16x32_bf16 v[144:147], a[192:195], a[128:131], v[144:147]\n"
"s_cmp_ge_u32 s46, 16256\n"
"s_cbranch_scc0 .Lslow1_%=\n"
".Lback1_%=:\n"
"v_mfma_f32_16x16x32_bf16 v[144:147], a[196:199], a[132:135], v[144:147]\n"
"s_add_u32 m0, s42, 16384\n"
"v_add_u32 v238, 128, v248\n"
"v_mfma_f32_16x16x32_bf16 v[148:151], a[192:195], a[136:139], v[148:151]\n"
"global_load_lds_dwordx4 v238, s[26:27] offset:0 sc1\n"
"v_mfma_f32_16x16x32_bf16 v[148:151], a[196:199], a[140:143], v[148:151]\n"
"v_add_f32 v163, v235, v163\n"
"v_mfma_f32_16x16x32_bf16 v[152:155], a[192:195], a[144:147], v[152:155]\n"
"v_mfma_f32_16x16x32_bf16 v[152:155], a[196:199], a[148:151], v[152:155]\n"
"v_mfma_f32_16x16x32_bf16 v[156:159], a[192:195], a[152:155], v[156:159]\n"
"global_load_dwordx4 a[192:195], v246, s[16:17] offset:-4096 sc1\n"
"v_mfma_f32_16x16x32_bf16 v[156:159], a[196:199], a[156:159], v[156:159]\n"
"v_mfma_f32_16x16x32_bf16 v[160:163], a[200:203], a[96:99], v[160:163]\n"
"global_load_dwordx4 a[196:199], v246, s[16:17] offset:-3072 sc1\n"
"v_mfma_f32_16x16x32_bf16 v[160:163], a[204:207], a[100:103], v[160:163]\n"
"v_mfma_f32_16x16x32_bf16 v[164:167], a[200:203], a[104:107], v[164:167]\n"
"v_mfma_f32_16x16x32_bf16 v[164:167], a[204:207], a[108:111], v[164:167]\n"
"v_mfma_f32_16x16x32_bf16 v[168:171], a[200:203], a[112:115], v[168:171]\n"
"v_mfma_f32_16x16x32_bf16 v[168:171], a[204:207], a[116:119], v[168:171]\n"
"v_mfma_f32_16x16x32_bf16 v[172:175], a[200:203], a[120:123], v[172:175]\n"
"v_mfma_f32_16x16x32_bf16 v[172:175], a[204:207], a[124:127], v[172:175]\n"
"v_mfma_f32_16x16x32_bf16 v[176:179], a[200:203], a[128:131], v[176:179]\n"
"v_mfma_f32_16x16x32_bf16 v[176:179], a[204:207], a[132:135], v[176:179]\n"
"v_mfma_f32_16x16x32_bf16 v[180:183], a[200:203], a[136:139], v[180:183]\n"
"v_mfma_f32_16x16x32_bf16 v[180:183], a[204:207], a[140:143], v[180:183]\n"
"v_mfma_f32_16x16x32_bf16 v[184:187], a[200:203], a[144:147], v[184:187]\n"
"v_mfma_f32_16x16x32_bf16 v[184:187], a[204:207], a[148:151], v[184:187]\n"
"v_mfma_f32_16x16x32_bf16 v[188:191], a[200:203], a[152:155], v[188:191]\n"
"global_load_dwordx4 a[200:203], v246, s[16:17] offset:-2048 sc1\n"
"v_mfma_f32_16x16x32_bf16 v[188:191], a[204:207], a[156:159], v[188:191]\n"
"v_mfma_f32_16x16x32_bf16 v[192:195], a[208:211], a[96:99], v[192:195]\n"
"global_load_dwordx4 a[204:207], v246, s[16:17] offset:-1024 sc1\n"
"v_mfma_f32_16x16x32_bf16 v[192:195], a[212:215], a[100:103], v[192:195]\n"
"v_mfma_f32_16x16x32_bf16 v[196:199], a[208:211], a[104:107], v[196:199]\n"
"v_mfma_f32_16x16x32_bf16 v[196:199], a[212:215], a[108:111], v[196:199]\n"
"v_mfma_f32_16x16x32_bf16 v[200:203], a[208:211], a[112:115], v[200:203]\n"
"v_add_u32 v249, 32768, v248\n"
"v_mfma_f32_16x16x32_bf16 v[200:203], a[212:215], a[116:119], v[200:203]\n"
"s_waitcnt lgkmcnt(1)\n"
"global_store_dwordx4 v249, v[224:227], s[24:25] sc1\n"
"v_mfma_f32_16x16x32_bf16 v[204:207], a[208:211], a[120:123], v[204:207]\n"
"v_mfma_f32_16x16x32_bf16 v[204:207], a[212:215], a[124:127], v[204:207]\n"
"v_add_u32 v238, s66, v238\n"
"v_mfma_f32_16x16x32_bf16 v[208:211], a[208:211], a[128:131], v[208:211]\n"
"global_load_lds_dwordx4 v238, s[26:27] offset:1024 sc1\n"
"v_mfma_f32_16x16x32_bf16 v[208:211], a[212:215], a[132:135], v[208:211]\n"
"v_add_f32 v164, v232, v164\n"
"v_mfma_f32_16x16x32_bf16 v[212:215], a[208:211], a[136:139], v[212:215]\n"
"v_add_f32 v165, v233, v165\n"
"v_mfma_f32_16x16x32_bf16 v[212:215], a[212:215], a[140:143], v[212:215]\n"
"v_add_f32 v166, v234, v166\n"
"v_mfma_f32_16x16x32_bf16 v[216:219], a[208:211], a[144:147], v[216:219]\n"
"v_add_f32 v167, v235, v167\n"
"v_mfma_f32_16x16x32_bf16 v[216:219], a[212:215], a[148:151], v[216:219]\n"
"v_add_f32 v168, v232, v168\n"
"v_mfma_f32_16x16x32_bf16 v[220:223], a[208:211], a[152:155], v[220:223]\n"
"global_load_dwordx4 a[208:211], v246, s[16:17] offset:0 sc1\n"
"v_mfma_f32_16x16x32_bf16 v[220:223], a[212:215], a[156:159], v[220:223]\n"
"v_mfma_f32_16x16x32_bf16 a[0:3], a[216:219], a[96:99], a[0:3]\n"
"global_load_dwordx4 a[212:215], v246, s[16:17] offset:1024 sc1\n"
"ds_read_b128 a[96:99], v240 offset:32768\n"
"v_mfma_f32_16x16x32_bf16 a[0:3], a[220:223], a[100:103], a[0:3]\n"
"ds_read_b128 a[100:103], v241 offset:32768\n"
"v_mfma_f32_16x16x32_bf16 a[4:7], a[216:219], a[104:107], a[4:7]\n"
"ds_read_b128 a[104:107], v240 offset:34816\n"
"v_mfma_f32_16x16x32_bf16 a[4:7], a[220:223], a[108:111], a[4:7]\n"
"ds_read_b128 a[108:111], v241 offset:34816\n"
"v_mfma_f32_16x16x32_bf16 a[8:11], a[216:219], a[112:115], a[8:11]\n"
"ds_read_b128 a[112:115], v240 offset:36864\n"
"v_mfma_f32_16x16x32_bf16 a[8:11], a[220:223], a[116:119], a[8:11]\n"
"ds_read_b128 a[116:119], v241 offset:36864\n"
"v_mfma_f32_16x16x32_bf16 a[12:15], a[216:219], a[120:123], a[12:15]\n"
"ds_read_b128 a[120:123], v240 offset:38912\n"
"v_mfma_f32_16x16x32_bf16 a[12:15], a[220:223], a[124:127], a[12:15]\n"
"ds_read_b128 a[124:127], v241 offset:38912\n"
"v_mfma_f32_16x16x32_bf16 a[16:19], a[216:219], a[128:131], a[16:19]\n"
"ds_read_b128 a[128:131], v240 offset:40960\n"
"v_mfma_f32_16x16x32_bf16 a[16:19], a[220:223], a[132:135], a[16:19]\n"
"ds_read_b128 a[132:135], v241 offset:40960\n"
"v_mfma_f32_16x16x32_bf16 a[20:23], a[216:219], a[136:139], a[20:23]\n"
"ds_read_b128 a[136:139], v240 offset:43008\n"
"v_mfma_f32_16x16x32_bf16 a[20:23], a[220:223], a[140:143], a[20:23]\n"
"ds_read_b128 a[140:143], v241 offset:43008\n"
"v_mfma_f32_16x16x32_bf16 a[24:27], a[216:219], a[144:147], a[24:27]\n"
"ds_read_b128 a[144:147], v240 offset:45056\n"
"v_mfma_f32_16x16x32_bf16 a[24:27], a[220:223], a[148:151], a[24:27]\n"
"ds_read_b128 a[148:151], v241 offset:45056\n"
"v_mfma_f32_16x16x32_bf16 a[28:31], a[216:219], a[152:155], a[28:31]\n"
"global_load_dwordx4 a[216:219], v246, s[16:17] offset:2048 sc1\n"
"ds_read_b128 a[152:155], v240 offset:47104\n"
"v_mfma_f32_16x16x32_bf16 a[28:31], a[220:223], a[156:159], a[28:31]\n"
"ds_read_b128 a[156:159], v241 offset:47104\n"
"s_waitcnt vmcnt(20) lgkmcnt(14)\n"
"v_mfma_f32_16x16x32_bf16 v[0:3], a[160:163], a[96:99], v[0:3]\n"
"global_load_dwordx4 a[220:223], v246, s[16:17] offset:3072 sc1\n"
"v_mfma_f32_16x16x32_bf16 v[0:3], a[164:167], a[100:103], v[0:3]\n"
"s_waitcnt lgkmcnt(12)\n"
"v_mfma_f32_16x16x32_bf16 v[4:7], a[160:163], a[104:107], v[4:7]\n"
"v_add_u32 v246, 0x2000, v246\n"
"v_mfma_f32_16x16x32_bf16 v[4:7], a[164:167], a[108:111], v[4:7]\n"
"s_waitcnt lgkmcnt(10)\n"
"v_mfma_f32_16x16x32_bf16 v[8:11], a[160:163], a[112:115], v[8:11]\n"
"v_add_f32 v169, v233, v169\n"
"v_mfma_f32_16x16x32_bf16 v[8:11], a[164:167], a[116:119], v[8:11]\n"
"s_waitcnt lgkmcnt(8)\n"
"v_mfma_f32_16x16x32_bf16 v[12:15], a[160:163], a[120:123], v[12:15]\n"
"v_add_u32 v238, s66, v238\n"
"v_mfma_f32_16x16x32_bf16 v[12:15], a[164:167], a[124:127], v[12:15]\n"
"s_waitcnt lgkmcnt(6)\n"
"v_mfma_f32_16x16x32_bf16 v[16:19], a[160:163], a[128:131], v[16:19]\n"
"global_load_lds_dwordx4 v238, s[26:27] offset:2048 sc1\n"
"v_mfma_f32_16x16x32_bf16 v[16:19], a[164:167], a[132:135], v[16:19]\n"
"s_waitcnt lgkmcnt(4)\n"
"v_mfma_f32_16x16x32_bf16 v[20:23], a[160:163], a[136:139], v[20:23]\n"
"s_mov_b64 exec, 1\n"
"global_atomic_add v250, v252, s[30:31] offset:0 sc1\n"
"s_mov_b64 exec, -1\n"
"v_mfma_f32_16x16x32_bf16 v[20:23], a[164:167], a[140:143], v[20:23]\n"
"s_waitcnt lgkmcnt(2)\n"
"v_mfma_f32_16x16x32_bf16 v[24:27], a[160:163], a[144:147], v[24:27]\n"
"v_add_u32 v249, 49152, v248\n"
"v_mfma_f32_16x16x32_bf16 v[24:27], a[164:167], a[148:151], v[24:27]\n"
"s_waitcnt lgkmcnt(0)\n"
"v_mfma_f32_16x16x32_bf16 v[28:31], a[160:163], a[152:155], v[28:31]\n"
"global_load_dwordx4 a[160:163], v246, s[16:17] offset:-4096 sc1\n"
"v_mfma_f32_16x16x32_bf16 v[28:31], a[164:167], a[156:159], v[28:31]\n"
"s_waitcnt vmcnt(20)\n"
"v_mfma_f32_16x16x32_bf16 v[32:35], a[168:171], a[96:99], v[32:35]\n"
"global_load_dwordx4 a[164:167], v246, s[16:17] offset:-3072 sc1\n"
"v_mfma_f32_16x16x32_bf16 v[32:35], a[172:175], a[100:103], v[32:35]\n"
"global_store_dwordx4 v249, v[228:231], s[24:25] sc1\n"
"v_mfma_f32_16x16x32_bf16 v[36:39], a[168:171], a[104:107], v[36:39]\n"
"v_add_f32 v170, v234, v170\n"
"v_mfma_f32_16x16x32_bf16 v[36:39], a[172:175], a[108:111], v[36:39]\n"
"v_add_f32 v171, v235, v171\n"
"v_mfma_f32_16x16x32_bf16 v[40:43], a[168:171], a[112:115], v[40:43]\n"
"v_add_f32 v172, v232, v172\n"
"v_mfma_f32_16x16x32_bf16 v[40:43], a[172:175], a[116:119], v[40:43]\n"
"v_add_f32 v173, v233, v173\n"
"v_mfma_f32_16x16x32_bf16 v[44:47], a[168:171], a[120:123], v[44:47]\n"
"v_add_f32 v174, v234, v174\n"
"v_mfma_f32_16x16x32_bf16 v[44:47], a[172:175], a[124:127], v[44:47]\n"
"v_add_f32 v175, v235, v175\n"
"v_mfma_f32_16x16x32_bf16 v[48:51], a[168:171], a[128:131], v[48:51]\n"
"v_add_f32 v176, v232, v176\n"
"v_mfma_f32_16x16x32_bf16 v[48:51], a[172:175], a[132:135], v[48:51]\n"
"v_add_f32 v177, v233, v177\n"
"v_mfma_f32_16x16x32_bf16 v[52:55], a[168:171], a[136:139], v[52:55]\n"
"v_add_f32 v178, v234, v178\n"
"v_mfma_f32_16x16x32_bf16 v[52:55], a[172:175], a[140:143], v[52:55]\n"
"v_add_f32 v179, v235, v179\n"
"v_mfma_f32_16x16x32_bf16 v[56:59], a[168:171], a[144:147], v[56:59]\n"
"v_add_f32 v180, v232, v180\n"
"v_mfma_f32_16x16x32_bf16 v[56:59], a[172:175], a[148:151], v[56:59]\n"
"v_add_f32 v181, v233, v181\n"
"v_mfma_f32_16x16x32_bf16 v[60:63], a[168:171], a[152:155], v[60:63]\n"
"global_load_dwordx4 a[168:171], v246, s[16:17] offset:-2048 sc1\n"
"v_mfma_f32_16x16x32_bf16 v[60:63], a[172:175], a[156:159], v[60:63]\n"
"s_waitcnt vmcnt(21)\n"
"v_mfma_f32_16x16x32_bf16 v[64:67], a[176:179], a[96:99], v[64:67]\n"
"global_load_dwordx4 a[172:175], v246, s[16:17] offset:-1024 sc1\n"
"v_mfma_f32_16x16x32_bf16 v[64:67], a[180:183], a[100:103], v[64:67]\n"
"v_add_f32 v182, v234, v182\n"
"v_mfma_f32_16x16x32_bf16 v[68:71], a[176:179], a[104:107], v[68:71]\n"
"v_add_f32 v183, v235, v183\n"
"v_mfma_f32_16x16x32_bf16 v[68:71], a[180:183], a[108:111], v[68:71]\n"
"v_add_f32 v184, v232, v184\n"
"v_mfma_f32_16x16x32_bf16 v[72:75], a[176:179], a[112:115], v[72:75]\n"
"v_add_f32 v185, v233, v185\n"
"v_mfma_f32_16x16x32_bf16 v[72:75], a[180:183], a[116:119], v[72:75]\n"
"v_add_u32 v238, s66, v238\n"
"v_mfma_f32_16x16x32_bf16 v[76:79], a[176:179], a[120:123], v[76:79]\n"
"global_load_lds_dwordx4 v238, s[26:27] offset:3072 sc1\n"
"v_mfma_f32_16x16x32_bf16 v[76:79], a[180:183], a[124:127], v[76:79]\n"
"v_add_f32 v186, v234, v186\n"
"v_mfma_f32_16x16x32_bf16 v[80:83], a[176:179], a[128:131], v[80:83]\n"
"ds_read_b128 v[224:227], v247 offset:32768\n"
"v_mfma_f32_16x16x32_bf16 v[80:83], a[180:183], a[132:135], v[80:83]\n"
"v_add_f32 v187, v235, v187\n"
"v_mfma_f32_16x16x32_bf16 v[84:87], a[176:179], a[136:139], v[84:87]\n"
"ds_read_b128 v[228:231], v247 offset:33792\n"
"v_mfma_f32_16x16x32_bf16 v[84:87], a[180:183], a[140:143], v[84:87]\n"
"v_add_f32 v188, v232, v188\n"
"v_mfma_f32_16x16x32_bf16 v[88:91], a[176:179], a[144:147], v[88:91]\n"
"v_add_f32 v189, v233, v189\n"
"v_mfma_f32_16x16x32_bf16 v[88:91], a[180:183], a[148:151], v[88:91]\n"
"v_add_f32 v190, v234, v190\n"
"v_mfma_f32_16x16x32_bf16 v[92:95], a[176:179], a[152:155], v[92:95]\n"
"global_load_dwordx4 a[176:179], v246, s[16:17] offset:0 sc1\n"
"v_mfma_f32_16x16x32_bf16 v[92:95], a[180:183], a[156:159], v[92:95]\n"
"s_waitcnt vmcnt(20)\n"
"v_mfma_f32_16x16x32_bf16 v[96:99], a[184:187], a[96:99], v[96:99]\n"
"global_load_dwordx4 a[180:183], v246, s[16:17] offset:1024 sc1\n"
"v_mfma_f32_16x16x32_bf16 v[96:99], a[188:191], a[100:103], v[96:99]\n"
"v_add_f32 v191, v235, v191\n"
"v_mfma_f32_16x16x32_bf16 v[100:103], a[184:187], a[104:107], v[100:103]\n"
"v_mfma_f32_16x16x32_bf16 v[100:103], a[188:191], a[108:111], v[100:103]\n"
"v_and_b32 v235, s67, v237\n"
"v_lshlrev_b32 v234, 16, v237\n"
"v_mfma_f32_16x16x32_bf16 v[104:107], a[184:187], a[112:115], v[104:107]\n"
"v_add_u32 v249, 256, v248\n"
"v_and_b32 v233, s67, v236\n"
"v_mfma_f32_16x16x32_bf16 v[104:107], a[188:191], a[116:119], v[104:107]\n"
"s_waitcnt lgkmcnt(1)\n"
"global_store_dwordx4 v249, v[224:227], s[24:25] sc1\n"
"v_mfma_f32_16x16x32_bf16 v[108:111], a[184:187], a[120:123], v[108:111]\n"
"v_lshlrev_b32 v232, 16, v236\n"
"v_mfma_f32_16x16x32_bf16 v[108:111], a[188:191], a[124:127], v[108:111]\n"
"v_lshrrev_b32 v237, 4, v247\n"
"v_mfma_f32_16x16x32_bf16 v[112:115], a[184:187], a[128:131], v[112:115]\n"
"ds_read_b128 v[224:227], v247 offset:34816\n"
"v_and_b32 v237, 0x30, v237\n"
"v_mfma_f32_16x16x32_bf16 v[112:115], a[188:191], a[132:135], v[112:115]\n"
"global_load_dwordx2 v[236:237], v237, s[18:19] offset:0\n"
"v_mfma_f32_16x16x32_bf16 v[116:119], a[184:187], a[136:139], v[116:119]\n"
"v_add_f32 v192, v232, v192\n"
"v_mfma_f32_16x16x32_bf16 v[116:119], a[188:191], a[140:143], v[116:119]\n"
"v_add_f32 v193, v233, v193\n"
"v_mfma_f32_16x16x32_bf16 v[120:123], a[184:187], a[144:147], v[120:123]\n"
"v_add_f32 v194, v234, v194\n"
"v_mfma_f32_16x16x32_bf16 v[120:123], a[188:191], a[148:151], v[120:123]\n"
"v_add_f32 v195, v235, v195\n"
"v_mfma_f32_16x16x32_bf16 v[124:127], a[184:187], a[152:155], v[124:127]\n"
"global_load_dwordx4 a[184:187], v246, s[16:17] offset:2048 sc1\n"
"v_mfma_f32_16x16x32_bf16 v[124:127], a[188:191], a[156:159], v[124:127]\n"
"s_waitcnt vmcnt(21)\n"
"v_mfma_f32_16x16x32_bf16 v[128:131], a[192:195], a[96:99], v[128:131]\n"
"global_load_dwordx4 a[188:191], v246, s[16:17] offset:3072 sc1\n"
"v_mfma_f32_16x16x32_bf16 v[128:131], a[196:199], a[100:103], v[128:131]\n"
"v_add_f32 v196, v232, v196\n"
"v_mfma_f32_16x16x32_bf16 v[132:135], a[192:195], a[104:107], v[132:135]\n"
"v_add_u32 v246, 0x2000, v246\n"
"v_mfma_f32_16x16x32_bf16 v[132:135], a[196:199], a[108:111], v[132:135]\n"
"s_add_u32 m0, s42, 49152\n"
"v_add_u32 v238, 384, v248\n"
"v_mfma_f32_16x16x32_bf16 v[136:139], a[192:195], a[112:115], v[136:139]\n"
"global_load_lds_dwordx4 v238, s[26:27] offset:0 sc1\n"
"v_mfma_f32_16x16x32_bf16 v[136:139], a[196:199], a[116:119], v[136:139]\n"
"v_add_f32 v197, v233, v197\n"
"v_mfma_f32_16x16x32_bf16 v[140:143], a[192:195], a[120:123], v[140:143]\n"
"v_add_f32 v198, v234, v198\n"
"v_mfma_f32_16x16x32_bf16 v[140:143], a[196:199], a[124:127], v[140:143]\n"
"v_add_f32 v199, v235, v199\n"
"v_mfma_f32_16x16x32_bf16 v[144:147], a[192:195], a[128:131], v[144:147]\n"
".Lpg10_%=:\n"
"s_cbranch_execz .Lpg14_%=\n"
"v_add_f32 v200, v232, v200\n"
"v_mfma_f32_16x16x32_bf16 v[144:147], a[196:199], a[132:135], v[144:147]\n"
"v_add_f32 v201, v233, v201\n"
"v_mfma_f32_16x16x32_bf16 v[148:151], a[192:195], a[136:139], v[148:151]\n"
"v_add_f32 v202, v234, v202\n"
"v_mfma_f32_16x16x32_bf16 v[148:151], a[196:199], a[140:143], v[148:151]\n"
"v_add_f32 v203, v235, v203\n"
"v_mfma_f32_16x16x32_bf16 v[152:155], a[192:195], a[144:147], v[152:155]\n"
"v_add_f32 v204, v232, v204\n"
"v_mfma_f32_16x16x32_bf16 v[152:155], a[196:199], a[148:151], v[152:155]\n"
"v_add_f32 v205, v233, v205\n"
"v_mfma_f32_16x16x32_bf16 v[156:159], a[192:195], a[152:155], v[156:159]\n"
"global_load_dwordx4 a[192:195], v246, s[16:17] offset:-4096 sc1\n"
"v_mfma_f32_16x16x32_bf16 v[156:159], a[196:199], a[156:159], v[156:159]\n"
"s_waitcnt vmcnt(22)\n"
"v_mfma_f32_16x16x32_bf16 v[160:163], a[200:203], a[96:99], v[160:163]\n"
"global_load_dwordx4 a[196:199], v246, s[16:17] offset:-3072 sc1\n"
"v_mfma_f32_16x16x32_bf16 v[160:163], a[204:207], a[100:103], v[160:163]\n"
"v_add_f32 v206, v234, v206\n"
"v_mfma_f32_16x16x32_bf16 v[164:167], a[200:203], a[104:107], v[164:167]\n"
"v_add_f32 v207, v235, v207\n"
"v_mfma_f32_16x16x32_bf16 v[164:167], a[204:207], a[108:111], v[164:167]\n"
"v_add_f32 v208, v232, v208\n"
"v_mfma_f32_16x16x32_bf16 v[168:171], a[200:203], a[112:115], v[168:171]\n"
"v_add_f32 v209, v233, v209\n"
"v_mfma_f32_16x16x32_bf16 v[168:171], a[204:207], a[116:119], v[168:171]\n"
"v_add_f32 v210, v234, v210\n"
"v_mfma_f32_16x16x32_bf16 v[172:175], a[200:203], a[120:123], v[172:175]\n"
"v_add_f32 v211, v235, v211\n"
"v_mfma_f32_16x16x32_bf16 v[172:175], a[204:207], a[124:127], v[172:175]\n"
"v_add_f32 v212, v232, v212\n"
"v_mfma_f32_16x16x32_bf16 v[176:179], a[200:203], a[128:131], v[176:179]\n"
"v_add_f32 v213, v233, v213\n"
"v_mfma_f32_16x16x32_bf16 v[176:179], a[204:207], a[132:135], v[176:179]\n"
"v_add_f32 v214, v234, v214\n"
"v_mfma_f32_16x16x32_bf16 v[180:183], a[200:203], a[136:139], v[180:183]\n"
"v_add_f32 v215, v235, v215\n"
"v_mfma_f32_16x16x32_bf16 v[180:183], a[204:207], a[140:143], v[180:183]\n"
"v_add_f32 v216, v232, v216\n"
"v_mfma_f32_16x16x32_bf16 v[184:187], a[200:203], a[144:147], v[184:187]\n"
"v_add_u32 v249, 16640, v248\n"
"v_mfma_f32_16x16x32_bf16 v[184:187], a[204:207], a[148:151], v[184:187]\n"
"s_waitcnt lgkmcnt(1)\n"
"global_store_dwordx4 v249, v[228:231], s[24:25] sc1\n"
"v_mfma_f32_16x16x32_bf16 v[188:191], a[200:203], a[152:155], v[188:191]\n"
"global_load_dwordx4 a[200:203], v246, s[16:17] offset:-2048 sc1\n"
"v_mfma_f32_16x16x32_bf16 v[188:191], a[204:207], a[156:159], v[188:191]\n"
"s_waitcnt vmcnt(21)\n"
"v_mfma_f32_16x16x32_bf16 v[192:195], a[208:211], a[96:99], v[192:195]\n"
"global_load_dwordx4 a[204:207], v246, s[16:17] offset:-1024 sc1\n"
"v_mfma_f32_16x16x32_bf16 v[192:195], a[212:215], a[100:103], v[192:195]\n"
"ds_read_b128 v[228:231], v247 offset:35840\n"
"v_mfma_f32_16x16x32_bf16 v[196:199], a[208:211], a[104:107], v[196:199]\n"
"v_add_u32 v238, s66, v238\n"
"v_mfma_f32_16x16x32_bf16 v[196:199], a[212:215], a[108:111], v[196:199]\n"
"global_load_lds_dwordx4 v238, s[26:27] offset:1024 sc1\n"
"v_mfma_f32_16x16x32_bf16 v[200:203], a[208:211], a[112:115], v[200:203]\n"
"v_mfma_f32_16x16x32_bf16 v[200:203], a[212:215], a[116:119], v[200:203]\n"
"v_mfma_f32_16x16x32_bf16 v[204:207], a[208:211], a[120:123], v[204:207]\n"
"v_mfma_f32_16x16x32_bf16 v[204:207], a[212:215], a[124:127], v[204:207]\n"
"v_mfma_f32_16x16x32_bf16 v[208:211], a[208:211], a[128:131], v[208:211]\n"
"v_mfma_f32_16x16x32_bf16 v[208:211], a[212:215], a[132:135], v[208:211]\n"
"v_mfma_f32_16x16x32_bf16 v[212:215], a[208:211], a[136:139], v[212:215]\n"
"v_mfma_f32_16x16x32_bf16 v[212:215], a[212:215], a[140:143], v[212:215]\n"
"v_mfma_f32_16x16x32_bf16 v[216:219], a[208:211], a[144:147], v[216:219]\n"
"v_mfma_f32_16x16x32_bf16 v[216:219], a[212:215], a[148:151], v[216:219]\n"
"v_mfma_f32_16x16x32_bf16 v[220:223], a[208:211], a[152:155], v[220:223]\n"
"global_load_dwordx4 a[208:211], v246, s[16:17] offset:0 sc1\n"
"v_mfma_f32_16x16x32_bf16 v[220:223], a[212:215], a[156:159], v[220:223]\n"
"s_waitcnt vmcnt(22)\n"
"v_mfma_f32_16x16x32_bf16 a[0:3], a[216:219], a[96:99], a[0:3]\n"
"global_load_dwordx4 a[212:215], v246, s[16:17] offset:1024 sc1\n"
"ds_read_b128 a[96:99], v242 offset:0\n"
"v_mfma_f32_16x16x32_bf16 a[0:3], a[220:223], a[100:103], a[0:3]\n"
"ds_read_b128 a[100:103], v243 offset:0\n"
"v_mfma_f32_16x16x32_bf16 a[4:7], a[216:219], a[104:107], a[4:7]\n"
"ds_read_b128 a[104:107], v242 offset:2048\n"
"v_mfma_f32_16x16x32_bf16 a[4:7], a[220:223], a[108:111], a[4:7]\n"
"ds_read_b128 a[108:111], v243 offset:2048\n"
"v_mfma_f32_16x16x32_bf16 a[8:11], a[216:219], a[112:115], a[8:11]\n"
"ds_read_b128 a[112:115], v242 offset:4096\n"
"v_mfma_f32_16x16x32_bf16 a[8:11], a[220:223], a[116:119], a[8:11]\n"
"ds_read_b128 a[116:119], v243 offset:4096\n"
"v_mfma_f32_16x16x32_bf16 a[12:15], a[216:219], a[120:123], a[12:15]\n"
"ds_read_b128 a[120:123], v242 offset:6144\n"
"v_mfma_f32_16x16x32_bf16 a[12:15], a[220:223], a[124:127], a[12:15]\n"
"ds_read_b128 a[124:127], v243 offset:6144\n"
"v_mfma_f32_16x16x32_bf16 a[16:19], a[216:219], a[128:131], a[16:19]\n"
"ds_read_b128 a[128:131], v242 offset:8192\n"
"v_mfma_f32_16x16x32_bf16 a[16:19], a[220:223], a[132:135], a[16:19]\n"
"ds_read_b128 a[132:135], v243 offset:8192\n"
"v_mfma_f32_16x16x32_bf16 a[20:23], a[216:219], a[136:139], a[20:23]\n"
"ds_read_b128 a[136:139], v242 offset:10240\n"
"v_mfma_f32_16x16x32_bf16 a[20:23], a[220:223], a[140:143], a[20:23]\n"
"ds_read_b128 a[140:143], v243 offset:10240\n"
"v_mfma_f32_16x16x32_bf16 a[24:27], a[216:219], a[144:147], a[24:27]\n"
"ds_read_b128 a[144:147], v242 offset:12288\n"
"v_mfma_f32_16x16x32_bf16 a[24:27], a[220:223], a[148:151], a[24:27]\n"
"ds_read_b128 a[148:151], v243 offset:12288\n"
"v_mfma_f32_16x16x32_bf16 a[28:31], a[216:219], a[152:155], a[28:31]\n"
"global_load_dwordx4 a[216:219], v246, s[16:17] offset:2048 sc1\n"
"ds_read_b128 a[152:155], v242 offset:14336\n"
"v_mfma_f32_16x16x32_bf16 a[28:31], a[220:223], a[156:159], a[28:31]\n"
"ds_read_b128 a[156:159], v243 offset:14336\n"
"s_waitcnt vmcnt(20) lgkmcnt(14)\n"
"v_mfma_f32_16x16x32_bf16 v[0:3], a[160:163], a[96:99], v[0:3]\n"
"global_load_dwordx4 a[220:223], v246, s[16:17] offset:3072 sc1\n"
"v_mfma_f32_16x16x32_bf16 v[0:3], a[164:167], a[100:103], v[0:3]\n"
"s_waitcnt lgkmcnt(12)\n"
"v_mfma_f32_16x16x32_bf16 v[4:7], a[160:163], a[104:107], v[4:7]\n"
"v_add_u32 v246, 0x2000, v246\n"
"v_mfma_f32_16x16x32_bf16 v[4:7], a[164:167], a[108:111], v[4:7]\n"
"s_waitcnt lgkmcnt(10)\n"
"v_mfma_f32_16x16x32_bf16 v[8:11], a[160:163], a[112:115], v[8:11]\n"
"v_add_u32 v249, 33024, v248\n"
"v_mfma_f32_16x16x32_bf16 v[8:11], a[164:167], a[116:119], v[8:11]\n"
"s_waitcnt lgkmcnt(8)\n"
"v_mfma_f32_16x16x32_bf16 v[12:15], a[160:163], a[120:123], v[12:15]\n"
"global_store_dwordx4 v249, v[224:227], s[24:25] sc1\n"
"v_mfma_f32_16x16x32_bf16 v[12:15], a[164:167], a[124:127], v[12:15]\n"
"s_waitcnt lgkmcnt(6)\n"
"v_mfma_f32_16x16x32_bf16 v[16:19], a[160:163], a[128:131], v[16:19]\n"
"v_add_u32 v238, s66, v238\n"
"v_mfma_f32_16x16x32_bf16 v[16:19], a[164:167], a[132:135], v[16:19]\n"
"s_waitcnt lgkmcnt(4)\n"
"v_mfma_f32_16x16x32_bf16 v[20:23], a[160:163], a[136:139], v[20:23]\n"
"global_load_lds_dwordx4 v238, s[26:27] offset:2048 sc1\n"
"v_mfma_f32_16x16x32_bf16 v[20:23], a[164:167], a[140:143], v[20:23]\n"
"s_waitcnt lgkmcnt(2)\n"
"v_mfma_f32_16x16x32_bf16 v[24:27], a[160:163], a[144:147], v[24:27]\n"
"global_load_dword v239, v250, s[34:35] sc1\n"
"v_mfma_f32_16x16x32_bf16 v[24:27], a[164:167], a[148:151], v[24:27]\n"
"s_waitcnt lgkmcnt(0)\n"
"v_mfma_f32_16x16x32_bf16 v[28:31], a[160:163], a[152:155], v[28:31]\n"
"global_load_dwordx4 a[160:163], v246, s[16:17] offset:-4096 sc1\n"
"v_mfma_f32_16x16x32_bf16 v[28:31], a[164:167], a[156:159], v[28:31]\n"
"s_waitcnt vmcnt(22)\n"
"v_mfma_f32_16x16x32_bf16 v[32:35], a[168:171], a[96:99], v[32:35]\n"
"global_load_dwordx4 a[164:167], v246, s[16:17] offset:-3072 sc1\n"
"v_mfma_f32_16x16x32_bf16 v[32:35], a[172:175], a[100:103], v[32:35]\n"
"v_add_f32 v217, v233, v217\n"
"v_mfma_f32_16x16x32_bf16 v[36:39], a[168:171], a[104:107], v[36:39]\n"
"v_add_f32 v218, v234, v218\n"
"v_mfma_f32_16x16x32_bf16 v[36:39], a[172:175], a[108:111], v[36:39]\n"
"v_add_f32 v219, v235, v219\n"
"v_mfma_f32_16x16x32_bf16 v[40:43], a[168:171], a[112:115], v[40:43]\n"
"v_add_f32 v220, v232, v220\n"
"v_mfma_f32_16x16x32_bf16 v[40:43], a[172:175], a[116:119], v[40:43]\n"
"v_add_f32 v221, v233, v221\n"
"v_mfma_f32_16x16x32_bf16 v[44:47], a[168:171], a[120:123], v[44:47]\n"
"v_add_f32 v222, v234, v222\n"
"v_mfma_f32_16x16x32_bf16 v[44:47], a[172:175], a[124:127], v[44:47]\n"
"v_add_f32 v223, v235, v223\n"
"v_mfma_f32_16x16x32_bf16 v[48:51], a[168:171], a[128:131], v[48:51]\n"
"s_add_u32 s64, s60, 45056\n"
"s_addc_u32 s65, s61, 0\n"
"v_mfma_f32_16x16x32_bf16 v[48:51], a[172:175], a[132:135], v[48:51]\n"
"v_mfma_f32_16x16x32_bf16 v[52:55], a[168:171], a[136:139], v[52:55]\n"
"v_mfma_f32_16x16x32_bf16 v[52:55], a[172:175], a[140:143], v[52:55]\n"
"v_mfma_f32_16x16x32_bf16 v[56:59], a[168:171], a[144:147], v[56:59]\n"
"v_mfma_f32_16x16x32_bf16 v[56:59], a[172:175], a[148:151], v[56:59]\n"
"v_mfma_f32_16x16x32_bf16 v[60:63], a[168:171], a[152:155], v[60:63]\n"
"global_load_dword a168, v250, s[64:65]\n"
"global_load_dwordx4 a[168:171], v246, s[16:17] offset:-2048 sc1\n"
"v_mfma_f32_16x16x32_bf16 v[60:63], a[172:175], a[156:159], v[60:63]\n"
"s_waitcnt vmcnt(22)\n"
"v_mfma_f32_16x16x32_bf16 v[64:67], a[176:179], a[96:99], v[64:67]\n"
"global_load_dwordx4 a[172:175], v246, s[16:17] offset:-1024 sc1\n"
"v_mfma_f32_16x16x32_bf16 v[64:67], a[180:183], a[100:103], v[64:67]\n"
"v_mfma_f32_16x16x32_bf16 v[68:71], a[176:179], a[104:107], v[68:71]\n"
"v_mfma_f32_16x16x32_bf16 v[68:71], a[180:183], a[108:111], v[68:71]\n"
"v_mfma_f32_16x16x32_bf16 v[72:75], a[176:179], a[112:115], v[72:75]\n"
"v_mfma_f32_16x16x32_bf16 v[72:75], a[180:183], a[116:119], v[72:75]\n"
"v_mfma_f32_16x16x32_bf16 v[76:79], a[176:179], a[120:123], v[76:79]\n"
"v_mfma_f32_16x16x32_bf16 v[76:79], a[180:183], a[124:127], v[76:79]\n"
"v_add_u32 v238, s66, v238\n"
"v_mfma_f32_16x16x32_bf16 v[80:83], a[176:179], a[128:131], v[80:83]\n"
"global_load_lds_dwordx4 v238, s[26:27] offset:3072 sc1\n"
"v_mfma_f32_16x16x32_bf16 v[80:83], a[180:183], a[132:135], v[80:83]\n"
"v_mfma_f32_16x16x32_bf16 v[84:87], a[176:179], a[136:139], v[84:87]\n"
"s_waitcnt vmcnt(22)\n"
"v_and_b32 v235, s67, v237\n"
"v_lshlrev_b32 v234, 16, v237\n"
"v_mfma_f32_16x16x32_bf16 v[84:87], a[180:183], a[140:143], v[84:87]\n"
"v_and_b32 v233, s67, v236\n"
"v_lshlrev_b32 v232, 16, v236\n"
"v_mfma_f32_16x16x32_bf16 v[88:91], a[176:179], a[144:147], v[88:91]\n"
"v_add_u32 v249, 49408, v248\n"
"v_lshrrev_b32 v237, 4, v247\n"
"v_mfma_f32_16x16x32_bf16 v[88:91], a[180:183], a[148:151], v[88:91]\n"
"global_store_dwordx4 v249, v[228:231], s[24:25] sc1\n"
"v_mfma_f32_16x16x32_bf16 v[92:95], a[176:179], a[152:155], v[92:95]\n"
"global_load_dwordx4 a[176:179], v246, s[16:17] offset:0 sc1\n"
"v_and_b32 v237, 0x30, v237\n"
"v_mfma_f32_16x16x32_bf16 v[92:95], a[180:183], a[156:159], v[92:95]\n"
"global_load_dwordx2 v[236:237], v237, s[18:19] offset:8\n"
"s_waitcnt vmcnt(23)\n"
"v_mfma_f32_16x16x32_bf16 v[96:99], a[184:187], a[96:99], v[96:99]\n"
"global_load_dwordx4 a[180:183], v246, s[16:17] offset:1024 sc1\n"
"v_mfma_f32_16x16x32_bf16 v[96:99], a[188:191], a[100:103], v[96:99]\n"
"v_xor_b32 v247, 0x10000, v247\n"
"v_mfma_f32_16x16x32_bf16 v[100:103], a[184:187], a[104:107], v[100:103]\n"
"v_add_f32 v0, v232, v0\n"
"v_mfma_f32_16x16x32_bf16 v[100:103], a[188:191], a[108:111], v[100:103]\n"
"v_add_f32 v1, v233, v1\n"
"v_mfma_f32_16x16x32_bf16 v[104:107], a[184:187], a[112:115], v[104:107]\n"
"v_add_f32 v2, v234, v2\n"
"v_mfma_f32_16x16x32_bf16 v[104:107], a[188:191], a[116:119], v[104:107]\n"
"v_add_f32 v3, v235, v3\n"
"v_mfma_f32_16x16x32_bf16 v[108:111], a[184:187], a[120:123], v[108:111]\n"
"v_add_f32 v4, v232, v4\n"
"v_mfma_f32_16x16x32_bf16 v[108:111], a[188:191], a[124:127], v[108:111]\n"
"v_add_f32 v5, v233, v5\n"
"v_mfma_f32_16x16x32_bf16 v[112:115], a[184:187], a[128:131], v[112:115]\n"
"v_add_f32 v6, v234, v6\n"
"v_mfma_f32_16x16x32_bf16 v[112:115], a[188:191], a[132:135], v[112:115]\n"
"v_add_f32 v7, v235, v7\n"
"v_mfma_f32_16x16x32_bf16 v[116:119], a[184:187], a[136:139], v[116:119]\n"
"v_add_f32 v8, v232, v8\n"
"v_mfma_f32_16x16x32_bf16 v[116:119], a[188:191], a[140:143], v[116:119]\n"
"v_add_f32 v9, v233, v9\n"
"v_mfma_f32_16x16x32_bf16 v[120:123], a[184:187], a[144:147], v[120:123]\n"
"v_add_f32 v10, v234, v10\n"
"v_mfma_f32_16x16x32_bf16 v[120:123], a[188:191], a[148:151], v[120:123]\n"
"v_add_f32 v11, v235, v11\n"
"v_mfma_f32_16x16x32_bf16 v[124:127], a[184:187], a[152:155], v[124:127]\n"
"global_load_dwordx4 a[184:187], v246, s[16:17] offset:2048 sc1\n"
"v_mfma_f32_16x16x32_bf16 v[124:127], a[188:191], a[156:159], v[124:127]\n"
"s_waitcnt vmcnt(22)\n"
"v_mfma_f32_16x16x32_bf16 v[128:131], a[192:195], a[96:99], v[128:131]\n"
"global_load_dwordx4 a[188:191], v246, s[16:17] offset:3072 sc1\n"
"v_mfma_f32_16x16x32_bf16 v[128:131], a[196:199], a[100:103], v[128:131]\n"
"v_add_f32 v12, v232, v12\n"
"v_mfma_f32_16x16x32_bf16 v[132:135], a[192:195], a[104:107], v[132:135]\n"
"v_add_u32 v246, 0x2000, v246\n"
"v_mfma_f32_16x16x32_bf16 v[132:135], a[196:199], a[108:111], v[132:135]\n"
"v_add_f32 v13, v233, v13\n"
"v_mfma_f32_16x16x32_bf16 v[136:139], a[192:195], a[112:115], v[136:139]\n"
"v_add_f32 v14, v234, v14\n"
"v_mfma_f32_16x16x32_bf16 v[136:139], a[196:199], a[116:119], v[136:139]\n"
"v_add_f32 v15, v235, v15\n"
"v_mfma_f32_16x16x32_bf16 v[140:143], a[192:195], a[120:123], v[140:143]\n"
"v_add_f32 v16, v232, v16\n"
"v_mfma_f32_16x16x32_bf16 v[140:143], a[196:199], a[124:127], v[140:143]\n"
"v_add_f32 v17, v233, v17\n"
"v_mfma_f32_16x16x32_bf16 v[144:147], a[192:195], a[128:131], v[144:147]\n"
"ds_read_b128 v[224:227], v247 offset:0\n"
"v_mfma_f32_16x16x32_bf16 v[144:147], a[196:199], a[132:135], v[144:147]\n"
"s_waitcnt vmcnt(12)\n"
"v_readfirstlane_b32 s46, v239\n"
"v_mfma_f32_16x16x32_bf16 v[148:151], a[192:195], a[136:139], v[148:151]\n"
"ds_read_b128 v[228:231], v247 offset:1024\n"
"v_mfma_f32_16x16x32_bf16 v[148:151], a[196:199], a[140:143], v[148:151]\n"
"v_add_f32 v18, v234, v18\n"
"v_mfma_f32_16x16x32_bf16 v[152:155], a[192:195], a[144:147], v[152:155]\n"
"s_sub_u32 s46, s46, s44\n"
"v_mfma_f32_16x16x32_bf16 v[152:155], a[196:199], a[148:151], v[152:155]\n"
"s_cmp_ge_u32 s46, 32512\n"
"s_cbranch_scc0 .Lslow2_%=\n"
".Lback2_%=:\n"
"v_mfma_f32_16x16x32_bf16 v[156:159], a[192:195], a[152:155], v[156:159]\n"
"global_load_dwordx4 a[192:195], v246, s[16:17] offset:-4096 sc1\n"
"v_mfma_f32_16x16x32_bf16 v[156:159], a[196:199], a[156:159], v[156:159]\n"
"v_mfma_f32_16x16x32_bf16 v[160:163], a[200:203], a[96:99], v[160:163]\n"
"global_load_dwordx4 a[196:199], v246, s[16:17] offset:-3072 sc1\n"
"v_mfma_f32_16x16x32_bf16 v[160:163], a[204:207], a[100:103], v[160:163]\n"
"s_add_u32 m0, s42, 81920\n"
"v_add_u32 v238, 640, v248\n"
"v_mfma_f32_16x16x32_bf16 v[164:167], a[200:203], a[104:107], v[164:167]\n"
"global_load_lds_dwordx4 v238, s[26:27] offset:0 sc1\n"
"v_mfma_f32_16x16x32_bf16 v[164:167], a[204:207], a[108:111], v[164:167]\n"
"v_add_f32 v19, v235, v19\n"
"v_mfma_f32_16x16x32_bf16 v[168:171], a[200:203], a[112:115], v[168:171]\n"
"v_add_u32 v249, 512, v248\n"
"v_mfma_f32_16x16x32_bf16 v[168:171], a[204:207], a[116:119], v[168:171]\n"
"s_waitcnt lgkmcnt(1)\n"
"global_store_dwordx4 v249, v[224:227], s[24:25] sc1\n"
"v_mfma_f32_16x16x32_bf16 v[172:175], a[200:203], a[120:123], v[172:175]\n"
"v_add_f32 v20, v232, v20\n"
"v_mfma_f32_16x16x32_bf16 v[172:175], a[204:207], a[124:127], v[172:175]\n"
"v_add_f32 v21, v233, v21\n"
"v_mfma_f32_16x16x32_bf16 v[176:179], a[200:203], a[128:131], v[176:179]\n"
"ds_read_b128 v[224:227], v247 offset:2048\n"
"v_mfma_f32_16x16x32_bf16 v[176:179], a[204:207], a[132:135], v[176:179]\n"
"v_add_f32 v22, v234, v22\n"
"v_mfma_f32_16x16x32_bf16 v[180:183], a[200:203], a[136:139], v[180:183]\n"
"v_add_f32 v23, v235, v23\n"
"v_mfma_f32_16x16x32_bf16 v[180:183], a[204:207], a[140:143], v[180:183]\n"
"v_add_f32 v24, v232, v24\n"
"v_mfma_f32_16x16x32_bf16 v[184:187], a[200:203], a[144:147], v[184:187]\n"
"v_add_f32 v25, v233, v25\n"
"v_mfma_f32_16x16x32_bf16 v[184:187], a[204:207], a[148:151], v[184:187]\n"
"v_add_f32 v26, v234, v26\n"
"v_mfma_f32_16x16x32_bf16 v[188:191], a[200:203], a[152:155], v[188:191]\n"
"global_load_dwordx4 a[200:203], v246, s[16:17] offset:-2048 sc1\n"
"v_mfma_f32_16x16x32_bf16 v[188:191], a[204:207], a[156:159], v[188:191]\n"
"v_mfma_f32_16x16x32_bf16 v[192:195], a[208:211], a[96:99], v[192:195]\n"
"global_load_dwordx4 a[204:207], v246, s[16:17] offset:-1024 sc1\n"
"v_mfma_f32_16x16x32_bf16 v[192:195], a[212:215], a[100:103], v[192:195]\n"
"v_add_f32 v27, v235, v27\n"
"v_mfma_f32_16x16x32_bf16 v[196:199], a[208:211], a[104:107], v[196:199]\n"
"v_add_f32 v28, v232, v28\n"
"v_mfma_f32_16x16x32_bf16 v[196:199], a[212:215], a[108:111], v[196:199]\n"
"v_add_f32 v29, v233, v29\n"
"v_mfma_f32_16x16x32_bf16 v[200:203], a[208:211], a[112:115], v[200:203]\n"
"v_add_f32 v30, v234, v30\n"
"v_mfma_f32_16x16x32_bf16 v[200:203], a[212:215], a[116:119], v[200:203]\n"
"v_add_f32 v31, v235, v31\n"
"v_mfma_f32_16x16x32_bf16 v[204:207], a[208:211], a[120:123], v[204:207]\n"
"v_mfma_f32_16x16x32_bf16 v[204:207], a[212:215], a[124:127], v[204:207]\n"
"v_mfma_f32_16x16x32_bf16 v[208:211], a[208:211], a[128:131], v[208:211]\n"
"v_mfma_f32_16x16x32_bf16 v[208:211], a[212:215], a[132:135], v[208:211]\n"
"v_mfma_f32_16x16x32_bf16 v[212:215], a[208:211], a[136:139], v[212:215]\n"
"v_mfma_f32_16x16x32_bf16 v[212:215], a[212:215], a[140:143], v[212:215]\n"
"v_mfma_f32_16x16x32_bf16 v[216:219], a[208:211], a[144:147], v[216:219]\n"
"v_mfma_f32_16x16x32_bf16 v[216:219], a[212:215], a[148:151], v[216:219]\n"
"v_mfma_f32_16x16x32_bf16 v[220:223], a[208:211], a[152:155], v[220:223]\n"
"global_load_dwordx4 a[208:211], v246, s[16:17] offset:0 sc1\n"
"v_mfma_f32_16x16x32_bf16 v[220:223], a[212:215], a[156:159], v[220:223]\n"
"v_mfma_f32_16x16x32_bf16 a[0:3], a[216:219], a[96:99], a[0:3]\n"
"global_load_dwordx4 a[212:215], v246, s[16:17] offset:1024 sc1\n"
"ds_read_b128 a[96:99], v242 offset:32768\n"
"v_mfma_f32_16x16x32_bf16 a[0:3], a[220:223], a[100:103], a[0:3]\n"
"ds_read_b128 a[100:103], v243 offset:32768\n"
"v_mfma_f32_16x16x32_bf16 a[4:7], a[216:219], a[104:107], a[4:7]\n"
"ds_read_b128 a[104:107], v242 offset:34816\n"
"v_mfma_f32_16x16x32_bf16 a[4:7], a[220:223], a[108:111], a[4:7]\n"
"ds_read_b128 a[108:111], v243 offset:34816\n"
"v_mfma_f32_16x16x32_bf16 a[8:11], a[216:219], a[112:115], a[8:11]\n"
"ds_read_b128 a[112:115], v242 offset:36864\n"
"v_mfma_f32_16x16x32_bf16 a[8:11], a[220:223], a[116:119], a[8:11]\n"
"ds_read_b128 a[116:119], v243 offset:36864\n"
"v_mfma_f32_16x16x32_bf16 a[12:15], a[216:219], a[120:123], a[12:15]\n"
"ds_read_b128 a[120:123], v242 offset:38912\n"
"v_mfma_f32_16x16x32_bf16 a[12:15], a[220:223], a[124:127], a[12:15]\n"
"ds_read_b128 a[124:127], v243 offset:38912\n"
"v_mfma_f32_16x16x32_bf16 a[16:19], a[216:219], a[128:131], a[16:19]\n"
"ds_read_b128 a[128:131], v242 offset:40960\n"
"v_mfma_f32_16x16x32_bf16 a[16:19], a[220:223], a[132:135], a[16:19]\n"
"ds_read_b128 a[132:135], v243 offset:40960\n"
"v_mfma_f32_16x16x32_bf16 a[20:23], a[216:219], a[136:139], a[20:23]\n"
"ds_read_b128 a[136:139], v242 offset:43008\n"
"v_mfma_f32_16x16x32_bf16 a[20:23], a[220:223], a[140:143], a[20:23]\n"
"ds_read_b128 a[140:143], v243 offset:43008\n"
"v_mfma_f32_16x16x32_bf16 a[24:27], a[216:219], a[144:147], a[24:27]\n"
"ds_read_b128 a[144:147], v242 offset:45056\n"
"v_mfma_f32_16x16x32_bf16 a[24:27], a[220:223], a[148:151], a[24:27]\n"
"ds_read_b128 a[148:151], v243 offset:45056\n"
"v_mfma_f32_16x16x32_bf16 a[28:31], a[216:219], a[152:155], a[28:31]\n"
"global_load_dwordx4 a[216:219], v246, s[16:17] offset:2048 sc1\n"
"ds_read_b128 a[152:155], v242 offset:47104\n"
"v_mfma_f32_16x16x32_bf16 a[28:31], a[220:223], a[156:159], a[28:31]\n"
"ds_read_b128 a[156:159], v243 offset:47104\n"
"s_waitcnt vmcnt(19) lgkmcnt(14)\n"
"v_mfma_f32_16x16x32_bf16 v[0:3], a[160:163], a[96:99], v[0:3]\n"
"global_load_dwordx4 a[220:223], v246, s[16:17] offset:3072 sc1\n"
"v_mfma_f32_16x16x32_bf16 v[0:3], a[164:167], a[100:103], v[0:3]\n"
"s_waitcnt lgkmcnt(12)\n"
"v_mfma_f32_16x16x32_bf16 v[4:7], a[160:163], a[104:107], v[4:7]\n"
"v_add_u32 v246, 0x2000, v246\n"
"v_mfma_f32_16x16x32_bf16 v[4:7], a[164:167], a[108:111], v[4:7]\n"
"s_waitcnt lgkmcnt(10)\n"
"v_mfma_f32_16x16x32_bf16 v[8:11], a[160:163], a[112:115], v[8:11]\n"
"v_add_u32 v249, 16896, v248\n"
"v_mfma_f32_16x16x32_bf16 v[8:11], a[164:167], a[116:119], v[8:11]\n"
"s_waitcnt lgkmcnt(8)\n"
"v_mfma_f32_16x16x32_bf16 v[12:15], a[160:163], a[120:123], v[12:15]\n"
"global_store_dwordx4 v249, v[228:231], s[24:25] sc1\n"
"v_mfma_f32_16x16x32_bf16 v[12:15], a[164:167], a[124:127], v[12:15]\n"
"s_waitcnt lgkmcnt(6)\n"
"v_mfma_f32_16x16x32_bf16 v[16:19], a[160:163], a[128:131], v[16:19]\n"
"v_add_u32 v238, s66, v238\n"
"v_mfma_f32_16x16x32_bf16 v[16:19], a[164:167], a[132:135], v[16:19]\n"
"s_waitcnt lgkmcnt(4)\n"
"v_mfma_f32_16x16x32_bf16 v[20:23], a[160:163], a[136:139], v[20:23]\n"
"ds_read_b128 v[228:231], v247 offset:3072\n"
"v_mfma_f32_16x16x32_bf16 v[20:23], a[164:167], a[140:143], v[20:23]\n"
"s_waitcnt lgkmcnt(3)\n"
"v_mfma_f32_16x16x32_bf16 v[24:27], a[160:163], a[144:147], v[24:27]\n"
"global_load_lds_dwordx4 v238, s[26:27] offset:1024 sc1\n"
"v_mfma_f32_16x16x32_bf16 v[24:27], a[164:167], a[148:151], v[24:27]\n"
"s_waitcnt lgkmcnt(1)\n"
"v_mfma_f32_16x16x32_bf16 v[28:31], a[160:163], a[152:155], v[28:31]\n"
"global_load_dwordx4 a[160:163], v246, s[16:17] offset:-4096 sc1\n"
"v_mfma_f32_16x16x32_bf16 v[28:31], a[164:167], a[156:159], v[28:31]\n"
"s_waitcnt vmcnt(20)\n"
"v_mfma_f32_16x16x32_bf16 v[32:35], a[168:171], a[96:99], v[32:35]\n"
"global_load_dwordx4 a[164:167], v246, s[16:17] offset:-3072 sc1\n"
"v_mfma_f32_16x16x32_bf16 v[32:35], a[172:175], a[100:103], v[32:35]\n"
"v_mfma_f32_16x16x32_bf16 v[36:39], a[168:171], a[104:107], v[36:39]\n"
"v_mfma_f32_16x16x32_bf16 v[36:39], a[172:175], a[108:111], v[36:39]\n"
"v_mfma_f32_16x16x32_bf16 v[40:43], a[168:171], a[112:115], v[40:43]\n"
"v_mfma_f32_16x16x32_bf16 v[40:43], a[172:175], a[116:119], v[40:43]\n"
"v_mfma_f32_16x16x32_bf16 v[44:47], a[168:171], a[120:123], v[44:47]\n"
"v_mfma_f32_16x16x32_bf16 v[44:47], a[172:175], a[124:127], v[44:47]\n"
"v_mfma_f32_16x16x32_bf16 v[48:51], a[168:171], a[128:131], v[48:51]\n"
"v_mfma_f32_16x16x32_bf16 v[48:51], a[172:175], a[132:135], v[48:51]\n"
"v_mfma_f32_16x16x32_bf16 v[52:55], a[168:171], a[136:139], v[52:55]\n"
"v_mfma_f32_16x16x32_bf16 v[52:55], a[172:175], a[140:143], v[52:55]\n"
"v_mfma_f32_16x16x32_bf16 v[56:59], a[168:171], a[144:147], v[56:59]\n"
"v_mfma_f32_16x16x32_bf16 v[56:59], a[172:175], a[148:151], v[56:59]\n"
"v_mfma_f32_16x16x32_bf16 v[60:63], a[168:171], a[152:155], v[60:63]\n"
"global_load_dwordx4 a[168:171], v246, s[16:17] offset:-2048 sc1\n"
"v_mfma_f32_16x16x32_bf16 v[60:63], a[172:175], a[156:159], v[60:63]\n"
"s_waitcnt vmcnt(17)\n"
"v_mfma_f32_16x16x32_bf16 v[64:67], a[176:179], a[96:99], v[64:67]\n"
"global_load_dwordx4 a[172:175], v246, s[16:17] offset:-1024 sc1\n"
"v_and_b32 v235, s67, v237\n"
"v_mfma_f32_16x16x32_bf16 v[64:67], a[180:183], a[100:103], v[64:67]\n"
"v_lshlrev_b32 v234, 16, v237\n"
"v_and_b32 v233, s67, v236\n"
"v_mfma_f32_16x16x32_bf16 v[68:71], a[176:179], a[104:107], v[68:71]\n"
"v_lshlrev_b32 v232, 16, v236\n"
"v_mfma_f32_16x16x32_bf16 v[68:71], a[180:183], a[108:111], v[68:71]\n"
"v_lshrrev_b32 v237, 4, v247\n"
"v_mfma_f32_16x16x32_bf16 v[72:75], a[176:179], a[112:115], v[72:75]\n"
"v_add_u32 v249, 33280, v248\n"
"v_and_b32 v237, 0x30, v237\n"
"v_mfma_f32_16x16x32_bf16 v[72:75], a[180:183], a[116:119], v[72:75]\n"
"global_store_dwordx4 v249, v[224:227], s[24:25] sc1\n"
"v_mfma_f32_16x16x32_bf16 v[76:79], a[176:179], a[120:123], v[76:79]\n"
"global_load_dwordx2 v[236:237], v237, s[18:19] offset:64\n"
"v_mfma_f32_16x16x32_bf16 v[76:79], a[180:183], a[124:127], v[76:79]\n"
"v_mfma_f32_16x16x32_bf16 v[80:83], a[176:179], a[128:131], v[80:83]\n"
"v_add_f32 v32, v232, v32\n"
"v_mfma_f32_16x16x32_bf16 v[80:83], a[180:183], a[132:135], v[80:83]\n"
"v_add_u32 v238, s66, v238\n"
"v_mfma_f32_16x16x32_bf16 v[84:87], a[176:179], a[136:139], v[84:87]\n"
"global_load_lds_dwordx4 v238, s[26:27] offset:2048 sc1\n"
"v_mfma_f32_16x16x32_bf16 v[84:87], a[180:183], a[140:143], v[84:87]\n"
"v_add_f32 v33, v233, v33\n"
"v_mfma_f32_16x16x32_bf16 v[88:91], a[176:179], a[144:147], v[88:91]\n"
"v_add_f32 v34, v234, v34\n"
"v_mfma_f32_16x16x32_bf16 v[88:91], a[180:183], a[148:151], v[88:91]\n"
"v_add_f32 v35, v235, v35\n"
"v_mfma_f32_16x16x32_bf16 v[92:95], a[176:179], a[152:155], v[92:95]\n"
"global_load_dwordx4 a[176:179], v246, s[16:17] offset:0 sc1\n"
"v_mfma_f32_16x16x32_bf16 v[92:95], a[180:183], a[156:159], v[92:95]\n"
"s_waitcnt vmcnt(20)\n"
"v_mfma_f32_16x16x32_bf16 v[96:99], a[184:187], a[96:99], v[96:99]\n"
"global_load_dwordx4 a[180:183], v246, s[16:17] offset:1024 sc1\n"
"v_mfma_f32_16x16x32_bf16 v[96:99], a[188:191], a[100:103], v[96:99]\n"
"v_add_f32 v36, v232, v36\n"
"v_mfma_f32_16x16x32_bf16 v[100:103], a[184:187], a[104:107], v[100:103]\n"
"v_add_f32 v37, v233, v37\n"
"v_mfma_f32_16x16x32_bf16 v[100:103], a[188:191], a[108:111], v[100:103]\n"
"v_add_f32 v38, v234, v38\n"
"v_mfma_f32_16x16x32_bf16 v[104:107], a[184:187], a[112:115], v[104:107]\n"
"v_add_f32 v39, v235, v39\n"
"v_mfma_f32_16x16x32_bf16 v[104:107], a[188:191], a[116:119], v[104:107]\n"
"v_add_f32 v40, v232, v40\n"
"v_mfma_f32_16x16x32_bf16 v[108:111], a[184:187], a[120:123], v[108:111]\n"
"s_mov_b64 exec, 1\n"
"global_atomic_add v250, v252, s[30:31] offset:0 sc1\n"
"s_mov_b64 exec, -1\n"
"v_mfma_f32_16x16x32_bf16 v[108:111], a[188:191], a[124:127], v[108:111]\n"
"v_add_f32 v41, v233, v41\n"
"v_mfma_f32_16x16x32_bf16 v[112:115], a[184:187], a[128:131], v[112:115]\n"
"v_add_f32 v42, v234, v42\n"
"v_mfma_f32_16x16x32_bf16 v[112:115], a[188:191], a[132:135], v[112:115]\n"
"v_add_f32 v43, v235, v43\n"
"v_mfma_f32_16x16x32_bf16 v[116:119], a[184:187], a[136:139], v[116:119]\n"
"v_add_f32 v44, v232, v44\n"
"v_mfma_f32_16x16x32_bf16 v[116:119], a[188:191], a[140:143], v[116:119]\n"
"v_add_f32 v45, v233, v45\n"
"v_mfma_f32_16x16x32_bf16 v[120:123], a[184:187], a[144:147], v[120:123]\n"
"v_add_f32 v46, v234, v46\n"
"v_mfma_f32_16x16x32_bf16 v[120:123], a[188:191], a[148:151], v[120:123]\n"
"v_add_f32 v47, v235, v47\n"
"v_mfma_f32_16x16x32_bf16 v[124:127], a[184:187], a[152:155], v[124:127]\n"
"global_load_dwordx4 a[184:187], v246, s[16:17] offset:2048 sc1\n"
"v_mfma_f32_16x16x32_bf16 v[124:127], a[188:191], a[156:159], v[124:127]\n"
"s_waitcnt vmcnt(21)\n"
"v_mfma_f32_16x16x32_bf16 v[128:131], a[192:195], a[96:99], v[128:131]\n"
"global_load_dwordx4 a[188:191], v246, s[16:17] offset:3072 sc1\n"
"v_mfma_f32_16x16x32_bf16 v[128:131], a[196:199], a[100:103], v[128:131]\n"
"v_add_f32 v48, v232, v48\n"
"v_mfma_f32_16x16x32_bf16 v[132:135], a[192:195], a[104:107], v[132:135]\n"
"v_add_u32 v246, 0x2000, v246\n"
"v_mfma_f32_16x16x32_bf16 v[132:135], a[196:199], a[108:111], v[132:135]\n"
"v_add_f32 v49, v233, v49\n"
"v_mfma_f32_16x16x32_bf16 v[136:139], a[192:195], a[112:115], v[136:139]\n"
"v_add_f32 v50, v234, v50\n"
"v_mfma_f32_16x16x32_bf16 v[136:139], a[196:199], a[116:119], v[136:139]\n"
"v_add_f32 v51, v235, v51\n"
"v_mfma_f32_16x16x32_bf16 v[140:143], a[192:195], a[120:123], v[140:143]\n"
"v_add_f32 v52, v232, v52\n"
"v_mfma_f32_16x16x32_bf16 v[140:143], a[196:199], a[124:127], v[140:143]\n"
"v_add_u32 v238, s66, v238\n"
"v_mfma_f32_16x16x32_bf16 v[144:147], a[192:195], a[128:131], v[144:147]\n"
"global_load_lds_dwordx4 v238, s[26:27] offset:3072 sc1\n"
"v_mfma_f32_16x16x32_bf16 v[144:147], a[196:199], a[132:135], v[144:147]\n"
"v_add_f32 v53, v233, v53\n"
"v_mfma_f32_16x16x32_bf16 v[148:151], a[192:195], a[136:139], v[148:151]\n"
"v_add_f32 v54, v234, v54\n"
"v_mfma_f32_16x16x32_bf16 v[148:151], a[196:199], a[140:143], v[148:151]\n"
"v_add_f32 v55, v235, v55\n"
"v_mfma_f32_16x16x32_bf16 v[152:155], a[192:195], a[144:147], v[152:155]\n"
"v_add_u32 v249, 49664, v248\n"
"v_mfma_f32_16x16x32_bf16 v[152:155], a[196:199], a[148:151], v[152:155]\n"
"s_waitcnt lgkmcnt(0)\n"
"global_store_dwordx4 v249, v[228:231], s[24:25] sc1\n"
"v_mfma_f32_16x16x32_bf16 v[156:159], a[192:195], a[152:155], v[156:159]\n"
"global_load_dwordx4 a[192:195], v246, s[16:17] offset:-4096 sc1\n"
"v_mfma_f32_16x16x32_bf16 v[156:159], a[196:199], a[156:159], v[156:159]\n"
"s_waitcnt vmcnt(21)\n"
"v_mfma_f32_16x16x32_bf16 v[160:163], a[200:203], a[96:99], v[160:163]\n"
"global_load_dwordx4 a[196:199], v246, s[16:17] offset:-3072 sc1\n"
"v_mfma_f32_16x16x32_bf16 v[160:163], a[204:207], a[100:103], v[160:163]\n"
"v_add_f32 v56, v232, v56\n"
"v_mfma_f32_16x16x32_bf16 v[164:167], a[200:203], a[104:107], v[164:167]\n"
"v_add_f32 v57, v233, v57\n"
"v_mfma_f32_16x16x32_bf16 v[164:167], a[204:207], a[108:111], v[164:167]\n"
"v_add_f32 v58, v234, v58\n"
"v_mfma_f32_16x16x32_bf16 v[168:171], a[200:203], a[112:115], v[168:171]\n"
"v_add_f32 v59, v235, v59\n"
"v_mfma_f32_16x16x32_bf16 v[168:171], a[204:207], a[116:119], v[168:171]\n"
"v_add_f32 v60, v232, v60\n"
"v_mfma_f32_16x16x32_bf16 v[172:175], a[200:203], a[120:123], v[172:175]\n"
"v_add_f32 v61, v233, v61\n"
"v_mfma_f32_16x16x32_bf16 v[172:175], a[204:207], a[124:127], v[172:175]\n"
"v_add_f32 v62, v234, v62\n"
"v_mfma_f32_16x16x32_bf16 v[176:179], a[200:203], a[128:131], v[176:179]\n"
"v_add_f32 v63, v235, v63\n"
"v_mfma_f32_16x16x32_bf16 v[176:179], a[204:207], a[132:135], v[176:179]\n"
"v_mfma_f32_16x16x32_bf16 v[180:183], a[200:203], a[136:139], v[180:183]\n"
"v_mfma_f32_16x16x32_bf16 v[180:183], a[204:207], a[140:143], v[180:183]\n"
"v_mfma_f32_16x16x32_bf16 v[184:187], a[200:203], a[144:147], v[184:187]\n"
"v_mfma_f32_16x16x32_bf16 v[184:187], a[204:207], a[148:151], v[184:187]\n"
"v_mfma_f32_16x16x32_bf16 v[188:191], a[200:203], a[152:155], v[188:191]\n"
"global_load_dwordx4 a[200:203], v246, s[16:17] offset:-2048 sc1\n"
"v_mfma_f32_16x16x32_bf16 v[188:191], a[204:207], a[156:159], v[188:191]\n"
"s_waitcnt vmcnt(21)\n"
"v_mfma_f32_16x16x32_bf16 v[192:195], a[208:211], a[96:99], v[192:195]\n"
"global_load_dwordx4 a[204:207], v246, s[16:17] offset:-1024 sc1\n"
"v_mfma_f32_16x16x32_bf16 v[192:195], a[212:215], a[100:103], v[192:195]\n"
"v_mfma_f32_16x16x32_bf16 v[196:199], a[208:211], a[104:107], v[196:199]\n"
"v_mfma_f32_16x16x32_bf16 v[196:199], a[212:215], a[108:111], v[196:199]\n"
"v_mfma_f32_16x16x32_bf16 v[200:203], a[208:211], a[112:115], v[200:203]\n"
"v_mfma_f32_16x16x32_bf16 v[200:203], a[212:215], a[116:119], v[200:203]\n"
"s_add_u32 m0, s42, 114688\n"
"v_add_u32 v238, 896, v248\n"
"v_mfma_f32_16x16x32_bf16 v[204:207], a[208:211], a[120:123], v[204:207]\n"
"global_load_lds_dwordx4 v238, s[26:27] offset:0 sc1\n"
"v_mfma_f32_16x16x32_bf16 v[204:207], a[212:215], a[124:127], v[204:207]\n"
"v_mfma_f32_16x16x32_bf16 v[208:211], a[208:211], a[128:131], v[208:211]\n"
"ds_read_b128 v[224:227], v247 offset:32768\n"
"v_mfma_f32_16x16x32_bf16 v[208:211], a[212:215], a[132:135], v[208:211]\n"
"v_mfma_f32_16x16x32_bf16 v[212:215], a[208:211], a[136:139], v[212:215]\n"
"ds_read_b128 v[228:231], v247 offset:33792\n"
"v_mfma_f32_16x16x32_bf16 v[212:215], a[212:215], a[140:143], v[212:215]\n"
"v_mfma_f32_16x16x32_bf16 v[216:219], a[208:211], a[144:147], v[216:219]\n"
"v_mfma_f32_16x16x32_bf16 v[216:219], a[212:215], a[148:151], v[216:219]\n"
"s_barrier\n"
"v_mfma_f32_16x16x32_bf16 v[220:223], a[208:211], a[152:155], v[220:223]\n"
"global_load_dwordx4 a[208:211], v246, s[16:17] offset:0 sc1\n"
"v_mfma_f32_16x16x32_bf16 v[220:223], a[212:215], a[156:159], v[220:223]\n"
"s_waitcnt vmcnt(22)\n"
"v_mfma_f32_16x16x32_bf16 a[0:3], a[216:219], a[96:99], a[0:3]\n"
"global_load_dwordx4 a[212:215], v246, s[16:17] offset:1024 sc1\n"
"ds_read_b128 a[96:99], v240 offset:16384\n"
"v_mfma_f32_16x16x32_bf16 a[0:3], a[220:223], a[100:103], a[0:3]\n"
"ds_read_b128 a[100:103], v241 offset:16384\n"
"v_mfma_f32_16x16x32_bf16 a[4:7], a[216:219], a[104:107], a[4:7]\n"
"ds_read_b128 a[104:107], v240 offset:18432\n"
"v_mfma_f32_16x16x32_bf16 a[4:7], a[220:223], a[108:111], a[4:7]\n"
"ds_read_b128 a[108:111], v241 offset:18432\n"
"v_mfma_f32_16x16x32_bf16 a[8:11], a[216:219], a[112:115], a[8:11]\n"
"ds_read_b128 a[112:115], v240 offset:20480\n"
"v_mfma_f32_16x16x32_bf16 a[8:11], a[220:223], a[116:119], a[8:11]\n"
"ds_read_b128 a[116:119], v241 offset:20480\n"
"v_mfma_f32_16x16x32_bf16 a[12:15], a[216:219], a[120:123], a[12:15]\n"
"ds_read_b128 a[120:123], v240 offset:22528\n"
"v_mfma_f32_16x16x32_bf16 a[12:15], a[220:223], a[124:127], a[12:15]\n"
"ds_read_b128 a[124:127], v241 offset:22528\n"
"v_mfma_f32_16x16x32_bf16 a[16:19], a[216:219], a[128:131], a[16:19]\n"
"ds_read_b128 a[128:131], v240 offset:24576\n"
"v_mfma_f32_16x16x32_bf16 a[16:19], a[220:223], a[132:135], a[16:19]\n"
"ds_read_b128 a[132:135], v241 offset:24576\n"
"v_mfma_f32_16x16x32_bf16 a[20:23], a[216:219], a[136:139], a[20:23]\n"
"ds_read_b128 a[136:139], v240 offset:26624\n"
"v_mfma_f32_16x16x32_bf16 a[20:23], a[220:223], a[140:143], a[20:23]\n"
"ds_read_b128 a[140:143], v241 offset:26624\n"
"v_mfma_f32_16x16x32_bf16 a[24:27], a[216:219], a[144:147], a[24:27]\n"
"ds_read_b128 a[144:147], v240 offset:28672\n"
"v_mfma_f32_16x16x32_bf16 a[24:27], a[220:223], a[148:151], a[24:27]\n"
"s_nop 0\n"
".Lpg11_%=:\n"
"s_cbranch_execz .Lpg15_%=\n"
"ds_read_b128 a[148:151], v241 offset:28672\n"
"v_mfma_f32_16x16x32_bf16 a[28:31], a[216:219], a[152:155], a[28:31]\n"
"global_load_dwordx4 a[216:219], v246, s[16:17] offset:2048 sc1\n"
"ds_read_b128 a[152:155], v240 offset:30720\n"
"v_mfma_f32_16x16x32_bf16 a[28:31], a[220:223], a[156:159], a[28:31]\n"
"ds_read_b128 a[156:159], v241 offset:30720\n"
"s_waitcnt vmcnt(20) lgkmcnt(14)\n"
"v_mfma_f32_16x16x32_bf16 v[0:3], a[160:163], a[96:99], v[0:3]\n"
"global_load_dwordx4 a[220:223], v246, s[16:17] offset:3072 sc1\n"
"v_mfma_f32_16x16x32_bf16 v[0:3], a[164:167], a[100:103], v[0:3]\n"
"s_waitcnt lgkmcnt(12)\n"
"v_mfma_f32_16x16x32_bf16 v[4:7], a[160:163], a[104:107], v[4:7]\n"
"v_add_u32 v246, 0x2000, v246\n"
"v_mfma_f32_16x16x32_bf16 v[4:7], a[164:167], a[108:111], v[4:7]\n"
"s_waitcnt lgkmcnt(10)\n"
"v_mfma_f32_16x16x32_bf16 v[8:11], a[160:163], a[112:115], v[8:11]\n"
"v_add_u32 v249, 768, v248\n"
"v_mfma_f32_16x16x32_bf16 v[8:11], a[164:167], a[116:119], v[8:11]\n"
"s_waitcnt lgkmcnt(8)\n"
"v_mfma_f32_16x16x32_bf16 v[12:15], a[160:163], a[120:123], v[12:15]\n"
"global_store_dwordx4 v249, v[224:227], s[24:25] sc1\n"
"v_mfma_f32_16x16x32_bf16 v[12:15], a[164:167], a[124:127], v[12:15]\n"
"s_waitcnt lgkmcnt(6)\n"
"v_mfma_f32_16x16x32_bf16 v[16:19], a[160:163], a[128:131], v[16:19]\n"
"v_add_u32 v238, s66, v238\n"
"v_mfma_f32_16x16x32_bf16 v[16:19], a[164:167], a[132:135], v[16:19]\n"
"s_waitcnt lgkmcnt(4)\n"
"v_mfma_f32_16x16x32_bf16 v[20:23], a[160:163], a[136:139], v[20:23]\n"
"ds_read_b128 v[224:227], v247 offset:34816\n"
"v_mfma_f32_16x16x32_bf16 v[20:23], a[164:167], a[140:143], v[20:23]\n"
"s_waitcnt lgkmcnt(3)\n"
"v_mfma_f32_16x16x32_bf16 v[24:27], a[160:163], a[144:147], v[24:27]\n"
"global_load_lds_dwordx4 v238, s[26:27] offset:1024 sc1\n"
"v_mfma_f32_16x16x32_bf16 v[24:27], a[164:167], a[148:151], v[24:27]\n"
"s_waitcnt lgkmcnt(1)\n"
"v_mfma_f32_16x16x32_bf16 v[28:31], a[160:163], a[152:155], v[28:31]\n"
"global_load_dwordx4 a[160:163], v246, s[16:17] offset:-4096 sc1\n"
"v_mfma_f32_16x16x32_bf16 v[28:31], a[164:167], a[156:159], v[28:31]\n"
"s_waitcnt vmcnt(22)\n"
"v_mfma_f32_16x16x32_bf16 v[32:35], a[168:171], a[96:99], v[32:35]\n"
"global_load_dwordx4 a[164:167], v246, s[16:17] offset:-3072 sc1\n"
"v_mfma_f32_16x16x32_bf16 v[32:35], a[172:175], a[100:103], v[32:35]\n"
"global_load_dword v239, v250, s[34:35] sc1\n"
"v_mfma_f32_16x16x32_bf16 v[36:39], a[168:171], a[104:107], v[36:39]\n"
"v_mfma_f32_16x16x32_bf16 v[36:39], a[172:175], a[108:111], v[36:39]\n"
"v_mfma_f32_16x16x32_bf16 v[40:43], a[168:171], a[112:115], v[40:43]\n"
"v_mfma_f32_16x16x32_bf16 v[40:43], a[172:175], a[116:119], v[40:43]\n"
"v_mfma_f32_16x16x32_bf16 v[44:47], a[168:171], a[120:123], v[44:47]\n"
"v_mfma_f32_16x16x32_bf16 v[44:47], a[172:175], a[124:127], v[44:47]\n"
"s_waitcnt vmcnt(22)\n"
"v_and_b32 v235, s67, v237\n"
"v_lshlrev_b32 v234, 16, v237\n"
"v_mfma_f32_16x16x32_bf16 v[48:51], a[168:171], a[128:131], v[48:51]\n"
"v_and_b32 v233, s67, v236\n"
"v_lshlrev_b32 v232, 16, v236\n"
"v_mfma_f32_16x16x32_bf16 v[48:51], a[172:175], a[132:135], v[48:51]\n"
"v_lshrrev_b32 v237, 4, v247\n"
"v_mfma_f32_16x16x32_bf16 v[52:55], a[168:171], a[136:139], v[52:55]\n"
"v_and_b32 v237, 0x30, v237\n"
"v_mfma_f32_16x16x32_bf16 v[52:55], a[172:175], a[140:143], v[52:55]\n"
"global_load_dwordx2 v[236:237], v237, s[18:19] offset:72\n"
"v_mfma_f32_16x16x32_bf16 v[56:59], a[168:171], a[144:147], v[56:59]\n"
"v_add_u32 v249, 17152, v248\n"
"v_mfma_f32_16x16x32_bf16 v[56:59], a[172:175], a[148:151], v[56:59]\n"
"global_store_dwordx4 v249, v[228:231], s[24:25] sc1\n"
"v_mfma_f32_16x16x32_bf16 v[60:63], a[168:171], a[152:155], v[60:63]\n"
"global_load_dwordx4 a[168:171], v246, s[16:17] offset:-2048 sc1\n"
"v_mfma_f32_16x16x32_bf16 v[60:63], a[172:175], a[156:159], v[60:63]\n"
"s_waitcnt vmcnt(22)\n"
"v_mfma_f32_16x16x32_bf16 v[64:67], a[176:179], a[96:99], v[64:67]\n"
"global_load_dwordx4 a[172:175], v246, s[16:17] offset:-1024 sc1\n"
"v_mfma_f32_16x16x32_bf16 v[64:67], a[180:183], a[100:103], v[64:67]\n"
"ds_read_b128 v[228:231], v247 offset:35840\n"
"v_mfma_f32_16x16x32_bf16 v[68:71], a[176:179], a[104:107], v[68:71]\n"
"v_mfma_f32_16x16x32_bf16 v[68:71], a[180:183], a[108:111], v[68:71]\n"
"v_mfma_f32_16x16x32_bf16 v[72:75], a[176:179], a[112:115], v[72:75]\n"
"s_add_u32 s64, s60, 49152\n"
"s_addc_u32 s65, s61, 0\n"
"v_mfma_f32_16x16x32_bf16 v[72:75], a[180:183], a[116:119], v[72:75]\n"
"v_mfma_f32_16x16x32_bf16 v[76:79], a[176:179], a[120:123], v[76:79]\n"
"v_mfma_f32_16x16x32_bf16 v[76:79], a[180:183], a[124:127], v[76:79]\n"
"v_mfma_f32_16x16x32_bf16 v[80:83], a[176:179], a[128:131], v[80:83]\n"
"v_mfma_f32_16x16x32_bf16 v[80:83], a[180:183], a[132:135], v[80:83]\n"
"v_add_u32 v238, s66, v238\n"
"v_mfma_f32_16x16x32_bf16 v[84:87], a[176:179], a[136:139], v[84:87]\n"
"global_load_lds_dwordx4 v238, s[26:27] offset:2048 sc1\n"
"v_mfma_f32_16x16x32_bf16 v[84:87], a[180:183], a[140:143], v[84:87]\n"
"v_mfma_f32_16x16x32_bf16 v[88:91], a[176:179], a[144:147], v[88:91]\n"
"v_mfma_f32_16x16x32_bf16 v[88:91], a[180:183], a[148:151], v[88:91]\n"
"v_mfma_f32_16x16x32_bf16 v[92:95], a[176:179], a[152:155], v[92:95]\n"
"global_load_dwordx4 a[176:179], v246, s[16:17] offset:0 sc1\n"
"v_mfma_f32_16x16x32_bf16 v[92:95], a[180:183], a[156:159], v[92:95]\n"
"s_waitcnt vmcnt(22)\n"
"v_mfma_f32_16x16x32_bf16 v[96:99], a[184:187], a[96:99], v[96:99]\n"
"global_load_dwordx4 a[180:183], v246, s[16:17] offset:1024 sc1\n"
"v_mfma_f32_16x16x32_bf16 v[96:99], a[188:191], a[100:103], v[96:99]\n"
"v_mfma_f32_16x16x32_bf16 v[100:103], a[184:187], a[104:107], v[100:103]\n"
"v_mfma_f32_16x16x32_bf16 v[100:103], a[188:191], a[108:111], v[100:103]\n"
"v_mfma_f32_16x16x32_bf16 v[104:107], a[184:187], a[112:115], v[104:107]\n"
"v_mfma_f32_16x16x32_bf16 v[104:107], a[188:191], a[116:119], v[104:107]\n"
"v_mfma_f32_16x16x32_bf16 v[108:111], a[184:187], a[120:123], v[108:111]\n"
"v_mfma_f32_16x16x32_bf16 v[108:111], a[188:191], a[124:127], v[108:111]\n"
"v_mfma_f32_16x16x32_bf16 v[112:115], a[184:187], a[128:131], v[112:115]\n"
"v_add_f32 v64, v232, v64\n"
"v_mfma_f32_16x16x32_bf16 v[112:115], a[188:191], a[132:135], v[112:115]\n"
"v_add_f32 v65, v233, v65\n"
"v_mfma_f32_16x16x32_bf16 v[116:119], a[184:187], a[136:139], v[116:119]\n"
"v_add_f32 v66, v234, v66\n"
"v_mfma_f32_16x16x32_bf16 v[116:119], a[188:191], a[140:143], v[116:119]\n"
"v_add_f32 v67, v235, v67\n"
"v_mfma_f32_16x16x32_bf16 v[120:123], a[184:187], a[144:147], v[120:123]\n"
"v_add_f32 v68, v232, v68\n"
"v_mfma_f32_16x16x32_bf16 v[120:123], a[188:191], a[148:151], v[120:123]\n"
"v_add_f32 v69, v233, v69\n"
"v_mfma_f32_16x16x32_bf16 v[124:127], a[184:187], a[152:155], v[124:127]\n"
"global_load_dword a184, v250, s[64:65]\n"
"global_load_dwordx4 a[184:187], v246, s[16:17] offset:2048 sc1\n"
"v_mfma_f32_16x16x32_bf16 v[124:127], a[188:191], a[156:159], v[124:127]\n"
"s_waitcnt vmcnt(21)\n"
"v_mfma_f32_16x16x32_bf16 v[128:131], a[192:195], a[96:99], v[128:131]\n"
"global_load_dwordx4 a[188:191], v246, s[16:17] offset:3072 sc1\n"
"v_mfma_f32_16x16x32_bf16 v[128:131], a[196:199], a[100:103], v[128:131]\n"
"v_add_f32 v70, v234, v70\n"
"v_mfma_f32_16x16x32_bf16 v[132:135], a[192:195], a[104:107], v[132:135]\n"
"v_add_u32 v246, 0x2000, v246\n"
"v_mfma_f32_16x16x32_bf16 v[132:135], a[196:199], a[108:111], v[132:135]\n"
"v_add_f32 v71, v235, v71\n"
"v_mfma_f32_16x16x32_bf16 v[136:139], a[192:195], a[112:115], v[136:139]\n"
"v_add_u32 v249, 33536, v248\n"
"v_mfma_f32_16x16x32_bf16 v[136:139], a[196:199], a[116:119], v[136:139]\n"
"s_waitcnt lgkmcnt(1)\n"
"global_store_dwordx4 v249, v[224:227], s[24:25] sc1\n"
"v_mfma_f32_16x16x32_bf16 v[140:143], a[192:195], a[120:123], v[140:143]\n"
"v_add_f32 v72, v232, v72\n"
"v_mfma_f32_16x16x32_bf16 v[140:143], a[196:199], a[124:127], v[140:143]\n"
"v_add_u32 v238, s66, v238\n"
"v_mfma_f32_16x16x32_bf16 v[144:147], a[192:195], a[128:131], v[144:147]\n"
"global_load_lds_dwordx4 v238, s[26:27] offset:3072 sc1\n"
"v_mfma_f32_16x16x32_bf16 v[144:147], a[196:199], a[132:135], v[144:147]\n"
"v_add_f32 v73, v233, v73\n"
"v_mfma_f32_16x16x32_bf16 v[148:151], a[192:195], a[136:139], v[148:151]\n"
"v_add_f32 v74, v234, v74\n"
"v_mfma_f32_16x16x32_bf16 v[148:151], a[196:199], a[140:143], v[148:151]\n"
"v_add_f32 v75, v235, v75\n"
"v_mfma_f32_16x16x32_bf16 v[152:155], a[192:195], a[144:147], v[152:155]\n"
"v_add_f32 v76, v232, v76\n"
"v_mfma_f32_16x16x32_bf16 v[152:155], a[196:199], a[148:151], v[152:155]\n"
"v_add_f32 v77, v233, v77\n"
"v_mfma_f32_16x16x32_bf16 v[156:159], a[192:195], a[152:155], v[156:159]\n"
"global_load_dwordx4 a[192:195], v246, s[16:17] offset:-4096 sc1\n"
"v_mfma_f32_16x16x32_bf16 v[156:159], a[196:199], a[156:159], v[156:159]\n"
"s_waitcnt vmcnt(23)\n"
"v_mfma_f32_16x16x32_bf16 v[160:163], a[200:203], a[96:99], v[160:163]\n"
"global_load_dwordx4 a[196:199], v246, s[16:17] offset:-3072 sc1\n"
"v_mfma_f32_16x16x32_bf16 v[160:163], a[204:207], a[100:103], v[160:163]\n"
"s_waitcnt vmcnt(14)\n"
"v_readfirstlane_b32 s46, v239\n"
"v_mfma_f32_16x16x32_bf16 v[164:167], a[200:203], a[104:107], v[164:167]\n"
"v_add_f32 v78, v234, v78\n"
"v_mfma_f32_16x16x32_bf16 v[164:167], a[204:207], a[108:111], v[164:167]\n"
"v_add_f32 v79, v235, v79\n"
"v_mfma_f32_16x16x32_bf16 v[168:171], a[200:203], a[112:115], v[168:171]\n"
"s_sub_u32 s46, s46, s44\n"
"v_mfma_f32_16x16x32_bf16 v[168:171], a[204:207], a[116:119], v[168:171]\n"
"s_cmp_ge_u32 s46, 48768\n"
"s_cbranch_scc0 .Lslow3_%=\n"
".Lback3_%=:\n"
"v_mfma_f32_16x16x32_bf16 v[172:175], a[200:203], a[120:123], v[172:175]\n"
"s_add_u32 m0, s42, 0\n"
"v_add_u32 v238, 0, v248\n"
"v_mfma_f32_16x16x32_bf16 v[172:175], a[204:207], a[124:127], v[172:175]\n"
"global_load_lds_dwordx4 v238, s[26:27] offset:0 sc1\n"
"v_mfma_f32_16x16x32_bf16 v[176:179], a[200:203], a[128:131], v[176:179]\n"
"v_add_f32 v80, v232, v80\n"
"v_mfma_f32_16x16x32_bf16 v[176:179], a[204:207], a[132:135], v[176:179]\n"
"v_add_f32 v81, v233, v81\n"
"v_mfma_f32_16x16x32_bf16 v[180:183], a[200:203], a[136:139], v[180:183]\n"
"v_add_f32 v82, v234, v82\n"
"v_mfma_f32_16x16x32_bf16 v[180:183], a[204:207], a[140:143], v[180:183]\n"
"v_add_f32 v83, v235, v83\n"
"v_mfma_f32_16x16x32_bf16 v[184:187], a[200:203], a[144:147], v[184:187]\n"
"v_add_f32 v84, v232, v84\n"
"v_mfma_f32_16x16x32_bf16 v[184:187], a[204:207], a[148:151], v[184:187]\n"
"v_add_f32 v85, v233, v85\n"
"v_mfma_f32_16x16x32_bf16 v[188:191], a[200:203], a[152:155], v[188:191]\n"
"global_load_dwordx4 a[200:203], v246, s[16:17] offset:-2048 sc1\n"
"v_mfma_f32_16x16x32_bf16 v[188:191], a[204:207], a[156:159], v[188:191]\n"
"v_mfma_f32_16x16x32_bf16 v[192:195], a[208:211], a[96:99], v[192:195]\n"
"global_load_dwordx4 a[204:207], v246, s[16:17] offset:-1024 sc1\n"
"v_mfma_f32_16x16x32_bf16 v[192:195], a[212:215], a[100:103], v[192:195]\n"
"v_add_f32 v86, v234, v86\n"
"v_mfma_f32_16x16x32_bf16 v[196:199], a[208:211], a[104:107], v[196:199]\n"
"v_add_f32 v87, v235, v87\n"
"v_mfma_f32_16x16x32_bf16 v[196:199], a[212:215], a[108:111], v[196:199]\n"
"v_add_f32 v88, v232, v88\n"
"v_mfma_f32_16x16x32_bf16 v[200:203], a[208:211], a[112:115], v[200:203]\n"
"v_add_f32 v89, v233, v89\n"
"v_mfma_f32_16x16x32_bf16 v[200:203], a[212:215], a[116:119], v[200:203]\n"
"v_add_f32 v90, v234, v90\n"
"v_mfma_f32_16x16x32_bf16 v[204:207], a[208:211], a[120:123], v[204:207]\n"
"v_add_f32 v91, v235, v91\n"
"v_mfma_f32_16x16x32_bf16 v[204:207], a[212:215], a[124:127], v[204:207]\n"
"v_add_f32 v92, v232, v92\n"
"v_mfma_f32_16x16x32_bf16 v[208:211], a[208:211], a[128:131], v[208:211]\n"
"v_add_f32 v93, v233, v93\n"
"v_mfma_f32_16x16x32_bf16 v[208:211], a[212:215], a[132:135], v[208:211]\n"
"v_add_f32 v94, v234, v94\n"
"v_mfma_f32_16x16x32_bf16 v[212:215], a[208:211], a[136:139], v[212:215]\n"
"v_add_f32 v95, v235, v95\n"
"v_mfma_f32_16x16x32_bf16 v[212:215], a[212:215], a[140:143], v[212:215]\n"
"v_mfma_f32_16x16x32_bf16 v[216:219], a[208:211], a[144:147], v[216:219]\n"
"v_add_u32 v249, 49920, v248\n"
"v_mfma_f32_16x16x32_bf16 v[216:219], a[212:215], a[148:151], v[216:219]\n"
"s_waitcnt lgkmcnt(0)\n"
"global_store_dwordx4 v249, v[228:231], s[24:25] sc1\n"
"v_mfma_f32_16x16x32_bf16 v[220:223], a[208:211], a[152:155], v[220:223]\n"
"global_load_dwordx4 a[208:211], v246, s[16:17] offset:0 sc1\n"
"v_mfma_f32_16x16x32_bf16 v[220:223], a[212:215], a[156:159], v[220:223]\n"
"v_mfma_f32_16x16x32_bf16 a[0:3], a[216:219], a[96:99], a[0:3]\n"
"global_load_dwordx4 a[212:215], v246, s[16:17] offset:1024 sc1\n"
"ds_read_b128 a[96:99], v240 offset:49152\n"
"v_mfma_f32_16x16x32_bf16 a[0:3], a[220:223], a[100:103], a[0:3]\n"
"ds_read_b128 a[100:103], v241 offset:49152\n"
"v_mfma_f32_16x16x32_bf16 a[4:7], a[216:219], a[104:107], a[4:7]\n"
"ds_read_b128 a[104:107], v240 offset:51200\n"
"v_mfma_f32_16x16x32_bf16 a[4:7], a[220:223], a[108:111], a[4:7]\n"
"ds_read_b128 a[108:111], v241 offset:51200\n"
"v_mfma_f32_16x16x32_bf16 a[8:11], a[216:219], a[112:115], a[8:11]\n"
"ds_read_b128 a[112:115], v240 offset:53248\n"
"v_mfma_f32_16x16x32_bf16 a[8:11], a[220:223], a[116:119], a[8:11]\n"
"ds_read_b128 a[116:119], v241 offset:53248\n"
"v_mfma_f32_16x16x32_bf16 a[12:15], a[216:219], a[120:123], a[12:15]\n"
"ds_read_b128 a[120:123], v240 offset:55296\n"
"v_mfma_f32_16x16x32_bf16 a[12:15], a[220:223], a[124:127], a[12:15]\n"
"ds_read_b128 a[124:127], v241 offset:55296\n"
"v_mfma_f32_16x16x32_bf16 a[16:19], a[216:219], a[128:131], a[16:19]\n"
"ds_read_b128 a[128:131], v240 offset:57344\n"
"v_mfma_f32_16x16x32_bf16 a[16:19], a[220:223], a[132:135], a[16:19]\n"
"ds_read_b128 a[132:135], v241 offset:57344\n"
"v_mfma_f32_16x16x32_bf16 a[20:23], a[216:219], a[136:139], a[20:23]\n"
"ds_read_b128 a[136:139], v240 offset:59392\n"
"v_mfma_f32_16x16x32_bf16 a[20:23], a[220:223], a[140:143], a[20:23]\n"
"ds_read_b128 a[140:143], v241 offset:59392\n"
"v_mfma_f32_16x16x32_bf16 a[24:27], a[216:219], a[144:147], a[24:27]\n"
"ds_read_b128 a[144:147], v240 offset:61440\n"
"v_mfma_f32_16x16x32_bf16 a[24:27], a[220:223], a[148:151], a[24:27]\n"
"ds_read_b128 a[148:151], v241 offset:61440\n"
"v_mfma_f32_16x16x32_bf16 a[28:31], a[216:219], a[152:155], a[28:31]\n"
"global_load_dwordx4 a[216:219], v246, s[16:17] offset:2048 sc1\n"
"ds_read_b128 a[152:155], v240 offset:63488\n"
"v_mfma_f32_16x16x32_bf16 a[28:31], a[220:223], a[156:159], a[28:31]\n"
"ds_read_b128 a[156:159], v241 offset:63488\n"
"s_waitcnt lgkmcnt(14)\n"
"v_mfma_f32_16x16x32_bf16 v[0:3], a[160:163], a[96:99], v[0:3]\n"
"global_load_dwordx4 a[220:223], v246, s[16:17] offset:3072 sc1\n"
"v_mfma_f32_16x16x32_bf16 v[0:3], a[164:167], a[100:103], v[0:3]\n"
"s_waitcnt lgkmcnt(12)\n"
"v_mfma_f32_16x16x32_bf16 v[4:7], a[160:163], a[104:107], v[4:7]\n"
"v_add_u32 v246, 0x2000, v246\n"
"v_mfma_f32_16x16x32_bf16 v[4:7], a[164:167], a[108:111], v[4:7]\n"
"s_waitcnt lgkmcnt(10)\n"
"v_mfma_f32_16x16x32_bf16 v[8:11], a[160:163], a[112:115], v[8:11]\n"
"v_xor_b32 v247, 0x10000, v247\n"
"v_mfma_f32_16x16x32_bf16 v[8:11], a[164:167], a[116:119], v[8:11]\n"
"s_waitcnt lgkmcnt(8)\n"
"v_mfma_f32_16x16x32_bf16 v[12:15], a[160:163], a[120:123], v[12:15]\n"
"v_add_u32 v238, s66, v238\n"
"v_mfma_f32_16x16x32_bf16 v[12:15], a[164:167], a[124:127], v[12:15]\n"
"s_waitcnt lgkmcnt(6)\n"
"v_mfma_f32_16x16x32_bf16 v[16:19], a[160:163], a[128:131], v[16:19]\n"
"global_load_lds_dwordx4 v238, s[26:27] offset:1024 sc1\n"
"v_mfma_f32_16x16x32_bf16 v[16:19], a[164:167], a[132:135], v[16:19]\n"
"s_waitcnt lgkmcnt(4)\n"
"v_mfma_f32_16x16x32_bf16 v[20:23], a[160:163], a[136:139], v[20:23]\n"
"v_mfma_f32_16x16x32_bf16 v[20:23], a[164:167], a[140:143], v[20:23]\n"
"s_waitcnt lgkmcnt(2)\n"
"v_mfma_f32_16x16x32_bf16 v[24:27], a[160:163], a[144:147], v[24:27]\n"
"s_waitcnt vmcnt(22)\n"
"v_and_b32 v235, s67, v237\n"
"v_lshlrev_b32 v234, 16, v237\n"
"v_mfma_f32_16x16x32_bf16 v[24:27], a[164:167], a[148:151], v[24:27]\n"
"v_and_b32 v233, s67, v236\n"
"s_waitcnt lgkmcnt(0)\n"
"v_mfma_f32_16x16x32_bf16 v[28:31], a[160:163], a[152:155], v[28:31]\n"
"global_load_dwordx4 a[160:163], v246, s[16:17] offset:-4096 sc1\n"
"v_lshlrev_b32 v232, 16, v236\n"
"v_mfma_f32_16x16x32_bf16 v[28:31], a[164:167], a[156:159], v[28:31]\n"
"v_lshrrev_b32 v237, 4, v247\n"
"s_waitcnt vmcnt(20)\n"
"v_mfma_f32_16x16x32_bf16 v[32:35], a[168:171], a[96:99], v[32:35]\n"
"global_load_dwordx4 a[164:167], v246, s[16:17] offset:-3072 sc1\n"
"v_and_b32 v237, 0x30, v237\n"
"v_mfma_f32_16x16x32_bf16 v[32:35], a[172:175], a[100:103], v[32:35]\n"
"global_load_dwordx2 v[236:237], v237, s[18:19] offset:200\n"
"v_mfma_f32_16x16x32_bf16 v[36:39], a[168:171], a[104:107], v[36:39]\n"
"v_add_f32 v96, v232, v96\n"
"v_mfma_f32_16x16x32_bf16 v[36:39], a[172:175], a[108:111], v[36:39]\n"
"v_add_f32 v97, v233, v97\n"
"v_mfma_f32_16x16x32_bf16 v[40:43], a[168:171], a[112:115], v[40:43]\n"
"v_add_f32 v98, v234, v98\n"
"v_mfma_f32_16x16x32_bf16 v[40:43], a[172:175], a[116:119], v[40:43]\n"
"v_add_f32 v99, v235, v99\n"
"v_mfma_f32_16x16x32_bf16 v[44:47], a[168:171], a[120:123], v[44:47]\n"
"v_add_f32 v100, v232, v100\n"
"v_mfma_f32_16x16x32_bf16 v[44:47], a[172:175], a[124:127], v[44:47]\n"
"v_add_f32 v101, v233, v101\n"
"v_mfma_f32_16x16x32_bf16 v[48:51], a[168:171], a[128:131], v[48:51]\n"
"v_add_f32 v102, v234, v102\n"
"v_mfma_f32_16x16x32_bf16 v[48:51], a[172:175], a[132:135], v[48:51]\n"
"v_add_f32 v103, v235, v103\n"
"v_mfma_f32_16x16x32_bf16 v[52:55], a[168:171], a[136:139], v[52:55]\n"
"v_add_f32 v104, v232, v104\n"
"v_mfma_f32_16x16x32_bf16 v[52:55], a[172:175], a[140:143], v[52:55]\n"
"v_add_f32 v105, v233, v105\n"
"v_mfma_f32_16x16x32_bf16 v[56:59], a[168:171], a[144:147], v[56:59]\n"
"v_add_f32 v106, v234, v106\n"
"v_mfma_f32_16x16x32_bf16 v[56:59], a[172:175], a[148:151], v[56:59]\n"
"v_add_f32 v107, v235, v107\n"
"v_mfma_f32_16x16x32_bf16 v[60:63], a[168:171], a[152:155], v[60:63]\n"
"global_load_dwordx4 a[168:171], v246, s[16:17] offset:-2048 sc1\n"
"v_mfma_f32_16x16x32_bf16 v[60:63], a[172:175], a[156:159], v[60:63]\n"
"s_waitcnt vmcnt(20)\n"
"v_mfma_f32_16x16x32_bf16 v[64:67], a[176:179], a[96:99], v[64:67]\n"
"global_load_dwordx4 a[172:175], v246, s[16:17] offset:-1024 sc1\n"
"v_mfma_f32_16x16x32_bf16 v[64:67], a[180:183], a[100:103], v[64:67]\n"
"v_add_f32 v108, v232, v108\n"
"v_mfma_f32_16x16x32_bf16 v[68:71], a[176:179], a[104:107], v[68:71]\n"
"v_add_f32 v109, v233, v109\n"
"v_mfma_f32_16x16x32_bf16 v[68:71], a[180:183], a[108:111], v[68:71]\n"
"v_add_f32 v110, v234, v110\n"
"v_mfma_f32_16x16x32_bf16 v[72:75], a[176:179], a[112:115], v[72:75]\n"
"v_add_f32 v111, v235, v111\n"
"v_mfma_f32_16x16x32_bf16 v[72:75], a[180:183], a[116:119], v[72:75]\n"
"v_add_u32 v238, s66, v238\n"
"v_mfma_f32_16x16x32_bf16 v[76:79], a[176:179], a[120:123], v[76:79]\n"
"global_load_lds_dwordx4 v238, s[26:27] offset:2048 sc1\n"
"v_mfma_f32_16x16x32_bf16 v[76:79], a[180:183], a[124:127], v[76:79]\n"
"v_add_f32 v112, v232, v112\n"
"v_mfma_f32_16x16x32_bf16 v[80:83], a[176:179], a[128:131], v[80:83]\n"
"v_add_f32 v113, v233, v113\n"
"v_mfma_f32_16x16x32_bf16 v[80:83], a[180:183], a[132:135], v[80:83]\n"
"v_add_f32 v114, v234, v114\n"
"v_mfma_f32_16x16x32_bf16 v[84:87], a[176:179], a[136:139], v[84:87]\n"
"v_add_f32 v115, v235, v115\n"
"v_mfma_f32_16x16x32_bf16 v[84:87], a[180:183], a[140:143], v[84:87]\n"
"v_add_f32 v116, v232, v116\n"
"v_mfma_f32_16x16x32_bf16 v[88:91], a[176:179], a[144:147], v[88:91]\n"
"v_mfma_f32_16x16x32_bf16 v[88:91], a[180:183], a[148:151], v[88:91]\n"
"v_mfma_f32_16x16x32_bf16 v[92:95], a[176:179], a[152:155], v[92:95]\n"
"global_load_dwordx4 a[176:179], v246, s[16:17] offset:0 sc1\n"
"v_mfma_f32_16x16x32_bf16 v[92:95], a[180:183], a[156:159], v[92:95]\n"
"s_waitcnt vmcnt(20)\n"
"v_mfma_f32_16x16x32_bf16 v[96:99], a[184:187], a[96:99], v[96:99]\n"
"global_load_dwordx4 a[180:183], v246, s[16:17] offset:1024 sc1\n"
"v_mfma_f32_16x16x32_bf16 v[96:99], a[188:191], a[100:103], v[96:99]\n"
"v_mfma_f32_16x16x32_bf16 v[100:103], a[184:187], a[104:107], v[100:103]\n"
"v_mfma_f32_16x16x32_bf16 v[100:103], a[188:191], a[108:111], v[100:103]\n"
"v_mfma_f32_16x16x32_bf16 v[104:107], a[184:187], a[112:115], v[104:107]\n"
"v_mfma_f32_16x16x32_bf16 v[104:107], a[188:191], a[116:119], v[104:107]\n"
"v_mfma_f32_16x16x32_bf16 v[108:111], a[184:187], a[120:123], v[108:111]\n"
"v_mfma_f32_16x16x32_bf16 v[108:111], a[188:191], a[124:127], v[108:111]\n"
"v_mfma_f32_16x16x32_bf16 v[112:115], a[184:187], a[128:131], v[112:115]\n"
"v_mfma_f32_16x16x32_bf16 v[112:115], a[188:191], a[132:135], v[112:115]\n"
"v_mfma_f32_16x16x32_bf16 v[116:119], a[184:187], a[136:139], v[116:119]\n"
"v_mfma_f32_16x16x32_bf16 v[116:119], a[188:191], a[140:143], v[116:119]\n"
"v_mfma_f32_16x16x32_bf16 v[120:123], a[184:187], a[144:147], v[120:123]\n"
"v_mfma_f32_16x16x32_bf16 v[120:123], a[188:191], a[148:151], v[120:123]\n"
"v_mfma_f32_16x16x32_bf16 v[124:127], a[184:187], a[152:155], v[124:127]\n"
"global_load_dwordx4 a[184:187], v246, s[16:17] offset:2048 sc1\n"
"v_mfma_f32_16x16x32_bf16 v[124:127], a[188:191], a[156:159], v[124:127]\n"
"s_waitcnt vmcnt(18)\n"
"v_mfma_f32_16x16x32_bf16 v[128:131], a[192:195], a[96:99], v[128:131]\n"
"global_load_dwordx4 a[188:191], v246, s[16:17] offset:3072 sc1\n"
"v_mfma_f32_16x16x32_bf16 v[128:131], a[196:199], a[100:103], v[128:131]\n"
"v_mfma_f32_16x16x32_bf16 v[132:135], a[192:195], a[104:107], v[132:135]\n"
"v_add_u32 v246, 0x2000, v246\n"
"v_mfma_f32_16x16x32_bf16 v[132:135], a[196:199], a[108:111], v[132:135]\n"
"v_add_u32 v238, s66, v238\n"
"v_mfma_f32_16x16x32_bf16 v[136:139], a[192:195], a[112:115], v[136:139]\n"
"global_load_lds_dwordx4 v238, s[26:27] offset:3072 sc1\n"
"v_mfma_f32_16x16x32_bf16 v[136:139], a[196:199], a[116:119], v[136:139]\n"
"v_mfma_f32_16x16x32_bf16 v[140:143], a[192:195], a[120:123], v[140:143]\n"
"v_mfma_f32_16x16x32_bf16 v[140:143], a[196:199], a[124:127], v[140:143]\n"
"v_mfma_f32_16x16x32_bf16 v[144:147], a[192:195], a[128:131], v[144:147]\n"
"v_add_f32 v117, v233, v117\n"
"v_mfma_f32_16x16x32_bf16 v[144:147], a[196:199], a[132:135], v[144:147]\n"
"v_add_f32 v118, v234, v118\n"
"v_mfma_f32_16x16x32_bf16 v[148:151], a[192:195], a[136:139], v[148:151]\n"
"v_add_f32 v119, v235, v119\n"
"v_mfma_f32_16x16x32_bf16 v[148:151], a[196:199], a[140:143], v[148:151]\n"
"v_add_f32 v120, v232, v120\n"
"v_mfma_f32_16x16x32_bf16 v[152:155], a[192:195], a[144:147], v[152:155]\n"
"v_add_f32 v121, v233, v121\n"
"v_mfma_f32_16x16x32_bf16 v[152:155], a[196:199], a[148:151], v[152:155]\n"
"v_add_f32 v122, v234, v122\n"
"v_mfma_f32_16x16x32_bf16 v[156:159], a[192:195], a[152:155], v[156:159]\n"
"global_load_dwordx4 a[192:195], v246, s[16:17] offset:-4096 sc1\n"
"v_mfma_f32_16x16x32_bf16 v[156:159], a[196:199], a[156:159], v[156:159]\n"
"s_waitcnt vmcnt(18)\n"
"v_mfma_f32_16x16x32_bf16 v[160:163], a[200:203], a[96:99], v[160:163]\n"
"global_load_dwordx4 a[196:199], v246, s[16:17] offset:-3072 sc1\n"
"v_mfma_f32_16x16x32_bf16 v[160:163], a[204:207], a[100:103], v[160:163]\n"
"v_add_f32 v123, v235, v123\n"
"v_mfma_f32_16x16x32_bf16 v[164:167], a[200:203], a[104:107], v[164:167]\n"
"v_add_f32 v124, v232, v124\n"
"v_mfma_f32_16x16x32_bf16 v[164:167], a[204:207], a[108:111], v[164:167]\n"
"v_add_f32 v125, v233, v125\n"
"v_mfma_f32_16x16x32_bf16 v[168:171], a[200:203], a[112:115], v[168:171]\n"
"v_add_f32 v126, v234, v126\n"
"v_mfma_f32_16x16x32_bf16 v[168:171], a[204:207], a[116:119], v[168:171]\n"
"v_add_f32 v127, v235, v127\n"
"v_mfma_f32_16x16x32_bf16 v[172:175], a[200:203], a[120:123], v[172:175]\n"
"v_mfma_f32_16x16x32_bf16 v[172:175], a[204:207], a[124:127], v[172:175]\n"
"v_mfma_f32_16x16x32_bf16 v[176:179], a[200:203], a[128:131], v[176:179]\n"
"v_mfma_f32_16x16x32_bf16 v[176:179], a[204:207], a[132:135], v[176:179]\n"
"v_mfma_f32_16x16x32_bf16 v[180:183], a[200:203], a[136:139], v[180:183]\n"
"v_mfma_f32_16x16x32_bf16 v[180:183], a[204:207], a[140:143], v[180:183]\n"
"v_mfma_f32_16x16x32_bf16 v[184:187], a[200:203], a[144:147], v[184:187]\n"
"v_mfma_f32_16x16x32_bf16 v[184:187], a[204:207], a[148:151], v[184:187]\n"
"v_mfma_f32_16x16x32_bf16 v[188:191], a[200:203], a[152:155], v[188:191]\n"
"global_load_dwordx4 a[200:203], v246, s[16:17] offset:-2048 sc1\n"
"v_mfma_f32_16x16x32_bf16 v[188:191], a[204:207], a[156:159], v[188:191]\n"
"s_waitcnt vmcnt(17)\n"
"v_mfma_f32_16x16x32_bf16 v[192:195], a[208:211], a[96:99], v[192:195]\n"
"global_load_dwordx4 a[204:207], v246, s[16:17] offset:-1024 sc1\n"
"v_mfma_f32_16x16x32_bf16 v[192:195], a[212:215], a[100:103], v[192:195]\n"
"s_add_u32 m0, s42, 32768\n"
"v_add_u32 v238, 256, v248\n"
"v_mfma_f32_16x16x32_bf16 v[196:199], a[208:211], a[104:107], v[196:199]\n"
"global_load_lds_dwordx4 v238, s[26:27] offset:0 sc1\n"
"v_mfma_f32_16x16x32_bf16 v[196:199], a[212:215], a[108:111], v[196:199]\n"
"v_mfma_f32_16x16x32_bf16 v[200:203], a[208:211], a[112:115], v[200:203]\n"
"v_mfma_f32_16x16x32_bf16 v[200:203], a[212:215], a[116:119], v[200:203]\n"
"v_mfma_f32_16x16x32_bf16 v[204:207], a[208:211], a[120:123], v[204:207]\n"
"v_mfma_f32_16x16x32_bf16 v[204:207], a[212:215], a[124:127], v[204:207]\n"
"v_mfma_f32_16x16x32_bf16 v[208:211], a[208:211], a[128:131], v[208:211]\n"
"v_mfma_f32_16x16x32_bf16 v[208:211], a[212:215], a[132:135], v[208:211]\n"
"v_mfma_f32_16x16x32_bf16 v[212:215], a[208:211], a[136:139], v[212:215]\n"
"v_mfma_f32_16x16x32_bf16 v[212:215], a[212:215], a[140:143], v[212:215]\n"
"v_mfma_f32_16x16x32_bf16 v[216:219], a[208:211], a[144:147], v[216:219]\n"
"s_mov_b64 exec, 1\n"
"global_atomic_add v250, v252, s[30:31] offset:0 sc1\n"
"s_mov_b64 exec, -1\n"
"v_mfma_f32_16x16x32_bf16 v[216:219], a[212:215], a[148:151], v[216:219]\n"
"s_barrier\n"
"v_mfma_f32_16x16x32_bf16 v[220:223], a[208:211], a[152:155], v[220:223]\n"
"global_load_dwordx4 a[208:211], v246, s[16:17] offset:0 sc1\n"
"v_mfma_f32_16x16x32_bf16 v[220:223], a[212:215], a[156:159], v[220:223]\n"
"s_waitcnt vmcnt(19)\n"
"v_mfma_f32_16x16x32_bf16 a[0:3], a[216:219], a[96:99], a[0:3]\n"
"global_load_dwordx4 a[212:215], v246, s[16:17] offset:1024 sc1\n"
"ds_read_b128 a[96:99], v242 offset:16384\n"
"v_mfma_f32_16x16x32_bf16 a[0:3], a[220:223], a[100:103], a[0:3]\n"
"ds_read_b128 a[100:103], v243 offset:16384\n"
"v_mfma_f32_16x16x32_bf16 a[4:7], a[216:219], a[104:107], a[4:7]\n"
"ds_read_b128 a[104:107], v242 offset:18432\n"
"v_mfma_f32_16x16x32_bf16 a[4:7], a[220:223], a[108:111], a[4:7]\n"
"ds_read_b128 a[108:111], v243 offset:18432\n"
"v_mfma_f32_16x16x32_bf16 a[8:11], a[216:219], a[112:115], a[8:11]\n"
"ds_read_b128 a[112:115], v242 offset:20480\n"
"v_mfma_f32_16x16x32_bf16 a[8:11], a[220:223], a[116:119], a[8:11]\n"
"ds_read_b128 a[116:119], v243 offset:20480\n"
"v_mfma_f32_16x16x32_bf16 a[12:15], a[216:219], a[120:123], a[12:15]\n"
"ds_read_b128 a[120:123], v242 offset:22528\n"
"v_mfma_f32_16x16x32_bf16 a[12:15], a[220:223], a[124:127], a[12:15]\n"
"ds_read_b128 a[124:127], v243 offset:22528\n"
"v_mfma_f32_16x16x32_bf16 a[16:19], a[216:219], a[128:131], a[16:19]\n"
"ds_read_b128 a[128:131], v242 offset:24576\n"
"v_mfma_f32_16x16x32_bf16 a[16:19], a[220:223], a[132:135], a[16:19]\n"
"ds_read_b128 a[132:135], v243 offset:24576\n"
"v_mfma_f32_16x16x32_bf16 a[20:23], a[216:219], a[136:139], a[20:23]\n"
"ds_read_b128 a[136:139], v242 offset:26624\n"
"v_mfma_f32_16x16x32_bf16 a[20:23], a[220:223], a[140:143], a[20:23]\n"
"ds_read_b128 a[140:143], v243 offset:26624\n"
"v_mfma_f32_16x16x32_bf16 a[24:27], a[216:219], a[144:147], a[24:27]\n"
"ds_read_b128 a[144:147], v242 offset:28672\n"
"v_mfma_f32_16x16x32_bf16 a[24:27], a[220:223], a[148:151], a[24:27]\n"
"ds_read_b128 a[148:151], v243 offset:28672\n"
"v_mfma_f32_16x16x32_bf16 a[28:31], a[216:219], a[152:155], a[28:31]\n"
"global_load_dwordx4 a[216:219], v246, s[16:17] offset:2048 sc1\n"
"ds_read_b128 a[152:155], v242 offset:30720\n"
"v_mfma_f32_16x16x32_bf16 a[28:31], a[220:223], a[156:159], a[28:31]\n"
"ds_read_b128 a[156:159], v243 offset:30720\n"
"s_waitcnt vmcnt(18) lgkmcnt(14)\n"
"v_mfma_f32_16x16x32_bf16 v[0:3], a[160:163], a[96:99], v[0:3]\n"
"global_load_dwordx4 a[220:223], v246, s[16:17] offset:3072 sc1\n"
"v_mfma_f32_16x16x32_bf16 v[0:3], a[164:167], a[100:103], v[0:3]\n"
"s_waitcnt lgkmcnt(12)\n"
"v_mfma_f32_16x16x32_bf16 v[4:7], a[160:163], a[104:107], v[4:7]\n"
"v_add_u32 v246, 0x2000, v246\n"
"s_waitcnt vmcnt(18)\n"
"v_and_b32 v235, s67, v237\n"
"v_mfma_f32_16x16x32_bf16 v[4:7], a[164:167], a[108:111], v[4:7]\n"
"v_lshlrev_b32 v234, 16, v237\n"
"s_waitcnt lgkmcnt(10)\n"
"v_mfma_f32_16x16x32_bf16 v[8:11], a[160:163], a[112:115], v[8:11]\n"
"v_add_u32 v238, s66, v238\n"
"v_and_b32 v233, s67, v236\n"
"v_mfma_f32_16x16x32_bf16 v[8:11], a[164:167], a[116:119], v[8:11]\n"
"v_lshlrev_b32 v232, 16, v236\n"
"s_waitcnt lgkmcnt(8)\n"
"v_mfma_f32_16x16x32_bf16 v[12:15], a[160:163], a[120:123], v[12:15]\n"
"global_load_lds_dwordx4 v238, s[26:27] offset:1024 sc1\n"
"v_mfma_f32_16x16x32_bf16 v[12:15], a[164:167], a[124:127], v[12:15]\n"
"s_waitcnt lgkmcnt(6)\n"
"v_mfma_f32_16x16x32_bf16 v[16:19], a[160:163], a[128:131], v[16:19]\n"
"v_accvgpr_read_b32 v249, a0\n"
"v_mfma_f32_16x16x32_bf16 v[16:19], a[164:167], a[132:135], v[16:19]\n"
"v_add_f32 v249, v232, v249\n"
"s_waitcnt lgkmcnt(4)\n"
"v_mfma_f32_16x16x32_bf16 v[20:23], a[160:163], a[136:139], v[20:23]\n"
"v_accvgpr_write_b32 a0, v249\n"
"v_mfma_f32_16x16x32_bf16 v[20:23], a[164:167], a[140:143], v[20:23]\n"
"s_waitcnt lgkmcnt(2)\n"
"v_mfma_f32_16x16x32_bf16 v[24:27], a[160:163], a[144:147], v[24:27]\n"
"v_accvgpr_read_b32 v249, a1\n"
"v_mfma_f32_16x16x32_bf16 v[24:27], a[164:167], a[148:151], v[24:27]\n"
"v_add_f32 v249, v233, v249\n"
"s_waitcnt lgkmcnt(0)\n"
"v_mfma_f32_16x16x32_bf16 v[28:31], a[160:163], a[152:155], v[28:31]\n"
"global_load_dwordx4 a[160:163], v246, s[16:17] offset:-4096 sc1\n"
"v_accvgpr_write_b32 a1, v249\n"
"v_mfma_f32_16x16x32_bf16 v[28:31], a[164:167], a[156:159], v[28:31]\n"
"s_waitcnt vmcnt(18)\n"
"v_mfma_f32_16x16x32_bf16 v[32:35], a[168:171], a[96:99], v[32:35]\n"
"global_load_dwordx4 a[164:167], v246, s[16:17] offset:-3072 sc1\n"
"v_mfma_f32_16x16x32_bf16 v[32:35], a[172:175], a[100:103], v[32:35]\n"
"v_accvgpr_read_b32 v249, a2\n"
"v_mfma_f32_16x16x32_bf16 v[36:39], a[168:171], a[104:107], v[36:39]\n"
"v_add_f32 v249, v234, v249\n"
"v_mfma_f32_16x16x32_bf16 v[36:39], a[172:175], a[108:111], v[36:39]\n"
"v_accvgpr_write_b32 a2, v249\n"
"v_mfma_f32_16x16x32_bf16 v[40:43], a[168:171], a[112:115], v[40:43]\n"
"v_accvgpr_read_b32 v249, a3\n"
"v_mfma_f32_16x16x32_bf16 v[40:43], a[172:175], a[116:119], v[40:43]\n"
"v_add_f32 v249, v235, v249\n"
"v_mfma_f32_16x16x32_bf16 v[44:47], a[168:171], a[120:123], v[44:47]\n"
"v_accvgpr_write_b32 a3, v249\n"
"v_mfma_f32_16x16x32_bf16 v[44:47], a[172:175], a[124:127], v[44:47]\n"
"v_accvgpr_read_b32 v249, a4\n"
"v_mfma_f32_16x16x32_bf16 v[48:51], a[168:171], a[128:131], v[48:51]\n"
"v_add_f32 v249, v232, v249\n"
"v_mfma_f32_16x16x32_bf16 v[48:51], a[172:175], a[132:135], v[48:51]\n"
"v_accvgpr_write_b32 a4, v249\n"
"v_mfma_f32_16x16x32_bf16 v[52:55], a[168:171], a[136:139], v[52:55]\n"
"v_accvgpr_read_b32 v249, a5\n"
"v_mfma_f32_16x16x32_bf16 v[52:55], a[172:175], a[140:143], v[52:55]\n"
"v_add_f32 v249, v233, v249\n"
"v_mfma_f32_16x16x32_bf16 v[56:59], a[168:171], a[144:147], v[56:59]\n"
"v_accvgpr_write_b32 a5, v249\n"
"v_mfma_f32_16x16x32_bf16 v[56:59], a[172:175], a[148:151], v[56:59]\n"
"v_accvgpr_read_b32 v249, a6\n"
"v_mfma_f32_16x16x32_bf16 v[60:63], a[168:171], a[152:155], v[60:63]\n"
"global_load_dwordx4 a[168:171], v246, s[16:17] offset:-2048 sc1\n"
"v_add_f32 v249, v234, v249\n"
"v_mfma_f32_16x16x32_bf16 v[60:63], a[172:175], a[156:159], v[60:63]\n"
"v_accvgpr_write_b32 a6, v249\n"
"s_waitcnt vmcnt(17)\n"
"v_mfma_f32_16x16x32_bf16 v[64:67], a[176:179], a[96:99], v[64:67]\n"
"global_load_dwordx4 a[172:175], v246, s[16:17] offset:-1024 sc1\n"
"v_mfma_f32_16x16x32_bf16 v[64:67], a[180:183], a[100:103], v[64:67]\n"
"v_accvgpr_read_b32 v249, a7\n"
"v_mfma_f32_16x16x32_bf16 v[68:71], a[176:179], a[104:107], v[68:71]\n"
"v_add_f32 v249, v235, v249\n"
"v_mfma_f32_16x16x32_bf16 v[68:71], a[180:183], a[108:111], v[68:71]\n"
"v_add_u32 v238, s66, v238\n"
"v_accvgpr_write_b32 a7, v249\n"
"v_mfma_f32_16x16x32_bf16 v[72:75], a[176:179], a[112:115], v[72:75]\n"
"global_load_lds_dwordx4 v238, s[26:27] offset:2048 sc1\n"
"v_mfma_f32_16x16x32_bf16 v[72:75], a[180:183], a[116:119], v[72:75]\n"
"v_accvgpr_read_b32 v249, a8\n"
"v_mfma_f32_16x16x32_bf16 v[76:79], a[176:179], a[120:123], v[76:79]\n"
"v_add_f32 v249, v232, v249\n"
"v_mfma_f32_16x16x32_bf16 v[76:79], a[180:183], a[124:127], v[76:79]\n"
"v_accvgpr_write_b32 a8, v249\n"
"v_mfma_f32_16x16x32_bf16 v[80:83], a[176:179], a[128:131], v[80:83]\n"
"v_accvgpr_read_b32 v249, a9\n"
"v_mfma_f32_16x16x32_bf16 v[80:83], a[180:183], a[132:135], v[80:83]\n"
"v_add_f32 v249, v233, v249\n"
"v_mfma_f32_16x16x32_bf16 v[84:87], a[176:179], a[136:139], v[84:87]\n"
"v_accvgpr_write_b32 a9, v249\n"
"v_mfma_f32_16x16x32_bf16 v[84:87], a[180:183], a[140:143], v[84:87]\n"
"v_accvgpr_read_b32 v249, a10\n"
"v_mfma_f32_16x16x32_bf16 v[88:91], a[176:179], a[144:147], v[88:91]\n"
"v_add_f32 v249, v234, v249\n"
"v_mfma_f32_16x16x32_bf16 v[88:91], a[180:183], a[148:151], v[88:91]\n"
"v_accvgpr_write_b32 a10, v249\n"
"v_mfma_f32_16x16x32_bf16 v[92:95], a[176:179], a[152:155], v[92:95]\n"
"s_nop 0\n"
".Lpg12_%=:\n"
"s_cbranch_execz .Lreta_%=\n"
"global_load_dwordx4 a[176:179], v246, s[16:17] offset:0 sc1\n"
"v_mfma_f32_16x16x32_bf16 v[92:95], a[180:183], a[156:159], v[92:95]\n"
"s_waitcnt vmcnt(18)\n"
"v_mfma_f32_16x16x32_bf16 v[96:99], a[184:187], a[96:99], v[96:99]\n"
"global_load_dwordx4 a[180:183], v246, s[16:17] offset:1024 sc1\n"
"v_mfma_f32_16x16x32_bf16 v[96:99], a[188:191], a[100:103], v[96:99]\n"
"v_accvgpr_read_b32 v249, a11\n"
"v_mfma_f32_16x16x32_bf16 v[100:103], a[184:187], a[104:107], v[100:103]\n"
"global_load_dword v239, v250, s[34:35] sc1\n"
"v_add_f32 v249, v235, v249\n"
"v_mfma_f32_16x16x32_bf16 v[100:103], a[188:191], a[108:111], v[100:103]\n"
"v_accvgpr_write_b32 a11, v249\n"
"v_mfma_f32_16x16x32_bf16 v[104:107], a[184:187], a[112:115], v[104:107]\n"
"v_accvgpr_read_b32 v249, a12\n"
"v_mfma_f32_16x16x32_bf16 v[104:107], a[188:191], a[116:119], v[104:107]\n"
"v_add_f32 v249, v232, v249\n"
"v_mfma_f32_16x16x32_bf16 v[108:111], a[184:187], a[120:123], v[108:111]\n"
"v_accvgpr_write_b32 a12, v249\n"
"v_mfma_f32_16x16x32_bf16 v[108:111], a[188:191], a[124:127], v[108:111]\n"
"v_accvgpr_read_b32 v249, a13\n"
"v_mfma_f32_16x16x32_bf16 v[112:115], a[184:187], a[128:131], v[112:115]\n"
"v_add_f32 v249, v233, v249\n"
"v_mfma_f32_16x16x32_bf16 v[112:115], a[188:191], a[132:135], v[112:115]\n"
"v_accvgpr_write_b32 a13, v249\n"
"v_mfma_f32_16x16x32_bf16 v[116:119], a[184:187], a[136:139], v[116:119]\n"
"v_accvgpr_read_b32 v249, a14\n"
"v_mfma_f32_16x16x32_bf16 v[116:119], a[188:191], a[140:143], v[116:119]\n"
"v_add_f32 v249, v234, v249\n"
"v_mfma_f32_16x16x32_bf16 v[120:123], a[184:187], a[144:147], v[120:123]\n"
"v_accvgpr_write_b32 a14, v249\n"
"v_mfma_f32_16x16x32_bf16 v[120:123], a[188:191], a[148:151], v[120:123]\n"
"v_accvgpr_read_b32 v249, a15\n"
"v_mfma_f32_16x16x32_bf16 v[124:127], a[184:187], a[152:155], v[124:127]\n"
"global_load_dwordx4 a[184:187], v246, s[16:17] offset:2048 sc1\n"
"v_add_f32 v249, v235, v249\n"
"v_mfma_f32_16x16x32_bf16 v[124:127], a[188:191], a[156:159], v[124:127]\n"
"v_accvgpr_write_b32 a15, v249\n"
"s_waitcnt vmcnt(18)\n"
"v_mfma_f32_16x16x32_bf16 v[128:131], a[192:195], a[96:99], v[128:131]\n"
"global_load_dwordx4 a[188:191], v246, s[16:17] offset:3072 sc1\n"
"v_mfma_f32_16x16x32_bf16 v[128:131], a[196:199], a[100:103], v[128:131]\n"
"v_add_u32 v238, s66, v238\n"
"v_mfma_f32_16x16x32_bf16 v[132:135], a[192:195], a[104:107], v[132:135]\n"
"v_add_u32 v246, 0x2000, v246\n"
"v_mfma_f32_16x16x32_bf16 v[132:135], a[196:199], a[108:111], v[132:135]\n"
"global_load_lds_dwordx4 v238, s[26:27] offset:3072 sc1\n"
"v_mfma_f32_16x16x32_bf16 v[136:139], a[192:195], a[112:115], v[136:139]\n"
"v_accvgpr_read_b32 v249, a16\n"
"v_mfma_f32_16x16x32_bf16 v[136:139], a[196:199], a[116:119], v[136:139]\n"
"v_add_f32 v249, v232, v249\n"
"v_mfma_f32_16x16x32_bf16 v[140:143], a[192:195], a[120:123], v[140:143]\n"
"v_accvgpr_write_b32 a16, v249\n"
"v_mfma_f32_16x16x32_bf16 v[140:143], a[196:199], a[124:127], v[140:143]\n"
"v_accvgpr_read_b32 v249, a17\n"
"v_mfma_f32_16x16x32_bf16 v[144:147], a[192:195], a[128:131], v[144:147]\n"
"v_add_f32 v249, v233, v249\n"
"v_mfma_f32_16x16x32_bf16 v[144:147], a[196:199], a[132:135], v[144:147]\n"
"v_accvgpr_write_b32 a17, v249\n"
"v_mfma_f32_16x16x32_bf16 v[148:151], a[192:195], a[136:139], v[148:151]\n"
"v_accvgpr_read_b32 v249, a18\n"
"v_mfma_f32_16x16x32_bf16 v[148:151], a[196:199], a[140:143], v[148:151]\n"
"v_add_f32 v249, v234, v249\n"
"v_mfma_f32_16x16x32_bf16 v[152:155], a[192:195], a[144:147], v[152:155]\n"
"v_accvgpr_write_b32 a18, v249\n"
"v_mfma_f32_16x16x32_bf16 v[152:155], a[196:199], a[148:151], v[152:155]\n"
"v_accvgpr_read_b32 v249, a19\n"
"v_mfma_f32_16x16x32_bf16 v[156:159], a[192:195], a[152:155], v[156:159]\n"
"global_load_dwordx4 a[192:195], v246, s[16:17] offset:-4096 sc1\n"
"v_add_f32 v249, v235, v249\n"
"v_mfma_f32_16x16x32_bf16 v[156:159], a[196:199], a[156:159], v[156:159]\n"
"v_accvgpr_write_b32 a19, v249\n"
"s_waitcnt vmcnt(19)\n"
"v_mfma_f32_16x16x32_bf16 v[160:163], a[200:203], a[96:99], v[160:163]\n"
"global_load_dwordx4 a[196:199], v246, s[16:17] offset:-3072 sc1\n"
"v_mfma_f32_16x16x32_bf16 v[160:163], a[204:207], a[100:103], v[160:163]\n"
"v_accvgpr_read_b32 v249, a20\n"
"v_mfma_f32_16x16x32_bf16 v[164:167], a[200:203], a[104:107], v[164:167]\n"
"v_add_f32 v249, v232, v249\n"
"v_mfma_f32_16x16x32_bf16 v[164:167], a[204:207], a[108:111], v[164:167]\n"
"v_accvgpr_write_b32 a20, v249\n"
"v_mfma_f32_16x16x32_bf16 v[168:171], a[200:203], a[112:115], v[168:171]\n"
"v_accvgpr_read_b32 v249, a21\n"
"v_mfma_f32_16x16x32_bf16 v[168:171], a[204:207], a[116:119], v[168:171]\n"
"v_add_f32 v249, v233, v249\n"
"v_mfma_f32_16x16x32_bf16 v[172:175], a[200:203], a[120:123], v[172:175]\n"
"v_accvgpr_write_b32 a21, v249\n"
"v_mfma_f32_16x16x32_bf16 v[172:175], a[204:207], a[124:127], v[172:175]\n"
"v_accvgpr_read_b32 v249, a22\n"
"v_mfma_f32_16x16x32_bf16 v[176:179], a[200:203], a[128:131], v[176:179]\n"
"v_add_f32 v249, v234, v249\n"
"v_mfma_f32_16x16x32_bf16 v[176:179], a[204:207], a[132:135], v[176:179]\n"
"v_accvgpr_write_b32 a22, v249\n"
"v_mfma_f32_16x16x32_bf16 v[180:183], a[200:203], a[136:139], v[180:183]\n"
"v_accvgpr_read_b32 v249, a23\n"
"v_mfma_f32_16x16x32_bf16 v[180:183], a[204:207], a[140:143], v[180:183]\n"
"v_add_f32 v249, v235, v249\n"
"v_mfma_f32_16x16x32_bf16 v[184:187], a[200:203], a[144:147], v[184:187]\n"
"v_accvgpr_write_b32 a23, v249\n"
"v_mfma_f32_16x16x32_bf16 v[184:187], a[204:207], a[148:151], v[184:187]\n"
"v_accvgpr_read_b32 v249, a24\n"
"v_mfma_f32_16x16x32_bf16 v[188:191], a[200:203], a[152:155], v[188:191]\n"
"global_load_dwordx4 a[200:203], v246, s[16:17] offset:-2048 sc1\n"
"v_add_f32 v249, v232, v249\n"
"v_mfma_f32_16x16x32_bf16 v[188:191], a[204:207], a[156:159], v[188:191]\n"
"v_accvgpr_write_b32 a24, v249\n"
"s_waitcnt vmcnt(17)\n"
"v_mfma_f32_16x16x32_bf16 v[192:195], a[208:211], a[96:99], v[192:195]\n"
"global_load_dwordx4 a[204:207], v246, s[16:17] offset:-1024 sc1\n"
"v_mfma_f32_16x16x32_bf16 v[192:195], a[212:215], a[100:103], v[192:195]\n"
"v_accvgpr_read_b32 v249, a25\n"
"v_mfma_f32_16x16x32_bf16 v[196:199], a[208:211], a[104:107], v[196:199]\n"
"v_add_f32 v249, v233, v249\n"
"v_mfma_f32_16x16x32_bf16 v[196:199], a[212:215], a[108:111], v[196:199]\n"
"v_accvgpr_write_b32 a25, v249\n"
"v_mfma_f32_16x16x32_bf16 v[200:203], a[208:211], a[112:115], v[200:203]\n"
"v_accvgpr_read_b32 v249, a26\n"
"v_mfma_f32_16x16x32_bf16 v[200:203], a[212:215], a[116:119], v[200:203]\n"
"v_add_f32 v249, v234, v249\n"
"v_mfma_f32_16x16x32_bf16 v[204:207], a[208:211], a[120:123], v[204:207]\n"
"v_accvgpr_write_b32 a26, v249\n"
"v_mfma_f32_16x16x32_bf16 v[204:207], a[212:215], a[124:127], v[204:207]\n"
"s_add_u32 s64, s60, 53248\n"
"s_addc_u32 s65, s61, 0\n"
"v_mfma_f32_16x16x32_bf16 v[208:211], a[208:211], a[128:131], v[208:211]\n"
"v_bfe_u32 v236, v247, 4, 6\n"
"v_mfma_f32_16x16x32_bf16 v[208:211], a[212:215], a[132:135], v[208:211]\n"
"v_min_u32 v236, 22, v236\n"
"v_mfma_f32_16x16x32_bf16 v[212:215], a[208:211], a[136:139], v[212:215]\n"
"v_lshlrev_b32 v236, 12, v236\n"
"v_mfma_f32_16x16x32_bf16 v[212:215], a[212:215], a[140:143], v[212:215]\n"
"v_mfma_f32_16x16x32_bf16 v[216:219], a[208:211], a[144:147], v[216:219]\n"
"v_mfma_f32_16x16x32_bf16 v[216:219], a[212:215], a[148:151], v[216:219]\n"
"v_mfma_f32_16x16x32_bf16 v[220:223], a[208:211], a[152:155], v[220:223]\n"
"global_load_dword a208, v250, s[64:65]\n"
"global_load_dwordx4 a[208:211], v246, s[16:17] offset:0 sc1\n"
"v_mfma_f32_16x16x32_bf16 v[220:223], a[212:215], a[156:159], v[220:223]\n"
"s_waitcnt vmcnt(18)\n"
"v_mfma_f32_16x16x32_bf16 a[0:3], a[216:219], a[96:99], a[0:3]\n"
"global_load_dwordx4 a[212:215], v246, s[16:17] offset:1024 sc1\n"
"ds_read_b128 a[96:99], v242 offset:49152\n"
"v_mfma_f32_16x16x32_bf16 a[0:3], a[220:223], a[100:103], a[0:3]\n"
"ds_read_b128 a[100:103], v243 offset:49152\n"
"v_mfma_f32_16x16x32_bf16 a[4:7], a[216:219], a[104:107], a[4:7]\n"
"ds_read_b128 a[104:107], v242 offset:51200\n"
"v_mfma_f32_16x16x32_bf16 a[4:7], a[220:223], a[108:111], a[4:7]\n"
"ds_read_b128 a[108:111], v243 offset:51200\n"
"v_mfma_f32_16x16x32_bf16 a[8:11], a[216:219], a[112:115], a[8:11]\n"
"ds_read_b128 a[112:115], v242 offset:53248\n"
"v_mfma_f32_16x16x32_bf16 a[8:11], a[220:223], a[116:119], a[8:11]\n"
"ds_read_b128 a[116:119], v243 offset:53248\n"
"v_mfma_f32_16x16x32_bf16 a[12:15], a[216:219], a[120:123], a[12:15]\n"
"ds_read_b128 a[120:123], v242 offset:55296\n"
"v_mfma_f32_16x16x32_bf16 a[12:15], a[220:223], a[124:127], a[12:15]\n"
"ds_read_b128 a[124:127], v243 offset:55296\n"
"v_mfma_f32_16x16x32_bf16 a[16:19], a[216:219], a[128:131], a[16:19]\n"
"ds_read_b128 a[128:131], v242 offset:57344\n"
"v_mfma_f32_16x16x32_bf16 a[16:19], a[220:223], a[132:135], a[16:19]\n"
"ds_read_b128 a[132:135], v243 offset:57344\n"
"v_mfma_f32_16x16x32_bf16 a[20:23], a[216:219], a[136:139], a[20:23]\n"
"ds_read_b128 a[136:139], v242 offset:59392\n"
"v_mfma_f32_16x16x32_bf16 a[20:23], a[220:223], a[140:143], a[20:23]\n"
"ds_read_b128 a[140:143], v243 offset:59392\n"
"v_mfma_f32_16x16x32_bf16 a[24:27], a[216:219], a[144:147], a[24:27]\n"
"ds_read_b128 a[144:147], v242 offset:61440\n"
"v_mfma_f32_16x16x32_bf16 a[24:27], a[220:223], a[148:151], a[24:27]\n"
"ds_read_b128 a[148:151], v243 offset:61440\n"
"v_mfma_f32_16x16x32_bf16 a[28:31], a[216:219], a[152:155], a[28:31]\n"
"global_load_dword a216, v236, s[60:61]\n"
"global_load_dwordx4 a[216:219], v246, s[16:17] offset:2048 sc1\n"
"ds_read_b128 a[152:155], v242 offset:63488\n"
"v_mfma_f32_16x16x32_bf16 a[28:31], a[220:223], a[156:159], a[28:31]\n"
"ds_read_b128 a[156:159], v243 offset:63488\n"
"s_waitcnt vmcnt(18) lgkmcnt(14)\n"
"v_mfma_f32_16x16x32_bf16 v[0:3], a[160:163], a[96:99], v[0:3]\n"
"global_load_dwordx4 a[220:223], v246, s[16:17] offset:3072 sc1\n"
"v_mfma_f32_16x16x32_bf16 v[0:3], a[164:167], a[100:103], v[0:3]\n"
"s_waitcnt lgkmcnt(12)\n"
"v_mfma_f32_16x16x32_bf16 v[4:7], a[160:163], a[104:107], v[4:7]\n"
"v_add_u32 v246, 0x2000, v246\n"
"v_mfma_f32_16x16x32_bf16 v[4:7], a[164:167], a[108:111], v[4:7]\n"
"s_waitcnt lgkmcnt(10)\n"
"v_mfma_f32_16x16x32_bf16 v[8:11], a[160:163], a[112:115], v[8:11]\n"
"s_waitcnt vmcnt(13)\n"
"v_readfirstlane_b32 s46, v239\n"
"v_mfma_f32_16x16x32_bf16 v[8:11], a[164:167], a[116:119], v[8:11]\n"
"s_waitcnt lgkmcnt(8)\n"
"v_mfma_f32_16x16x32_bf16 v[12:15], a[160:163], a[120:123], v[12:15]\n"
"v_mfma_f32_16x16x32_bf16 v[12:15], a[164:167], a[124:127], v[12:15]\n"
"s_waitcnt lgkmcnt(6)\n"
"v_mfma_f32_16x16x32_bf16 v[16:19], a[160:163], a[128:131], v[16:19]\n"
"s_sub_u32 s46, s46, s44\n"
"v_mfma_f32_16x16x32_bf16 v[16:19], a[164:167], a[132:135], v[16:19]\n"
"s_waitcnt lgkmcnt(4)\n"
"v_mfma_f32_16x16x32_bf16 v[20:23], a[160:163], a[136:139], v[20:23]\n"
"s_cmp_ge_u32 s46, 65024\n"
"s_cbranch_scc0 .Lslow4_%=\n"
".Lback4_%=:\n"
"v_mfma_f32_16x16x32_bf16 v[20:23], a[164:167], a[140:143], v[20:23]\n"
"s_waitcnt lgkmcnt(2)\n"
"v_mfma_f32_16x16x32_bf16 v[24:27], a[160:163], a[144:147], v[24:27]\n"
"s_add_u32 m0, s42, 65536\n"
"v_add_u32 v238, 512, v248\n"
"v_mfma_f32_16x16x32_bf16 v[24:27], a[164:167], a[148:151], v[24:27]\n"
"s_waitcnt lgkmcnt(0)\n"
"v_mfma_f32_16x16x32_bf16 v[28:31], a[160:163], a[152:155], v[28:31]\n"
"global_load_dwordx4 a[160:163], v246, s[16:17] offset:-4096 sc1\n"
"v_mfma_f32_16x16x32_bf16 v[28:31], a[164:167], a[156:159], v[28:31]\n"
"v_mfma_f32_16x16x32_bf16 v[32:35], a[168:171], a[96:99], v[32:35]\n"
"global_load_dwordx4 a[164:167], v246, s[16:17] offset:-3072 sc1\n"
"v_mfma_f32_16x16x32_bf16 v[32:35], a[172:175], a[100:103], v[32:35]\n"
"global_load_lds_dwordx4 v238, s[26:27] offset:0 sc1\n"
"v_mfma_f32_16x16x32_bf16 v[36:39], a[168:171], a[104:107], v[36:39]\n"
"v_accvgpr_read_b32 v249, a27\n"
"v_mfma_f32_16x16x32_bf16 v[36:39], a[172:175], a[108:111], v[36:39]\n"
"v_add_f32 v249, v235, v249\n"
"v_mfma_f32_16x16x32_bf16 v[40:43], a[168:171], a[112:115], v[40:43]\n"
"v_accvgpr_write_b32 a27, v249\n"
"v_mfma_f32_16x16x32_bf16 v[40:43], a[172:175], a[116:119], v[40:43]\n"
"v_accvgpr_read_b32 v249, a28\n"
"v_mfma_f32_16x16x32_bf16 v[44:47], a[168:171], a[120:123], v[44:47]\n"
"v_add_f32 v249, v232, v249\n"
"v_mfma_f32_16x16x32_bf16 v[44:47], a[172:175], a[124:127], v[44:47]\n"
"v_accvgpr_write_b32 a28, v249\n"
"v_mfma_f32_16x16x32_bf16 v[48:51], a[168:171], a[128:131], v[48:51]\n"
"v_accvgpr_read_b32 v249, a29\n"
"v_mfma_f32_16x16x32_bf16 v[48:51], a[172:175], a[132:135], v[48:51]\n"
"v_add_f32 v249, v233, v249\n"
"v_mfma_f32_16x16x32_bf16 v[52:55], a[168:171], a[136:139], v[52:55]\n"
"v_accvgpr_write_b32 a29, v249\n"
"v_mfma_f32_16x16x32_bf16 v[52:55], a[172:175], a[140:143], v[52:55]\n"
"v_accvgpr_read_b32 v249, a30\n"
"v_mfma_f32_16x16x32_bf16 v[56:59], a[168:171], a[144:147], v[56:59]\n"
"v_add_f32 v249, v234, v249\n"
"v_mfma_f32_16x16x32_bf16 v[56:59], a[172:175], a[148:151], v[56:59]\n"
"v_accvgpr_write_b32 a30, v249\n"
"v_mfma_f32_16x16x32_bf16 v[60:63], a[168:171], a[152:155], v[60:63]\n"
"global_load_dwordx4 a[168:171], v246, s[16:17] offset:-2048 sc1\n"
"v_mfma_f32_16x16x32_bf16 v[60:63], a[172:175], a[156:159], v[60:63]\n"
"v_mfma_f32_16x16x32_bf16 v[64:67], a[176:179], a[96:99], v[64:67]\n"
"global_load_dwordx4 a[172:175], v246, s[16:17] offset:-1024 sc1\n"
"v_mfma_f32_16x16x32_bf16 v[64:67], a[180:183], a[100:103], v[64:67]\n"
"v_accvgpr_read_b32 v249, a31\n"
"v_mfma_f32_16x16x32_bf16 v[68:71], a[176:179], a[104:107], v[68:71]\n"
"v_add_f32 v249, v235, v249\n"
"v_mfma_f32_16x16x32_bf16 v[68:71], a[180:183], a[108:111], v[68:71]\n"
"v_add_u32 v238, s66, v238\n"
"v_accvgpr_write_b32 a31, v249\n"
"v_mfma_f32_16x16x32_bf16 v[72:75], a[176:179], a[112:115], v[72:75]\n"
"global_load_lds_dwordx4 v238, s[26:27] offset:1024 sc1\n"
"v_mfma_f32_16x16x32_bf16 v[72:75], a[180:183], a[116:119], v[72:75]\n"
"v_mfma_f32_16x16x32_bf16 v[76:79], a[176:179], a[120:123], v[76:79]\n"
"v_mfma_f32_16x16x32_bf16 v[76:79], a[180:183], a[124:127], v[76:79]\n"
"v_mfma_f32_16x16x32_bf16 v[80:83], a[176:179], a[128:131], v[80:83]\n"
"v_mfma_f32_16x16x32_bf16 v[80:83], a[180:183], a[132:135], v[80:83]\n"
"v_mfma_f32_16x16x32_bf16 v[84:87], a[176:179], a[136:139], v[84:87]\n"
"v_mfma_f32_16x16x32_bf16 v[84:87], a[180:183], a[140:143], v[84:87]\n"
"v_mfma_f32_16x16x32_bf16 v[88:91], a[176:179], a[144:147], v[88:91]\n"
"s_mov_b64 exec, 0\n"
"s_cmp_eq_u32 %1, 0\n"
"s_cbranch_scc1 .Lpg16_%=\n"
"s_cmp_eq_u32 %1, 1\n"
"s_cbranch_scc1 .Lpg17_%=\n"
"s_cmp_eq_u32 %1, 2\n"
"s_cbranch_scc1 .Lpg18_%=\n"
"s_cmp_eq_u32 %1, 3\n"
"s_cbranch_scc1 .Lpg19_%=\n"
".Lretb_%=:\n"
"s_mov_b64 exec, -1\n"
"v_mfma_f32_16x16x32_bf16 v[88:91], a[180:183], a[148:151], v[88:91]\n"
"v_mfma_f32_16x16x32_bf16 v[92:95], a[176:179], a[152:155], v[92:95]\n"
"global_load_dwordx4 a[176:179], v246, s[16:17] offset:0 sc1\n"
"v_mfma_f32_16x16x32_bf16 v[92:95], a[180:183], a[156:159], v[92:95]\n"
"s_waitcnt vmcnt(18)\n"
"v_mfma_f32_16x16x32_bf16 v[96:99], a[184:187], a[96:99], v[96:99]\n"
"global_load_dwordx4 a[180:183], v246, s[16:17] offset:1024 sc1\n"
"v_mfma_f32_16x16x32_bf16 v[96:99], a[188:191], a[100:103], v[96:99]\n"
"v_mfma_f32_16x16x32_bf16 v[100:103], a[184:187], a[104:107], v[100:103]\n"
"v_mfma_f32_16x16x32_bf16 v[100:103], a[188:191], a[108:111], v[100:103]\n"
"v_mfma_f32_16x16x32_bf16 v[104:107], a[184:187], a[112:115], v[104:107]\n"
"v_mfma_f32_16x16x32_bf16 v[104:107], a[188:191], a[116:119], v[104:107]\n"
"v_mfma_f32_16x16x32_bf16 v[108:111], a[184:187], a[120:123], v[108:111]\n"
"v_add_u32 v238, s66, v238\n"
"v_mfma_f32_16x16x32_bf16 v[108:111], a[188:191], a[124:127], v[108:111]\n"
"global_load_lds_dwordx4 v238, s[26:27] offset:2048 sc1\n"
"v_mfma_f32_16x16x32_bf16 v[112:115], a[184:187], a[128:131], v[112:115]\n"
"v_mfma_f32_16x16x32_bf16 v[112:115], a[188:191], a[132:135], v[112:115]\n"
"v_mfma_f32_16x16x32_bf16 v[116:119], a[184:187], a[136:139], v[116:119]\n"
"v_mfma_f32_16x16x32_bf16 v[116:119], a[188:191], a[140:143], v[116:119]\n"
"v_mfma_f32_16x16x32_bf16 v[120:123], a[184:187], a[144:147], v[120:123]\n"
"v_mfma_f32_16x16x32_bf16 v[120:123], a[188:191], a[148:151], v[120:123]\n"
"v_mfma_f32_16x16x32_bf16 v[124:127], a[184:187], a[152:155], v[124:127]\n"
"global_load_dwordx4 a[184:187], v246, s[16:17] offset:2048 sc1\n"
"v_mfma_f32_16x16x32_bf16 v[124:127], a[188:191], a[156:159], v[124:127]\n"
"s_waitcnt vmcnt(18)\n"
"v_mfma_f32_16x16x32_bf16 v[128:131], a[192:195], a[96:99], v[128:131]\n"
"global_load_dwordx4 a[188:191], v246, s[16:17] offset:3072 sc1\n"
"v_mfma_f32_16x16x32_bf16 v[128:131], a[196:199], a[100:103], v[128:131]\n"
"v_mfma_f32_16x16x32_bf16 v[132:135], a[192:195], a[104:107], v[132:135]\n"
"v_add_u32 v246, 0x2000, v246\n"
"v_mfma_f32_16x16x32_bf16 v[132:135], a[196:199], a[108:111], v[132:135]\n"
"v_mfma_f32_16x16x32_bf16 v[136:139], a[192:195], a[112:115], v[136:139]\n"
"v_mfma_f32_16x16x32_bf16 v[136:139], a[196:199], a[116:119], v[136:139]\n"
"v_mfma_f32_16x16x32_bf16 v[140:143], a[192:195], a[120:123], v[140:143]\n"
"v_mfma_f32_16x16x32_bf16 v[140:143], a[196:199], a[124:127], v[140:143]\n"
"v_mfma_f32_16x16x32_bf16 v[144:147], a[192:195], a[128:131], v[144:147]\n"
"v_mfma_f32_16x16x32_bf16 v[144:147], a[196:199], a[132:135], v[144:147]\n"
"v_add_u32 v238, s66, v238\n"
"v_mfma_f32_16x16x32_bf16 v[148:151], a[192:195], a[136:139], v[148:151]\n"
"global_load_lds_dwordx4 v238, s[26:27] offset:3072 sc1\n"
"v_mfma_f32_16x16x32_bf16 v[148:151], a[196:199], a[140:143], v[148:151]\n"
"v_mfma_f32_16x16x32_bf16 v[152:155], a[192:195], a[144:147], v[152:155]\n"
"v_mfma_f32_16x16x32_bf16 v[152:155], a[196:199], a[148:151], v[152:155]\n"
"v_mfma_f32_16x16x32_bf16 v[156:159], a[192:195], a[152:155], v[156:159]\n"
"global_load_dwordx4 a[192:195], v246, s[16:17] offset:-4096 sc1\n"
"v_mfma_f32_16x16x32_bf16 v[156:159], a[196:199], a[156:159], v[156:159]\n"
"s_waitcnt vmcnt(19)\n"
"v_mfma_f32_16x16x32_bf16 v[160:163], a[200:203], a[96:99], v[160:163]\n"
"global_load_dwordx4 a[196:199], v246, s[16:17] offset:-3072 sc1\n"
"v_mfma_f32_16x16x32_bf16 v[160:163], a[204:207], a[100:103], v[160:163]\n"
"v_mfma_f32_16x16x32_bf16 v[164:167], a[200:203], a[104:107], v[164:167]\n"
"v_mfma_f32_16x16x32_bf16 v[164:167], a[204:207], a[108:111], v[164:167]\n"
"v_mfma_f32_16x16x32_bf16 v[168:171], a[200:203], a[112:115], v[168:171]\n"
"v_mfma_f32_16x16x32_bf16 v[168:171], a[204:207], a[116:119], v[168:171]\n"
"v_mfma_f32_16x16x32_bf16 v[172:175], a[200:203], a[120:123], v[172:175]\n"
"v_mfma_f32_16x16x32_bf16 v[172:175], a[204:207], a[124:127], v[172:175]\n"
"v_mfma_f32_16x16x32_bf16 v[176:179], a[200:203], a[128:131], v[176:179]\n"
"v_mfma_f32_16x16x32_bf16 v[176:179], a[204:207], a[132:135], v[176:179]\n"
"v_mfma_f32_16x16x32_bf16 v[180:183], a[200:203], a[136:139], v[180:183]\n"
"v_mfma_f32_16x16x32_bf16 v[180:183], a[204:207], a[140:143], v[180:183]\n"
"v_mfma_f32_16x16x32_bf16 v[184:187], a[200:203], a[144:147], v[184:187]\n"
"s_add_u32 m0, s42, 98304\n"
"v_add_u32 v238, 768, v248\n"
"v_mfma_f32_16x16x32_bf16 v[184:187], a[204:207], a[148:151], v[184:187]\n"
"global_load_lds_dwordx4 v238, s[26:27] offset:0 sc1\n"
"v_mfma_f32_16x16x32_bf16 v[188:191], a[200:203], a[152:155], v[188:191]\n"
"global_load_dwordx4 a[200:203], v246, s[16:17] offset:-2048 sc1\n"
"v_mfma_f32_16x16x32_bf16 v[188:191], a[204:207], a[156:159], v[188:191]\n"
"s_waitcnt vmcnt(19)\n"
"v_mfma_f32_16x16x32_bf16 v[192:195], a[208:211], a[96:99], v[192:195]\n"
"global_load_dwordx4 a[204:207], v246, s[16:17] offset:-1024 sc1\n"
"v_mfma_f32_16x16x32_bf16 v[192:195], a[212:215], a[100:103], v[192:195]\n"
"v_mfma_f32_16x16x32_bf16 v[196:199], a[208:211], a[104:107], v[196:199]\n"
"v_mfma_f32_16x16x32_bf16 v[196:199], a[212:215], a[108:111], v[196:199]\n"
"v_mfma_f32_16x16x32_bf16 v[200:203], a[208:211], a[112:115], v[200:203]\n"
"v_mfma_f32_16x16x32_bf16 v[200:203], a[212:215], a[116:119], v[200:203]\n"
"v_mfma_f32_16x16x32_bf16 v[204:207], a[208:211], a[120:123], v[204:207]\n"
"v_mfma_f32_16x16x32_bf16 v[204:207], a[212:215], a[124:127], v[204:207]\n"
"v_mfma_f32_16x16x32_bf16 v[208:211], a[208:211], a[128:131], v[208:211]\n"
"v_mfma_f32_16x16x32_bf16 v[208:211], a[212:215], a[132:135], v[208:211]\n"
"v_mfma_f32_16x16x32_bf16 v[212:215], a[208:211], a[136:139], v[212:215]\n"
"v_mfma_f32_16x16x32_bf16 v[212:215], a[212:215], a[140:143], v[212:215]\n"
"v_mfma_f32_16x16x32_bf16 v[216:219], a[208:211], a[144:147], v[216:219]\n"
"v_mfma_f32_16x16x32_bf16 v[216:219], a[212:215], a[148:151], v[216:219]\n"
"s_barrier\n"
"v_mfma_f32_16x16x32_bf16 v[220:223], a[208:211], a[152:155], v[220:223]\n"
"global_load_dwordx4 a[208:211], v246, s[16:17] offset:0 sc1\n"
"v_mfma_f32_16x16x32_bf16 v[220:223], a[212:215], a[156:159], v[220:223]\n"
"s_waitcnt vmcnt(18)\n"
"v_mfma_f32_16x16x32_bf16 a[0:3], a[216:219], a[96:99], a[0:3]\n"
"global_load_dwordx4 a[212:215], v246, s[16:17] offset:1024 sc1\n"
"ds_read_b128 a[96:99], v240 offset:0\n"
"v_mfma_f32_16x16x32_bf16 a[0:3], a[220:223], a[100:103], a[0:3]\n"
"ds_read_b128 a[100:103], v241 offset:0\n"
"v_mfma_f32_16x16x32_bf16 a[4:7], a[216:219], a[104:107], a[4:7]\n"
"ds_read_b128 a[104:107], v240 offset:2048\n"
"v_mfma_f32_16x16x32_bf16 a[4:7], a[220:223], a[108:111], a[4:7]\n"
"ds_read_b128 a[108:111], v241 offset:2048\n"
"v_mfma_f32_16x16x32_bf16 a[8:11], a[216:219], a[112:115], a[8:11]\n"
"ds_read_b128 a[112:115], v240 offset:4096\n"
"v_mfma_f32_16x16x32_bf16 a[8:11], a[220:223], a[116:119], a[8:11]\n"
"ds_read_b128 a[116:119], v241 offset:4096\n"
"v_mfma_f32_16x16x32_bf16 a[12:15], a[216:219], a[120:123], a[12:15]\n"
"ds_read_b128 a[120:123], v240 offset:6144\n"
"v_mfma_f32_16x16x32_bf16 a[12:15], a[220:223], a[124:127], a[12:15]\n"
"ds_read_b128 a[124:127], v241 offset:6144\n"
"v_mfma_f32_16x16x32_bf16 a[16:19], a[216:219], a[128:131], a[16:19]\n"
"ds_read_b128 a[128:131], v240 offset:8192\n"
"v_mfma_f32_16x16x32_bf16 a[16:19], a[220:223], a[132:135], a[16:19]\n"
"ds_read_b128 a[132:135], v241 offset:8192\n"
"v_mfma_f32_16x16x32_bf16 a[20:23], a[216:219], a[136:139], a[20:23]\n"
"ds_read_b128 a[136:139], v240 offset:10240\n"
"v_mfma_f32_16x16x32_bf16 a[20:23], a[220:223], a[140:143], a[20:23]\n"
"ds_read_b128 a[140:143], v241 offset:10240\n"
"v_mfma_f32_16x16x32_bf16 a[24:27], a[216:219], a[144:147], a[24:27]\n"
"ds_read_b128 a[144:147], v240 offset:12288\n"
"v_mfma_f32_16x16x32_bf16 a[24:27], a[220:223], a[148:151], a[24:27]\n"
"ds_read_b128 a[148:151], v241 offset:12288\n"
"v_mfma_f32_16x16x32_bf16 a[28:31], a[216:219], a[152:155], a[28:31]\n"
"global_load_dwordx4 a[216:219], v246, s[16:17] offset:2048 sc1\n"
"ds_read_b128 a[152:155], v240 offset:14336\n"
"v_mfma_f32_16x16x32_bf16 a[28:31], a[220:223], a[156:159], a[28:31]\n"
"ds_read_b128 a[156:159], v241 offset:14336\n"
"s_waitcnt vmcnt(18) lgkmcnt(14)\n"
"v_mfma_f32_16x16x32_bf16 v[128:131], a[160:163], a[96:99], v[128:131]\n"
"global_load_dwordx4 a[220:223], v246, s[16:17] offset:3072 sc1\n"
"v_mfma_f32_16x16x32_bf16 v[128:131], a[164:167], a[100:103], v[128:131]\n"
"s_waitcnt lgkmcnt(12)\n"
"v_mfma_f32_16x16x32_bf16 v[132:135], a[160:163], a[104:107], v[132:135]\n"
"v_add_u32 v246, 0x2000, v246\n"
"v_mfma_f32_16x16x32_bf16 v[132:135], a[164:167], a[108:111], v[132:135]\n"
"s_waitcnt lgkmcnt(10)\n"
"v_mfma_f32_16x16x32_bf16 v[136:139], a[160:163], a[112:115], v[136:139]\n"
"v_add_u32 v238, s66, v238\n"
"v_mfma_f32_16x16x32_bf16 v[136:139], a[164:167], a[116:119], v[136:139]\n"
"s_waitcnt lgkmcnt(8)\n"
"v_mfma_f32_16x16x32_bf16 v[140:143], a[160:163], a[120:123], v[140:143]\n"
"global_load_lds_dwordx4 v238, s[26:27] offset:1024 sc1\n"
"v_mfma_f32_16x16x32_bf16 v[140:143], a[164:167], a[124:127], v[140:143]\n"
"s_waitcnt lgkmcnt(6)\n"
"v_mfma_f32_16x16x32_bf16 v[144:147], a[160:163], a[128:131], v[144:147]\n"
"v_mfma_f32_16x16x32_bf16 v[144:147], a[164:167], a[132:135], v[144:147]\n"
"s_waitcnt lgkmcnt(4)\n"
"v_mfma_f32_16x16x32_bf16 v[148:151], a[160:163], a[136:139], v[148:151]\n"
"v_mfma_f32_16x16x32_bf16 v[148:151], a[164:167], a[140:143], v[148:151]\n"
"s_waitcnt lgkmcnt(2)\n"
"v_mfma_f32_16x16x32_bf16 v[152:155], a[160:163], a[144:147], v[152:155]\n"
"v_mfma_f32_16x16x32_bf16 v[152:155], a[164:167], a[148:151], v[152:155]\n"
"s_waitcnt lgkmcnt(0)\n"
"v_mfma_f32_16x16x32_bf16 v[156:159], a[160:163], a[152:155], v[156:159]\n"
"global_load_dwordx4 a[160:163], v246, s[16:17] offset:-4096 sc1\n"
"v_mfma_f32_16x16x32_bf16 v[156:159], a[164:167], a[156:159], v[156:159]\n"
"s_waitcnt vmcnt(18)\n"
"v_mfma_f32_16x16x32_bf16 v[160:163], a[168:171], a[96:99], v[160:163]\n"
"global_load_dwordx4 a[164:167], v246, s[16:17] offset:-3072 sc1\n"
"v_mfma_f32_16x16x32_bf16 v[160:163], a[172:175], a[100:103], v[160:163]\n"
"v_mfma_f32_16x16x32_bf16 v[164:167], a[168:171], a[104:107], v[164:167]\n"
"v_mfma_f32_16x16x32_bf16 v[164:167], a[172:175], a[108:111], v[164:167]\n"
"v_mfma_f32_16x16x32_bf16 v[168:171], a[168:171], a[112:115], v[168:171]\n"
"v_mfma_f32_16x16x32_bf16 v[168:171], a[172:175], a[116:119], v[168:171]\n"
"v_mfma_f32_16x16x32_bf16 v[172:175], a[168:171], a[120:123], v[172:175]\n"
"v_mfma_f32_16x16x32_bf16 v[172:175], a[172:175], a[124:127], v[172:175]\n"
"v_mfma_f32_16x16x32_bf16 v[176:179], a[168:171], a[128:131], v[176:179]\n"
"v_add_u32 v238, s66, v238\n"
"v_mfma_f32_16x16x32_bf16 v[176:179], a[172:175], a[132:135], v[176:179]\n"
"global_load_lds_dwordx4 v238, s[26:27] offset:2048 sc1\n"
"v_mfma_f32_16x16x32_bf16 v[180:183], a[168:171], a[136:139], v[180:183]\n"
"v_mfma_f32_16x16x32_bf16 v[180:183], a[172:175], a[140:143], v[180:183]\n"
"v_mfma_f32_16x16x32_bf16 v[184:187], a[168:171], a[144:147], v[184:187]\n"
"v_mfma_f32_16x16x32_bf16 v[184:187], a[172:175], a[148:151], v[184:187]\n"
"v_mfma_f32_16x16x32_bf16 v[188:191], a[168:171], a[152:155], v[188:191]\n"
"global_load_dwordx4 a[168:171], v246, s[16:17] offset:-2048 sc1\n"
"v_mfma_f32_16x16x32_bf16 v[188:191], a[172:175], a[156:159], v[188:191]\n"
"s_waitcnt vmcnt(18)\n"
"v_mfma_f32_16x16x32_bf16 v[192:195], a[176:179], a[96:99], v[192:195]\n"
"global_load_dwordx4 a[172:175], v246, s[16:17] offset:-1024 sc1\n"
"v_mfma_f32_16x16x32_bf16 v[192:195], a[180:183], a[100:103], v[192:195]\n"
"v_mfma_f32_16x16x32_bf16 v[196:199], a[176:179], a[104:107], v[196:199]\n"
"v_mfma_f32_16x16x32_bf16 v[196:199], a[180:183], a[108:111], v[196:199]\n"
"v_mfma_f32_16x16x32_bf16 v[200:203], a[176:179], a[112:115], v[200:203]\n"
"v_mfma_f32_16x16x32_bf16 v[200:203], a[180:183], a[116:119], v[200:203]\n"
"v_mfma_f32_16x16x32_bf16 v[204:207], a[176:179], a[120:123], v[204:207]\n"
"v_mfma_f32_16x16x32_bf16 v[204:207], a[180:183], a[124:127], v[204:207]\n"
"v_mfma_f32_16x16x32_bf16 v[208:211], a[176:179], a[128:131], v[208:211]\n"
"v_mfma_f32_16x16x32_bf16 v[208:211], a[180:183], a[132:135], v[208:211]\n"
"v_mfma_f32_16x16x32_bf16 v[212:215], a[176:179], a[136:139], v[212:215]\n"
"v_mfma_f32_16x16x32_bf16 v[212:215], a[180:183], a[140:143], v[212:215]\n"
"v_add_u32 v238, s66, v238\n"
"v_mfma_f32_16x16x32_bf16 v[216:219], a[176:179], a[144:147], v[216:219]\n"
"global_load_lds_dwordx4 v238, s[26:27] offset:3072 sc1\n"
"v_mfma_f32_16x16x32_bf16 v[216:219], a[180:183], a[148:151], v[216:219]\n"
"v_mfma_f32_16x16x32_bf16 v[220:223], a[176:179], a[152:155], v[220:223]\n"
"global_load_dwordx4 a[176:179], v246, s[16:17] offset:0 sc1\n"
"v_bfe_u32 v237, v247, 6, 4\n"
"v_mfma_f32_16x16x32_bf16 v[220:223], a[180:183], a[156:159], v[220:223]\n"
"v_and_b32 v238, 48, v247\n"
"s_waitcnt vmcnt(18)\n"
"v_mfma_f32_16x16x32_bf16 a[0:3], a[184:187], a[96:99], a[0:3]\n"
"global_load_dwordx4 a[180:183], v246, s[16:17] offset:1024 sc1\n"
"ds_read_b128 a[96:99], v240 offset:32768\n"
"v_mfma_f32_16x16x32_bf16 a[0:3], a[188:191], a[100:103], a[0:3]\n"
"ds_read_b128 a[100:103], v241 offset:32768\n"
"v_lshl_add_u32 v237, v237, 11, v238\n"
"v_mfma_f32_16x16x32_bf16 a[4:7], a[184:187], a[104:107], a[4:7]\n"
"ds_read_b128 a[104:107], v240 offset:34816\n"
"s_add_u32 s43, s51, s42\n"
"v_mfma_f32_16x16x32_bf16 a[4:7], a[188:191], a[108:111], a[4:7]\n"
"ds_read_b128 a[108:111], v241 offset:34816\n"
"v_mfma_f32_16x16x32_bf16 a[8:11], a[184:187], a[112:115], a[8:11]\n"
"ds_read_b128 a[112:115], v240 offset:36864\n"
"v_mfma_f32_16x16x32_bf16 a[8:11], a[188:191], a[116:119], a[8:11]\n"
"ds_read_b128 a[116:119], v241 offset:36864\n"
"v_mfma_f32_16x16x32_bf16 a[12:15], a[184:187], a[120:123], a[12:15]\n"
"ds_read_b128 a[120:123], v240 offset:38912\n"
"v_mfma_f32_16x16x32_bf16 a[12:15], a[188:191], a[124:127], a[12:15]\n"
"ds_read_b128 a[124:127], v241 offset:38912\n"
"v_mfma_f32_16x16x32_bf16 a[16:19], a[184:187], a[128:131], a[16:19]\n"
"ds_read_b128 a[128:131], v240 offset:40960\n"
"v_mfma_f32_16x16x32_bf16 a[16:19], a[188:191], a[132:135], a[16:19]\n"
"ds_read_b128 a[132:135], v241 offset:40960\n"
"v_mfma_f32_16x16x32_bf16 a[20:23], a[184:187], a[136:139], a[20:23]\n"
"ds_read_b128 a[136:139], v240 offset:43008\n"
"v_mfma_f32_16x16x32_bf16 a[20:23], a[188:191], a[140:143], a[20:23]\n"
"ds_read_b128 a[140:143], v241 offset:43008\n"
"v_mfma_f32_16x16x32_bf16 a[24:27], a[184:187], a[144:147], a[24:27]\n"
"ds_read_b128 a[144:147], v240 offset:45056\n"
"v_mfma_f32_16x16x32_bf16 a[24:27], a[188:191], a[148:151], a[24:27]\n"
"ds_read_b128 a[148:151], v241 offset:45056\n"
"v_mfma_f32_16x16x32_bf16 a[28:31], a[184:187], a[152:155], a[28:31]\n"
"global_load_dwordx4 a[184:187], v246, s[16:17] offset:2048 sc1\n"
"ds_read_b128 a[152:155], v240 offset:47104\n"
"v_mfma_f32_16x16x32_bf16 a[28:31], a[188:191], a[156:159], a[28:31]\n"
"ds_read_b128 a[156:159], v241 offset:47104\n"
"s_waitcnt vmcnt(17) lgkmcnt(14)\n"
"v_mfma_f32_16x16x32_bf16 v[128:131], a[192:195], a[96:99], v[128:131]\n"
"global_load_dwordx4 a[188:191], v246, s[16:17] offset:3072 sc1\n"
"v_mfma_f32_16x16x32_bf16 v[128:131], a[196:199], a[100:103], v[128:131]\n"
"s_waitcnt lgkmcnt(12)\n"
"v_mfma_f32_16x16x32_bf16 v[132:135], a[192:195], a[104:107], v[132:135]\n"
"v_add_u32 v246, 0x2000, v246\n"
"v_mfma_f32_16x16x32_bf16 v[132:135], a[196:199], a[108:111], v[132:135]\n"
"s_waitcnt lgkmcnt(10)\n"
"v_mfma_f32_16x16x32_bf16 v[136:139], a[192:195], a[112:115], v[136:139]\n"
"s_add_u32 m0, s43, 0\n"
"v_add_u32 v238, 64, v237\n"
"v_mfma_f32_16x16x32_bf16 v[136:139], a[196:199], a[116:119], v[136:139]\n"
"global_load_lds_dwordx4 v238, s[22:23] offset:0\n"
"s_waitcnt lgkmcnt(8)\n"
"v_mfma_f32_16x16x32_bf16 v[140:143], a[192:195], a[120:123], v[140:143]\n"
"v_mfma_f32_16x16x32_bf16 v[140:143], a[196:199], a[124:127], v[140:143]\n"
"s_waitcnt lgkmcnt(6)\n"
"v_mfma_f32_16x16x32_bf16 v[144:147], a[192:195], a[128:131], v[144:147]\n"
"v_mfma_f32_16x16x32_bf16 v[144:147], a[196:199], a[132:135], v[144:147]\n"
"s_waitcnt lgkmcnt(4)\n"
"v_mfma_f32_16x16x32_bf16 v[148:151], a[192:195], a[136:139], v[148:151]\n"
"v_mfma_f32_16x16x32_bf16 v[148:151], a[196:199], a[140:143], v[148:151]\n"
"s_waitcnt lgkmcnt(2)\n"
"v_mfma_f32_16x16x32_bf16 v[152:155], a[192:195], a[144:147], v[152:155]\n"
"v_mfma_f32_16x16x32_bf16 v[152:155], a[196:199], a[148:151], v[152:155]\n"
"s_waitcnt lgkmcnt(0)\n"
"v_mfma_f32_16x16x32_bf16 v[156:159], a[192:195], a[152:155], v[156:159]\n"
"global_load_dwordx4 a[192:195], v246, s[16:17] offset:-4096 sc1\n"
"v_mfma_f32_16x16x32_bf16 v[156:159], a[196:199], a[156:159], v[156:159]\n"
"s_waitcnt vmcnt(17)\n"
"v_mfma_f32_16x16x32_bf16 v[160:163], a[200:203], a[96:99], v[160:163]\n"
"global_load_dwordx4 a[196:199], v246, s[16:17] offset:-3072 sc1\n"
"v_mfma_f32_16x16x32_bf16 v[160:163], a[204:207], a[100:103], v[160:163]\n"
"v_mfma_f32_16x16x32_bf16 v[164:167], a[200:203], a[104:107], v[164:167]\n"
"v_mfma_f32_16x16x32_bf16 v[164:167], a[204:207], a[108:111], v[164:167]\n"
"v_mfma_f32_16x16x32_bf16 v[168:171], a[200:203], a[112:115], v[168:171]\n"
"v_mfma_f32_16x16x32_bf16 v[168:171], a[204:207], a[116:119], v[168:171]\n"
"v_mfma_f32_16x16x32_bf16 v[172:175], a[200:203], a[120:123], v[172:175]\n"
"v_mfma_f32_16x16x32_bf16 v[172:175], a[204:207], a[124:127], v[172:175]\n"
"v_add_u32 v238, 31744, v238\n"
"global_load_lds_dwordx4 v238, s[22:23] offset:1024\n"
"v_mfma_f32_16x16x32_bf16 v[176:179], a[200:203], a[128:131], v[176:179]\n"
"v_mfma_f32_16x16x32_bf16 v[176:179], a[204:207], a[132:135], v[176:179]\n"
"v_mfma_f32_16x16x32_bf16 v[180:183], a[200:203], a[136:139], v[180:183]\n"
"v_mfma_f32_16x16x32_bf16 v[180:183], a[204:207], a[140:143], v[180:183]\n"
"v_mfma_f32_16x16x32_bf16 v[184:187], a[200:203], a[144:147], v[184:187]\n"
"v_mfma_f32_16x16x32_bf16 v[184:187], a[204:207], a[148:151], v[184:187]\n"
"v_mfma_f32_16x16x32_bf16 v[188:191], a[200:203], a[152:155], v[188:191]\n"
"global_load_dwordx4 a[200:203], v246, s[16:17] offset:-2048 sc1\n"
"v_mfma_f32_16x16x32_bf16 v[188:191], a[204:207], a[156:159], v[188:191]\n"
"s_waitcnt vmcnt(18)\n"
"v_mfma_f32_16x16x32_bf16 v[192:195], a[208:211], a[96:99], v[192:195]\n"
"global_load_dwordx4 a[204:207], v246, s[16:17] offset:-1024 sc1\n"
"v_mfma_f32_16x16x32_bf16 v[192:195], a[212:215], a[100:103], v[192:195]\n"
"v_mfma_f32_16x16x32_bf16 v[196:199], a[208:211], a[104:107], v[196:199]\n"
".Lpg13_%=:\n"
"s_cbranch_execz .Lreta_%=\n"
"v_mfma_f32_16x16x32_bf16 v[196:199], a[212:215], a[108:111], v[196:199]\n"
"v_mfma_f32_16x16x32_bf16 v[200:203], a[208:211], a[112:115], v[200:203]\n"
"v_mfma_f32_16x16x32_bf16 v[200:203], a[212:215], a[116:119], v[200:203]\n"
"v_mfma_f32_16x16x32_bf16 v[204:207], a[208:211], a[120:123], v[204:207]\n"
"v_mfma_f32_16x16x32_bf16 v[204:207], a[212:215], a[124:127], v[204:207]\n"
"v_mfma_f32_16x16x32_bf16 v[208:211], a[208:211], a[128:131], v[208:211]\n"
"v_mfma_f32_16x16x32_bf16 v[208:211], a[212:215], a[132:135], v[208:211]\n"
"v_add_u32 v238, 31744, v238\n"
"global_load_lds_dwordx4 v238, s[22:23] offset:2048\n"
"v_mfma_f32_16x16x32_bf16 v[212:215], a[208:211], a[136:139], v[212:215]\n"
"v_mfma_f32_16x16x32_bf16 v[212:215], a[212:215], a[140:143], v[212:215]\n"
"v_mfma_f32_16x16x32_bf16 v[216:219], a[208:211], a[144:147], v[216:219]\n"
"v_mfma_f32_16x16x32_bf16 v[216:219], a[212:215], a[148:151], v[216:219]\n"
"s_waitcnt vmcnt(11)\n"
"s_barrier\n"
"s_mov_b64 exec, 1\n"
"global_atomic_add v250, v252, s[30:31] offset:0 sc1\n"
"s_mov_b64 exec, -1\n"
"v_mfma_f32_16x16x32_bf16 v[220:223], a[208:211], a[152:155], v[220:223]\n"
"global_load_dwordx4 a[208:211], v246, s[16:17] offset:0 sc1\n"
"v_mfma_f32_16x16x32_bf16 v[220:223], a[212:215], a[156:159], v[220:223]\n"
"v_mfma_f32_16x16x32_bf16 a[0:3], a[216:219], a[96:99], a[0:3]\n"
"global_load_dwordx4 a[212:215], v246, s[16:17] offset:1024 sc1\n"
"ds_read_b128 a[96:99], v242 offset:0\n"
"v_mfma_f32_16x16x32_bf16 a[0:3], a[220:223], a[100:103], a[0:3]\n"
"ds_read_b128 a[100:103], v243 offset:0\n"
"v_mfma_f32_16x16x32_bf16 a[4:7], a[216:219], a[104:107], a[4:7]\n"
"ds_read_b128 a[104:107], v242 offset:2048\n"
"v_mfma_f32_16x16x32_bf16 a[4:7], a[220:223], a[108:111], a[4:7]\n"
"ds_read_b128 a[108:111], v243 offset:2048\n"
"v_mfma_f32_16x16x32_bf16 a[8:11], a[216:219], a[112:115], a[8:11]\n"
"ds_read_b128 a[112:115], v242 offset:4096\n"
"v_mfma_f32_16x16x32_bf16 a[8:11], a[220:223], a[116:119], a[8:11]\n"
"ds_read_b128 a[116:119], v243 offset:4096\n"
"v_mfma_f32_16x16x32_bf16 a[12:15], a[216:219], a[120:123], a[12:15]\n"
"ds_read_b128 a[120:123], v242 offset:6144\n"
"v_mfma_f32_16x16x32_bf16 a[12:15], a[220:223], a[124:127], a[12:15]\n"
"ds_read_b128 a[124:127], v243 offset:6144\n"
"v_mfma_f32_16x16x32_bf16 a[16:19], a[216:219], a[128:131], a[16:19]\n"
"ds_read_b128 a[128:131], v242 offset:8192\n"
"v_mfma_f32_16x16x32_bf16 a[16:19], a[220:223], a[132:135], a[16:19]\n"
"ds_read_b128 a[132:135], v243 offset:8192\n"
"v_mfma_f32_16x16x32_bf16 a[20:23], a[216:219], a[136:139], a[20:23]\n"
"ds_read_b128 a[136:139], v242 offset:10240\n"
"v_mfma_f32_16x16x32_bf16 a[20:23], a[220:223], a[140:143], a[20:23]\n"
"ds_read_b128 a[140:143], v243 offset:10240\n"
"v_add_u32 v238, 31744, v238\n"
"v_mfma_f32_16x16x32_bf16 a[24:27], a[216:219], a[144:147], a[24:27]\n"
"ds_read_b128 a[144:147], v242 offset:12288\n"
"global_load_lds_dwordx4 v238, s[22:23] offset:3072\n"
"v_mfma_f32_16x16x32_bf16 a[24:27], a[220:223], a[148:151], a[24:27]\n"
"ds_read_b128 a[148:151], v243 offset:12288\n"
"v_mfma_f32_16x16x32_bf16 a[28:31], a[216:219], a[152:155], a[28:31]\n"
"global_load_dwordx4 a[216:219], v246, s[16:17] offset:2048 sc1\n"
"ds_read_b128 a[152:155], v242 offset:14336\n"
"v_mfma_f32_16x16x32_bf16 a[28:31], a[220:223], a[156:159], a[28:31]\n"
"ds_read_b128 a[156:159], v243 offset:14336\n"
"s_waitcnt lgkmcnt(14)\n"
"v_mfma_f32_16x16x32_bf16 v[128:131], a[160:163], a[96:99], v[128:131]\n"
"global_load_dwordx4 a[220:223], v246, s[16:17] offset:3072 sc1\n"
"v_mfma_f32_16x16x32_bf16 v[128:131], a[164:167], a[100:103], v[128:131]\n"
"s_waitcnt lgkmcnt(12)\n"
"v_mfma_f32_16x16x32_bf16 v[132:135], a[160:163], a[104:107], v[132:135]\n"
"v_add_u32 v246, 0x2000, v246\n"
"v_mfma_f32_16x16x32_bf16 v[132:135], a[164:167], a[108:111], v[132:135]\n"
"s_waitcnt lgkmcnt(10)\n"
"v_mfma_f32_16x16x32_bf16 v[136:139], a[160:163], a[112:115], v[136:139]\n"
"s_add_u32 s64, s60, 57344\n"
"s_addc_u32 s65, s61, 0\n"
"v_mfma_f32_16x16x32_bf16 v[136:139], a[164:167], a[116:119], v[136:139]\n"
"s_waitcnt lgkmcnt(8)\n"
"v_mfma_f32_16x16x32_bf16 v[140:143], a[160:163], a[120:123], v[140:143]\n"
"v_mfma_f32_16x16x32_bf16 v[140:143], a[164:167], a[124:127], v[140:143]\n"
"s_waitcnt lgkmcnt(6)\n"
"v_mfma_f32_16x16x32_bf16 v[144:147], a[160:163], a[128:131], v[144:147]\n"
"v_mfma_f32_16x16x32_bf16 v[144:147], a[164:167], a[132:135], v[144:147]\n"
"s_waitcnt lgkmcnt(4)\n"
"v_mfma_f32_16x16x32_bf16 v[148:151], a[160:163], a[136:139], v[148:151]\n"
"v_mfma_f32_16x16x32_bf16 v[148:151], a[164:167], a[140:143], v[148:151]\n"
"s_waitcnt lgkmcnt(2)\n"
"v_mfma_f32_16x16x32_bf16 v[152:155], a[160:163], a[144:147], v[152:155]\n"
"v_mfma_f32_16x16x32_bf16 v[152:155], a[164:167], a[148:151], v[152:155]\n"
"s_waitcnt lgkmcnt(0)\n"
"v_mfma_f32_16x16x32_bf16 v[156:159], a[160:163], a[152:155], v[156:159]\n"
"global_load_dwordx4 a[160:163], v246, s[16:17] offset:-4096 sc1\n"
"v_mfma_f32_16x16x32_bf16 v[156:159], a[164:167], a[156:159], v[156:159]\n"
"v_mfma_f32_16x16x32_bf16 v[160:163], a[168:171], a[96:99], v[160:163]\n"
"global_load_dwordx4 a[164:167], v246, s[16:17] offset:-3072 sc1\n"
"v_mfma_f32_16x16x32_bf16 v[160:163], a[172:175], a[100:103], v[160:163]\n"
"s_add_u32 m0, s43, 4096\n"
"v_add_u32 v238, 131136, v237\n"
"v_mfma_f32_16x16x32_bf16 v[164:167], a[168:171], a[104:107], v[164:167]\n"
"global_load_lds_dwordx4 v238, s[22:23] offset:0\n"
"v_mfma_f32_16x16x32_bf16 v[164:167], a[172:175], a[108:111], v[164:167]\n"
"v_mfma_f32_16x16x32_bf16 v[168:171], a[168:171], a[112:115], v[168:171]\n"
"v_mfma_f32_16x16x32_bf16 v[168:171], a[172:175], a[116:119], v[168:171]\n"
"v_mfma_f32_16x16x32_bf16 v[172:175], a[168:171], a[120:123], v[172:175]\n"
"v_mfma_f32_16x16x32_bf16 v[172:175], a[172:175], a[124:127], v[172:175]\n"
"v_mfma_f32_16x16x32_bf16 v[176:179], a[168:171], a[128:131], v[176:179]\n"
"v_mfma_f32_16x16x32_bf16 v[176:179], a[172:175], a[132:135], v[176:179]\n"
"v_mfma_f32_16x16x32_bf16 v[180:183], a[168:171], a[136:139], v[180:183]\n"
"v_mfma_f32_16x16x32_bf16 v[180:183], a[172:175], a[140:143], v[180:183]\n"
"v_mfma_f32_16x16x32_bf16 v[184:187], a[168:171], a[144:147], v[184:187]\n"
"v_mfma_f32_16x16x32_bf16 v[184:187], a[172:175], a[148:151], v[184:187]\n"
"v_mfma_f32_16x16x32_bf16 v[188:191], a[168:171], a[152:155], v[188:191]\n"
"global_load_dword a168, v250, s[64:65]\n"
"global_load_dwordx4 a[168:171], v246, s[16:17] offset:-2048 sc1\n"
"v_mfma_f32_16x16x32_bf16 v[188:191], a[172:175], a[156:159], v[188:191]\n"
"s_waitcnt vmcnt(20)\n"
"v_mfma_f32_16x16x32_bf16 v[192:195], a[176:179], a[96:99], v[192:195]\n"
"global_load_dwordx4 a[172:175], v246, s[16:17] offset:-1024 sc1\n"
"v_mfma_f32_16x16x32_bf16 v[192:195], a[180:183], a[100:103], v[192:195]\n"
"v_mfma_f32_16x16x32_bf16 v[196:199], a[176:179], a[104:107], v[196:199]\n"
"v_mfma_f32_16x16x32_bf16 v[196:199], a[180:183], a[108:111], v[196:199]\n"
"v_mfma_f32_16x16x32_bf16 v[200:203], a[176:179], a[112:115], v[200:203]\n"
"v_add_u32 v238, 31744, v238\n"
"global_load_lds_dwordx4 v238, s[22:23] offset:1024\n"
"v_mfma_f32_16x16x32_bf16 v[200:203], a[180:183], a[116:119], v[200:203]\n"
"v_mfma_f32_16x16x32_bf16 v[204:207], a[176:179], a[120:123], v[204:207]\n"
"v_mfma_f32_16x16x32_bf16 v[204:207], a[180:183], a[124:127], v[204:207]\n"
"v_mfma_f32_16x16x32_bf16 v[208:211], a[176:179], a[128:131], v[208:211]\n"
"v_mfma_f32_16x16x32_bf16 v[208:211], a[180:183], a[132:135], v[208:211]\n"
"v_mfma_f32_16x16x32_bf16 v[212:215], a[176:179], a[136:139], v[212:215]\n"
"v_mfma_f32_16x16x32_bf16 v[212:215], a[180:183], a[140:143], v[212:215]\n"
"v_mfma_f32_16x16x32_bf16 v[216:219], a[176:179], a[144:147], v[216:219]\n"
"v_mfma_f32_16x16x32_bf16 v[216:219], a[180:183], a[148:151], v[216:219]\n"
"v_mfma_f32_16x16x32_bf16 v[220:223], a[176:179], a[152:155], v[220:223]\n"
"global_load_dwordx4 a[176:179], v246, s[16:17] offset:0 sc1\n"
"v_mfma_f32_16x16x32_bf16 v[220:223], a[180:183], a[156:159], v[220:223]\n"
"s_waitcnt vmcnt(21)\n"
"v_mfma_f32_16x16x32_bf16 a[0:3], a[184:187], a[96:99], a[0:3]\n"
"global_load_dwordx4 a[180:183], v246, s[16:17] offset:1024 sc1\n"
"ds_read_b128 a[96:99], v242 offset:32768\n"
"v_mfma_f32_16x16x32_bf16 a[0:3], a[188:191], a[100:103], a[0:3]\n"
"ds_read_b128 a[100:103], v243 offset:32768\n"
"v_mfma_f32_16x16x32_bf16 a[4:7], a[184:187], a[104:107], a[4:7]\n"
"ds_read_b128 a[104:107], v242 offset:34816\n"
"v_mfma_f32_16x16x32_bf16 a[4:7], a[188:191], a[108:111], a[4:7]\n"
"ds_read_b128 a[108:111], v243 offset:34816\n"
"v_mfma_f32_16x16x32_bf16 a[8:11], a[184:187], a[112:115], a[8:11]\n"
"ds_read_b128 a[112:115], v242 offset:36864\n"
"v_mfma_f32_16x16x32_bf16 a[8:11], a[188:191], a[116:119], a[8:11]\n"
"ds_read_b128 a[116:119], v243 offset:36864\n"
"v_mfma_f32_16x16x32_bf16 a[12:15], a[184:187], a[120:123], a[12:15]\n"
"ds_read_b128 a[120:123], v242 offset:38912\n"
"v_add_u32 v238, 31744, v238\n"
"v_mfma_f32_16x16x32_bf16 a[12:15], a[188:191], a[124:127], a[12:15]\n"
"ds_read_b128 a[124:127], v243 offset:38912\n"
"global_load_lds_dwordx4 v238, s[22:23] offset:2048\n"
"v_mfma_f32_16x16x32_bf16 a[16:19], a[184:187], a[128:131], a[16:19]\n"
"ds_read_b128 a[128:131], v242 offset:40960\n"
"v_mfma_f32_16x16x32_bf16 a[16:19], a[188:191], a[132:135], a[16:19]\n"
"ds_read_b128 a[132:135], v243 offset:40960\n"
"v_mfma_f32_16x16x32_bf16 a[20:23], a[184:187], a[136:139], a[20:23]\n"
"ds_read_b128 a[136:139], v242 offset:43008\n"
"v_mfma_f32_16x16x32_bf16 a[20:23], a[188:191], a[140:143], a[20:23]\n"
"ds_read_b128 a[140:143], v243 offset:43008\n"
"v_mfma_f32_16x16x32_bf16 a[24:27], a[184:187], a[144:147], a[24:27]\n"
"ds_read_b128 a[144:147], v242 offset:45056\n"
"v_mfma_f32_16x16x32_bf16 a[24:27], a[188:191], a[148:151], a[24:27]\n"
"ds_read_b128 a[148:151], v243 offset:45056\n"
"v_mfma_f32_16x16x32_bf16 a[28:31], a[184:187], a[152:155], a[28:31]\n"
"global_load_dwordx4 a[184:187], v246, s[16:17] offset:2048 sc1\n"
"ds_read_b128 a[152:155], v242 offset:47104\n"
"v_mfma_f32_16x16x32_bf16 a[28:31], a[188:191], a[156:159], a[28:31]\n"
"ds_read_b128 a[156:159], v243 offset:47104\n"
"s_waitcnt vmcnt(21) lgkmcnt(14)\n"
"v_mfma_f32_16x16x32_bf16 v[128:131], a[192:195], a[96:99], v[128:131]\n"
"global_load_dwordx4 a[188:191], v246, s[16:17] offset:3072 sc1\n"
"v_mfma_f32_16x16x32_bf16 v[128:131], a[196:199], a[100:103], v[128:131]\n"
"s_waitcnt lgkmcnt(12)\n"
"v_mfma_f32_16x16x32_bf16 v[132:135], a[192:195], a[104:107], v[132:135]\n"
"v_add_u32 v246, 0x2000, v246\n"
"v_mfma_f32_16x16x32_bf16 v[132:135], a[196:199], a[108:111], v[132:135]\n"
"s_waitcnt lgkmcnt(10)\n"
"v_mfma_f32_16x16x32_bf16 v[136:139], a[192:195], a[112:115], v[136:139]\n"
"v_mfma_f32_16x16x32_bf16 v[136:139], a[196:199], a[116:119], v[136:139]\n"
"s_waitcnt lgkmcnt(8)\n"
"v_mfma_f32_16x16x32_bf16 v[140:143], a[192:195], a[120:123], v[140:143]\n"
"v_mfma_f32_16x16x32_bf16 v[140:143], a[196:199], a[124:127], v[140:143]\n"
"s_waitcnt lgkmcnt(6)\n"
"v_mfma_f32_16x16x32_bf16 v[144:147], a[192:195], a[128:131], v[144:147]\n"
"v_mfma_f32_16x16x32_bf16 v[144:147], a[196:199], a[132:135], v[144:147]\n"
"v_add_u32 v238, 31744, v238\n"
"s_waitcnt lgkmcnt(4)\n"
"v_mfma_f32_16x16x32_bf16 v[148:151], a[192:195], a[136:139], v[148:151]\n"
"global_load_lds_dwordx4 v238, s[22:23] offset:3072\n"
"v_mfma_f32_16x16x32_bf16 v[148:151], a[196:199], a[140:143], v[148:151]\n"
"s_waitcnt lgkmcnt(2)\n"
"v_mfma_f32_16x16x32_bf16 v[152:155], a[192:195], a[144:147], v[152:155]\n"
"v_mfma_f32_16x16x32_bf16 v[152:155], a[196:199], a[148:151], v[152:155]\n"
"s_waitcnt lgkmcnt(0)\n"
"v_mfma_f32_16x16x32_bf16 v[156:159], a[192:195], a[152:155], v[156:159]\n"
"global_load_dwordx4 a[192:195], v246, s[16:17] offset:-4096 sc1\n"
"v_mfma_f32_16x16x32_bf16 v[156:159], a[196:199], a[156:159], v[156:159]\n"
"s_waitcnt vmcnt(21)\n"
"v_mfma_f32_16x16x32_bf16 v[160:163], a[200:203], a[96:99], v[160:163]\n"
"global_load_dwordx4 a[196:199], v246, s[16:17] offset:-3072 sc1\n"
"v_mfma_f32_16x16x32_bf16 v[160:163], a[204:207], a[100:103], v[160:163]\n"
"v_mfma_f32_16x16x32_bf16 v[164:167], a[200:203], a[104:107], v[164:167]\n"
"v_mfma_f32_16x16x32_bf16 v[164:167], a[204:207], a[108:111], v[164:167]\n"
"v_mfma_f32_16x16x32_bf16 v[168:171], a[200:203], a[112:115], v[168:171]\n"
"v_mfma_f32_16x16x32_bf16 v[168:171], a[204:207], a[116:119], v[168:171]\n"
"v_mfma_f32_16x16x32_bf16 v[172:175], a[200:203], a[120:123], v[172:175]\n"
"v_bfe_u32 v236, v247, 4, 4\n"
"v_mfma_f32_16x16x32_bf16 v[172:175], a[204:207], a[124:127], v[172:175]\n"
"v_bfe_u32 v249, v247, 8, 2\n"
"v_mfma_f32_16x16x32_bf16 v[176:179], a[200:203], a[128:131], v[176:179]\n"
"v_lshl_add_u32 v236, v236, 2, v249\n"
"v_mfma_f32_16x16x32_bf16 v[176:179], a[204:207], a[132:135], v[176:179]\n"
"v_lshl_add_u32 v236, v236, 4, s43\n"
"v_mfma_f32_16x16x32_bf16 v[180:183], a[200:203], a[136:139], v[180:183]\n"
"v_mfma_f32_16x16x32_bf16 v[180:183], a[204:207], a[140:143], v[180:183]\n"
"v_mfma_f32_16x16x32_bf16 v[184:187], a[200:203], a[144:147], v[184:187]\n"
"v_mfma_f32_16x16x32_bf16 v[184:187], a[204:207], a[148:151], v[184:187]\n"
"v_mfma_f32_16x16x32_bf16 v[188:191], a[200:203], a[152:155], v[188:191]\n"
"global_load_dwordx4 a[200:203], v246, s[16:17] offset:-2048 sc1\n"
"v_mfma_f32_16x16x32_bf16 v[188:191], a[204:207], a[156:159], v[188:191]\n"
"s_waitcnt vmcnt(19)\n"
"v_mfma_f32_16x16x32_bf16 v[192:195], a[208:211], a[96:99], v[192:195]\n"
"global_load_dwordx4 a[204:207], v246, s[16:17] offset:-1024 sc1\n"
"v_mfma_f32_16x16x32_bf16 v[192:195], a[212:215], a[100:103], v[192:195]\n"
"v_mfma_f32_16x16x32_bf16 v[196:199], a[208:211], a[104:107], v[196:199]\n"
"v_mfma_f32_16x16x32_bf16 v[196:199], a[212:215], a[108:111], v[196:199]\n"
"v_mfma_f32_16x16x32_bf16 v[200:203], a[208:211], a[112:115], v[200:203]\n"
"v_mfma_f32_16x16x32_bf16 v[200:203], a[212:215], a[116:119], v[200:203]\n"
"v_mfma_f32_16x16x32_bf16 v[204:207], a[208:211], a[120:123], v[204:207]\n"
"v_mfma_f32_16x16x32_bf16 v[204:207], a[212:215], a[124:127], v[204:207]\n"
"v_mfma_f32_16x16x32_bf16 v[208:211], a[208:211], a[128:131], v[208:211]\n"
"v_mfma_f32_16x16x32_bf16 v[208:211], a[212:215], a[132:135], v[208:211]\n"
"v_mfma_f32_16x16x32_bf16 v[212:215], a[208:211], a[136:139], v[212:215]\n"
"v_mfma_f32_16x16x32_bf16 v[212:215], a[212:215], a[140:143], v[212:215]\n"
"v_mfma_f32_16x16x32_bf16 v[216:219], a[208:211], a[144:147], v[216:219]\n"
"v_mfma_f32_16x16x32_bf16 v[216:219], a[212:215], a[148:151], v[216:219]\n"
"v_mfma_f32_16x16x32_bf16 v[220:223], a[208:211], a[152:155], v[220:223]\n"
"global_load_dwordx4 a[208:211], v246, s[16:17] offset:0 sc1\n"
"v_mfma_f32_16x16x32_bf16 v[220:223], a[212:215], a[156:159], v[220:223]\n"
"s_waitcnt vmcnt(18)\n"
"v_mfma_f32_16x16x32_bf16 a[0:3], a[216:219], a[96:99], a[0:3]\n"
"global_load_dwordx4 a[212:215], v246, s[16:17] offset:1024 sc1\n"
"ds_read_b128 a[96:99], v240 offset:0\n"
"v_mfma_f32_16x16x32_bf16 a[0:3], a[220:223], a[100:103], a[0:3]\n"
"ds_read_b128 a[100:103], v241 offset:0\n"
"v_mfma_f32_16x16x32_bf16 a[4:7], a[216:219], a[104:107], a[4:7]\n"
"ds_read_b128 a[104:107], v240 offset:2048\n"
"v_mfma_f32_16x16x32_bf16 a[4:7], a[220:223], a[108:111], a[4:7]\n"
"ds_read_b128 a[108:111], v241 offset:2048\n"
"v_mfma_f32_16x16x32_bf16 a[8:11], a[216:219], a[112:115], a[8:11]\n"
"ds_read_b128 a[112:115], v240 offset:4096\n"
"ds_read_b128 v[232:235], v236 offset:0\n"
"v_mfma_f32_16x16x32_bf16 a[8:11], a[220:223], a[116:119], a[8:11]\n"
"ds_read_b128 a[116:119], v241 offset:4096\n"
"v_mfma_f32_16x16x32_bf16 a[12:15], a[216:219], a[120:123], a[12:15]\n"
"ds_read_b128 a[120:123], v240 offset:6144\n"
"v_mfma_f32_16x16x32_bf16 a[12:15], a[220:223], a[124:127], a[12:15]\n"
"ds_read_b128 a[124:127], v241 offset:6144\n"
"v_mfma_f32_16x16x32_bf16 a[16:19], a[216:219], a[128:131], a[16:19]\n"
"ds_read_b128 a[128:131], v240 offset:8192\n"
"v_mfma_f32_16x16x32_bf16 a[16:19], a[220:223], a[132:135], a[16:19]\n"
"ds_read_b128 a[132:135], v241 offset:8192\n"
"v_mfma_f32_16x16x32_bf16 a[20:23], a[216:219], a[136:139], a[20:23]\n"
"ds_read_b128 a[136:139], v240 offset:10240\n"
"v_mfma_f32_16x16x32_bf16 a[20:23], a[220:223], a[140:143], a[20:23]\n"
"ds_read_b128 a[140:143], v241 offset:10240\n"
"v_mfma_f32_16x16x32_bf16 a[24:27], a[216:219], a[144:147], a[24:27]\n"
"ds_read_b128 a[144:147], v240 offset:12288\n"
"v_mfma_f32_16x16x32_bf16 a[24:27], a[220:223], a[148:151], a[24:27]\n"
"ds_read_b128 a[148:151], v241 offset:12288\n"
"v_mfma_f32_16x16x32_bf16 a[28:31], a[216:219], a[152:155], a[28:31]\n"
"global_load_dwordx4 a[216:219], v246, s[16:17] offset:2048 sc1\n"
"ds_read_b128 a[152:155], v240 offset:14336\n"
"v_mfma_f32_16x16x32_bf16 a[28:31], a[220:223], a[156:159], a[28:31]\n"
"ds_read_b128 a[156:159], v241 offset:14336\n"
"s_waitcnt vmcnt(18) lgkmcnt(15)\n"
"v_mfma_f32_16x16x32_bf16 v[0:3], a[160:163], a[96:99], v[0:3]\n"
"global_load_dwordx4 a[220:223], v246, s[16:17] offset:3072 sc1\n"
"v_mfma_f32_16x16x32_bf16 v[0:3], a[164:167], a[100:103], v[0:3]\n"
"s_waitcnt lgkmcnt(13)\n"
"v_mfma_f32_16x16x32_bf16 v[4:7], a[160:163], a[104:107], v[4:7]\n"
"v_add_u32 v246, 0x2000, v246\n"
"v_mfma_f32_16x16x32_bf16 v[224:227], v[252:255], a[224:227], 0\n"
"v_mfma_f32_16x16x32_bf16 v[4:7], a[164:167], a[108:111], v[4:7]\n"
"s_waitcnt lgkmcnt(10)\n"
"v_mfma_f32_16x16x32_bf16 v[8:11], a[160:163], a[112:115], v[8:11]\n"
"v_mfma_f32_16x16x32_bf16 v[8:11], a[164:167], a[116:119], v[8:11]\n"
"s_waitcnt lgkmcnt(8)\n"
"v_mfma_f32_16x16x32_bf16 v[12:15], a[160:163], a[120:123], v[12:15]\n"
"v_mfma_f32_16x16x32_bf16 v[12:15], a[164:167], a[124:127], v[12:15]\n"
"s_nop 0\n"
"v_mul_f32 v224, v128, v224\n"
"s_waitcnt lgkmcnt(6)\n"
"v_mfma_f32_16x16x32_bf16 v[16:19], a[160:163], a[128:131], v[16:19]\n"
"global_load_dword v239, v250, s[34:35] sc1\n"
"v_mul_f32 v225, v129, v225\n"
"v_mfma_f32_16x16x32_bf16 v[16:19], a[164:167], a[132:135], v[16:19]\n"
"v_mfma_f32_16x16x32_bf16 v[228:231], v[250:253], a[224:227], 0\n"
"s_waitcnt lgkmcnt(4)\n"
"v_mfma_f32_16x16x32_bf16 v[20:23], a[160:163], a[136:139], v[20:23]\n"
"v_mul_f32 v226, v130, v226\n"
"v_mul_f32 v227, v131, v227\n"
"v_mfma_f32_16x16x32_bf16 v[20:23], a[164:167], a[140:143], v[20:23]\n"
"v_cvt_pk_bf16_f32 v224, v224, v225\n"
"s_waitcnt lgkmcnt(2)\n"
"v_mfma_f32_16x16x32_bf16 v[24:27], a[160:163], a[144:147], v[24:27]\n"
"v_cvt_pk_bf16_f32 v225, v226, v227\n"
"ds_write_b64 v244, v[224:225] offset:16384\n"
"v_mfma_f32_16x16x32_bf16 v[24:27], a[164:167], a[148:151], v[24:27]\n"
"v_mul_f32 v228, v160, v228\n"
"s_waitcnt lgkmcnt(1)\n"
"v_mfma_f32_16x16x32_bf16 v[28:31], a[160:163], a[152:155], v[28:31]\n"
"global_load_dwordx4 a[160:163], v246, s[16:17] offset:-4096 sc1\n"
"v_mul_f32 v229, v161, v229\n"
"v_mfma_f32_16x16x32_bf16 v[28:31], a[164:167], a[156:159], v[28:31]\n"
"v_mfma_f32_16x16x32_bf16 v[224:227], v[252:255], v[232:235], 0\n"
"s_waitcnt vmcnt(17)\n"
"v_mfma_f32_16x16x32_bf16 v[32:35], a[168:171], a[96:99], v[32:35]\n"
"global_load_dwordx4 a[164:167], v246, s[16:17] offset:-3072 sc1\n"
"v_mul_f32 v230, v162, v230\n"
"v_mfma_f32_16x16x32_bf16 v[32:35], a[172:175], a[100:103], v[32:35]\n"
"v_mul_f32 v231, v163, v231\n"
"v_cvt_pk_bf16_f32 v228, v228, v229\n"
"v_mfma_f32_16x16x32_bf16 v[36:39], a[168:171], a[104:107], v[36:39]\n"
"v_cvt_pk_bf16_f32 v229, v230, v231\n"
"v_mfma_f32_16x16x32_bf16 v[36:39], a[172:175], a[108:111], v[36:39]\n"
"ds_write_b64 v244, v[228:229] offset:16392\n"
"v_mfma_f32_16x16x32_bf16 v[40:43], a[168:171], a[112:115], v[40:43]\n"
"v_mul_f32 v224, v192, v224\n"
"v_mul_f32 v225, v193, v225\n"
"v_mfma_f32_16x16x32_bf16 v[40:43], a[172:175], a[116:119], v[40:43]\n"
"v_mfma_f32_16x16x32_bf16 v[228:231], v[250:253], v[232:235], 0\n"
"v_mfma_f32_16x16x32_bf16 v[44:47], a[168:171], a[120:123], v[44:47]\n"
"ds_read_b128 v[232:235], v236 offset:1024\n"
"v_mul_f32 v226, v194, v226\n"
"v_mfma_f32_16x16x32_bf16 v[44:47], a[172:175], a[124:127], v[44:47]\n"
"v_mul_f32 v227, v195, v227\n"
"v_mfma_f32_16x16x32_bf16 v[48:51], a[168:171], a[128:131], v[48:51]\n"
"v_cvt_pk_bf16_f32 v224, v224, v225\n"
"v_mfma_f32_16x16x32_bf16 v[48:51], a[172:175], a[132:135], v[48:51]\n"
"v_cvt_pk_bf16_f32 v225, v226, v227\n"
"ds_write_b64 v245, v[224:225] offset:16384\n"
"v_mfma_f32_16x16x32_bf16 v[52:55], a[168:171], a[136:139], v[52:55]\n"
"v_accvgpr_read_b32 v249, a0\n"
"v_mfma_f32_16x16x32_bf16 v[52:55], a[172:175], a[140:143], v[52:55]\n"
"v_mul_f32_e64 v228, v249, v228\n"
"v_mfma_f32_16x16x32_bf16 v[224:227], v[252:255], a[228:231], 0\n"
"v_mfma_f32_16x16x32_bf16 v[56:59], a[168:171], a[144:147], v[56:59]\n"
"v_accvgpr_read_b32 v249, a1\n"
"v_mul_f32_e64 v229, v249, v229\n"
"v_mfma_f32_16x16x32_bf16 v[56:59], a[172:175], a[148:151], v[56:59]\n"
"v_accvgpr_read_b32 v249, a2\n"
"v_mul_f32_e64 v230, v249, v230\n"
"v_mfma_f32_16x16x32_bf16 v[60:63], a[168:171], a[152:155], v[60:63]\n"
"global_load_dwordx4 a[168:171], v246, s[16:17] offset:-2048 sc1\n"
"v_accvgpr_read_b32 v249, a3\n"
"v_mfma_f32_16x16x32_bf16 v[60:63], a[172:175], a[156:159], v[60:63]\n"
"v_mul_f32_e64 v231, v249, v231\n"
"s_waitcnt vmcnt(16)\n"
"v_mfma_f32_16x16x32_bf16 v[64:67], a[176:179], a[96:99], v[64:67]\n"
"global_load_dwordx4 a[172:175], v246, s[16:17] offset:-1024 sc1\n"
"v_cvt_pk_bf16_f32 v228, v228, v229\n"
"v_mfma_f32_16x16x32_bf16 v[64:67], a[180:183], a[100:103], v[64:67]\n"
"v_cvt_pk_bf16_f32 v229, v230, v231\n"
"ds_write_b64 v245, v[228:229] offset:16392\n"
"v_mfma_f32_16x16x32_bf16 v[68:71], a[176:179], a[104:107], v[68:71]\n"
"v_mul_f32 v224, v132, v224\n"
"v_mul_f32 v225, v133, v225\n"
"v_mfma_f32_16x16x32_bf16 v[68:71], a[180:183], a[108:111], v[68:71]\n"
"v_mfma_f32_16x16x32_bf16 v[228:231], v[250:253], a[228:231], 0\n"
"v_mul_f32 v226, v134, v226\n"
"v_mfma_f32_16x16x32_bf16 v[72:75], a[176:179], a[112:115], v[72:75]\n"
"v_mul_f32 v227, v135, v227\n"
"v_mfma_f32_16x16x32_bf16 v[72:75], a[180:183], a[116:119], v[72:75]\n"
"v_cvt_pk_bf16_f32 v224, v224, v225\n"
"v_cvt_pk_bf16_f32 v225, v226, v227\n"
"v_mfma_f32_16x16x32_bf16 v[76:79], a[176:179], a[120:123], v[76:79]\n"
"ds_write_b64 v244, v[224:225] offset:18432\n"
"v_mfma_f32_16x16x32_bf16 v[76:79], a[180:183], a[124:127], v[76:79]\n"
"v_mul_f32 v228, v164, v228\n"
"v_mfma_f32_16x16x32_bf16 v[80:83], a[176:179], a[128:131], v[80:83]\n"
"v_mul_f32 v229, v165, v229\n"
"v_mfma_f32_16x16x32_bf16 v[80:83], a[180:183], a[132:135], v[80:83]\n"
"s_waitcnt lgkmcnt(3)\n"
"v_mfma_f32_16x16x32_bf16 v[224:227], v[252:255], v[232:235], 0\n"
"v_mul_f32 v230, v166, v230\n"
"v_mfma_f32_16x16x32_bf16 v[84:87], a[176:179], a[136:139], v[84:87]\n"
"v_mul_f32 v231, v167, v231\n"
"v_mfma_f32_16x16x32_bf16 v[84:87], a[180:183], a[140:143], v[84:87]\n"
"v_cvt_pk_bf16_f32 v228, v228, v229\n"
"v_mfma_f32_16x16x32_bf16 v[88:91], a[176:179], a[144:147], v[88:91]\n"
"v_cvt_pk_bf16_f32 v229, v230, v231\n"
"v_mfma_f32_16x16x32_bf16 v[88:91], a[180:183], a[148:151], v[88:91]\n"
"ds_write_b64 v244, v[228:229] offset:18440\n"
"v_mfma_f32_16x16x32_bf16 v[92:95], a[176:179], a[152:155], v[92:95]\n"
"global_load_dwordx4 a[176:179], v246, s[16:17] offset:0 sc1\n"
"v_mul_f32 v224, v196, v224\n"
"v_mfma_f32_16x16x32_bf16 v[92:95], a[180:183], a[156:159], v[92:95]\n"
"v_mul_f32 v225, v197, v225\n"
"s_waitcnt vmcnt(15)\n"
"v_mfma_f32_16x16x32_bf16 v[96:99], a[184:187], a[96:99], v[96:99]\n"
"global_load_dwordx4 a[180:183], v246, s[16:17] offset:1024 sc1\n"
"ds_read_b128 a[96:99], v240 offset:32768\n"
"v_mfma_f32_16x16x32_bf16 v[96:99], a[188:191], a[100:103], v[96:99]\n"
"ds_read_b128 a[100:103], v241 offset:32768\n"
"v_mfma_f32_16x16x32_bf16 v[228:231], v[250:253], v[232:235], 0\n"
"v_mfma_f32_16x16x32_bf16 v[100:103], a[184:187], a[104:107], v[100:103]\n"
"ds_read_b128 a[104:107], v240 offset:34816\n"
"ds_read_b128 v[232:235], v236 offset:2048\n"
"v_mfma_f32_16x16x32_bf16 v[100:103], a[188:191], a[108:111], v[100:103]\n"
"ds_read_b128 a[108:111], v241 offset:34816\n"
"v_mul_f32 v226, v198, v226\n"
"v_mfma_f32_16x16x32_bf16 v[104:107], a[184:187], a[112:115], v[104:107]\n"
"ds_read_b128 a[112:115], v240 offset:36864\n"
"v_mul_f32 v227, v199, v227\n"
"v_mfma_f32_16x16x32_bf16 v[104:107], a[188:191], a[116:119], v[104:107]\n"
"ds_read_b128 a[116:119], v241 offset:36864\n"
"v_cvt_pk_bf16_f32 v224, v224, v225\n"
"v_mfma_f32_16x16x32_bf16 v[108:111], a[184:187], a[120:123], v[108:111]\n"
"ds_read_b128 a[120:123], v240 offset:38912\n"
"v_cvt_pk_bf16_f32 v225, v226, v227\n"
"v_mfma_f32_16x16x32_bf16 v[108:111], a[188:191], a[124:127], v[108:111]\n"
"ds_read_b128 a[124:127], v241 offset:38912\n"
"ds_write_b64 v245, v[224:225] offset:18432\n"
"v_mfma_f32_16x16x32_bf16 v[112:115], a[184:187], a[128:131], v[112:115]\n"
"ds_read_b128 a[128:131], v240 offset:40960\n"
"v_accvgpr_read_b32 v249, a4\n"
"v_mfma_f32_16x16x32_bf16 v[112:115], a[188:191], a[132:135], v[112:115]\n"
"ds_read_b128 a[132:135], v241 offset:40960\n"
"v_mul_f32_e64 v228, v249, v228\n"
"v_mfma_f32_16x16x32_bf16 v[116:119], a[184:187], a[136:139], v[116:119]\n"
"ds_read_b128 a[136:139], v240 offset:43008\n"
"v_mfma_f32_16x16x32_bf16 v[224:227], v[252:255], a[232:235], 0\n"
"v_mfma_f32_16x16x32_bf16 v[116:119], a[188:191], a[140:143], v[116:119]\n"
"ds_read_b128 a[140:143], v241 offset:43008\n"
"v_accvgpr_read_b32 v249, a5\n"
"v_mfma_f32_16x16x32_bf16 v[120:123], a[184:187], a[144:147], v[120:123]\n"
"ds_read_b128 a[144:147], v240 offset:45056\n"
"v_mul_f32_e64 v229, v249, v229\n"
"v_mfma_f32_16x16x32_bf16 v[120:123], a[188:191], a[148:151], v[120:123]\n"
"ds_read_b128 a[148:151], v241 offset:45056\n"
"v_accvgpr_read_b32 v249, a6\n"
"v_mfma_f32_16x16x32_bf16 v[124:127], a[184:187], a[152:155], v[124:127]\n"
"global_load_dwordx4 a[184:187], v246, s[16:17] offset:2048 sc1\n"
"ds_read_b128 a[152:155], v240 offset:47104\n"
"v_mfma_f32_16x16x32_bf16 v[124:127], a[188:191], a[156:159], v[124:127]\n"
"ds_read_b128 a[156:159], v241 offset:47104\n"
"s_waitcnt vmcnt(14) lgkmcnt(15)\n"
"v_mfma_f32_16x16x32_bf16 v[0:3], a[192:195], a[96:99], v[0:3]\n"
"global_load_dwordx4 a[188:191], v246, s[16:17] offset:3072 sc1\n"
"v_mul_f32_e64 v230, v249, v230\n"
"v_mfma_f32_16x16x32_bf16 v[0:3], a[196:199], a[100:103], v[0:3]\n"
"v_accvgpr_read_b32 v249, a7\n"
"s_waitcnt lgkmcnt(13)\n"
"v_mfma_f32_16x16x32_bf16 v[4:7], a[192:195], a[104:107], v[4:7]\n"
"v_add_u32 v246, 0x2000, v246\n"
"v_mul_f32_e64 v231, v249, v231\n"
"v_mfma_f32_16x16x32_bf16 v[4:7], a[196:199], a[108:111], v[4:7]\n"
"v_cvt_pk_bf16_f32 v228, v228, v229\n"
"s_waitcnt lgkmcnt(11)\n"
"v_mfma_f32_16x16x32_bf16 v[8:11], a[192:195], a[112:115], v[8:11]\n"
"v_cvt_pk_bf16_f32 v229, v230, v231\n"
"ds_write_b64 v245, v[228:229] offset:18440\n"
"v_mfma_f32_16x16x32_bf16 v[8:11], a[196:199], a[116:119], v[8:11]\n"
"v_mul_f32 v224, v136, v224\n"
"s_waitcnt lgkmcnt(10)\n"
"v_mfma_f32_16x16x32_bf16 v[12:15], a[192:195], a[120:123], v[12:15]\n"
"v_mul_f32 v225, v137, v225\n"
"v_mfma_f32_16x16x32_bf16 v[228:231], v[250:253], a[232:235], 0\n"
"v_mfma_f32_16x16x32_bf16 v[12:15], a[196:199], a[124:127], v[12:15]\n"
"v_mul_f32 v226, v138, v226\n"
"s_waitcnt lgkmcnt(7)\n"
"v_mfma_f32_16x16x32_bf16 v[16:19], a[192:195], a[128:131], v[16:19]\n"
"v_mul_f32 v227, v139, v227\n"
"v_cvt_pk_bf16_f32 v224, v224, v225\n"
"v_mfma_f32_16x16x32_bf16 v[16:19], a[196:199], a[132:135], v[16:19]\n"
"v_cvt_pk_bf16_f32 v225, v226, v227\n"
"s_waitcnt lgkmcnt(5)\n"
"v_mfma_f32_16x16x32_bf16 v[20:23], a[192:195], a[136:139], v[20:23]\n"
"ds_write_b64 v244, v[224:225] offset:20480\n"
"v_mul_f32 v228, v168, v228\n"
"v_mfma_f32_16x16x32_bf16 v[20:23], a[196:199], a[140:143], v[20:23]\n"
"v_mul_f32 v229, v169, v229\n"
"s_waitcnt lgkmcnt(4)\n"
"v_mfma_f32_16x16x32_bf16 v[24:27], a[192:195], a[144:147], v[24:27]\n"
"v_mfma_f32_16x16x32_bf16 v[224:227], v[252:255], v[232:235], 0\n"
"v_mul_f32 v230, v170, v230\n"
"v_mfma_f32_16x16x32_bf16 v[24:27], a[196:199], a[148:151], v[24:27]\n"
"v_mul_f32 v231, v171, v231\n"
"s_waitcnt lgkmcnt(2)\n"
"v_mfma_f32_16x16x32_bf16 v[28:31], a[192:195], a[152:155], v[28:31]\n"
"global_load_dwordx4 a[192:195], v246, s[16:17] offset:-4096 sc1\n"
"v_cvt_pk_bf16_f32 v228, v228, v229\n"
"v_mfma_f32_16x16x32_bf16 v[28:31], a[196:199], a[156:159], v[28:31]\n"
"v_cvt_pk_bf16_f32 v229, v230, v231\n"
"s_waitcnt vmcnt(14)\n"
"v_mfma_f32_16x16x32_bf16 v[32:35], a[200:203], a[96:99], v[32:35]\n"
"global_load_dwordx4 a[196:199], v246, s[16:17] offset:-3072 sc1\n"
"ds_write_b64 v244, v[228:229] offset:20488\n"
"v_mfma_f32_16x16x32_bf16 v[32:35], a[204:207], a[100:103], v[32:35]\n"
"v_mul_f32 v224, v200, v224\n"
"v_mul_f32 v225, v201, v225\n"
"v_mfma_f32_16x16x32_bf16 v[36:39], a[200:203], a[104:107], v[36:39]\n"
"v_mfma_f32_16x16x32_bf16 v[228:231], v[250:253], v[232:235], 0\n"
"ds_read_b128 v[232:235], v236 offset:3072\n"
"v_mfma_f32_16x16x32_bf16 v[36:39], a[204:207], a[108:111], v[36:39]\n"
"v_mul_f32 v226, v202, v226\n"
"v_mul_f32 v227, v203, v227\n"
"v_mfma_f32_16x16x32_bf16 v[40:43], a[200:203], a[112:115], v[40:43]\n"
"v_cvt_pk_bf16_f32 v224, v224, v225\n"
"v_cvt_pk_bf16_f32 v225, v226, v227\n"
"v_mfma_f32_16x16x32_bf16 v[40:43], a[204:207], a[116:119], v[40:43]\n"
"ds_write_b64 v245, v[224:225] offset:20480\n"
"v_accvgpr_read_b32 v249, a8\n"
"v_mfma_f32_16x16x32_bf16 v[44:47], a[200:203], a[120:123], v[44:47]\n"
"v_mul_f32_e64 v228, v249, v228\n"
"v_mfma_f32_16x16x32_bf16 v[224:227], v[252:255], a[236:239], 0\n"
"v_mfma_f32_16x16x32_bf16 v[44:47], a[204:207], a[124:127], v[44:47]\n"
"v_accvgpr_read_b32 v249, a9\n"
"v_mul_f32_e64 v229, v249, v229\n"
"v_mfma_f32_16x16x32_bf16 v[48:51], a[200:203], a[128:131], v[48:51]\n"
"v_accvgpr_read_b32 v249, a10\n"
"v_mul_f32_e64 v230, v249, v230\n"
"v_mfma_f32_16x16x32_bf16 v[48:51], a[204:207], a[132:135], v[48:51]\n"
"v_accvgpr_read_b32 v249, a11\n"
"v_mul_f32_e64 v231, v249, v231\n"
"v_mfma_f32_16x16x32_bf16 v[52:55], a[200:203], a[136:139], v[52:55]\n"
"v_cvt_pk_bf16_f32 v228, v228, v229\n"
"v_cvt_pk_bf16_f32 v229, v230, v231\n"
"v_mfma_f32_16x16x32_bf16 v[52:55], a[204:207], a[140:143], v[52:55]\n"
"ds_write_b64 v245, v[228:229] offset:20488\n"
"v_mul_f32 v224, v140, v224\n"
"v_mfma_f32_16x16x32_bf16 v[56:59], a[200:203], a[144:147], v[56:59]\n"
"v_mul_f32 v225, v141, v225\n"
"v_mfma_f32_16x16x32_bf16 v[228:231], v[250:253], a[236:239], 0\n"
"v_mfma_f32_16x16x32_bf16 v[56:59], a[204:207], a[148:151], v[56:59]\n"
"v_mul_f32 v226, v142, v226\n"
"v_mul_f32 v227, v143, v227\n"
"v_mfma_f32_16x16x32_bf16 v[60:63], a[200:203], a[152:155], v[60:63]\n"
"global_load_dwordx4 a[200:203], v246, s[16:17] offset:-2048 sc1\n"
"v_cvt_pk_bf16_f32 v224, v224, v225\n"
"v_mfma_f32_16x16x32_bf16 v[60:63], a[204:207], a[156:159], v[60:63]\n"
"v_cvt_pk_bf16_f32 v225, v226, v227\n"
"s_waitcnt vmcnt(14)\n"
"v_mfma_f32_16x16x32_bf16 v[64:67], a[208:211], a[96:99], v[64:67]\n"
"global_load_dwordx4 a[204:207], v246, s[16:17] offset:-1024 sc1\n"
"ds_write_b64 v244, v[224:225] offset:22528\n"
"v_mfma_f32_16x16x32_bf16 v[64:67], a[212:215], a[100:103], v[64:67]\n"
"v_mul_f32 v228, v172, v228\n"
"v_mul_f32 v229, v173, v229\n"
"v_mfma_f32_16x16x32_bf16 v[68:71], a[208:211], a[104:107], v[68:71]\n"
"s_waitcnt lgkmcnt(3)\n"
"v_mfma_f32_16x16x32_bf16 v[224:227], v[252:255], v[232:235], 0\n"
"v_mul_f32 v230, v174, v230\n"
"v_mfma_f32_16x16x32_bf16 v[68:71], a[212:215], a[108:111], v[68:71]\n"
"v_mul_f32 v231, v175, v231\n"
"v_cvt_pk_bf16_f32 v228, v228, v229\n"
"v_mfma_f32_16x16x32_bf16 v[72:75], a[208:211], a[112:115], v[72:75]\n"
"v_cvt_pk_bf16_f32 v229, v230, v231\n"
"ds_write_b64 v244, v[228:229] offset:22536\n"
"s_nop 0\n"
".Lpg14_%=:\n"
"s_cbranch_execz .Lreta_%=\n"
"v_mfma_f32_16x16x32_bf16 v[72:75], a[212:215], a[116:119], v[72:75]\n"
"v_mul_f32 v224, v204, v224\n"
"v_mul_f32 v225, v205, v225\n"
"v_mfma_f32_16x16x32_bf16 v[76:79], a[208:211], a[120:123], v[76:79]\n"
"v_mfma_f32_16x16x32_bf16 v[228:231], v[250:253], v[232:235], 0\n"
"ds_read_b128 v[232:235], v236 offset:4096\n"
"v_mfma_f32_16x16x32_bf16 v[76:79], a[212:215], a[124:127], v[76:79]\n"
"v_mul_f32 v226, v206, v226\n"
"v_mul_f32 v227, v207, v227\n"
"v_mfma_f32_16x16x32_bf16 v[80:83], a[208:211], a[128:131], v[80:83]\n"
"v_cvt_pk_bf16_f32 v224, v224, v225\n"
"v_cvt_pk_bf16_f32 v225, v226, v227\n"
"v_mfma_f32_16x16x32_bf16 v[80:83], a[212:215], a[132:135], v[80:83]\n"
"ds_write_b64 v245, v[224:225] offset:22528\n"
"v_accvgpr_read_b32 v249, a12\n"
"v_mfma_f32_16x16x32_bf16 v[84:87], a[208:211], a[136:139], v[84:87]\n"
"v_mul_f32_e64 v228, v249, v228\n"
"v_mfma_f32_16x16x32_bf16 v[224:227], v[252:255], a[240:243], 0\n"
"v_mfma_f32_16x16x32_bf16 v[84:87], a[212:215], a[140:143], v[84:87]\n"
"v_accvgpr_read_b32 v249, a13\n"
"v_mul_f32_e64 v229, v249, v229\n"
"v_mfma_f32_16x16x32_bf16 v[88:91], a[208:211], a[144:147], v[88:91]\n"
"v_accvgpr_read_b32 v249, a14\n"
"v_mul_f32_e64 v230, v249, v230\n"
"v_mfma_f32_16x16x32_bf16 v[88:91], a[212:215], a[148:151], v[88:91]\n"
"v_accvgpr_read_b32 v249, a15\n"
"v_mul_f32_e64 v231, v249, v231\n"
"v_mfma_f32_16x16x32_bf16 v[92:95], a[208:211], a[152:155], v[92:95]\n"
"global_load_dwordx4 a[208:211], v246, s[16:17] offset:0 sc1\n"
"v_cvt_pk_bf16_f32 v228, v228, v229\n"
"v_mfma_f32_16x16x32_bf16 v[92:95], a[212:215], a[156:159], v[92:95]\n"
"v_cvt_pk_bf16_f32 v229, v230, v231\n"
"s_waitcnt vmcnt(14)\n"
"v_mfma_f32_16x16x32_bf16 v[96:99], a[216:219], a[96:99], v[96:99]\n"
"global_load_dwordx4 a[212:215], v246, s[16:17] offset:1024 sc1\n"
"ds_read_b128 a[96:99], v242 offset:0\n"
"v_mfma_f32_16x16x32_bf16 v[96:99], a[220:223], a[100:103], v[96:99]\n"
"ds_read_b128 a[100:103], v243 offset:0\n"
"ds_write_b64 v245, v[228:229] offset:22536\n"
"v_mfma_f32_16x16x32_bf16 v[100:103], a[216:219], a[104:107], v[100:103]\n"
"ds_read_b128 a[104:107], v242 offset:2048\n"
"v_mul_f32 v224, v144, v224\n"
"v_mfma_f32_16x16x32_bf16 v[100:103], a[220:223], a[108:111], v[100:103]\n"
"ds_read_b128 a[108:111], v243 offset:2048\n"
"v_mul_f32 v225, v145, v225\n"
"v_mfma_f32_16x16x32_bf16 v[104:107], a[216:219], a[112:115], v[104:107]\n"
"ds_read_b128 a[112:115], v242 offset:4096\n"
"v_mfma_f32_16x16x32_bf16 v[228:231], v[250:253], a[240:243], 0\n"
"v_mfma_f32_16x16x32_bf16 v[104:107], a[220:223], a[116:119], v[104:107]\n"
"ds_read_b128 a[116:119], v243 offset:4096\n"
"v_mul_f32 v226, v146, v226\n"
"v_mfma_f32_16x16x32_bf16 v[108:111], a[216:219], a[120:123], v[108:111]\n"
"ds_read_b128 a[120:123], v242 offset:6144\n"
"v_mul_f32 v227, v147, v227\n"
"v_mfma_f32_16x16x32_bf16 v[108:111], a[220:223], a[124:127], v[108:111]\n"
"ds_read_b128 a[124:127], v243 offset:6144\n"
"v_cvt_pk_bf16_f32 v224, v224, v225\n"
"v_mfma_f32_16x16x32_bf16 v[112:115], a[216:219], a[128:131], v[112:115]\n"
"ds_read_b128 a[128:131], v242 offset:8192\n"
"v_cvt_pk_bf16_f32 v225, v226, v227\n"
"v_mfma_f32_16x16x32_bf16 v[112:115], a[220:223], a[132:135], v[112:115]\n"
"ds_read_b128 a[132:135], v243 offset:8192\n"
"ds_write_b64 v244, v[224:225] offset:24576\n"
"v_mfma_f32_16x16x32_bf16 v[116:119], a[216:219], a[136:139], v[116:119]\n"
"ds_read_b128 a[136:139], v242 offset:10240\n"
"v_mul_f32 v228, v176, v228\n"
"v_mfma_f32_16x16x32_bf16 v[116:119], a[220:223], a[140:143], v[116:119]\n"
"ds_read_b128 a[140:143], v243 offset:10240\n"
"v_mul_f32 v229, v177, v229\n"
"v_mfma_f32_16x16x32_bf16 v[120:123], a[216:219], a[144:147], v[120:123]\n"
"ds_read_b128 a[144:147], v242 offset:12288\n"
"s_waitcnt lgkmcnt(15)\n"
"v_mfma_f32_16x16x32_bf16 v[224:227], v[252:255], v[232:235], 0\n"
"v_mfma_f32_16x16x32_bf16 v[120:123], a[220:223], a[148:151], v[120:123]\n"
"ds_read_b128 a[148:151], v243 offset:12288\n"
"v_mul_f32 v230, v178, v230\n"
"v_mfma_f32_16x16x32_bf16 v[124:127], a[216:219], a[152:155], v[124:127]\n"
"global_load_dwordx4 a[216:219], v246, s[16:17] offset:2048 sc1\n"
"ds_read_b128 a[152:155], v242 offset:14336\n"
"v_mfma_f32_16x16x32_bf16 v[124:127], a[220:223], a[156:159], v[124:127]\n"
"ds_read_b128 a[156:159], v243 offset:14336\n"
"s_waitcnt vmcnt(13) lgkmcnt(15)\n"
"v_mfma_f32_16x16x32_bf16 v[0:3], a[160:163], a[96:99], v[0:3]\n"
"global_load_dwordx4 a[220:223], v246, s[16:17] offset:3072 sc1\n"
"v_mul_f32 v231, v179, v231\n"
"v_mfma_f32_16x16x32_bf16 v[0:3], a[164:167], a[100:103], v[0:3]\n"
"v_cvt_pk_bf16_f32 v228, v228, v229\n"
"s_waitcnt lgkmcnt(13)\n"
"v_mfma_f32_16x16x32_bf16 v[4:7], a[160:163], a[104:107], v[4:7]\n"
"v_add_u32 v246, 0x2000, v246\n"
"v_cvt_pk_bf16_f32 v229, v230, v231\n"
"v_mfma_f32_16x16x32_bf16 v[4:7], a[164:167], a[108:111], v[4:7]\n"
"ds_write_b64 v244, v[228:229] offset:24584\n"
"s_waitcnt lgkmcnt(12)\n"
"v_mfma_f32_16x16x32_bf16 v[8:11], a[160:163], a[112:115], v[8:11]\n"
"v_mul_f32 v224, v208, v224\n"
"v_mul_f32 v225, v209, v225\n"
"v_mfma_f32_16x16x32_bf16 v[8:11], a[164:167], a[116:119], v[8:11]\n"
"v_mfma_f32_16x16x32_bf16 v[228:231], v[250:253], v[232:235], 0\n"
"s_waitcnt lgkmcnt(10)\n"
"v_mfma_f32_16x16x32_bf16 v[12:15], a[160:163], a[120:123], v[12:15]\n"
"ds_read_b128 v[232:235], v236 offset:5120\n"
"v_mul_f32 v226, v210, v226\n"
"v_mfma_f32_16x16x32_bf16 v[12:15], a[164:167], a[124:127], v[12:15]\n"
"v_mul_f32 v227, v211, v227\n"
"s_waitcnt lgkmcnt(9)\n"
"v_mfma_f32_16x16x32_bf16 v[16:19], a[160:163], a[128:131], v[16:19]\n"
"v_cvt_pk_bf16_f32 v224, v224, v225\n"
"v_cvt_pk_bf16_f32 v225, v226, v227\n"
"v_mfma_f32_16x16x32_bf16 v[16:19], a[164:167], a[132:135], v[16:19]\n"
"ds_write_b64 v245, v[224:225] offset:24576\n"
"s_waitcnt lgkmcnt(7)\n"
"v_mfma_f32_16x16x32_bf16 v[20:23], a[160:163], a[136:139], v[20:23]\n"
"v_accvgpr_read_b32 v249, a16\n"
"v_mul_f32_e64 v228, v249, v228\n"
"v_mfma_f32_16x16x32_bf16 v[20:23], a[164:167], a[140:143], v[20:23]\n"
"v_mfma_f32_16x16x32_bf16 v[224:227], v[252:255], a[244:247], 0\n"
"s_waitcnt lgkmcnt(5)\n"
"v_mfma_f32_16x16x32_bf16 v[24:27], a[160:163], a[144:147], v[24:27]\n"
"v_accvgpr_read_b32 v249, a17\n"
"v_mul_f32_e64 v229, v249, v229\n"
"v_mfma_f32_16x16x32_bf16 v[24:27], a[164:167], a[148:151], v[24:27]\n"
"v_accvgpr_read_b32 v249, a18\n"
"s_waitcnt lgkmcnt(3)\n"
"v_mfma_f32_16x16x32_bf16 v[28:31], a[160:163], a[152:155], v[28:31]\n"
"global_load_dwordx4 a[160:163], v246, s[16:17] offset:-4096 sc1\n"
"v_mul_f32_e64 v230, v249, v230\n"
"v_mfma_f32_16x16x32_bf16 v[28:31], a[164:167], a[156:159], v[28:31]\n"
"v_accvgpr_read_b32 v249, a19\n"
"s_waitcnt vmcnt(13)\n"
"v_mfma_f32_16x16x32_bf16 v[32:35], a[168:171], a[96:99], v[32:35]\n"
"global_load_dwordx4 a[164:167], v246, s[16:17] offset:-3072 sc1\n"
"v_mul_f32_e64 v231, v249, v231\n"
"v_mfma_f32_16x16x32_bf16 v[32:35], a[172:175], a[100:103], v[32:35]\n"
"v_cvt_pk_bf16_f32 v228, v228, v229\n"
"v_cvt_pk_bf16_f32 v229, v230, v231\n"
"v_mfma_f32_16x16x32_bf16 v[36:39], a[168:171], a[104:107], v[36:39]\n"
"ds_write_b64 v245, v[228:229] offset:24584\n"
"v_mul_f32 v224, v148, v224\n"
"v_mfma_f32_16x16x32_bf16 v[36:39], a[172:175], a[108:111], v[36:39]\n"
"v_mul_f32 v225, v149, v225\n"
"v_mfma_f32_16x16x32_bf16 v[228:231], v[250:253], a[244:247], 0\n"
"v_mfma_f32_16x16x32_bf16 v[40:43], a[168:171], a[112:115], v[40:43]\n"
"v_readfirstlane_b32 s46, v239\n"
"v_mul_f32 v226, v150, v226\n"
"v_mfma_f32_16x16x32_bf16 v[40:43], a[172:175], a[116:119], v[40:43]\n"
"v_mul_f32 v227, v151, v227\n"
"v_cvt_pk_bf16_f32 v224, v224, v225\n"
"v_mfma_f32_16x16x32_bf16 v[44:47], a[168:171], a[120:123], v[44:47]\n"
"v_cvt_pk_bf16_f32 v225, v226, v227\n"
"ds_write_b64 v244, v[224:225] offset:26624\n"
"v_mfma_f32_16x16x32_bf16 v[44:47], a[172:175], a[124:127], v[44:47]\n"
"s_sub_u32 s46, s46, s44\n"
"v_mul_f32 v228, v180, v228\n"
"v_mfma_f32_16x16x32_bf16 v[48:51], a[168:171], a[128:131], v[48:51]\n"
"s_cmp_ge_u32 s46, 81280\n"
"s_cbranch_scc0 .Lslow5_%=\n"
".Lback5_%=:\n"
"v_mfma_f32_16x16x32_bf16 v[48:51], a[172:175], a[132:135], v[48:51]\n"
"v_mul_f32 v229, v181, v229\n"
"s_waitcnt lgkmcnt(3)\n"
"v_mfma_f32_16x16x32_bf16 v[224:227], v[252:255], v[232:235], 0\n"
"v_mfma_f32_16x16x32_bf16 v[52:55], a[168:171], a[136:139], v[52:55]\n"
"v_mul_f32 v230, v182, v230\n"
"v_mul_f32 v231, v183, v231\n"
"v_mfma_f32_16x16x32_bf16 v[52:55], a[172:175], a[140:143], v[52:55]\n"
"v_cvt_pk_bf16_f32 v228, v228, v229\n"
"v_cvt_pk_bf16_f32 v229, v230, v231\n"
"v_mfma_f32_16x16x32_bf16 v[56:59], a[168:171], a[144:147], v[56:59]\n"
"ds_write_b64 v244, v[228:229] offset:26632\n"
"v_mul_f32 v224, v212, v224\n"
"v_mfma_f32_16x16x32_bf16 v[56:59], a[172:175], a[148:151], v[56:59]\n"
"v_mul_f32 v225, v213, v225\n"
"v_mfma_f32_16x16x32_bf16 v[228:231], v[250:253], v[232:235], 0\n"
"v_mfma_f32_16x16x32_bf16 v[60:63], a[168:171], a[152:155], v[60:63]\n"
"global_load_dwordx4 a[168:171], v246, s[16:17] offset:-2048 sc1\n"
"ds_read_b128 v[232:235], v236 offset:6144\n"
"v_mfma_f32_16x16x32_bf16 v[60:63], a[172:175], a[156:159], v[60:63]\n"
"v_mul_f32 v226, v214, v226\n"
"s_waitcnt vmcnt(13)\n"
"v_mfma_f32_16x16x32_bf16 v[64:67], a[176:179], a[96:99], v[64:67]\n"
"global_load_dwordx4 a[172:175], v246, s[16:17] offset:-1024 sc1\n"
"v_mul_f32 v227, v215, v227\n"
"v_mfma_f32_16x16x32_bf16 v[64:67], a[180:183], a[100:103], v[64:67]\n"
"v_cvt_pk_bf16_f32 v224, v224, v225\n"
"v_cvt_pk_bf16_f32 v225, v226, v227\n"
"v_mfma_f32_16x16x32_bf16 v[68:71], a[176:179], a[104:107], v[68:71]\n"
"ds_write_b64 v245, v[224:225] offset:26624\n"
"v_accvgpr_read_b32 v249, a20\n"
"v_mfma_f32_16x16x32_bf16 v[68:71], a[180:183], a[108:111], v[68:71]\n"
"v_mul_f32_e64 v228, v249, v228\n"
"v_mfma_f32_16x16x32_bf16 v[224:227], v[252:255], a[248:251], 0\n"
"v_mfma_f32_16x16x32_bf16 v[72:75], a[176:179], a[112:115], v[72:75]\n"
"v_accvgpr_read_b32 v249, a21\n"
"v_mul_f32_e64 v229, v249, v229\n"
"v_mfma_f32_16x16x32_bf16 v[72:75], a[180:183], a[116:119], v[72:75]\n"
"v_accvgpr_read_b32 v249, a22\n"
"v_mul_f32_e64 v230, v249, v230\n"
"v_mfma_f32_16x16x32_bf16 v[76:79], a[176:179], a[120:123], v[76:79]\n"
"v_accvgpr_read_b32 v249, a23\n"
"v_mul_f32_e64 v231, v249, v231\n"
"v_mfma_f32_16x16x32_bf16 v[76:79], a[180:183], a[124:127], v[76:79]\n"
"v_cvt_pk_bf16_f32 v228, v228, v229\n"
"v_cvt_pk_bf16_f32 v229, v230, v231\n"
"v_mfma_f32_16x16x32_bf16 v[80:83], a[176:179], a[128:131], v[80:83]\n"
"ds_write_b64 v245, v[228:229] offset:26632\n"
"v_mul_f32 v224, v152, v224\n"
"v_mfma_f32_16x16x32_bf16 v[80:83], a[180:183], a[132:135], v[80:83]\n"
"v_mul_f32 v225, v153, v225\n"
"v_mfma_f32_16x16x32_bf16 v[228:231], v[250:253], a[248:251], 0\n"
"v_mfma_f32_16x16x32_bf16 v[84:87], a[176:179], a[136:139], v[84:87]\n"
"v_mul_f32 v226, v154, v226\n"
"v_mul_f32 v227, v155, v227\n"
"v_mfma_f32_16x16x32_bf16 v[84:87], a[180:183], a[140:143], v[84:87]\n"
"v_cvt_pk_bf16_f32 v224, v224, v225\n"
"v_cvt_pk_bf16_f32 v225, v226, v227\n"
"v_mfma_f32_16x16x32_bf16 v[88:91], a[176:179], a[144:147], v[88:91]\n"
"ds_write_b64 v244, v[224:225] offset:28672\n"
"v_mul_f32 v228, v184, v228\n"
"v_mfma_f32_16x16x32_bf16 v[88:91], a[180:183], a[148:151], v[88:91]\n"
"v_mul_f32 v229, v185, v229\n"
"s_waitcnt lgkmcnt(3)\n"
"v_mfma_f32_16x16x32_bf16 v[224:227], v[252:255], v[232:235], 0\n"
"v_mfma_f32_16x16x32_bf16 v[92:95], a[176:179], a[152:155], v[92:95]\n"
"global_load_dwordx4 a[176:179], v246, s[16:17] offset:0 sc1\n"
"v_mul_f32 v230, v186, v230\n"
"v_mfma_f32_16x16x32_bf16 v[92:95], a[180:183], a[156:159], v[92:95]\n"
"v_mul_f32 v231, v187, v231\n"
"s_waitcnt vmcnt(13)\n"
"v_mfma_f32_16x16x32_bf16 v[96:99], a[184:187], a[96:99], v[96:99]\n"
"global_load_dwordx4 a[180:183], v246, s[16:17] offset:1024 sc1\n"
"ds_read_b128 a[96:99], v242 offset:32768\n"
"v_mfma_f32_16x16x32_bf16 v[96:99], a[188:191], a[100:103], v[96:99]\n"
"ds_read_b128 a[100:103], v243 offset:32768\n"
"v_cvt_pk_bf16_f32 v228, v228, v229\n"
"v_mfma_f32_16x16x32_bf16 v[100:103], a[184:187], a[104:107], v[100:103]\n"
"ds_read_b128 a[104:107], v242 offset:34816\n"
"v_cvt_pk_bf16_f32 v229, v230, v231\n"
"v_mfma_f32_16x16x32_bf16 v[100:103], a[188:191], a[108:111], v[100:103]\n"
"ds_read_b128 a[108:111], v243 offset:34816\n"
"ds_write_b64 v244, v[228:229] offset:28680\n"
"v_mfma_f32_16x16x32_bf16 v[104:107], a[184:187], a[112:115], v[104:107]\n"
"ds_read_b128 a[112:115], v242 offset:36864\n"
"v_mul_f32 v224, v216, v224\n"
"v_mfma_f32_16x16x32_bf16 v[104:107], a[188:191], a[116:119], v[104:107]\n"
"ds_read_b128 a[116:119], v243 offset:36864\n"
"v_mul_f32 v225, v217, v225\n"
"v_mfma_f32_16x16x32_bf16 v[108:111], a[184:187], a[120:123], v[108:111]\n"
"ds_read_b128 a[120:123], v242 offset:38912\n"
"v_mfma_f32_16x16x32_bf16 v[228:231], v[250:253], v[232:235], 0\n"
"v_mfma_f32_16x16x32_bf16 v[108:111], a[188:191], a[124:127], v[108:111]\n"
"ds_read_b128 a[124:127], v243 offset:38912\n"
"ds_read_b128 v[232:235], v236 offset:7168\n"
"v_mfma_f32_16x16x32_bf16 v[112:115], a[184:187], a[128:131], v[112:115]\n"
"ds_read_b128 a[128:131], v242 offset:40960\n"
"v_mul_f32 v226, v218, v226\n"
"v_mfma_f32_16x16x32_bf16 v[112:115], a[188:191], a[132:135], v[112:115]\n"
"ds_read_b128 a[132:135], v243 offset:40960\n"
"v_mul_f32 v227, v219, v227\n"
"v_mfma_f32_16x16x32_bf16 v[116:119], a[184:187], a[136:139], v[116:119]\n"
"ds_read_b128 a[136:139], v242 offset:43008\n"
"v_cvt_pk_bf16_f32 v224, v224, v225\n"
"v_mfma_f32_16x16x32_bf16 v[116:119], a[188:191], a[140:143], v[116:119]\n"
"ds_read_b128 a[140:143], v243 offset:43008\n"
"v_cvt_pk_bf16_f32 v225, v226, v227\n"
"v_mfma_f32_16x16x32_bf16 v[120:123], a[184:187], a[144:147], v[120:123]\n"
"ds_read_b128 a[144:147], v242 offset:45056\n"
"ds_write_b64 v245, v[224:225] offset:28672\n"
"v_mfma_f32_16x16x32_bf16 v[120:123], a[188:191], a[148:151], v[120:123]\n"
"ds_read_b128 a[148:151], v243 offset:45056\n"
"v_accvgpr_read_b32 v249, a24\n"
"v_mfma_f32_16x16x32_bf16 v[124:127], a[184:187], a[152:155], v[124:127]\n"
"global_load_dwordx4 a[184:187], v246, s[16:17] offset:2048 sc1\n"
"ds_read_b128 a[152:155], v242 offset:47104\n"
"v_mfma_f32_16x16x32_bf16 v[124:127], a[188:191], a[156:159], v[124:127]\n"
"ds_read_b128 a[156:159], v243 offset:47104\n"
"s_waitcnt vmcnt(13) lgkmcnt(15)\n"
"v_mfma_f32_16x16x32_bf16 v[0:3], a[192:195], a[96:99], v[0:3]\n"
"global_load_dwordx4 a[188:191], v246, s[16:17] offset:3072 sc1\n"
"v_mul_f32_e64 v228, v249, v228\n"
"v_mfma_f32_16x16x32_bf16 v[0:3], a[196:199], a[100:103], v[0:3]\n"
"v_mfma_f32_16x16x32_bf16 v[224:227], v[252:255], a[252:255], 0\n"
"v_mfma_f32_16x16x32_bf16 v[4:7], a[192:195], a[104:107], v[4:7]\n"
"v_add_u32 v246, 0x2000, v246\n"
"v_accvgpr_read_b32 v249, a25\n"
"v_mfma_f32_16x16x32_bf16 v[4:7], a[196:199], a[108:111], v[4:7]\n"
"v_mul_f32_e64 v229, v249, v229\n"
"s_waitcnt lgkmcnt(12)\n"
"v_mfma_f32_16x16x32_bf16 v[8:11], a[192:195], a[112:115], v[8:11]\n"
"v_accvgpr_read_b32 v249, a26\n"
"v_mul_f32_e64 v230, v249, v230\n"
"v_mfma_f32_16x16x32_bf16 v[8:11], a[196:199], a[116:119], v[8:11]\n"
"v_accvgpr_read_b32 v249, a27\n"
"s_waitcnt lgkmcnt(10)\n"
"v_mfma_f32_16x16x32_bf16 v[12:15], a[192:195], a[120:123], v[12:15]\n"
"v_mul_f32_e64 v231, v249, v231\n"
"v_cvt_pk_bf16_f32 v228, v228, v229\n"
"v_mfma_f32_16x16x32_bf16 v[12:15], a[196:199], a[124:127], v[12:15]\n"
"v_cvt_pk_bf16_f32 v229, v230, v231\n"
"s_waitcnt lgkmcnt(7)\n"
"v_mfma_f32_16x16x32_bf16 v[16:19], a[192:195], a[128:131], v[16:19]\n"
"ds_write_b64 v245, v[228:229] offset:28680\n"
"v_mul_f32 v224, v156, v224\n"
"v_mfma_f32_16x16x32_bf16 v[16:19], a[196:199], a[132:135], v[16:19]\n"
"v_mul_f32 v225, v157, v225\n"
"s_waitcnt lgkmcnt(6)\n"
"v_mfma_f32_16x16x32_bf16 v[20:23], a[192:195], a[136:139], v[20:23]\n"
"v_mfma_f32_16x16x32_bf16 v[228:231], v[250:253], a[252:255], 0\n"
"v_mul_f32 v226, v158, v226\n"
"v_mfma_f32_16x16x32_bf16 v[20:23], a[196:199], a[140:143], v[20:23]\n"
"v_mul_f32 v227, v159, v227\n"
"s_waitcnt lgkmcnt(3)\n"
"v_mfma_f32_16x16x32_bf16 v[24:27], a[192:195], a[144:147], v[24:27]\n"
"v_cvt_pk_bf16_f32 v224, v224, v225\n"
"v_cvt_pk_bf16_f32 v225, v226, v227\n"
"v_mfma_f32_16x16x32_bf16 v[24:27], a[196:199], a[148:151], v[24:27]\n"
"ds_write_b64 v244, v[224:225] offset:30720\n"
"s_waitcnt lgkmcnt(2)\n"
"v_mfma_f32_16x16x32_bf16 v[28:31], a[192:195], a[152:155], v[28:31]\n"
"global_load_dwordx4 a[192:195], v246, s[16:17] offset:-4096 sc1\n"
"v_mul_f32 v228, v188, v228\n"
"v_mfma_f32_16x16x32_bf16 v[28:31], a[196:199], a[156:159], v[28:31]\n"
"v_mul_f32 v229, v189, v229\n"
"s_waitcnt vmcnt(13)\n"
"v_mfma_f32_16x16x32_bf16 v[32:35], a[200:203], a[96:99], v[32:35]\n"
"global_load_dwordx4 a[196:199], v246, s[16:17] offset:-3072 sc1\n"
"v_mfma_f32_16x16x32_bf16 v[224:227], v[252:255], v[232:235], 0\n"
"v_mfma_f32_16x16x32_bf16 v[32:35], a[204:207], a[100:103], v[32:35]\n"
"v_mul_f32 v230, v190, v230\n"
"v_mul_f32 v231, v191, v231\n"
"v_mfma_f32_16x16x32_bf16 v[36:39], a[200:203], a[104:107], v[36:39]\n"
"v_cvt_pk_bf16_f32 v228, v228, v229\n"
"v_cvt_pk_bf16_f32 v229, v230, v231\n"
"v_mfma_f32_16x16x32_bf16 v[36:39], a[204:207], a[108:111], v[36:39]\n"
"ds_write_b64 v244, v[228:229] offset:30728\n"
"v_mul_f32 v224, v220, v224\n"
"v_mfma_f32_16x16x32_bf16 v[40:43], a[200:203], a[112:115], v[40:43]\n"
"v_mul_f32 v225, v221, v225\n"
"v_mfma_f32_16x16x32_bf16 v[228:231], v[250:253], v[232:235], 0\n"
"v_mfma_f32_16x16x32_bf16 v[40:43], a[204:207], a[116:119], v[40:43]\n"
"v_mul_f32 v226, v222, v226\n"
"v_mul_f32 v227, v223, v227\n"
"v_mfma_f32_16x16x32_bf16 v[44:47], a[200:203], a[120:123], v[44:47]\n"
"v_cvt_pk_bf16_f32 v224, v224, v225\n"
"v_cvt_pk_bf16_f32 v225, v226, v227\n"
"v_mfma_f32_16x16x32_bf16 v[44:47], a[204:207], a[124:127], v[44:47]\n"
"ds_write_b64 v245, v[224:225] offset:30720\n"
"v_accvgpr_read_b32 v249, a28\n"
"v_mfma_f32_16x16x32_bf16 v[48:51], a[200:203], a[128:131], v[48:51]\n"
"v_mul_f32_e64 v228, v249, v228\n"
"v_accvgpr_read_b32 v249, a29\n"
"v_mfma_f32_16x16x32_bf16 v[48:51], a[204:207], a[132:135], v[48:51]\n"
"v_mul_f32_e64 v229, v249, v229\n"
"v_accvgpr_read_b32 v249, a30\n"
"v_mfma_f32_16x16x32_bf16 v[52:55], a[200:203], a[136:139], v[52:55]\n"
"v_mul_f32_e64 v230, v249, v230\n"
"v_accvgpr_read_b32 v249, a31\n"
"v_mfma_f32_16x16x32_bf16 v[52:55], a[204:207], a[140:143], v[52:55]\n"
"v_mul_f32_e64 v231, v249, v231\n"
"v_cvt_pk_bf16_f32 v228, v228, v229\n"
"v_mfma_f32_16x16x32_bf16 v[56:59], a[200:203], a[144:147], v[56:59]\n"
"v_cvt_pk_bf16_f32 v229, v230, v231\n"
"ds_write_b64 v245, v[228:229] offset:30728\n"
"v_mfma_f32_16x16x32_bf16 v[56:59], a[204:207], a[148:151], v[56:59]\n"
"s_add_u32 s64, s60, 61440\n"
"s_addc_u32 s65, s61, 0\n"
"v_mfma_f32_16x16x32_bf16 v[60:63], a[200:203], a[152:155], v[60:63]\n"
"global_load_dwordx4 a[200:203], v246, s[16:17] offset:-2048 sc1\n"
"v_mfma_f32_16x16x32_bf16 v[60:63], a[204:207], a[156:159], v[60:63]\n"
"s_waitcnt vmcnt(13)\n"
"v_mfma_f32_16x16x32_bf16 v[64:67], a[208:211], a[96:99], v[64:67]\n"
"global_load_dwordx4 a[204:207], v246, s[16:17] offset:-1024 sc1\n"
"v_mfma_f32_16x16x32_bf16 v[64:67], a[212:215], a[100:103], v[64:67]\n"
"v_mfma_f32_16x16x32_bf16 v[68:71], a[208:211], a[104:107], v[68:71]\n"
"v_mfma_f32_16x16x32_bf16 v[68:71], a[212:215], a[108:111], v[68:71]\n"
"v_mfma_f32_16x16x32_bf16 v[72:75], a[208:211], a[112:115], v[72:75]\n"
"v_mfma_f32_16x16x32_bf16 v[72:75], a[212:215], a[116:119], v[72:75]\n"
"v_mfma_f32_16x16x32_bf16 v[76:79], a[208:211], a[120:123], v[76:79]\n"
"v_mfma_f32_16x16x32_bf16 v[76:79], a[212:215], a[124:127], v[76:79]\n"
"v_mfma_f32_16x16x32_bf16 v[80:83], a[208:211], a[128:131], v[80:83]\n"
"v_mfma_f32_16x16x32_bf16 v[80:83], a[212:215], a[132:135], v[80:83]\n"
"v_mfma_f32_16x16x32_bf16 v[84:87], a[208:211], a[136:139], v[84:87]\n"
"v_mfma_f32_16x16x32_bf16 v[84:87], a[212:215], a[140:143], v[84:87]\n"
"v_mfma_f32_16x16x32_bf16 v[88:91], a[208:211], a[144:147], v[88:91]\n"
"v_mfma_f32_16x16x32_bf16 v[88:91], a[212:215], a[148:151], v[88:91]\n"
"s_waitcnt lgkmcnt(0)\n"
"s_barrier\n"
"v_mfma_f32_16x16x32_bf16 v[92:95], a[208:211], a[152:155], v[92:95]\n"
"global_load_dword a208, v250, s[64:65]\n"
"global_load_dwordx4 a[208:211], v246, s[16:17] offset:0 sc1\n"
"v_mfma_f32_16x16x32_bf16 v[92:95], a[212:215], a[156:159], v[92:95]\n"
"s_waitcnt vmcnt(14)\n"
"v_mfma_f32_16x16x32_bf16 v[96:99], a[216:219], a[96:99], v[96:99]\n"
"global_load_dwordx4 a[212:215], v246, s[16:17] offset:1024 sc1\n"
"ds_read_b128 a[96:99], v240 offset:16384\n"
"v_mfma_f32_16x16x32_bf16 v[96:99], a[220:223], a[100:103], v[96:99]\n"
"ds_read_b128 a[100:103], v241 offset:16384\n"
"v_mfma_f32_16x16x32_bf16 v[100:103], a[216:219], a[104:107], v[100:103]\n"
"ds_read_b128 a[104:107], v240 offset:18432\n"
"v_mfma_f32_16x16x32_bf16 v[100:103], a[220:223], a[108:111], v[100:103]\n"
"ds_read_b128 a[108:111], v241 offset:18432\n"
"v_mfma_f32_16x16x32_bf16 v[104:107], a[216:219], a[112:115], v[104:107]\n"
"ds_read_b128 a[112:115], v240 offset:20480\n"
"v_mfma_f32_16x16x32_bf16 v[104:107], a[220:223], a[116:119], v[104:107]\n"
"ds_read_b128 a[116:119], v241 offset:20480\n"
"v_mfma_f32_16x16x32_bf16 v[108:111], a[216:219], a[120:123], v[108:111]\n"
"ds_read_b128 a[120:123], v240 offset:22528\n"
"v_mfma_f32_16x16x32_bf16 v[108:111], a[220:223], a[124:127], v[108:111]\n"
"ds_read_b128 a[124:127], v241 offset:22528\n"
"v_mfma_f32_16x16x32_bf16 v[112:115], a[216:219], a[128:131], v[112:115]\n"
"ds_read_b128 a[128:131], v240 offset:24576\n"
"v_mfma_f32_16x16x32_bf16 v[112:115], a[220:223], a[132:135], v[112:115]\n"
"ds_read_b128 a[132:135], v241 offset:24576\n"
"v_mfma_f32_16x16x32_bf16 v[116:119], a[216:219], a[136:139], v[116:119]\n"
"ds_read_b128 a[136:139], v240 offset:26624\n"
"v_mfma_f32_16x16x32_bf16 v[116:119], a[220:223], a[140:143], v[116:119]\n"
"ds_read_b128 a[140:143], v241 offset:26624\n"
"v_mfma_f32_16x16x32_bf16 v[120:123], a[216:219], a[144:147], v[120:123]\n"
"ds_read_b128 a[144:147], v240 offset:28672\n"
"v_mfma_f32_16x16x32_bf16 v[120:123], a[220:223], a[148:151], v[120:123]\n"
"ds_read_b128 a[148:151], v241 offset:28672\n"
"v_mfma_f32_16x16x32_bf16 v[124:127], a[216:219], a[152:155], v[124:127]\n"
"global_load_dwordx4 a[216:219], v246, s[16:17] offset:2048 sc1\n"
"ds_read_b128 a[152:155], v240 offset:30720\n"
"v_mfma_f32_16x16x32_bf16 v[124:127], a[220:223], a[156:159], v[124:127]\n"
"ds_read_b128 a[156:159], v241 offset:30720\n"
"s_waitcnt vmcnt(14) lgkmcnt(14)\n"
"v_mfma_f32_16x16x32_bf16 v[128:131], a[160:163], a[96:99], v[128:131]\n"
"global_load_dwordx4 a[220:223], v246, s[16:17] offset:3072 sc1\n"
"v_mfma_f32_16x16x32_bf16 v[128:131], a[164:167], a[100:103], v[128:131]\n"
"s_waitcnt lgkmcnt(12)\n"
"v_mfma_f32_16x16x32_bf16 v[132:135], a[160:163], a[104:107], v[132:135]\n"
"v_add_u32 v246, 0x2000, v246\n"
"v_mfma_f32_16x16x32_bf16 v[232:235], v[252:255], a[32:35], 0\n"
"v_mfma_f32_16x16x32_bf16 v[132:135], a[164:167], a[108:111], v[132:135]\n"
"s_waitcnt lgkmcnt(10)\n"
"v_mfma_f32_16x16x32_bf16 v[136:139], a[160:163], a[112:115], v[136:139]\n"
"v_mfma_f32_16x16x32_bf16 v[136:139], a[164:167], a[116:119], v[136:139]\n"
"s_waitcnt lgkmcnt(8)\n"
"v_mfma_f32_16x16x32_bf16 v[140:143], a[160:163], a[120:123], v[140:143]\n"
"ds_read_b128 v[224:227], v247 offset:16384\n"
"v_mfma_f32_16x16x32_bf16 v[140:143], a[164:167], a[124:127], v[140:143]\n"
"v_mul_f32 v232, v0, v232\n"
"s_waitcnt lgkmcnt(7)\n"
"v_mfma_f32_16x16x32_bf16 v[144:147], a[160:163], a[128:131], v[144:147]\n"
"ds_read_b128 v[228:231], v247 offset:17408\n"
"v_mul_f32 v233, v1, v233\n"
"v_mfma_f32_16x16x32_bf16 v[144:147], a[164:167], a[132:135], v[144:147]\n"
"v_mfma_f32_16x16x32_bf16 v[236:239], v[252:255], a[36:39], 0\n"
"s_waitcnt lgkmcnt(6)\n"
"v_mfma_f32_16x16x32_bf16 v[148:151], a[160:163], a[136:139], v[148:151]\n"
"v_mul_f32 v234, v2, v234\n"
"v_mfma_f32_16x16x32_bf16 v[148:151], a[164:167], a[140:143], v[148:151]\n"
"v_mul_f32 v235, v3, v235\n"
"s_waitcnt lgkmcnt(4)\n"
"v_mfma_f32_16x16x32_bf16 v[152:155], a[160:163], a[144:147], v[152:155]\n"
"v_cvt_pk_bf16_f32 v232, v232, v233\n"
"v_mfma_f32_16x16x32_bf16 v[152:155], a[164:167], a[148:151], v[152:155]\n"
"v_cvt_pk_bf16_f32 v233, v234, v235\n"
"s_waitcnt lgkmcnt(2)\n"
"v_mfma_f32_16x16x32_bf16 v[156:159], a[160:163], a[152:155], v[156:159]\n"
"global_load_dwordx4 a[160:163], v246, s[16:17] offset:-4096 sc1\n"
"ds_write_b64 v244, v[232:233] offset:0\n"
"v_mfma_f32_16x16x32_bf16 v[156:159], a[164:167], a[156:159], v[156:159]\n"
"s_waitcnt vmcnt(14)\n"
"v_mfma_f32_16x16x32_bf16 v[160:163], a[168:171], a[96:99], v[160:163]\n"
"global_load_dwordx4 a[164:167], v246, s[16:17] offset:-3072 sc1\n"
"v_mul_f32 v236, v4, v236\n"
"v_mfma_f32_16x16x32_bf16 v[160:163], a[172:175], a[100:103], v[160:163]\n"
"v_mul_f32 v237, v5, v237\n"
"v_mfma_f32_16x16x32_bf16 v[164:167], a[168:171], a[104:107], v[164:167]\n"
"v_add_u32 v249, 128, v248\n"
"v_mfma_f32_16x16x32_bf16 v[232:235], v[252:255], a[40:43], 0\n"
"v_mfma_f32_16x16x32_bf16 v[164:167], a[172:175], a[108:111], v[164:167]\n"
"s_waitcnt lgkmcnt(2)\n"
"global_store_dwordx4 v249, v[224:227], s[24:25] sc1\n"
"v_mfma_f32_16x16x32_bf16 v[168:171], a[168:171], a[112:115], v[168:171]\n"
"v_mul_f32 v238, v6, v238\n"
"v_mul_f32 v239, v7, v239\n"
"v_mfma_f32_16x16x32_bf16 v[168:171], a[172:175], a[116:119], v[168:171]\n"
"v_cvt_pk_bf16_f32 v236, v236, v237\n"
"v_mfma_f32_16x16x32_bf16 v[172:175], a[168:171], a[120:123], v[172:175]\n"
"ds_read_b128 v[224:227], v247 offset:18432\n"
"v_cvt_pk_bf16_f32 v237, v238, v239\n"
"v_mfma_f32_16x16x32_bf16 v[172:175], a[172:175], a[124:127], v[172:175]\n"
"ds_write_b64 v244, v[236:237] offset:2048\n"
"v_mfma_f32_16x16x32_bf16 v[176:179], a[168:171], a[128:131], v[176:179]\n"
"v_mfma_f32_16x16x32_bf16 v[176:179], a[172:175], a[132:135], v[176:179]\n"
"v_mul_f32 v232, v8, v232\n"
"v_mfma_f32_16x16x32_bf16 v[180:183], a[168:171], a[136:139], v[180:183]\n"
"v_mul_f32 v233, v9, v233\n"
"v_mfma_f32_16x16x32_bf16 v[180:183], a[172:175], a[140:143], v[180:183]\n"
"v_mfma_f32_16x16x32_bf16 v[236:239], v[252:255], a[44:47], 0\n"
"v_mfma_f32_16x16x32_bf16 v[184:187], a[168:171], a[144:147], v[184:187]\n"
"v_mul_f32 v234, v10, v234\n"
"v_mfma_f32_16x16x32_bf16 v[184:187], a[172:175], a[148:151], v[184:187]\n"
"v_mul_f32 v235, v11, v235\n"
"v_mfma_f32_16x16x32_bf16 v[188:191], a[168:171], a[152:155], v[188:191]\n"
"global_load_dwordx4 a[168:171], v246, s[16:17] offset:-2048 sc1\n"
"v_cvt_pk_bf16_f32 v232, v232, v233\n"
"v_mfma_f32_16x16x32_bf16 v[188:191], a[172:175], a[156:159], v[188:191]\n"
"v_cvt_pk_bf16_f32 v233, v234, v235\n"
"s_waitcnt vmcnt(15)\n"
"v_mfma_f32_16x16x32_bf16 v[192:195], a[176:179], a[96:99], v[192:195]\n"
"global_load_dwordx4 a[172:175], v246, s[16:17] offset:-1024 sc1\n"
"ds_write_b64 v244, v[232:233] offset:4096\n"
"v_mfma_f32_16x16x32_bf16 v[192:195], a[180:183], a[100:103], v[192:195]\n"
"v_mfma_f32_16x16x32_bf16 v[196:199], a[176:179], a[104:107], v[196:199]\n"
"v_mul_f32 v236, v12, v236\n"
"v_mfma_f32_16x16x32_bf16 v[196:199], a[180:183], a[108:111], v[196:199]\n"
"v_mul_f32 v237, v13, v237\n"
"v_mfma_f32_16x16x32_bf16 v[200:203], a[176:179], a[112:115], v[200:203]\n"
"v_mfma_f32_16x16x32_bf16 v[232:235], v[252:255], a[48:51], 0\n"
"v_mfma_f32_16x16x32_bf16 v[200:203], a[180:183], a[116:119], v[200:203]\n"
"v_mul_f32 v238, v14, v238\n"
"v_mfma_f32_16x16x32_bf16 v[204:207], a[176:179], a[120:123], v[204:207]\n"
"v_mul_f32 v239, v15, v239\n"
"v_mfma_f32_16x16x32_bf16 v[204:207], a[180:183], a[124:127], v[204:207]\n"
"v_cvt_pk_bf16_f32 v236, v236, v237\n"
"v_mfma_f32_16x16x32_bf16 v[208:211], a[176:179], a[128:131], v[208:211]\n"
"v_cvt_pk_bf16_f32 v237, v238, v239\n"
"v_mfma_f32_16x16x32_bf16 v[208:211], a[180:183], a[132:135], v[208:211]\n"
"ds_write_b64 v244, v[236:237] offset:6144\n"
"v_mfma_f32_16x16x32_bf16 v[212:215], a[176:179], a[136:139], v[212:215]\n"
"v_mfma_f32_16x16x32_bf16 v[212:215], a[180:183], a[140:143], v[212:215]\n"
"v_mul_f32 v232, v16, v232\n"
"v_mfma_f32_16x16x32_bf16 v[216:219], a[176:179], a[144:147], v[216:219]\n"
"v_mul_f32 v233, v17, v233\n"
"v_mfma_f32_16x16x32_bf16 v[216:219], a[180:183], a[148:151], v[216:219]\n"
"v_mfma_f32_16x16x32_bf16 v[236:239], v[252:255], a[52:55], 0\n"
"v_mfma_f32_16x16x32_bf16 v[220:223], a[176:179], a[152:155], v[220:223]\n"
"global_load_dwordx4 a[176:179], v246, s[16:17] offset:0 sc1\n"
"v_mul_f32 v234, v18, v234\n"
"v_mfma_f32_16x16x32_bf16 v[220:223], a[180:183], a[156:159], v[220:223]\n"
"v_mul_f32 v235, v19, v235\n"
"s_waitcnt vmcnt(15)\n"
"v_mfma_f32_16x16x32_bf16 a[0:3], a[184:187], a[96:99], a[0:3]\n"
"global_load_dwordx4 a[180:183], v246, s[16:17] offset:1024 sc1\n"
"ds_read_b128 a[96:99], v240 offset:49152\n"
"v_mfma_f32_16x16x32_bf16 a[0:3], a[188:191], a[100:103], a[0:3]\n"
"ds_read_b128 a[100:103], v241 offset:49152\n"
"v_cvt_pk_bf16_f32 v232, v232, v233\n"
"v_mfma_f32_16x16x32_bf16 a[4:7], a[184:187], a[104:107], a[4:7]\n"
"ds_read_b128 a[104:107], v240 offset:51200\n"
"v_cvt_pk_bf16_f32 v233, v234, v235\n"
"v_mfma_f32_16x16x32_bf16 a[4:7], a[188:191], a[108:111], a[4:7]\n"
"ds_read_b128 a[108:111], v241 offset:51200\n"
"ds_write_b64 v244, v[232:233] offset:8192\n"
"v_mfma_f32_16x16x32_bf16 a[8:11], a[184:187], a[112:115], a[8:11]\n"
"ds_read_b128 a[112:115], v240 offset:53248\n"
"v_mul_f32 v236, v20, v236\n"
"v_mfma_f32_16x16x32_bf16 a[8:11], a[188:191], a[116:119], a[8:11]\n"
"ds_read_b128 a[116:119], v241 offset:53248\n"
"v_mul_f32 v237, v21, v237\n"
"v_mfma_f32_16x16x32_bf16 a[12:15], a[184:187], a[120:123], a[12:15]\n"
"ds_read_b128 a[120:123], v240 offset:55296\n"
"v_mfma_f32_16x16x32_bf16 v[232:235], v[252:255], a[56:59], 0\n"
"v_mfma_f32_16x16x32_bf16 a[12:15], a[188:191], a[124:127], a[12:15]\n"
"ds_read_b128 a[124:127], v241 offset:55296\n"
"v_mul_f32 v238, v22, v238\n"
"v_mfma_f32_16x16x32_bf16 a[16:19], a[184:187], a[128:131], a[16:19]\n"
"ds_read_b128 a[128:131], v240 offset:57344\n"
"v_mul_f32 v239, v23, v239\n"
"v_mfma_f32_16x16x32_bf16 a[16:19], a[188:191], a[132:135], a[16:19]\n"
"ds_read_b128 a[132:135], v241 offset:57344\n"
"v_cvt_pk_bf16_f32 v236, v236, v237\n"
"v_mfma_f32_16x16x32_bf16 a[20:23], a[184:187], a[136:139], a[20:23]\n"
"ds_read_b128 a[136:139], v240 offset:59392\n"
"v_cvt_pk_bf16_f32 v237, v238, v239\n"
"v_mfma_f32_16x16x32_bf16 a[20:23], a[188:191], a[140:143], a[20:23]\n"
"ds_read_b128 a[140:143], v241 offset:59392\n"
"ds_write_b64 v244, v[236:237] offset:10240\n"
".Lpg15_%=:\n"
"s_cbranch_execz .Lreta_%=\n"
"v_mfma_f32_16x16x32_bf16 a[24:27], a[184:187], a[144:147], a[24:27]\n"
"ds_read_b128 a[144:147], v240 offset:61440\n"
"v_mfma_f32_16x16x32_bf16 a[24:27], a[188:191], a[148:151], a[24:27]\n"
"ds_read_b128 a[148:151], v241 offset:61440\n"
"v_mul_f32 v232, v24, v232\n"
"v_mfma_f32_16x16x32_bf16 a[28:31], a[184:187], a[152:155], a[28:31]\n"
"global_load_dwordx4 a[184:187], v246, s[16:17] offset:2048 sc1\n"
"ds_read_b128 a[152:155], v240 offset:63488\n"
"v_mfma_f32_16x16x32_bf16 a[28:31], a[188:191], a[156:159], a[28:31]\n"
"ds_read_b128 a[156:159], v241 offset:63488\n"
"s_waitcnt vmcnt(15) lgkmcnt(15)\n"
"v_mfma_f32_16x16x32_bf16 v[128:131], a[192:195], a[96:99], v[128:131]\n"
"global_load_dwordx4 a[188:191], v246, s[16:17] offset:3072 sc1\n"
"v_mul_f32 v233, v25, v233\n"
"v_mfma_f32_16x16x32_bf16 v[128:131], a[196:199], a[100:103], v[128:131]\n"
"v_mfma_f32_16x16x32_bf16 v[236:239], v[252:255], a[60:63], 0\n"
"s_waitcnt lgkmcnt(14)\n"
"v_mfma_f32_16x16x32_bf16 v[132:135], a[192:195], a[104:107], v[132:135]\n"
"v_add_u32 v246, 0x2000, v246\n"
"v_mul_f32 v234, v26, v234\n"
"v_mfma_f32_16x16x32_bf16 v[132:135], a[196:199], a[108:111], v[132:135]\n"
"v_mul_f32 v235, v27, v235\n"
"s_waitcnt lgkmcnt(11)\n"
"v_mfma_f32_16x16x32_bf16 v[136:139], a[192:195], a[112:115], v[136:139]\n"
"v_add_u32 v249, 16512, v248\n"
"v_cvt_pk_bf16_f32 v232, v232, v233\n"
"v_mfma_f32_16x16x32_bf16 v[136:139], a[196:199], a[116:119], v[136:139]\n"
"v_cvt_pk_bf16_f32 v233, v234, v235\n"
"s_waitcnt lgkmcnt(9)\n"
"v_mfma_f32_16x16x32_bf16 v[140:143], a[192:195], a[120:123], v[140:143]\n"
"global_store_dwordx4 v249, v[228:231], s[24:25] sc1\n"
"v_mfma_f32_16x16x32_bf16 v[140:143], a[196:199], a[124:127], v[140:143]\n"
"ds_write_b64 v244, v[232:233] offset:12288\n"
"s_waitcnt lgkmcnt(8)\n"
"v_mfma_f32_16x16x32_bf16 v[144:147], a[192:195], a[128:131], v[144:147]\n"
"v_mul_f32 v236, v28, v236\n"
"v_mul_f32 v237, v29, v237\n"
"v_mfma_f32_16x16x32_bf16 v[144:147], a[196:199], a[132:135], v[144:147]\n"
"v_mfma_f32_16x16x32_bf16 v[232:235], v[250:253], a[32:35], 0\n"
"s_waitcnt lgkmcnt(6)\n"
"v_mfma_f32_16x16x32_bf16 v[148:151], a[192:195], a[136:139], v[148:151]\n"
"ds_read_b128 v[228:231], v247 offset:19456\n"
"v_mul_f32 v238, v30, v238\n"
"v_mfma_f32_16x16x32_bf16 v[148:151], a[196:199], a[140:143], v[148:151]\n"
"v_mul_f32 v239, v31, v239\n"
"s_waitcnt lgkmcnt(4)\n"
"v_mfma_f32_16x16x32_bf16 v[152:155], a[192:195], a[144:147], v[152:155]\n"
"v_cvt_pk_bf16_f32 v236, v236, v237\n"
"v_cvt_pk_bf16_f32 v237, v238, v239\n"
"v_mfma_f32_16x16x32_bf16 v[152:155], a[196:199], a[148:151], v[152:155]\n"
"ds_write_b64 v244, v[236:237] offset:14336\n"
"s_waitcnt lgkmcnt(3)\n"
"v_mfma_f32_16x16x32_bf16 v[156:159], a[192:195], a[152:155], v[156:159]\n"
"global_load_dwordx4 a[192:195], v246, s[16:17] offset:-4096 sc1\n"
"v_mfma_f32_16x16x32_bf16 v[156:159], a[196:199], a[156:159], v[156:159]\n"
"v_mul_f32 v232, v32, v232\n"
"s_waitcnt vmcnt(16)\n"
"v_mfma_f32_16x16x32_bf16 v[160:163], a[200:203], a[96:99], v[160:163]\n"
"global_load_dwordx4 a[196:199], v246, s[16:17] offset:-3072 sc1\n"
"v_mul_f32 v233, v33, v233\n"
"v_mfma_f32_16x16x32_bf16 v[160:163], a[204:207], a[100:103], v[160:163]\n"
"v_mfma_f32_16x16x32_bf16 v[236:239], v[250:253], a[36:39], 0\n"
"v_mfma_f32_16x16x32_bf16 v[164:167], a[200:203], a[104:107], v[164:167]\n"
"v_mul_f32 v234, v34, v234\n"
"v_mfma_f32_16x16x32_bf16 v[164:167], a[204:207], a[108:111], v[164:167]\n"
"v_mul_f32 v235, v35, v235\n"
"v_mfma_f32_16x16x32_bf16 v[168:171], a[200:203], a[112:115], v[168:171]\n"
"v_add_u32 v249, 32896, v248\n"
"v_cvt_pk_bf16_f32 v232, v232, v233\n"
"v_mfma_f32_16x16x32_bf16 v[168:171], a[204:207], a[116:119], v[168:171]\n"
"global_store_dwordx4 v249, v[224:227], s[24:25] sc1\n"
"v_mfma_f32_16x16x32_bf16 v[172:175], a[200:203], a[120:123], v[172:175]\n"
"v_cvt_pk_bf16_f32 v233, v234, v235\n"
"ds_write_b64 v244, v[232:233] offset:8\n"
"v_mfma_f32_16x16x32_bf16 v[172:175], a[204:207], a[124:127], v[172:175]\n"
"s_add_u32 s64, s60, 65536\n"
"s_addc_u32 s65, s61, 0\n"
"v_mfma_f32_16x16x32_bf16 v[176:179], a[200:203], a[128:131], v[176:179]\n"
"v_mul_f32 v236, v36, v236\n"
"v_mfma_f32_16x16x32_bf16 v[176:179], a[204:207], a[132:135], v[176:179]\n"
"v_mul_f32 v237, v37, v237\n"
"v_mfma_f32_16x16x32_bf16 v[180:183], a[200:203], a[136:139], v[180:183]\n"
"v_mfma_f32_16x16x32_bf16 v[232:235], v[250:253], a[40:43], 0\n"
"v_mfma_f32_16x16x32_bf16 v[180:183], a[204:207], a[140:143], v[180:183]\n"
"v_mul_f32 v238, v38, v238\n"
"v_mfma_f32_16x16x32_bf16 v[184:187], a[200:203], a[144:147], v[184:187]\n"
"v_mul_f32 v239, v39, v239\n"
"v_mfma_f32_16x16x32_bf16 v[184:187], a[204:207], a[148:151], v[184:187]\n"
"v_cvt_pk_bf16_f32 v236, v236, v237\n"
"v_mfma_f32_16x16x32_bf16 v[188:191], a[200:203], a[152:155], v[188:191]\n"
"global_load_dwordx4 a[200:203], v246, s[16:17] offset:-2048 sc1\n"
"v_cvt_pk_bf16_f32 v237, v238, v239\n"
"v_mfma_f32_16x16x32_bf16 v[188:191], a[204:207], a[156:159], v[188:191]\n"
"ds_write_b64 v244, v[236:237] offset:2056\n"
"s_waitcnt vmcnt(16)\n"
"v_mfma_f32_16x16x32_bf16 v[192:195], a[208:211], a[96:99], v[192:195]\n"
"global_load_dwordx4 a[204:207], v246, s[16:17] offset:-1024 sc1\n"
"v_mfma_f32_16x16x32_bf16 v[192:195], a[212:215], a[100:103], v[192:195]\n"
"v_mul_f32 v232, v40, v232\n"
"v_mfma_f32_16x16x32_bf16 v[196:199], a[208:211], a[104:107], v[196:199]\n"
"v_mul_f32 v233, v41, v233\n"
"v_mfma_f32_16x16x32_bf16 v[196:199], a[212:215], a[108:111], v[196:199]\n"
"v_mfma_f32_16x16x32_bf16 v[236:239], v[250:253], a[44:47], 0\n"
"v_mfma_f32_16x16x32_bf16 v[200:203], a[208:211], a[112:115], v[200:203]\n"
"v_mul_f32 v234, v42, v234\n"
"v_mfma_f32_16x16x32_bf16 v[200:203], a[212:215], a[116:119], v[200:203]\n"
"v_mul_f32 v235, v43, v235\n"
"v_mfma_f32_16x16x32_bf16 v[204:207], a[208:211], a[120:123], v[204:207]\n"
"v_cvt_pk_bf16_f32 v232, v232, v233\n"
"v_mfma_f32_16x16x32_bf16 v[204:207], a[212:215], a[124:127], v[204:207]\n"
"v_cvt_pk_bf16_f32 v233, v234, v235\n"
"v_mfma_f32_16x16x32_bf16 v[208:211], a[208:211], a[128:131], v[208:211]\n"
"ds_write_b64 v244, v[232:233] offset:4104\n"
"v_mfma_f32_16x16x32_bf16 v[208:211], a[212:215], a[132:135], v[208:211]\n"
"v_mfma_f32_16x16x32_bf16 v[212:215], a[208:211], a[136:139], v[212:215]\n"
"v_mul_f32 v236, v44, v236\n"
"v_mfma_f32_16x16x32_bf16 v[212:215], a[212:215], a[140:143], v[212:215]\n"
"v_mul_f32 v237, v45, v237\n"
"v_mfma_f32_16x16x32_bf16 v[216:219], a[208:211], a[144:147], v[216:219]\n"
"v_mfma_f32_16x16x32_bf16 v[232:235], v[250:253], a[48:51], 0\n"
"v_mfma_f32_16x16x32_bf16 v[216:219], a[212:215], a[148:151], v[216:219]\n"
"v_mul_f32 v238, v46, v238\n"
"v_mfma_f32_16x16x32_bf16 v[220:223], a[208:211], a[152:155], v[220:223]\n"
"global_load_dword a208, v250, s[64:65]\n"
"global_load_dwordx4 a[208:211], v246, s[16:17] offset:0 sc1\n"
"v_mul_f32 v239, v47, v239\n"
"v_mfma_f32_16x16x32_bf16 v[220:223], a[212:215], a[156:159], v[220:223]\n"
"v_cvt_pk_bf16_f32 v236, v236, v237\n"
"s_waitcnt vmcnt(17)\n"
"v_mfma_f32_16x16x32_bf16 a[0:3], a[216:219], a[96:99], a[0:3]\n"
"global_load_dwordx4 a[212:215], v246, s[16:17] offset:1024 sc1\n"
"ds_read_b128 a[96:99], v242 offset:16384\n"
"v_mfma_f32_16x16x32_bf16 a[0:3], a[220:223], a[100:103], a[0:3]\n"
"ds_read_b128 a[100:103], v243 offset:16384\n"
"v_cvt_pk_bf16_f32 v237, v238, v239\n"
"v_mfma_f32_16x16x32_bf16 a[4:7], a[216:219], a[104:107], a[4:7]\n"
"ds_read_b128 a[104:107], v242 offset:18432\n"
"ds_write_b64 v244, v[236:237] offset:6152\n"
"v_mfma_f32_16x16x32_bf16 a[4:7], a[220:223], a[108:111], a[4:7]\n"
"ds_read_b128 a[108:111], v243 offset:18432\n"
"v_mul_f32 v232, v48, v232\n"
"v_mfma_f32_16x16x32_bf16 a[8:11], a[216:219], a[112:115], a[8:11]\n"
"ds_read_b128 a[112:115], v242 offset:20480\n"
"v_mul_f32 v233, v49, v233\n"
"v_mfma_f32_16x16x32_bf16 a[8:11], a[220:223], a[116:119], a[8:11]\n"
"ds_read_b128 a[116:119], v243 offset:20480\n"
"v_mfma_f32_16x16x32_bf16 v[236:239], v[250:253], a[52:55], 0\n"
"v_mfma_f32_16x16x32_bf16 a[12:15], a[216:219], a[120:123], a[12:15]\n"
"ds_read_b128 a[120:123], v242 offset:22528\n"
"v_mul_f32 v234, v50, v234\n"
"v_mfma_f32_16x16x32_bf16 a[12:15], a[220:223], a[124:127], a[12:15]\n"
"ds_read_b128 a[124:127], v243 offset:22528\n"
"v_mul_f32 v235, v51, v235\n"
"v_mfma_f32_16x16x32_bf16 a[16:19], a[216:219], a[128:131], a[16:19]\n"
"ds_read_b128 a[128:131], v242 offset:24576\n"
"v_cvt_pk_bf16_f32 v232, v232, v233\n"
"v_mfma_f32_16x16x32_bf16 a[16:19], a[220:223], a[132:135], a[16:19]\n"
"ds_read_b128 a[132:135], v243 offset:24576\n"
"v_cvt_pk_bf16_f32 v233, v234, v235\n"
"v_mfma_f32_16x16x32_bf16 a[20:23], a[216:219], a[136:139], a[20:23]\n"
"ds_read_b128 a[136:139], v242 offset:26624\n"
"ds_write_b64 v244, v[232:233] offset:8200\n"
"v_mfma_f32_16x16x32_bf16 a[20:23], a[220:223], a[140:143], a[20:23]\n"
"ds_read_b128 a[140:143], v243 offset:26624\n"
"v_mfma_f32_16x16x32_bf16 a[24:27], a[216:219], a[144:147], a[24:27]\n"
"ds_read_b128 a[144:147], v242 offset:28672\n"
"v_mul_f32 v236, v52, v236\n"
"v_mfma_f32_16x16x32_bf16 a[24:27], a[220:223], a[148:151], a[24:27]\n"
"ds_read_b128 a[148:151], v243 offset:28672\n"
"v_mul_f32 v237, v53, v237\n"
"v_mfma_f32_16x16x32_bf16 a[28:31], a[216:219], a[152:155], a[28:31]\n"
"global_load_dwordx4 a[216:219], v246, s[16:17] offset:2048 sc1\n"
"ds_read_b128 a[152:155], v242 offset:30720\n"
"v_mfma_f32_16x16x32_bf16 a[28:31], a[220:223], a[156:159], a[28:31]\n"
"ds_read_b128 a[156:159], v243 offset:30720\n"
"s_waitcnt vmcnt(17) lgkmcnt(15)\n"
"v_mfma_f32_16x16x32_bf16 v[128:131], a[160:163], a[96:99], v[128:131]\n"
"global_load_dwordx4 a[220:223], v246, s[16:17] offset:3072 sc1\n"
"v_mfma_f32_16x16x32_bf16 v[232:235], v[250:253], a[56:59], 0\n"
"v_mfma_f32_16x16x32_bf16 v[128:131], a[164:167], a[100:103], v[128:131]\n"
"v_mul_f32 v238, v54, v238\n"
"s_waitcnt lgkmcnt(13)\n"
"v_mfma_f32_16x16x32_bf16 v[132:135], a[160:163], a[104:107], v[132:135]\n"
"v_add_u32 v246, 0x2000, v246\n"
"v_mul_f32 v239, v55, v239\n"
"v_mfma_f32_16x16x32_bf16 v[132:135], a[164:167], a[108:111], v[132:135]\n"
"v_cvt_pk_bf16_f32 v236, v236, v237\n"
"s_waitcnt lgkmcnt(11)\n"
"v_mfma_f32_16x16x32_bf16 v[136:139], a[160:163], a[112:115], v[136:139]\n"
"v_add_u32 v249, 49280, v248\n"
"v_cvt_pk_bf16_f32 v237, v238, v239\n"
"v_mfma_f32_16x16x32_bf16 v[136:139], a[164:167], a[116:119], v[136:139]\n"
"ds_write_b64 v244, v[236:237] offset:10248\n"
"s_waitcnt lgkmcnt(10)\n"
"v_mfma_f32_16x16x32_bf16 v[140:143], a[160:163], a[120:123], v[140:143]\n"
"global_store_dwordx4 v249, v[228:231], s[24:25] sc1\n"
"v_mfma_f32_16x16x32_bf16 v[140:143], a[164:167], a[124:127], v[140:143]\n"
"v_mul_f32 v232, v56, v232\n"
"s_waitcnt lgkmcnt(8)\n"
"v_mfma_f32_16x16x32_bf16 v[144:147], a[160:163], a[128:131], v[144:147]\n"
"v_mul_f32 v233, v57, v233\n"
"v_mfma_f32_16x16x32_bf16 v[236:239], v[250:253], a[60:63], 0\n"
"v_mfma_f32_16x16x32_bf16 v[144:147], a[164:167], a[132:135], v[144:147]\n"
"v_mul_f32 v234, v58, v234\n"
"s_waitcnt lgkmcnt(5)\n"
"v_mfma_f32_16x16x32_bf16 v[148:151], a[160:163], a[136:139], v[148:151]\n"
"ds_read_b128 v[224:227], v247 offset:49152\n"
"v_mul_f32 v235, v59, v235\n"
"v_mfma_f32_16x16x32_bf16 v[148:151], a[164:167], a[140:143], v[148:151]\n"
"v_cvt_pk_bf16_f32 v232, v232, v233\n"
"s_waitcnt lgkmcnt(4)\n"
"v_mfma_f32_16x16x32_bf16 v[152:155], a[160:163], a[144:147], v[152:155]\n"
"ds_read_b128 v[228:231], v247 offset:50176\n"
"v_cvt_pk_bf16_f32 v233, v234, v235\n"
"v_mfma_f32_16x16x32_bf16 v[152:155], a[164:167], a[148:151], v[152:155]\n"
"ds_write_b64 v244, v[232:233] offset:12296\n"
"s_waitcnt lgkmcnt(4)\n"
"v_mfma_f32_16x16x32_bf16 v[156:159], a[160:163], a[152:155], v[156:159]\n"
"global_load_dwordx4 a[160:163], v246, s[16:17] offset:-4096 sc1\n"
"v_mul_f32 v236, v60, v236\n"
"v_mfma_f32_16x16x32_bf16 v[156:159], a[164:167], a[156:159], v[156:159]\n"
"v_mul_f32 v237, v61, v237\n"
"s_waitcnt vmcnt(17)\n"
"v_mfma_f32_16x16x32_bf16 v[160:163], a[168:171], a[96:99], v[160:163]\n"
"global_load_dwordx4 a[164:167], v246, s[16:17] offset:-3072 sc1\n"
"v_mfma_f32_16x16x32_bf16 v[232:235], v[252:255], a[64:67], 0\n"
"v_mfma_f32_16x16x32_bf16 v[160:163], a[172:175], a[100:103], v[160:163]\n"
"v_mul_f32 v238, v62, v238\n"
"v_mfma_f32_16x16x32_bf16 v[164:167], a[168:171], a[104:107], v[164:167]\n"
"v_mul_f32 v239, v63, v239\n"
"v_mfma_f32_16x16x32_bf16 v[164:167], a[172:175], a[108:111], v[164:167]\n"
"v_cvt_pk_bf16_f32 v236, v236, v237\n"
"v_mfma_f32_16x16x32_bf16 v[168:171], a[168:171], a[112:115], v[168:171]\n"
"v_cvt_pk_bf16_f32 v237, v238, v239\n"
"v_mfma_f32_16x16x32_bf16 v[168:171], a[172:175], a[116:119], v[168:171]\n"
"ds_write_b64 v244, v[236:237] offset:14344\n"
"v_mfma_f32_16x16x32_bf16 v[172:175], a[168:171], a[120:123], v[172:175]\n"
"v_add_u32 v249, 384, v248\n"
"v_mfma_f32_16x16x32_bf16 v[172:175], a[172:175], a[124:127], v[172:175]\n"
"s_waitcnt lgkmcnt(3)\n"
"global_store_dwordx4 v249, v[224:227], s[24:25] sc1\n"
"v_mfma_f32_16x16x32_bf16 v[176:179], a[168:171], a[128:131], v[176:179]\n"
"v_mul_f32 v232, v64, v232\n"
"v_mul_f32 v233, v65, v233\n"
"v_mfma_f32_16x16x32_bf16 v[176:179], a[172:175], a[132:135], v[176:179]\n"
"v_mfma_f32_16x16x32_bf16 v[236:239], v[252:255], a[68:71], 0\n"
"v_mfma_f32_16x16x32_bf16 v[180:183], a[168:171], a[136:139], v[180:183]\n"
"ds_read_b128 v[224:227], v247 offset:51200\n"
"v_mul_f32 v234, v66, v234\n"
"v_mfma_f32_16x16x32_bf16 v[180:183], a[172:175], a[140:143], v[180:183]\n"
"v_mul_f32 v235, v67, v235\n"
"v_mfma_f32_16x16x32_bf16 v[184:187], a[168:171], a[144:147], v[184:187]\n"
"v_cvt_pk_bf16_f32 v232, v232, v233\n"
"v_mfma_f32_16x16x32_bf16 v[184:187], a[172:175], a[148:151], v[184:187]\n"
"v_cvt_pk_bf16_f32 v233, v234, v235\n"
"v_mfma_f32_16x16x32_bf16 v[188:191], a[168:171], a[152:155], v[188:191]\n"
"global_load_dwordx4 a[168:171], v246, s[16:17] offset:-2048 sc1\n"
"ds_write_b64 v245, v[232:233] offset:0\n"
"v_mfma_f32_16x16x32_bf16 v[188:191], a[172:175], a[156:159], v[188:191]\n"
"s_waitcnt vmcnt(18)\n"
"v_mfma_f32_16x16x32_bf16 v[192:195], a[176:179], a[96:99], v[192:195]\n"
"global_load_dwordx4 a[172:175], v246, s[16:17] offset:-1024 sc1\n"
"v_mul_f32 v236, v68, v236\n"
"v_mfma_f32_16x16x32_bf16 v[192:195], a[180:183], a[100:103], v[192:195]\n"
"v_mul_f32 v237, v69, v237\n"
"v_mfma_f32_16x16x32_bf16 v[196:199], a[176:179], a[104:107], v[196:199]\n"
"v_mfma_f32_16x16x32_bf16 v[232:235], v[252:255], a[72:75], 0\n"
"v_mfma_f32_16x16x32_bf16 v[196:199], a[180:183], a[108:111], v[196:199]\n"
"v_mul_f32 v238, v70, v238\n"
"v_mfma_f32_16x16x32_bf16 v[200:203], a[176:179], a[112:115], v[200:203]\n"
"v_mul_f32 v239, v71, v239\n"
"v_mfma_f32_16x16x32_bf16 v[200:203], a[180:183], a[116:119], v[200:203]\n"
"v_cvt_pk_bf16_f32 v236, v236, v237\n"
"v_mfma_f32_16x16x32_bf16 v[204:207], a[176:179], a[120:123], v[204:207]\n"
"v_cvt_pk_bf16_f32 v237, v238, v239\n"
"v_mfma_f32_16x16x32_bf16 v[204:207], a[180:183], a[124:127], v[204:207]\n"
"ds_write_b64 v245, v[236:237] offset:2048\n"
"v_mfma_f32_16x16x32_bf16 v[208:211], a[176:179], a[128:131], v[208:211]\n"
"v_mfma_f32_16x16x32_bf16 v[208:211], a[180:183], a[132:135], v[208:211]\n"
"v_mul_f32 v232, v72, v232\n"
"v_mfma_f32_16x16x32_bf16 v[212:215], a[176:179], a[136:139], v[212:215]\n"
"v_mul_f32 v233, v73, v233\n"
"v_mfma_f32_16x16x32_bf16 v[212:215], a[180:183], a[140:143], v[212:215]\n"
"v_mfma_f32_16x16x32_bf16 v[236:239], v[252:255], a[76:79], 0\n"
"v_mfma_f32_16x16x32_bf16 v[216:219], a[176:179], a[144:147], v[216:219]\n"
"v_mul_f32 v234, v74, v234\n"
"v_mfma_f32_16x16x32_bf16 v[216:219], a[180:183], a[148:151], v[216:219]\n"
"v_mul_f32 v235, v75, v235\n"
"v_mfma_f32_16x16x32_bf16 v[220:223], a[176:179], a[152:155], v[220:223]\n"
"global_load_dwordx4 a[176:179], v246, s[16:17] offset:0 sc1\n"
"v_cvt_pk_bf16_f32 v232, v232, v233\n"
"v_mfma_f32_16x16x32_bf16 v[220:223], a[180:183], a[156:159], v[220:223]\n"
"v_cvt_pk_bf16_f32 v233, v234, v235\n"
"s_waitcnt vmcnt(18)\n"
"v_mfma_f32_16x16x32_bf16 a[0:3], a[184:187], a[96:99], a[0:3]\n"
"global_load_dwordx4 a[180:183], v246, s[16:17] offset:1024 sc1\n"
"ds_read_b128 a[96:99], v242 offset:49152\n"
"v_mfma_f32_16x16x32_bf16 a[0:3], a[188:191], a[100:103], a[0:3]\n"
"ds_read_b128 a[100:103], v243 offset:49152\n"
"ds_write_b64 v245, v[232:233] offset:4096\n"
"v_mfma_f32_16x16x32_bf16 a[4:7], a[184:187], a[104:107], a[4:7]\n"
"ds_read_b128 a[104:107], v242 offset:51200\n"
"v_mul_f32 v236, v76, v236\n"
"v_mfma_f32_16x16x32_bf16 a[4:7], a[188:191], a[108:111], a[4:7]\n"
"ds_read_b128 a[108:111], v243 offset:51200\n"
"v_mul_f32 v237, v77, v237\n"
"v_mfma_f32_16x16x32_bf16 a[8:11], a[184:187], a[112:115], a[8:11]\n"
"ds_read_b128 a[112:115], v242 offset:53248\n"
"v_mfma_f32_16x16x32_bf16 v[232:235], v[252:255], a[80:83], 0\n"
"v_mfma_f32_16x16x32_bf16 a[8:11], a[188:191], a[116:119], a[8:11]\n"
"ds_read_b128 a[116:119], v243 offset:53248\n"
"v_mul_f32 v238, v78, v238\n"
"v_mfma_f32_16x16x32_bf16 a[12:15], a[184:187], a[120:123], a[12:15]\n"
"ds_read_b128 a[120:123], v242 offset:55296\n"
"v_mul_f32 v239, v79, v239\n"
"v_mfma_f32_16x16x32_bf16 a[12:15], a[188:191], a[124:127], a[12:15]\n"
"ds_read_b128 a[124:127], v243 offset:55296\n"
"v_cvt_pk_bf16_f32 v236, v236, v237\n"
"v_mfma_f32_16x16x32_bf16 a[16:19], a[184:187], a[128:131], a[16:19]\n"
"ds_read_b128 a[128:131], v242 offset:57344\n"
"v_cvt_pk_bf16_f32 v237, v238, v239\n"
"v_mfma_f32_16x16x32_bf16 a[16:19], a[188:191], a[132:135], a[16:19]\n"
"ds_read_b128 a[132:135], v243 offset:57344\n"
"ds_write_b64 v245, v[236:237] offset:6144\n"
"v_mfma_f32_16x16x32_bf16 a[20:23], a[184:187], a[136:139], a[20:23]\n"
"ds_read_b128 a[136:139], v242 offset:59392\n"
"v_mfma_f32_16x16x32_bf16 a[20:23], a[188:191], a[140:143], a[20:23]\n"
"ds_read_b128 a[140:143], v243 offset:59392\n"
"v_mul_f32 v232, v80, v232\n"
"v_mfma_f32_16x16x32_bf16 a[24:27], a[184:187], a[144:147], a[24:27]\n"
"ds_read_b128 a[144:147], v242 offset:61440\n"
"v_mul_f32 v233, v81, v233\n"
"v_mfma_f32_16x16x32_bf16 a[24:27], a[188:191], a[148:151], a[24:27]\n"
"ds_read_b128 a[148:151], v243 offset:61440\n"
"v_mfma_f32_16x16x32_bf16 v[236:239], v[252:255], a[84:87], 0\n"
"v_mfma_f32_16x16x32_bf16 a[28:31], a[184:187], a[152:155], a[28:31]\n"
"global_load_dwordx4 a[184:187], v246, s[16:17] offset:2048 sc1\n"
"ds_read_b128 a[152:155], v242 offset:63488\n"
"v_mfma_f32_16x16x32_bf16 a[28:31], a[188:191], a[156:159], a[28:31]\n"
"ds_read_b128 a[156:159], v243 offset:63488\n"
"s_waitcnt vmcnt(17) lgkmcnt(15)\n"
"v_mfma_f32_16x16x32_bf16 v[128:131], a[192:195], a[96:99], v[128:131]\n"
"global_load_dwordx4 a[188:191], v246, s[16:17] offset:3072 sc1\n"
"v_mul_f32 v234, v82, v234\n"
"v_mfma_f32_16x16x32_bf16 v[128:131], a[196:199], a[100:103], v[128:131]\n"
"v_mul_f32 v235, v83, v235\n"
"s_waitcnt lgkmcnt(13)\n"
"v_mfma_f32_16x16x32_bf16 v[132:135], a[192:195], a[104:107], v[132:135]\n"
"v_add_u32 v246, 0x2000, v246\n"
"v_cvt_pk_bf16_f32 v232, v232, v233\n"
"v_mfma_f32_16x16x32_bf16 v[132:135], a[196:199], a[108:111], v[132:135]\n"
"v_cvt_pk_bf16_f32 v233, v234, v235\n"
"s_waitcnt lgkmcnt(11)\n"
"v_mfma_f32_16x16x32_bf16 v[136:139], a[192:195], a[112:115], v[136:139]\n"
"v_add_u32 v249, 16768, v248\n"
"ds_write_b64 v245, v[232:233] offset:8192\n"
"v_mfma_f32_16x16x32_bf16 v[136:139], a[196:199], a[116:119], v[136:139]\n"
"v_mul_f32 v236, v84, v236\n"
"s_waitcnt lgkmcnt(10)\n"
"v_mfma_f32_16x16x32_bf16 v[140:143], a[192:195], a[120:123], v[140:143]\n"
"global_store_dwordx4 v249, v[228:231], s[24:25] sc1\n"
"v_mfma_f32_16x16x32_bf16 v[140:143], a[196:199], a[124:127], v[140:143]\n"
"v_mul_f32 v237, v85, v237\n"
"s_waitcnt lgkmcnt(8)\n"
"v_mfma_f32_16x16x32_bf16 v[144:147], a[192:195], a[128:131], v[144:147]\n"
"v_mfma_f32_16x16x32_bf16 v[232:235], v[252:255], a[88:91], 0\n"
"v_mul_f32 v238, v86, v238\n"
"v_mfma_f32_16x16x32_bf16 v[144:147], a[196:199], a[132:135], v[144:147]\n"
"v_mul_f32 v239, v87, v239\n"
"s_waitcnt lgkmcnt(5)\n"
"v_mfma_f32_16x16x32_bf16 v[148:151], a[192:195], a[136:139], v[148:151]\n"
"ds_read_b128 v[228:231], v247 offset:52224\n"
"v_cvt_pk_bf16_f32 v236, v236, v237\n"
"v_mfma_f32_16x16x32_bf16 v[148:151], a[196:199], a[140:143], v[148:151]\n"
"v_cvt_pk_bf16_f32 v237, v238, v239\n"
"s_waitcnt lgkmcnt(4)\n"
"v_mfma_f32_16x16x32_bf16 v[152:155], a[192:195], a[144:147], v[152:155]\n"
"ds_write_b64 v245, v[236:237] offset:10240\n"
"v_mfma_f32_16x16x32_bf16 v[152:155], a[196:199], a[148:151], v[152:155]\n"
"v_mul_f32 v232, v88, v232\n"
"s_waitcnt lgkmcnt(3)\n"
"v_mfma_f32_16x16x32_bf16 v[156:159], a[192:195], a[152:155], v[156:159]\n"
"global_load_dwordx4 a[192:195], v246, s[16:17] offset:-4096 sc1\n"
"v_mul_f32 v233, v89, v233\n"
"v_mfma_f32_16x16x32_bf16 v[156:159], a[196:199], a[156:159], v[156:159]\n"
"v_mfma_f32_16x16x32_bf16 v[236:239], v[252:255], a[92:95], 0\n"
"s_waitcnt vmcnt(17)\n"
"v_mfma_f32_16x16x32_bf16 v[160:163], a[200:203], a[96:99], v[160:163]\n"
"global_load_dwordx4 a[196:199], v246, s[16:17] offset:-3072 sc1\n"
"v_mul_f32 v234, v90, v234\n"
"v_mfma_f32_16x16x32_bf16 v[160:163], a[204:207], a[100:103], v[160:163]\n"
"v_mul_f32 v235, v91, v235\n"
"v_mfma_f32_16x16x32_bf16 v[164:167], a[200:203], a[104:107], v[164:167]\n"
"v_cvt_pk_bf16_f32 v232, v232, v233\n"
"v_mfma_f32_16x16x32_bf16 v[164:167], a[204:207], a[108:111], v[164:167]\n"
"v_cvt_pk_bf16_f32 v233, v234, v235\n"
"v_mfma_f32_16x16x32_bf16 v[168:171], a[200:203], a[112:115], v[168:171]\n"
"ds_write_b64 v245, v[232:233] offset:12288\n"
"v_mfma_f32_16x16x32_bf16 v[168:171], a[204:207], a[116:119], v[168:171]\n"
"v_mfma_f32_16x16x32_bf16 v[172:175], a[200:203], a[120:123], v[172:175]\n"
"v_mul_f32 v236, v92, v236\n"
"v_mfma_f32_16x16x32_bf16 v[172:175], a[204:207], a[124:127], v[172:175]\n"
"v_mul_f32 v237, v93, v237\n"
"v_mfma_f32_16x16x32_bf16 v[176:179], a[200:203], a[128:131], v[176:179]\n"
"v_add_u32 v249, 33152, v248\n"
"v_mfma_f32_16x16x32_bf16 v[232:235], v[250:253], a[64:67], 0\n"
"v_mfma_f32_16x16x32_bf16 v[176:179], a[204:207], a[132:135], v[176:179]\n"
"global_store_dwordx4 v249, v[224:227], s[24:25] sc1\n"
"v_mfma_f32_16x16x32_bf16 v[180:183], a[200:203], a[136:139], v[180:183]\n"
"v_mul_f32 v238, v94, v238\n"
"v_mul_f32 v239, v95, v239\n"
"v_mfma_f32_16x16x32_bf16 v[180:183], a[204:207], a[140:143], v[180:183]\n"
"v_cvt_pk_bf16_f32 v236, v236, v237\n"
"v_mfma_f32_16x16x32_bf16 v[184:187], a[200:203], a[144:147], v[184:187]\n"
"v_cvt_pk_bf16_f32 v237, v238, v239\n"
"v_mfma_f32_16x16x32_bf16 v[184:187], a[204:207], a[148:151], v[184:187]\n"
"ds_write_b64 v245, v[236:237] offset:14336\n"
"v_mfma_f32_16x16x32_bf16 v[188:191], a[200:203], a[152:155], v[188:191]\n"
"global_load_dwordx4 a[200:203], v246, s[16:17] offset:-2048 sc1\n"
"v_mfma_f32_16x16x32_bf16 v[188:191], a[204:207], a[156:159], v[188:191]\n"
"v_mul_f32 v232, v96, v232\n"
"s_waitcnt vmcnt(17)\n"
"v_mfma_f32_16x16x32_bf16 v[192:195], a[208:211], a[96:99], v[192:195]\n"
"global_load_dwordx4 a[204:207], v246, s[16:17] offset:-1024 sc1\n"
"v_mul_f32 v233, v97, v233\n"
"v_mfma_f32_16x16x32_bf16 v[192:195], a[212:215], a[100:103], v[192:195]\n"
"v_mfma_f32_16x16x32_bf16 v[236:239], v[250:253], a[68:71], 0\n"
"v_mfma_f32_16x16x32_bf16 v[196:199], a[208:211], a[104:107], v[196:199]\n"
"v_mul_f32 v234, v98, v234\n"
"v_mfma_f32_16x16x32_bf16 v[196:199], a[212:215], a[108:111], v[196:199]\n"
"v_mul_f32 v235, v99, v235\n"
"v_mfma_f32_16x16x32_bf16 v[200:203], a[208:211], a[112:115], v[200:203]\n"
"v_cvt_pk_bf16_f32 v232, v232, v233\n"
"v_mfma_f32_16x16x32_bf16 v[200:203], a[212:215], a[116:119], v[200:203]\n"
"v_cvt_pk_bf16_f32 v233, v234, v235\n"
"v_mfma_f32_16x16x32_bf16 v[204:207], a[208:211], a[120:123], v[204:207]\n"
"ds_write_b64 v245, v[232:233] offset:8\n"
"v_mfma_f32_16x16x32_bf16 v[204:207], a[212:215], a[124:127], v[204:207]\n"
"v_mfma_f32_16x16x32_bf16 v[208:211], a[208:211], a[128:131], v[208:211]\n"
"v_mul_f32 v236, v100, v236\n"
"v_mfma_f32_16x16x32_bf16 v[208:211], a[212:215], a[132:135], v[208:211]\n"
"v_mul_f32 v237, v101, v237\n"
"v_mfma_f32_16x16x32_bf16 v[212:215], a[208:211], a[136:139], v[212:215]\n"
"v_mfma_f32_16x16x32_bf16 v[232:235], v[250:253], a[72:75], 0\n"
"v_mfma_f32_16x16x32_bf16 v[212:215], a[212:215], a[140:143], v[212:215]\n"
"v_mul_f32 v238, v102, v238\n"
"v_mfma_f32_16x16x32_bf16 v[216:219], a[208:211], a[144:147], v[216:219]\n"
"v_mul_f32 v239, v103, v239\n"
"v_mfma_f32_16x16x32_bf16 v[216:219], a[212:215], a[148:151], v[216:219]\n"
"v_cvt_pk_bf16_f32 v236, v236, v237\n"
"v_mfma_f32_16x16x32_bf16 v[220:223], a[208:211], a[152:155], v[220:223]\n"
"global_load_dwordx4 a[208:211], v246, s[16:17] offset:0 sc1\n"
"v_cvt_pk_bf16_f32 v237, v238, v239\n"
"v_mfma_f32_16x16x32_bf16 v[220:223], a[212:215], a[156:159], v[220:223]\n"
"ds_write_b64 v245, v[236:237] offset:2056\n"
"s_waitcnt vmcnt(17)\n"
"v_mfma_f32_16x16x32_bf16 a[0:3], a[216:219], a[96:99], a[0:3]\n"
"global_load_dwordx4 a[212:215], v246, s[16:17] offset:1024 sc1\n"
"ds_read_b128 a[96:99], v240 offset:16384\n"
"v_mfma_f32_16x16x32_bf16 a[0:3], a[220:223], a[100:103], a[0:3]\n"
"ds_read_b128 a[100:103], v241 offset:16384\n"
"v_mul_f32 v232, v104, v232\n"
"v_mfma_f32_16x16x32_bf16 a[4:7], a[216:219], a[104:107], a[4:7]\n"
"ds_read_b128 a[104:107], v240 offset:18432\n"
"v_mul_f32 v233, v105, v233\n"
"v_mfma_f32_16x16x32_bf16 a[4:7], a[220:223], a[108:111], a[4:7]\n"
"ds_read_b128 a[108:111], v241 offset:18432\n"
"v_mfma_f32_16x16x32_bf16 v[236:239], v[250:253], a[76:79], 0\n"
"v_mfma_f32_16x16x32_bf16 a[8:11], a[216:219], a[112:115], a[8:11]\n"
"ds_read_b128 a[112:115], v240 offset:20480\n"
"v_mul_f32 v234, v106, v234\n"
"v_mfma_f32_16x16x32_bf16 a[8:11], a[220:223], a[116:119], a[8:11]\n"
"ds_read_b128 a[116:119], v241 offset:20480\n"
"v_mul_f32 v235, v107, v235\n"
"v_mfma_f32_16x16x32_bf16 a[12:15], a[216:219], a[120:123], a[12:15]\n"
"ds_read_b128 a[120:123], v240 offset:22528\n"
"v_cvt_pk_bf16_f32 v232, v232, v233\n"
"v_mfma_f32_16x16x32_bf16 a[12:15], a[220:223], a[124:127], a[12:15]\n"
"ds_read_b128 a[124:127], v241 offset:22528\n"
"v_cvt_pk_bf16_f32 v233, v234, v235\n"
"v_mfma_f32_16x16x32_bf16 a[16:19], a[216:219], a[128:131], a[16:19]\n"
"ds_read_b128 a[128:131], v240 offset:24576\n"
"ds_write_b64 v245, v[232:233] offset:4104\n"
"v_mfma_f32_16x16x32_bf16 a[16:19], a[220:223], a[132:135], a[16:19]\n"
"ds_read_b128 a[132:135], v241 offset:24576\n"
"v_mfma_f32_16x16x32_bf16 a[20:23], a[216:219], a[136:139], a[20:23]\n"
"ds_read_b128 a[136:139], v240 offset:26624\n"
"v_mul_f32 v236, v108, v236\n"
"v_mfma_f32_16x16x32_bf16 a[20:23], a[220:223], a[140:143], a[20:23]\n"
"ds_read_b128 a[140:143], v241 offset:26624\n"
"v_mul_f32 v237, v109, v237\n"
"v_mfma_f32_16x16x32_bf16 a[24:27], a[216:219], a[144:147], a[24:27]\n"
"ds_read_b128 a[144:147], v240 offset:28672\n"
"v_mfma_f32_16x16x32_bf16 v[232:235], v[250:253], a[80:83], 0\n"
"v_mfma_f32_16x16x32_bf16 a[24:27], a[220:223], a[148:151], a[24:27]\n"
"ds_read_b128 a[148:151], v241 offset:28672\n"
"v_mul_f32 v238, v110, v238\n"
"v_mfma_f32_16x16x32_bf16 a[28:31], a[216:219], a[152:155], a[28:31]\n"
"global_load_dwordx4 a[216:219], v246, s[16:17] offset:2048 sc1\n"
"ds_read_b128 a[152:155], v240 offset:30720\n"
"v_mfma_f32_16x16x32_bf16 a[28:31], a[220:223], a[156:159], a[28:31]\n"
"ds_read_b128 a[156:159], v241 offset:30720\n"
"s_waitcnt vmcnt(16) lgkmcnt(15)\n"
"v_mfma_f32_16x16x32_bf16 v[0:3], a[160:163], a[96:99], v[0:3]\n"
"global_load_dwordx4 a[220:223], v246, s[16:17] offset:3072 sc1\n"
"v_mul_f32 v239, v111, v239\n"
"v_mfma_f32_16x16x32_bf16 v[0:3], a[164:167], a[100:103], v[0:3]\n"
"v_cvt_pk_bf16_f32 v236, v236, v237\n"
"s_waitcnt lgkmcnt(13)\n"
"v_mfma_f32_16x16x32_bf16 v[4:7], a[160:163], a[104:107], v[4:7]\n"
"v_add_u32 v246, 0x2000, v246\n"
"v_cvt_pk_bf16_f32 v237, v238, v239\n"
"v_mfma_f32_16x16x32_bf16 v[4:7], a[164:167], a[108:111], v[4:7]\n"
"ds_write_b64 v245, v[236:237] offset:6152\n"
"s_waitcnt lgkmcnt(12)\n"
"v_mfma_f32_16x16x32_bf16 v[8:11], a[160:163], a[112:115], v[8:11]\n"
"v_add_u32 v249, 49536, v248\n"
"v_mul_f32 v232, v112, v232\n"
"v_mfma_f32_16x16x32_bf16 v[8:11], a[164:167], a[116:119], v[8:11]\n"
"v_mul_f32 v233, v113, v233\n"
"s_waitcnt lgkmcnt(10)\n"
"v_mfma_f32_16x16x32_bf16 v[12:15], a[160:163], a[120:123], v[12:15]\n"
"global_store_dwordx4 v249, v[228:231], s[24:25] sc1\n"
"v_mfma_f32_16x16x32_bf16 v[12:15], a[164:167], a[124:127], v[12:15]\n"
"v_mfma_f32_16x16x32_bf16 v[236:239], v[250:253], a[84:87], 0\n"
"s_waitcnt lgkmcnt(7)\n"
"v_mfma_f32_16x16x32_bf16 v[16:19], a[160:163], a[128:131], v[16:19]\n"
"v_xor_b32 v247, 0x10000, v247\n"
"v_mul_f32 v234, v114, v234\n"
"v_mfma_f32_16x16x32_bf16 v[16:19], a[164:167], a[132:135], v[16:19]\n"
"v_mul_f32 v235, v115, v235\n"
"s_waitcnt lgkmcnt(5)\n"
"v_mfma_f32_16x16x32_bf16 v[20:23], a[160:163], a[136:139], v[20:23]\n"
"v_cvt_pk_bf16_f32 v232, v232, v233\n"
"v_cvt_pk_bf16_f32 v233, v234, v235\n"
"v_mfma_f32_16x16x32_bf16 v[20:23], a[164:167], a[140:143], v[20:23]\n"
"ds_write_b64 v245, v[232:233] offset:8200\n"
"s_waitcnt lgkmcnt(4)\n"
"v_mfma_f32_16x16x32_bf16 v[24:27], a[160:163], a[144:147], v[24:27]\n"
"v_mul_f32 v236, v116, v236\n"
"v_mfma_f32_16x16x32_bf16 v[24:27], a[164:167], a[148:151], v[24:27]\n"
"v_mul_f32 v237, v117, v237\n"
"s_waitcnt lgkmcnt(2)\n"
"v_mfma_f32_16x16x32_bf16 v[28:31], a[160:163], a[152:155], v[28:31]\n"
"global_load_dwordx4 a[160:163], v246, s[16:17] offset:-4096 sc1\n"
"v_mfma_f32_16x16x32_bf16 v[232:235], v[250:253], a[88:91], 0\n"
"v_mfma_f32_16x16x32_bf16 v[28:31], a[164:167], a[156:159], v[28:31]\n"
"v_mul_f32 v238, v118, v238\n"
"s_waitcnt vmcnt(16)\n"
"v_mfma_f32_16x16x32_bf16 v[32:35], a[168:171], a[96:99], v[32:35]\n"
"global_load_dwordx4 a[164:167], v246, s[16:17] offset:-3072 sc1\n"
"v_mul_f32 v239, v119, v239\n"
"v_mfma_f32_16x16x32_bf16 v[32:35], a[172:175], a[100:103], v[32:35]\n"
"ds_read_b128 v[224:227], v247 offset:16384\n"
"v_cvt_pk_bf16_f32 v236, v236, v237\n"
"v_mfma_f32_16x16x32_bf16 v[36:39], a[168:171], a[104:107], v[36:39]\n"
"v_cvt_pk_bf16_f32 v237, v238, v239\n"
"v_mfma_f32_16x16x32_bf16 v[36:39], a[172:175], a[108:111], v[36:39]\n"
"ds_read_b128 v[228:231], v247 offset:17408\n"
"ds_write_b64 v245, v[236:237] offset:10248\n"
"v_mfma_f32_16x16x32_bf16 v[40:43], a[168:171], a[112:115], v[40:43]\n"
"v_mfma_f32_16x16x32_bf16 v[40:43], a[172:175], a[116:119], v[40:43]\n"
"v_mul_f32 v232, v120, v232\n"
"v_mfma_f32_16x16x32_bf16 v[44:47], a[168:171], a[120:123], v[44:47]\n"
"v_mul_f32 v233, v121, v233\n"
"v_mfma_f32_16x16x32_bf16 v[44:47], a[172:175], a[124:127], v[44:47]\n"
"v_mfma_f32_16x16x32_bf16 v[236:239], v[250:253], a[92:95], 0\n"
"v_mfma_f32_16x16x32_bf16 v[48:51], a[168:171], a[128:131], v[48:51]\n"
"v_mul_f32 v234, v122, v234\n"
"v_mfma_f32_16x16x32_bf16 v[48:51], a[172:175], a[132:135], v[48:51]\n"
"v_mul_f32 v235, v123, v235\n"
"v_mfma_f32_16x16x32_bf16 v[52:55], a[168:171], a[136:139], v[52:55]\n"
"v_add_u32 v249, 640, v248\n"
"v_cvt_pk_bf16_f32 v232, v232, v233\n"
"v_mfma_f32_16x16x32_bf16 v[52:55], a[172:175], a[140:143], v[52:55]\n"
"s_waitcnt lgkmcnt(2)\n"
"global_store_dwordx4 v249, v[224:227], s[24:25] sc1\n"
"v_mfma_f32_16x16x32_bf16 v[56:59], a[168:171], a[144:147], v[56:59]\n"
"v_cvt_pk_bf16_f32 v233, v234, v235\n"
"ds_write_b64 v245, v[232:233] offset:12296\n"
"v_mfma_f32_16x16x32_bf16 v[56:59], a[172:175], a[148:151], v[56:59]\n"
"v_mfma_f32_16x16x32_bf16 v[60:63], a[168:171], a[152:155], v[60:63]\n"
"global_load_dwordx4 a[168:171], v246, s[16:17] offset:-2048 sc1\n"
"v_mul_f32 v236, v124, v236\n"
"s_nop 0\n"
".Lpg16_%=:\n"
"s_cbranch_execz .Lpg20_%=\n"
"v_mfma_f32_16x16x32_bf16 v[60:63], a[172:175], a[156:159], v[60:63]\n"
"v_mul_f32 v237, v125, v237\n"
"s_waitcnt vmcnt(17)\n"
"v_mfma_f32_16x16x32_bf16 v[64:67], a[176:179], a[96:99], v[64:67]\n"
"global_load_dwordx4 a[172:175], v246, s[16:17] offset:-1024 sc1\n"
"v_mul_f32 v238, v126, v238\n"
"v_mfma_f32_16x16x32_bf16 v[64:67], a[180:183], a[100:103], v[64:67]\n"
"ds_read_b128 v[224:227], v247 offset:18432\n"
"v_mul_f32 v239, v127, v239\n"
"v_mfma_f32_16x16x32_bf16 v[68:71], a[176:179], a[104:107], v[68:71]\n"
"v_mfma_f32_16x16x32_bf16 v[68:71], a[180:183], a[108:111], v[68:71]\n"
"v_cvt_pk_bf16_f32 v236, v236, v237\n"
"v_mfma_f32_16x16x32_bf16 v[72:75], a[176:179], a[112:115], v[72:75]\n"
"v_cvt_pk_bf16_f32 v237, v238, v239\n"
"v_mfma_f32_16x16x32_bf16 v[72:75], a[180:183], a[116:119], v[72:75]\n"
"ds_write_b64 v245, v[236:237] offset:14344\n"
"v_mfma_f32_16x16x32_bf16 v[76:79], a[176:179], a[120:123], v[76:79]\n"
"v_mfma_f32_16x16x32_bf16 v[76:79], a[180:183], a[124:127], v[76:79]\n"
"v_mfma_f32_16x16x32_bf16 v[80:83], a[176:179], a[128:131], v[80:83]\n"
"v_mfma_f32_16x16x32_bf16 v[80:83], a[180:183], a[132:135], v[80:83]\n"
"v_mfma_f32_16x16x32_bf16 v[84:87], a[176:179], a[136:139], v[84:87]\n"
"v_mfma_f32_16x16x32_bf16 v[84:87], a[180:183], a[140:143], v[84:87]\n"
"v_mfma_f32_16x16x32_bf16 v[88:91], a[176:179], a[144:147], v[88:91]\n"
"v_mfma_f32_16x16x32_bf16 v[88:91], a[180:183], a[148:151], v[88:91]\n"
"v_mfma_f32_16x16x32_bf16 v[92:95], a[176:179], a[152:155], v[92:95]\n"
"global_load_dwordx4 a[176:179], v246, s[16:17] offset:0 sc1\n"
"v_mfma_f32_16x16x32_bf16 v[92:95], a[180:183], a[156:159], v[92:95]\n"
"s_waitcnt vmcnt(17)\n"
"v_mfma_f32_16x16x32_bf16 v[96:99], a[184:187], a[96:99], v[96:99]\n"
"global_load_dwordx4 a[180:183], v246, s[16:17] offset:1024 sc1\n"
"ds_read_b128 a[96:99], v240 offset:49152\n"
"v_mfma_f32_16x16x32_bf16 v[96:99], a[188:191], a[100:103], v[96:99]\n"
"ds_read_b128 a[100:103], v241 offset:49152\n"
"v_mfma_f32_16x16x32_bf16 v[100:103], a[184:187], a[104:107], v[100:103]\n"
"ds_read_b128 a[104:107], v240 offset:51200\n"
"v_mfma_f32_16x16x32_bf16 v[100:103], a[188:191], a[108:111], v[100:103]\n"
"ds_read_b128 a[108:111], v241 offset:51200\n"
"v_mfma_f32_16x16x32_bf16 v[104:107], a[184:187], a[112:115], v[104:107]\n"
"ds_read_b128 a[112:115], v240 offset:53248\n"
"v_mfma_f32_16x16x32_bf16 v[104:107], a[188:191], a[116:119], v[104:107]\n"
"ds_read_b128 a[116:119], v241 offset:53248\n"
"v_mfma_f32_16x16x32_bf16 v[108:111], a[184:187], a[120:123], v[108:111]\n"
"ds_read_b128 a[120:123], v240 offset:55296\n"
"v_mfma_f32_16x16x32_bf16 v[108:111], a[188:191], a[124:127], v[108:111]\n"
"ds_read_b128 a[124:127], v241 offset:55296\n"
"v_mfma_f32_16x16x32_bf16 v[112:115], a[184:187], a[128:131], v[112:115]\n"
"ds_read_b128 a[128:131], v240 offset:57344\n"
"v_mfma_f32_16x16x32_bf16 v[112:115], a[188:191], a[132:135], v[112:115]\n"
"ds_read_b128 a[132:135], v241 offset:57344\n"
"v_mfma_f32_16x16x32_bf16 v[116:119], a[184:187], a[136:139], v[116:119]\n"
"ds_read_b128 a[136:139], v240 offset:59392\n"
"v_mfma_f32_16x16x32_bf16 v[116:119], a[188:191], a[140:143], v[116:119]\n"
"ds_read_b128 a[140:143], v241 offset:59392\n"
"v_mfma_f32_16x16x32_bf16 v[120:123], a[184:187], a[144:147], v[120:123]\n"
"ds_read_b128 a[144:147], v240 offset:61440\n"
"v_mfma_f32_16x16x32_bf16 v[120:123], a[188:191], a[148:151], v[120:123]\n"
"ds_read_b128 a[148:151], v241 offset:61440\n"
"v_mfma_f32_16x16x32_bf16 v[124:127], a[184:187], a[152:155], v[124:127]\n"
"global_load_dwordx4 a[184:187], v246, s[16:17] offset:2048 sc1\n"
"ds_read_b128 a[152:155], v240 offset:63488\n"
"v_mfma_f32_16x16x32_bf16 v[124:127], a[188:191], a[156:159], v[124:127]\n"
"ds_read_b128 a[156:159], v241 offset:63488\n"
"s_waitcnt vmcnt(16) lgkmcnt(14)\n"
"v_mfma_f32_16x16x32_bf16 v[0:3], a[192:195], a[96:99], v[0:3]\n"
"global_load_dwordx4 a[188:191], v246, s[16:17] offset:3072 sc1\n"
"v_mfma_f32_16x16x32_bf16 v[0:3], a[196:199], a[100:103], v[0:3]\n"
"s_waitcnt lgkmcnt(12)\n"
"v_mfma_f32_16x16x32_bf16 v[4:7], a[192:195], a[104:107], v[4:7]\n"
"v_add_u32 v246, 0x2000, v246\n"
"v_mfma_f32_16x16x32_bf16 v[4:7], a[196:199], a[108:111], v[4:7]\n"
"s_waitcnt lgkmcnt(10)\n"
"v_mfma_f32_16x16x32_bf16 v[8:11], a[192:195], a[112:115], v[8:11]\n"
"v_add_u32 v249, 17024, v248\n"
"v_mfma_f32_16x16x32_bf16 v[8:11], a[196:199], a[116:119], v[8:11]\n"
"s_waitcnt lgkmcnt(8)\n"
"v_mfma_f32_16x16x32_bf16 v[12:15], a[192:195], a[120:123], v[12:15]\n"
"global_store_dwordx4 v249, v[228:231], s[24:25] sc1\n"
"v_mfma_f32_16x16x32_bf16 v[12:15], a[196:199], a[124:127], v[12:15]\n"
"s_waitcnt lgkmcnt(6)\n"
"v_mfma_f32_16x16x32_bf16 v[16:19], a[192:195], a[128:131], v[16:19]\n"
"s_add_u32 s64, s60, 69632\n"
"s_addc_u32 s65, s61, 0\n"
"v_mfma_f32_16x16x32_bf16 v[16:19], a[196:199], a[132:135], v[16:19]\n"
"s_waitcnt lgkmcnt(4)\n"
"v_mfma_f32_16x16x32_bf16 v[20:23], a[192:195], a[136:139], v[20:23]\n"
"ds_read_b128 v[228:231], v247 offset:19456\n"
"v_mfma_f32_16x16x32_bf16 v[20:23], a[196:199], a[140:143], v[20:23]\n"
"s_waitcnt lgkmcnt(3)\n"
"v_mfma_f32_16x16x32_bf16 v[24:27], a[192:195], a[144:147], v[24:27]\n"
"s_add_u32 s18, s18, 2048\n"
"s_addc_u32 s19, s19, 0\n"
"v_mfma_f32_16x16x32_bf16 v[24:27], a[196:199], a[148:151], v[24:27]\n"
"v_lshrrev_b32 v237, 4, v247\n"
"s_waitcnt lgkmcnt(1)\n"
"v_mfma_f32_16x16x32_bf16 v[28:31], a[192:195], a[152:155], v[28:31]\n"
"global_load_dwordx4 a[192:195], v246, s[16:17] offset:-4096 sc1\n"
"v_and_b32 v237, 0x30, v237\n"
"v_mfma_f32_16x16x32_bf16 v[28:31], a[196:199], a[156:159], v[28:31]\n"
"global_load_dwordx2 v[236:237], v237, s[18:19] offset:128\n"
"s_waitcnt vmcnt(17)\n"
"v_mfma_f32_16x16x32_bf16 v[32:35], a[200:203], a[96:99], v[32:35]\n"
"global_load_dwordx4 a[196:199], v246, s[16:17] offset:-3072 sc1\n"
"v_mfma_f32_16x16x32_bf16 v[32:35], a[204:207], a[100:103], v[32:35]\n"
"v_mfma_f32_16x16x32_bf16 v[36:39], a[200:203], a[104:107], v[36:39]\n"
"v_mfma_f32_16x16x32_bf16 v[36:39], a[204:207], a[108:111], v[36:39]\n"
"v_mfma_f32_16x16x32_bf16 v[40:43], a[200:203], a[112:115], v[40:43]\n"
"v_mfma_f32_16x16x32_bf16 v[40:43], a[204:207], a[116:119], v[40:43]\n"
"v_mfma_f32_16x16x32_bf16 v[44:47], a[200:203], a[120:123], v[44:47]\n"
"v_mfma_f32_16x16x32_bf16 v[44:47], a[204:207], a[124:127], v[44:47]\n"
"v_mfma_f32_16x16x32_bf16 v[48:51], a[200:203], a[128:131], v[48:51]\n"
"v_mfma_f32_16x16x32_bf16 v[48:51], a[204:207], a[132:135], v[48:51]\n"
"v_mfma_f32_16x16x32_bf16 v[52:55], a[200:203], a[136:139], v[52:55]\n"
"v_mfma_f32_16x16x32_bf16 v[52:55], a[204:207], a[140:143], v[52:55]\n"
"v_mfma_f32_16x16x32_bf16 v[56:59], a[200:203], a[144:147], v[56:59]\n"
"v_add_u32 v249, 33408, v248\n"
"v_mfma_f32_16x16x32_bf16 v[56:59], a[204:207], a[148:151], v[56:59]\n"
"global_store_dwordx4 v249, v[224:227], s[24:25] sc1\n"
"v_mfma_f32_16x16x32_bf16 v[60:63], a[200:203], a[152:155], v[60:63]\n"
"global_load_dword a200, v250, s[64:65]\n"
"global_load_dwordx4 a[200:203], v246, s[16:17] offset:-2048 sc1\n"
"v_mfma_f32_16x16x32_bf16 v[60:63], a[204:207], a[156:159], v[60:63]\n"
"s_waitcnt vmcnt(19)\n"
"v_mfma_f32_16x16x32_bf16 v[64:67], a[208:211], a[96:99], v[64:67]\n"
"global_load_dwordx4 a[204:207], v246, s[16:17] offset:-1024 sc1\n"
"v_mfma_f32_16x16x32_bf16 v[64:67], a[212:215], a[100:103], v[64:67]\n"
"v_mfma_f32_16x16x32_bf16 v[68:71], a[208:211], a[104:107], v[68:71]\n"
"v_mfma_f32_16x16x32_bf16 v[68:71], a[212:215], a[108:111], v[68:71]\n"
"v_mfma_f32_16x16x32_bf16 v[72:75], a[208:211], a[112:115], v[72:75]\n"
"v_mfma_f32_16x16x32_bf16 v[72:75], a[212:215], a[116:119], v[72:75]\n"
"v_mfma_f32_16x16x32_bf16 v[76:79], a[208:211], a[120:123], v[76:79]\n"
"v_mfma_f32_16x16x32_bf16 v[76:79], a[212:215], a[124:127], v[76:79]\n"
"v_mfma_f32_16x16x32_bf16 v[80:83], a[208:211], a[128:131], v[80:83]\n"
"v_mfma_f32_16x16x32_bf16 v[80:83], a[212:215], a[132:135], v[80:83]\n"
"v_mfma_f32_16x16x32_bf16 v[84:87], a[208:211], a[136:139], v[84:87]\n"
"v_mfma_f32_16x16x32_bf16 v[84:87], a[212:215], a[140:143], v[84:87]\n"
"v_mfma_f32_16x16x32_bf16 v[88:91], a[208:211], a[144:147], v[88:91]\n"
"v_mfma_f32_16x16x32_bf16 v[88:91], a[212:215], a[148:151], v[88:91]\n"
"v_mfma_f32_16x16x32_bf16 v[92:95], a[208:211], a[152:155], v[92:95]\n"
"global_load_dwordx4 a[208:211], v246, s[16:17] offset:0 sc1\n"
"v_mfma_f32_16x16x32_bf16 v[92:95], a[212:215], a[156:159], v[92:95]\n"
"s_waitcnt vmcnt(19)\n"
"v_mfma_f32_16x16x32_bf16 v[96:99], a[216:219], a[96:99], v[96:99]\n"
"global_load_dwordx4 a[212:215], v246, s[16:17] offset:1024 sc1\n"
"ds_read_b128 a[96:99], v242 offset:16384\n"
"v_mfma_f32_16x16x32_bf16 v[96:99], a[220:223], a[100:103], v[96:99]\n"
"ds_read_b128 a[100:103], v243 offset:16384\n"
"v_mfma_f32_16x16x32_bf16 v[100:103], a[216:219], a[104:107], v[100:103]\n"
"ds_read_b128 a[104:107], v242 offset:18432\n"
"v_mfma_f32_16x16x32_bf16 v[100:103], a[220:223], a[108:111], v[100:103]\n"
"ds_read_b128 a[108:111], v243 offset:18432\n"
"v_mfma_f32_16x16x32_bf16 v[104:107], a[216:219], a[112:115], v[104:107]\n"
"ds_read_b128 a[112:115], v242 offset:20480\n"
"v_mfma_f32_16x16x32_bf16 v[104:107], a[220:223], a[116:119], v[104:107]\n"
"ds_read_b128 a[116:119], v243 offset:20480\n"
"v_mfma_f32_16x16x32_bf16 v[108:111], a[216:219], a[120:123], v[108:111]\n"
"ds_read_b128 a[120:123], v242 offset:22528\n"
"v_mfma_f32_16x16x32_bf16 v[108:111], a[220:223], a[124:127], v[108:111]\n"
"ds_read_b128 a[124:127], v243 offset:22528\n"
"v_mfma_f32_16x16x32_bf16 v[112:115], a[216:219], a[128:131], v[112:115]\n"
"ds_read_b128 a[128:131], v242 offset:24576\n"
"v_mfma_f32_16x16x32_bf16 v[112:115], a[220:223], a[132:135], v[112:115]\n"
"ds_read_b128 a[132:135], v243 offset:24576\n"
"v_mfma_f32_16x16x32_bf16 v[116:119], a[216:219], a[136:139], v[116:119]\n"
"ds_read_b128 a[136:139], v242 offset:26624\n"
"v_mfma_f32_16x16x32_bf16 v[116:119], a[220:223], a[140:143], v[116:119]\n"
"ds_read_b128 a[140:143], v243 offset:26624\n"
"v_mfma_f32_16x16x32_bf16 v[120:123], a[216:219], a[144:147], v[120:123]\n"
"ds_read_b128 a[144:147], v242 offset:28672\n"
"v_mfma_f32_16x16x32_bf16 v[120:123], a[220:223], a[148:151], v[120:123]\n"
"ds_read_b128 a[148:151], v243 offset:28672\n"
"v_mfma_f32_16x16x32_bf16 v[124:127], a[216:219], a[152:155], v[124:127]\n"
"global_load_dwordx4 a[216:219], v246, s[16:17] offset:2048 sc1\n"
"ds_read_b128 a[152:155], v242 offset:30720\n"
"v_mfma_f32_16x16x32_bf16 v[124:127], a[220:223], a[156:159], v[124:127]\n"
"ds_read_b128 a[156:159], v243 offset:30720\n"
"s_waitcnt vmcnt(18) lgkmcnt(14)\n"
"v_mfma_f32_16x16x32_bf16 v[0:3], a[160:163], a[96:99], v[0:3]\n"
"global_load_dwordx4 a[220:223], v246, s[16:17] offset:3072 sc1\n"
"v_mfma_f32_16x16x32_bf16 v[0:3], a[164:167], a[100:103], v[0:3]\n"
"s_waitcnt lgkmcnt(12)\n"
"v_mfma_f32_16x16x32_bf16 v[4:7], a[160:163], a[104:107], v[4:7]\n"
"v_add_u32 v246, 0x2000, v246\n"
"v_mfma_f32_16x16x32_bf16 v[4:7], a[164:167], a[108:111], v[4:7]\n"
"s_waitcnt lgkmcnt(10)\n"
"v_mfma_f32_16x16x32_bf16 v[8:11], a[160:163], a[112:115], v[8:11]\n"
"v_add_u32 v249, 49792, v248\n"
"v_mfma_f32_16x16x32_bf16 v[8:11], a[164:167], a[116:119], v[8:11]\n"
"s_waitcnt lgkmcnt(8)\n"
"v_mfma_f32_16x16x32_bf16 v[12:15], a[160:163], a[120:123], v[12:15]\n"
"global_store_dwordx4 v249, v[228:231], s[24:25] sc1\n"
"v_mfma_f32_16x16x32_bf16 v[12:15], a[164:167], a[124:127], v[12:15]\n"
"s_waitcnt lgkmcnt(6)\n"
"v_mfma_f32_16x16x32_bf16 v[16:19], a[160:163], a[128:131], v[16:19]\n"
"v_mfma_f32_16x16x32_bf16 v[16:19], a[164:167], a[132:135], v[16:19]\n"
"s_waitcnt lgkmcnt(4)\n"
"v_mfma_f32_16x16x32_bf16 v[20:23], a[160:163], a[136:139], v[20:23]\n"
"v_mfma_f32_16x16x32_bf16 v[20:23], a[164:167], a[140:143], v[20:23]\n"
"s_waitcnt lgkmcnt(2)\n"
"v_mfma_f32_16x16x32_bf16 v[24:27], a[160:163], a[144:147], v[24:27]\n"
"ds_read_b128 v[224:227], v247 offset:49152\n"
"v_mfma_f32_16x16x32_bf16 v[24:27], a[164:167], a[148:151], v[24:27]\n"
"s_waitcnt lgkmcnt(1)\n"
"v_mfma_f32_16x16x32_bf16 v[28:31], a[160:163], a[152:155], v[28:31]\n"
"global_load_dwordx4 a[160:163], v246, s[16:17] offset:-4096 sc1\n"
"v_mfma_f32_16x16x32_bf16 v[28:31], a[164:167], a[156:159], v[28:31]\n"
"s_waitcnt vmcnt(18)\n"
"v_mfma_f32_16x16x32_bf16 v[32:35], a[168:171], a[96:99], v[32:35]\n"
"global_load_dwordx4 a[164:167], v246, s[16:17] offset:-3072 sc1\n"
"v_mfma_f32_16x16x32_bf16 v[32:35], a[172:175], a[100:103], v[32:35]\n"
"ds_read_b128 v[228:231], v247 offset:50176\n"
"v_mfma_f32_16x16x32_bf16 v[36:39], a[168:171], a[104:107], v[36:39]\n"
"v_mfma_f32_16x16x32_bf16 v[36:39], a[172:175], a[108:111], v[36:39]\n"
"v_mfma_f32_16x16x32_bf16 v[40:43], a[168:171], a[112:115], v[40:43]\n"
"v_mfma_f32_16x16x32_bf16 v[40:43], a[172:175], a[116:119], v[40:43]\n"
"v_mfma_f32_16x16x32_bf16 v[44:47], a[168:171], a[120:123], v[44:47]\n"
"v_mfma_f32_16x16x32_bf16 v[44:47], a[172:175], a[124:127], v[44:47]\n"
"v_add_u32 v249, 896, v248\n"
"v_mfma_f32_16x16x32_bf16 v[48:51], a[168:171], a[128:131], v[48:51]\n"
"s_waitcnt lgkmcnt(1)\n"
"global_store_dwordx4 v249, v[224:227], s[24:25] sc1\n"
"v_mfma_f32_16x16x32_bf16 v[48:51], a[172:175], a[132:135], v[48:51]\n"
"v_mfma_f32_16x16x32_bf16 v[52:55], a[168:171], a[136:139], v[52:55]\n"
"v_mfma_f32_16x16x32_bf16 v[52:55], a[172:175], a[140:143], v[52:55]\n"
"ds_read_b128 v[224:227], v247 offset:51200\n"
"v_mfma_f32_16x16x32_bf16 v[56:59], a[168:171], a[144:147], v[56:59]\n"
"v_mfma_f32_16x16x32_bf16 v[56:59], a[172:175], a[148:151], v[56:59]\n"
"v_mfma_f32_16x16x32_bf16 v[60:63], a[168:171], a[152:155], v[60:63]\n"
"global_load_dwordx4 a[168:171], v246, s[16:17] offset:-2048 sc1\n"
"v_mfma_f32_16x16x32_bf16 v[60:63], a[172:175], a[156:159], v[60:63]\n"
"s_waitcnt vmcnt(19)\n"
"v_mfma_f32_16x16x32_bf16 v[64:67], a[176:179], a[96:99], v[64:67]\n"
"global_load_dwordx4 a[172:175], v246, s[16:17] offset:-1024 sc1\n"
"v_mfma_f32_16x16x32_bf16 v[64:67], a[180:183], a[100:103], v[64:67]\n"
"v_mfma_f32_16x16x32_bf16 v[68:71], a[176:179], a[104:107], v[68:71]\n"
"v_mfma_f32_16x16x32_bf16 v[68:71], a[180:183], a[108:111], v[68:71]\n"
"v_mfma_f32_16x16x32_bf16 v[72:75], a[176:179], a[112:115], v[72:75]\n"
"s_mov_b64 exec, 1\n"
"global_atomic_add v250, v252, s[30:31] offset:0 sc1\n"
"s_mov_b64 exec, -1\n"
"v_mfma_f32_16x16x32_bf16 v[72:75], a[180:183], a[116:119], v[72:75]\n"
"v_mfma_f32_16x16x32_bf16 v[76:79], a[176:179], a[120:123], v[76:79]\n"
"v_mfma_f32_16x16x32_bf16 v[76:79], a[180:183], a[124:127], v[76:79]\n"
"v_mfma_f32_16x16x32_bf16 v[80:83], a[176:179], a[128:131], v[80:83]\n"
"v_mfma_f32_16x16x32_bf16 v[80:83], a[180:183], a[132:135], v[80:83]\n"
"v_mfma_f32_16x16x32_bf16 v[84:87], a[176:179], a[136:139], v[84:87]\n"
"v_mfma_f32_16x16x32_bf16 v[84:87], a[180:183], a[140:143], v[84:87]\n"
"v_mfma_f32_16x16x32_bf16 v[88:91], a[176:179], a[144:147], v[88:91]\n"
"v_mfma_f32_16x16x32_bf16 v[88:91], a[180:183], a[148:151], v[88:91]\n"
"v_mfma_f32_16x16x32_bf16 v[92:95], a[176:179], a[152:155], v[92:95]\n"
"global_load_dwordx4 a[176:179], v246, s[16:17] offset:0 sc1\n"
"v_mfma_f32_16x16x32_bf16 v[92:95], a[180:183], a[156:159], v[92:95]\n"
"s_waitcnt vmcnt(20)\n"
"v_mfma_f32_16x16x32_bf16 v[96:99], a[184:187], a[96:99], v[96:99]\n"
"global_load_dwordx4 a[180:183], v246, s[16:17] offset:1024 sc1\n"
"ds_read_b128 a[96:99], v242 offset:49152\n"
"v_mfma_f32_16x16x32_bf16 v[96:99], a[188:191], a[100:103], v[96:99]\n"
"ds_read_b128 a[100:103], v243 offset:49152\n"
"v_mfma_f32_16x16x32_bf16 v[100:103], a[184:187], a[104:107], v[100:103]\n"
"ds_read_b128 a[104:107], v242 offset:51200\n"
"v_mfma_f32_16x16x32_bf16 v[100:103], a[188:191], a[108:111], v[100:103]\n"
"ds_read_b128 a[108:111], v243 offset:51200\n"
"v_mfma_f32_16x16x32_bf16 v[104:107], a[184:187], a[112:115], v[104:107]\n"
"ds_read_b128 a[112:115], v242 offset:53248\n"
"v_mfma_f32_16x16x32_bf16 v[104:107], a[188:191], a[116:119], v[104:107]\n"
"ds_read_b128 a[116:119], v243 offset:53248\n"
"v_mfma_f32_16x16x32_bf16 v[108:111], a[184:187], a[120:123], v[108:111]\n"
"ds_read_b128 a[120:123], v242 offset:55296\n"
"v_mfma_f32_16x16x32_bf16 v[108:111], a[188:191], a[124:127], v[108:111]\n"
"ds_read_b128 a[124:127], v243 offset:55296\n"
"v_mfma_f32_16x16x32_bf16 v[112:115], a[184:187], a[128:131], v[112:115]\n"
"ds_read_b128 a[128:131], v242 offset:57344\n"
"v_mfma_f32_16x16x32_bf16 v[112:115], a[188:191], a[132:135], v[112:115]\n"
"ds_read_b128 a[132:135], v243 offset:57344\n"
"v_mfma_f32_16x16x32_bf16 v[116:119], a[184:187], a[136:139], v[116:119]\n"
"ds_read_b128 a[136:139], v242 offset:59392\n"
"v_mfma_f32_16x16x32_bf16 v[116:119], a[188:191], a[140:143], v[116:119]\n"
"ds_read_b128 a[140:143], v243 offset:59392\n"
"v_mfma_f32_16x16x32_bf16 v[120:123], a[184:187], a[144:147], v[120:123]\n"
"ds_read_b128 a[144:147], v242 offset:61440\n"
"v_mfma_f32_16x16x32_bf16 v[120:123], a[188:191], a[148:151], v[120:123]\n"
"ds_read_b128 a[148:151], v243 offset:61440\n"
"v_mfma_f32_16x16x32_bf16 v[124:127], a[184:187], a[152:155], v[124:127]\n"
"global_load_dwordx4 a[184:187], v246, s[16:17] offset:2048 sc1\n"
"ds_read_b128 a[152:155], v242 offset:63488\n"
"v_mfma_f32_16x16x32_bf16 v[124:127], a[188:191], a[156:159], v[124:127]\n"
"ds_read_b128 a[156:159], v243 offset:63488\n"
"s_waitcnt vmcnt(18) lgkmcnt(14)\n"
"v_mfma_f32_16x16x32_bf16 v[0:3], a[192:195], a[96:99], v[0:3]\n"
"global_load_dwordx4 a[188:191], v246, s[16:17] offset:3072 sc1\n"
"v_and_b32 v235, s67, v237\n"
"v_mfma_f32_16x16x32_bf16 v[0:3], a[196:199], a[100:103], v[0:3]\n"
"v_lshlrev_b32 v234, 16, v237\n"
"s_waitcnt lgkmcnt(12)\n"
"v_mfma_f32_16x16x32_bf16 v[4:7], a[192:195], a[104:107], v[4:7]\n"
"v_add_u32 v246, 0x2000, v246\n"
"v_and_b32 v233, s67, v236\n"
"v_mfma_f32_16x16x32_bf16 v[4:7], a[196:199], a[108:111], v[4:7]\n"
"v_lshlrev_b32 v232, 16, v236\n"
"s_waitcnt lgkmcnt(10)\n"
"v_mfma_f32_16x16x32_bf16 v[8:11], a[192:195], a[112:115], v[8:11]\n"
"v_add_u32 v249, 17280, v248\n"
"v_lshrrev_b32 v237, 4, v247\n"
"v_mfma_f32_16x16x32_bf16 v[8:11], a[196:199], a[116:119], v[8:11]\n"
"v_and_b32 v237, 0x30, v237\n"
"s_waitcnt lgkmcnt(8)\n"
"v_mfma_f32_16x16x32_bf16 v[12:15], a[192:195], a[120:123], v[12:15]\n"
"global_store_dwordx4 v249, v[228:231], s[24:25] sc1\n"
"v_mfma_f32_16x16x32_bf16 v[12:15], a[196:199], a[124:127], v[12:15]\n"
"global_load_dwordx2 v[236:237], v237, s[18:19] offset:136\n"
"s_waitcnt lgkmcnt(6)\n"
"v_mfma_f32_16x16x32_bf16 v[16:19], a[192:195], a[128:131], v[16:19]\n"
"v_add_f32 v128, v232, v128\n"
"v_mfma_f32_16x16x32_bf16 v[16:19], a[196:199], a[132:135], v[16:19]\n"
"s_waitcnt lgkmcnt(4)\n"
"v_mfma_f32_16x16x32_bf16 v[20:23], a[192:195], a[136:139], v[20:23]\n"
"ds_read_b128 v[228:231], v247 offset:52224\n"
"v_mfma_f32_16x16x32_bf16 v[20:23], a[196:199], a[140:143], v[20:23]\n"
"s_waitcnt lgkmcnt(3)\n"
"v_mfma_f32_16x16x32_bf16 v[24:27], a[192:195], a[144:147], v[24:27]\n"
"v_add_f32 v129, v233, v129\n"
"v_mfma_f32_16x16x32_bf16 v[24:27], a[196:199], a[148:151], v[24:27]\n"
"s_waitcnt lgkmcnt(1)\n"
"v_mfma_f32_16x16x32_bf16 v[28:31], a[192:195], a[152:155], v[28:31]\n"
"global_load_dwordx4 a[192:195], v246, s[16:17] offset:-4096 sc1\n"
"v_mfma_f32_16x16x32_bf16 v[28:31], a[196:199], a[156:159], v[28:31]\n"
"s_waitcnt vmcnt(18)\n"
"v_mfma_f32_16x16x32_bf16 v[32:35], a[200:203], a[96:99], v[32:35]\n"
"global_load_dwordx4 a[196:199], v246, s[16:17] offset:-3072 sc1\n"
"v_mfma_f32_16x16x32_bf16 v[32:35], a[204:207], a[100:103], v[32:35]\n"
"v_add_u32 v249, 33664, v248\n"
"v_mfma_f32_16x16x32_bf16 v[36:39], a[200:203], a[104:107], v[36:39]\n"
"global_store_dwordx4 v249, v[224:227], s[24:25] sc1\n"
"v_mfma_f32_16x16x32_bf16 v[36:39], a[204:207], a[108:111], v[36:39]\n"
"v_add_f32 v130, v234, v130\n"
"v_mfma_f32_16x16x32_bf16 v[40:43], a[200:203], a[112:115], v[40:43]\n"
"v_add_f32 v131, v235, v131\n"
"v_mfma_f32_16x16x32_bf16 v[40:43], a[204:207], a[116:119], v[40:43]\n"
"v_add_f32 v132, v232, v132\n"
"v_mfma_f32_16x16x32_bf16 v[44:47], a[200:203], a[120:123], v[44:47]\n"
"v_add_f32 v133, v233, v133\n"
"v_mfma_f32_16x16x32_bf16 v[44:47], a[204:207], a[124:127], v[44:47]\n"
"v_add_f32 v134, v234, v134\n"
"v_mfma_f32_16x16x32_bf16 v[48:51], a[200:203], a[128:131], v[48:51]\n"
"v_add_f32 v135, v235, v135\n"
"v_mfma_f32_16x16x32_bf16 v[48:51], a[204:207], a[132:135], v[48:51]\n"
"v_add_f32 v136, v232, v136\n"
"v_mfma_f32_16x16x32_bf16 v[52:55], a[200:203], a[136:139], v[52:55]\n"
"v_add_f32 v137, v233, v137\n"
"v_mfma_f32_16x16x32_bf16 v[52:55], a[204:207], a[140:143], v[52:55]\n"
"v_add_f32 v138, v234, v138\n"
"v_mfma_f32_16x16x32_bf16 v[56:59], a[200:203], a[144:147], v[56:59]\n"
"v_add_f32 v139, v235, v139\n"
"v_mfma_f32_16x16x32_bf16 v[56:59], a[204:207], a[148:151], v[56:59]\n"
"v_add_f32 v140, v232, v140\n"
"v_mfma_f32_16x16x32_bf16 v[60:63], a[200:203], a[152:155], v[60:63]\n"
"global_load_dwordx4 a[200:203], v246, s[16:17] offset:-2048 sc1\n"
"v_mfma_f32_16x16x32_bf16 v[60:63], a[204:207], a[156:159], v[60:63]\n"
"s_waitcnt vmcnt(19)\n"
"v_mfma_f32_16x16x32_bf16 v[64:67], a[208:211], a[96:99], v[64:67]\n"
"global_load_dwordx4 a[204:207], v246, s[16:17] offset:-1024 sc1\n"
"v_mfma_f32_16x16x32_bf16 v[64:67], a[212:215], a[100:103], v[64:67]\n"
"v_add_f32 v141, v233, v141\n"
"v_mfma_f32_16x16x32_bf16 v[68:71], a[208:211], a[104:107], v[68:71]\n"
"v_add_f32 v142, v234, v142\n"
"v_mfma_f32_16x16x32_bf16 v[68:71], a[212:215], a[108:111], v[68:71]\n"
"v_add_f32 v143, v235, v143\n"
"v_mfma_f32_16x16x32_bf16 v[72:75], a[208:211], a[112:115], v[72:75]\n"
"v_add_f32 v144, v232, v144\n"
"v_mfma_f32_16x16x32_bf16 v[72:75], a[212:215], a[116:119], v[72:75]\n"
"v_add_f32 v145, v233, v145\n"
"v_mfma_f32_16x16x32_bf16 v[76:79], a[208:211], a[120:123], v[76:79]\n"
"v_add_f32 v146, v234, v146\n"
"v_mfma_f32_16x16x32_bf16 v[76:79], a[212:215], a[124:127], v[76:79]\n"
"v_add_f32 v147, v235, v147\n"
"v_mfma_f32_16x16x32_bf16 v[80:83], a[208:211], a[128:131], v[80:83]\n"
"v_add_f32 v148, v232, v148\n"
"v_mfma_f32_16x16x32_bf16 v[80:83], a[212:215], a[132:135], v[80:83]\n"
"v_add_f32 v149, v233, v149\n"
"v_mfma_f32_16x16x32_bf16 v[84:87], a[208:211], a[136:139], v[84:87]\n"
"v_add_f32 v150, v234, v150\n"
"v_mfma_f32_16x16x32_bf16 v[84:87], a[212:215], a[140:143], v[84:87]\n"
"v_add_u32 v249, 50048, v248\n"
"v_mfma_f32_16x16x32_bf16 v[88:91], a[208:211], a[144:147], v[88:91]\n"
"s_waitcnt lgkmcnt(0)\n"
"global_store_dwordx4 v249, v[228:231], s[24:25] sc1\n"
"v_mfma_f32_16x16x32_bf16 v[88:91], a[212:215], a[148:151], v[88:91]\n"
"v_xor_b32 v247, 0x10000, v247\n"
"s_barrier\n"
"v_mfma_f32_16x16x32_bf16 v[92:95], a[208:211], a[152:155], v[92:95]\n"
"global_load_dwordx4 a[208:211], v246, s[16:17] offset:0 sc1\n"
"v_mfma_f32_16x16x32_bf16 v[92:95], a[212:215], a[156:159], v[92:95]\n"
"s_waitcnt vmcnt(20)\n"
"v_mfma_f32_16x16x32_bf16 v[96:99], a[216:219], a[96:99], v[96:99]\n"
"global_load_dwordx4 a[212:215], v246, s[16:17] offset:1024 sc1\n"
"ds_read_b128 a[96:99], v240 offset:0\n"
"v_mfma_f32_16x16x32_bf16 v[96:99], a[220:223], a[100:103], v[96:99]\n"
"ds_read_b128 a[100:103], v241 offset:0\n"
"v_mfma_f32_16x16x32_bf16 v[100:103], a[216:219], a[104:107], v[100:103]\n"
"ds_read_b128 a[104:107], v240 offset:2048\n"
"v_mfma_f32_16x16x32_bf16 v[100:103], a[220:223], a[108:111], v[100:103]\n"
"ds_read_b128 a[108:111], v241 offset:2048\n"
"v_mfma_f32_16x16x32_bf16 v[104:107], a[216:219], a[112:115], v[104:107]\n"
"ds_read_b128 a[112:115], v240 offset:4096\n"
"v_mfma_f32_16x16x32_bf16 v[104:107], a[220:223], a[116:119], v[104:107]\n"
"ds_read_b128 a[116:119], v241 offset:4096\n"
"v_mfma_f32_16x16x32_bf16 v[108:111], a[216:219], a[120:123], v[108:111]\n"
"ds_read_b128 a[120:123], v240 offset:6144\n"
"v_mfma_f32_16x16x32_bf16 v[108:111], a[220:223], a[124:127], v[108:111]\n"
"ds_read_b128 a[124:127], v241 offset:6144\n"
"v_mfma_f32_16x16x32_bf16 v[112:115], a[216:219], a[128:131], v[112:115]\n"
"ds_read_b128 a[128:131], v240 offset:8192\n"
"v_mfma_f32_16x16x32_bf16 v[112:115], a[220:223], a[132:135], v[112:115]\n"
"ds_read_b128 a[132:135], v241 offset:8192\n"
"v_mfma_f32_16x16x32_bf16 v[116:119], a[216:219], a[136:139], v[116:119]\n"
"ds_read_b128 a[136:139], v240 offset:10240\n"
"v_mfma_f32_16x16x32_bf16 v[116:119], a[220:223], a[140:143], v[116:119]\n"
"ds_read_b128 a[140:143], v241 offset:10240\n"
"v_mfma_f32_16x16x32_bf16 v[120:123], a[216:219], a[144:147], v[120:123]\n"
"ds_read_b128 a[144:147], v240 offset:12288\n"
"v_mfma_f32_16x16x32_bf16 v[120:123], a[220:223], a[148:151], v[120:123]\n"
"ds_read_b128 a[148:151], v241 offset:12288\n"
"v_mfma_f32_16x16x32_bf16 v[124:127], a[216:219], a[152:155], v[124:127]\n"
"global_load_dwordx4 a[216:219], v246, s[16:17] offset:2048 sc1\n"
"ds_read_b128 a[152:155], v240 offset:14336\n"
"v_mfma_f32_16x16x32_bf16 v[124:127], a[220:223], a[156:159], v[124:127]\n"
"ds_read_b128 a[156:159], v241 offset:14336\n"
"s_waitcnt vmcnt(19) lgkmcnt(14)\n"
"v_mfma_f32_16x16x32_bf16 v[0:3], a[160:163], a[96:99], v[0:3]\n"
"global_load_dwordx4 a[220:223], v246, s[16:17] offset:3072 sc1\n"
"v_mfma_f32_16x16x32_bf16 v[0:3], a[164:167], a[100:103], v[0:3]\n"
"s_waitcnt lgkmcnt(12)\n"
"v_mfma_f32_16x16x32_bf16 v[4:7], a[160:163], a[104:107], v[4:7]\n"
"v_add_u32 v246, 0x2000, v246\n"
"v_mfma_f32_16x16x32_bf16 v[4:7], a[164:167], a[108:111], v[4:7]\n"
"s_waitcnt lgkmcnt(10)\n"
"v_mfma_f32_16x16x32_bf16 v[8:11], a[160:163], a[112:115], v[8:11]\n"
"global_load_dword v239, v250, s[34:35] sc1\n"
"v_mfma_f32_16x16x32_bf16 v[8:11], a[164:167], a[116:119], v[8:11]\n"
"s_waitcnt lgkmcnt(8)\n"
"v_mfma_f32_16x16x32_bf16 v[12:15], a[160:163], a[120:123], v[12:15]\n"
"v_add_f32 v151, v235, v151\n"
"v_mfma_f32_16x16x32_bf16 v[12:15], a[164:167], a[124:127], v[12:15]\n"
"s_waitcnt lgkmcnt(6)\n"
"v_mfma_f32_16x16x32_bf16 v[16:19], a[160:163], a[128:131], v[16:19]\n"
"ds_read_b128 v[224:227], v247 offset:0\n"
"v_mfma_f32_16x16x32_bf16 v[16:19], a[164:167], a[132:135], v[16:19]\n"
"s_waitcnt lgkmcnt(5)\n"
"v_mfma_f32_16x16x32_bf16 v[20:23], a[160:163], a[136:139], v[20:23]\n"
"ds_read_b128 v[228:231], v247 offset:1024\n"
"v_mfma_f32_16x16x32_bf16 v[20:23], a[164:167], a[140:143], v[20:23]\n"
"s_waitcnt lgkmcnt(4)\n"
"v_mfma_f32_16x16x32_bf16 v[24:27], a[160:163], a[144:147], v[24:27]\n"
"v_add_f32 v152, v232, v152\n"
"v_mfma_f32_16x16x32_bf16 v[24:27], a[164:167], a[148:151], v[24:27]\n"
"s_waitcnt lgkmcnt(2)\n"
"v_mfma_f32_16x16x32_bf16 v[28:31], a[160:163], a[152:155], v[28:31]\n"
"global_load_dwordx4 a[160:163], v246, s[16:17] offset:-4096 sc1\n"
"v_mfma_f32_16x16x32_bf16 v[28:31], a[164:167], a[156:159], v[28:31]\n"
"s_waitcnt vmcnt(19)\n"
"v_mfma_f32_16x16x32_bf16 v[32:35], a[168:171], a[96:99], v[32:35]\n"
"global_load_dwordx4 a[164:167], v246, s[16:17] offset:-3072 sc1\n"
"v_mfma_f32_16x16x32_bf16 v[32:35], a[172:175], a[100:103], v[32:35]\n"
"v_add_f32 v153, v233, v153\n"
"v_mfma_f32_16x16x32_bf16 v[36:39], a[168:171], a[104:107], v[36:39]\n"
"v_add_f32 v154, v234, v154\n"
"v_mfma_f32_16x16x32_bf16 v[36:39], a[172:175], a[108:111], v[36:39]\n"
"v_add_f32 v155, v235, v155\n"
"v_mfma_f32_16x16x32_bf16 v[40:43], a[168:171], a[112:115], v[40:43]\n"
"v_add_u32 v249, 0, v248\n"
"v_mfma_f32_16x16x32_bf16 v[40:43], a[172:175], a[116:119], v[40:43]\n"
"s_waitcnt lgkmcnt(1)\n"
"global_store_dwordx4 v249, v[224:227], s[24:25] sc1\n"
"v_mfma_f32_16x16x32_bf16 v[44:47], a[168:171], a[120:123], v[44:47]\n"
"v_add_f32 v156, v232, v156\n"
"v_mfma_f32_16x16x32_bf16 v[44:47], a[172:175], a[124:127], v[44:47]\n"
"v_add_f32 v157, v233, v157\n"
"v_mfma_f32_16x16x32_bf16 v[48:51], a[168:171], a[128:131], v[48:51]\n"
"ds_read_b128 v[224:227], v247 offset:2048\n"
"v_mfma_f32_16x16x32_bf16 v[48:51], a[172:175], a[132:135], v[48:51]\n"
"v_add_f32 v158, v234, v158\n"
"v_mfma_f32_16x16x32_bf16 v[52:55], a[168:171], a[136:139], v[52:55]\n"
"v_add_f32 v159, v235, v159\n"
"v_mfma_f32_16x16x32_bf16 v[52:55], a[172:175], a[140:143], v[52:55]\n"
"v_mfma_f32_16x16x32_bf16 v[56:59], a[168:171], a[144:147], v[56:59]\n"
"v_mfma_f32_16x16x32_bf16 v[56:59], a[172:175], a[148:151], v[56:59]\n"
"v_mfma_f32_16x16x32_bf16 v[60:63], a[168:171], a[152:155], v[60:63]\n"
"global_load_dwordx4 a[168:171], v246, s[16:17] offset:-2048 sc1\n"
"v_mfma_f32_16x16x32_bf16 v[60:63], a[172:175], a[156:159], v[60:63]\n"
"s_waitcnt vmcnt(19)\n"
"v_mfma_f32_16x16x32_bf16 v[64:67], a[176:179], a[96:99], v[64:67]\n"
"global_load_dwordx4 a[172:175], v246, s[16:17] offset:-1024 sc1\n"
"v_mfma_f32_16x16x32_bf16 v[64:67], a[180:183], a[100:103], v[64:67]\n"
"v_mfma_f32_16x16x32_bf16 v[68:71], a[176:179], a[104:107], v[68:71]\n"
"v_mfma_f32_16x16x32_bf16 v[68:71], a[180:183], a[108:111], v[68:71]\n"
"v_mfma_f32_16x16x32_bf16 v[72:75], a[176:179], a[112:115], v[72:75]\n"
"v_mfma_f32_16x16x32_bf16 v[72:75], a[180:183], a[116:119], v[72:75]\n"
"v_mfma_f32_16x16x32_bf16 v[76:79], a[176:179], a[120:123], v[76:79]\n"
"v_mfma_f32_16x16x32_bf16 v[76:79], a[180:183], a[124:127], v[76:79]\n"
"v_mfma_f32_16x16x32_bf16 v[80:83], a[176:179], a[128:131], v[80:83]\n"
"v_mfma_f32_16x16x32_bf16 v[80:83], a[180:183], a[132:135], v[80:83]\n"
"v_mfma_f32_16x16x32_bf16 v[84:87], a[176:179], a[136:139], v[84:87]\n"
"v_mfma_f32_16x16x32_bf16 v[84:87], a[180:183], a[140:143], v[84:87]\n"
"v_mfma_f32_16x16x32_bf16 v[88:91], a[176:179], a[144:147], v[88:91]\n"
"v_mfma_f32_16x16x32_bf16 v[88:91], a[180:183], a[148:151], v[88:91]\n"
"v_mfma_f32_16x16x32_bf16 v[92:95], a[176:179], a[152:155], v[92:95]\n"
"global_load_dwordx4 a[176:179], v246, s[16:17] offset:0 sc1\n"
"v_mfma_f32_16x16x32_bf16 v[92:95], a[180:183], a[156:159], v[92:95]\n"
"s_waitcnt vmcnt(19)\n"
"v_mfma_f32_16x16x32_bf16 v[96:99], a[184:187], a[96:99], v[96:99]\n"
"global_load_dwordx4 a[180:183], v246, s[16:17] offset:1024 sc1\n"
"v_mfma_f32_16x16x32_bf16 v[96:99], a[188:191], a[100:103], v[96:99]\n"
"v_mfma_f32_16x16x32_bf16 v[100:103], a[184:187], a[104:107], v[100:103]\n"
"v_mfma_f32_16x16x32_bf16 v[100:103], a[188:191], a[108:111], v[100:103]\n"
"v_mfma_f32_16x16x32_bf16 v[104:107], a[184:187], a[112:115], v[104:107]\n"
"v_mfma_f32_16x16x32_bf16 v[104:107], a[188:191], a[116:119], v[104:107]\n"
"v_mfma_f32_16x16x32_bf16 v[108:111], a[184:187], a[120:123], v[108:111]\n"
"v_mfma_f32_16x16x32_bf16 v[108:111], a[188:191], a[124:127], v[108:111]\n"
"v_mfma_f32_16x16x32_bf16 v[112:115], a[184:187], a[128:131], v[112:115]\n"
"s_waitcnt vmcnt(18)\n"
"v_and_b32 v235, s67, v237\n"
"v_lshlrev_b32 v234, 16, v237\n"
"v_mfma_f32_16x16x32_bf16 v[112:115], a[188:191], a[132:135], v[112:115]\n"
"v_and_b32 v233, s67, v236\n"
"v_lshlrev_b32 v232, 16, v236\n"
"v_mfma_f32_16x16x32_bf16 v[116:119], a[184:187], a[136:139], v[116:119]\n"
"v_lshrrev_b32 v237, 4, v247\n"
"v_mfma_f32_16x16x32_bf16 v[116:119], a[188:191], a[140:143], v[116:119]\n"
"v_and_b32 v237, 0x30, v237\n"
"v_mfma_f32_16x16x32_bf16 v[120:123], a[184:187], a[144:147], v[120:123]\n"
"v_add_u32 v249, 16384, v248\n"
"global_load_dwordx2 v[236:237], v237, s[18:19] offset:192\n"
"v_mfma_f32_16x16x32_bf16 v[120:123], a[188:191], a[148:151], v[120:123]\n"
"s_waitcnt lgkmcnt(1)\n"
"global_store_dwordx4 v249, v[228:231], s[24:25] sc1\n"
"v_mfma_f32_16x16x32_bf16 v[124:127], a[184:187], a[152:155], v[124:127]\n"
"global_load_dwordx4 a[184:187], v246, s[16:17] offset:2048 sc1\n"
"v_mfma_f32_16x16x32_bf16 v[124:127], a[188:191], a[156:159], v[124:127]\n"
"s_waitcnt vmcnt(19)\n"
"v_mfma_f32_16x16x32_bf16 v[128:131], a[192:195], a[96:99], v[128:131]\n"
"global_load_dwordx4 a[188:191], v246, s[16:17] offset:3072 sc1\n"
"v_mfma_f32_16x16x32_bf16 v[128:131], a[196:199], a[100:103], v[128:131]\n"
"ds_read_b128 v[228:231], v247 offset:3072\n"
"v_mfma_f32_16x16x32_bf16 v[132:135], a[192:195], a[104:107], v[132:135]\n"
"v_add_u32 v246, 0x2000, v246\n"
"v_mfma_f32_16x16x32_bf16 v[132:135], a[196:199], a[108:111], v[132:135]\n"
"v_add_f32 v160, v232, v160\n"
"v_mfma_f32_16x16x32_bf16 v[136:139], a[192:195], a[112:115], v[136:139]\n"
"s_waitcnt vmcnt(11)\n"
"v_readfirstlane_b32 s46, v239\n"
"v_mfma_f32_16x16x32_bf16 v[136:139], a[196:199], a[116:119], v[136:139]\n"
"v_add_f32 v161, v233, v161\n"
"v_mfma_f32_16x16x32_bf16 v[140:143], a[192:195], a[120:123], v[140:143]\n"
"v_add_f32 v162, v234, v162\n"
"v_mfma_f32_16x16x32_bf16 v[140:143], a[196:199], a[124:127], v[140:143]\n"
"s_sub_u32 s46, s46, s44\n"
"v_mfma_f32_16x16x32_bf16 v[144:147], a[192:195], a[128:131], v[144:147]\n"
"s_cmp_ge_u32 s46, 97536\n"
"s_cbranch_scc0 .Lslow6_%=\n"
".Lback6_%=:\n"
"v_mfma_f32_16x16x32_bf16 v[144:147], a[196:199], a[132:135], v[144:147]\n"
"s_add_u32 m0, s42, 16384\n"
"v_add_u32 v238, 128, v248\n"
"v_mfma_f32_16x16x32_bf16 v[148:151], a[192:195], a[136:139], v[148:151]\n"
"global_load_lds_dwordx4 v238, s[26:27] offset:0 sc1\n"
"v_mfma_f32_16x16x32_bf16 v[148:151], a[196:199], a[140:143], v[148:151]\n"
"v_add_f32 v163, v235, v163\n"
"v_mfma_f32_16x16x32_bf16 v[152:155], a[192:195], a[144:147], v[152:155]\n"
"v_mfma_f32_16x16x32_bf16 v[152:155], a[196:199], a[148:151], v[152:155]\n"
"v_mfma_f32_16x16x32_bf16 v[156:159], a[192:195], a[152:155], v[156:159]\n"
"global_load_dwordx4 a[192:195], v246, s[16:17] offset:-4096 sc1\n"
".Lpg17_%=:\n"
"s_cbranch_execz .Lpg21_%=\n"
"v_mfma_f32_16x16x32_bf16 v[156:159], a[196:199], a[156:159], v[156:159]\n"
"v_mfma_f32_16x16x32_bf16 v[160:163], a[200:203], a[96:99], v[160:163]\n"
"global_load_dwordx4 a[196:199], v246, s[16:17] offset:-3072 sc1\n"
"v_mfma_f32_16x16x32_bf16 v[160:163], a[204:207], a[100:103], v[160:163]\n"
"v_mfma_f32_16x16x32_bf16 v[164:167], a[200:203], a[104:107], v[164:167]\n"
"v_mfma_f32_16x16x32_bf16 v[164:167], a[204:207], a[108:111], v[164:167]\n"
"v_mfma_f32_16x16x32_bf16 v[168:171], a[200:203], a[112:115], v[168:171]\n"
"v_mfma_f32_16x16x32_bf16 v[168:171], a[204:207], a[116:119], v[168:171]\n"
"v_mfma_f32_16x16x32_bf16 v[172:175], a[200:203], a[120:123], v[172:175]\n"
"v_mfma_f32_16x16x32_bf16 v[172:175], a[204:207], a[124:127], v[172:175]\n"
"v_mfma_f32_16x16x32_bf16 v[176:179], a[200:203], a[128:131], v[176:179]\n"
"v_mfma_f32_16x16x32_bf16 v[176:179], a[204:207], a[132:135], v[176:179]\n"
"v_mfma_f32_16x16x32_bf16 v[180:183], a[200:203], a[136:139], v[180:183]\n"
"v_mfma_f32_16x16x32_bf16 v[180:183], a[204:207], a[140:143], v[180:183]\n"
"v_mfma_f32_16x16x32_bf16 v[184:187], a[200:203], a[144:147], v[184:187]\n"
"v_mfma_f32_16x16x32_bf16 v[184:187], a[204:207], a[148:151], v[184:187]\n"
"v_mfma_f32_16x16x32_bf16 v[188:191], a[200:203], a[152:155], v[188:191]\n"
"global_load_dwordx4 a[200:203], v246, s[16:17] offset:-2048 sc1\n"
"v_mfma_f32_16x16x32_bf16 v[188:191], a[204:207], a[156:159], v[188:191]\n"
"v_mfma_f32_16x16x32_bf16 v[192:195], a[208:211], a[96:99], v[192:195]\n"
"global_load_dwordx4 a[204:207], v246, s[16:17] offset:-1024 sc1\n"
"v_mfma_f32_16x16x32_bf16 v[192:195], a[212:215], a[100:103], v[192:195]\n"
"v_mfma_f32_16x16x32_bf16 v[196:199], a[208:211], a[104:107], v[196:199]\n"
"v_mfma_f32_16x16x32_bf16 v[196:199], a[212:215], a[108:111], v[196:199]\n"
"v_mfma_f32_16x16x32_bf16 v[200:203], a[208:211], a[112:115], v[200:203]\n"
"v_add_u32 v249, 32768, v248\n"
"v_mfma_f32_16x16x32_bf16 v[200:203], a[212:215], a[116:119], v[200:203]\n"
"s_waitcnt lgkmcnt(1)\n"
"global_store_dwordx4 v249, v[224:227], s[24:25] sc1\n"
"v_mfma_f32_16x16x32_bf16 v[204:207], a[208:211], a[120:123], v[204:207]\n"
"v_mfma_f32_16x16x32_bf16 v[204:207], a[212:215], a[124:127], v[204:207]\n"
"v_add_u32 v238, s66, v238\n"
"v_mfma_f32_16x16x32_bf16 v[208:211], a[208:211], a[128:131], v[208:211]\n"
"global_load_lds_dwordx4 v238, s[26:27] offset:1024 sc1\n"
"v_mfma_f32_16x16x32_bf16 v[208:211], a[212:215], a[132:135], v[208:211]\n"
"v_add_f32 v164, v232, v164\n"
"v_mfma_f32_16x16x32_bf16 v[212:215], a[208:211], a[136:139], v[212:215]\n"
"v_add_f32 v165, v233, v165\n"
"v_mfma_f32_16x16x32_bf16 v[212:215], a[212:215], a[140:143], v[212:215]\n"
"v_add_f32 v166, v234, v166\n"
"v_mfma_f32_16x16x32_bf16 v[216:219], a[208:211], a[144:147], v[216:219]\n"
"v_add_f32 v167, v235, v167\n"
"v_mfma_f32_16x16x32_bf16 v[216:219], a[212:215], a[148:151], v[216:219]\n"
"v_add_f32 v168, v232, v168\n"
"v_mfma_f32_16x16x32_bf16 v[220:223], a[208:211], a[152:155], v[220:223]\n"
"global_load_dwordx4 a[208:211], v246, s[16:17] offset:0 sc1\n"
"v_mfma_f32_16x16x32_bf16 v[220:223], a[212:215], a[156:159], v[220:223]\n"
"v_mfma_f32_16x16x32_bf16 a[0:3], a[216:219], a[96:99], a[0:3]\n"
"global_load_dwordx4 a[212:215], v246, s[16:17] offset:1024 sc1\n"
"ds_read_b128 a[96:99], v240 offset:32768\n"
"v_mfma_f32_16x16x32_bf16 a[0:3], a[220:223], a[100:103], a[0:3]\n"
"ds_read_b128 a[100:103], v241 offset:32768\n"
"v_mfma_f32_16x16x32_bf16 a[4:7], a[216:219], a[104:107], a[4:7]\n"
"ds_read_b128 a[104:107], v240 offset:34816\n"
"v_mfma_f32_16x16x32_bf16 a[4:7], a[220:223], a[108:111], a[4:7]\n"
"ds_read_b128 a[108:111], v241 offset:34816\n"
"v_mfma_f32_16x16x32_bf16 a[8:11], a[216:219], a[112:115], a[8:11]\n"
"ds_read_b128 a[112:115], v240 offset:36864\n"
"v_mfma_f32_16x16x32_bf16 a[8:11], a[220:223], a[116:119], a[8:11]\n"
"ds_read_b128 a[116:119], v241 offset:36864\n"
"v_mfma_f32_16x16x32_bf16 a[12:15], a[216:219], a[120:123], a[12:15]\n"
"ds_read_b128 a[120:123], v240 offset:38912\n"
"v_mfma_f32_16x16x32_bf16 a[12:15], a[220:223], a[124:127], a[12:15]\n"
"ds_read_b128 a[124:127], v241 offset:38912\n"
"v_mfma_f32_16x16x32_bf16 a[16:19], a[216:219], a[128:131], a[16:19]\n"
"ds_read_b128 a[128:131], v240 offset:40960\n"
"v_mfma_f32_16x16x32_bf16 a[16:19], a[220:223], a[132:135], a[16:19]\n"
"ds_read_b128 a[132:135], v241 offset:40960\n"
"v_mfma_f32_16x16x32_bf16 a[20:23], a[216:219], a[136:139], a[20:23]\n"
"ds_read_b128 a[136:139], v240 offset:43008\n"
"v_mfma_f32_16x16x32_bf16 a[20:23], a[220:223], a[140:143], a[20:23]\n"
"ds_read_b128 a[140:143], v241 offset:43008\n"
"v_mfma_f32_16x16x32_bf16 a[24:27], a[216:219], a[144:147], a[24:27]\n"
"ds_read_b128 a[144:147], v240 offset:45056\n"
"v_mfma_f32_16x16x32_bf16 a[24:27], a[220:223], a[148:151], a[24:27]\n"
"ds_read_b128 a[148:151], v241 offset:45056\n"
"v_mfma_f32_16x16x32_bf16 a[28:31], a[216:219], a[152:155], a[28:31]\n"
"global_load_dwordx4 a[216:219], v246, s[16:17] offset:2048 sc1\n"
"ds_read_b128 a[152:155], v240 offset:47104\n"
"v_mfma_f32_16x16x32_bf16 a[28:31], a[220:223], a[156:159], a[28:31]\n"
"ds_read_b128 a[156:159], v241 offset:47104\n"
"s_waitcnt vmcnt(19) lgkmcnt(14)\n"
"v_mfma_f32_16x16x32_bf16 v[0:3], a[160:163], a[96:99], v[0:3]\n"
"global_load_dwordx4 a[220:223], v246, s[16:17] offset:3072 sc1\n"
"v_mfma_f32_16x16x32_bf16 v[0:3], a[164:167], a[100:103], v[0:3]\n"
"s_waitcnt lgkmcnt(12)\n"
"v_mfma_f32_16x16x32_bf16 v[4:7], a[160:163], a[104:107], v[4:7]\n"
"v_add_u32 v246, 0x2000, v246\n"
"v_mfma_f32_16x16x32_bf16 v[4:7], a[164:167], a[108:111], v[4:7]\n"
"s_waitcnt lgkmcnt(10)\n"
"v_mfma_f32_16x16x32_bf16 v[8:11], a[160:163], a[112:115], v[8:11]\n"
"v_add_f32 v169, v233, v169\n"
"v_mfma_f32_16x16x32_bf16 v[8:11], a[164:167], a[116:119], v[8:11]\n"
"s_waitcnt lgkmcnt(8)\n"
"v_mfma_f32_16x16x32_bf16 v[12:15], a[160:163], a[120:123], v[12:15]\n"
"v_add_u32 v238, s66, v238\n"
"v_mfma_f32_16x16x32_bf16 v[12:15], a[164:167], a[124:127], v[12:15]\n"
"s_waitcnt lgkmcnt(6)\n"
"v_mfma_f32_16x16x32_bf16 v[16:19], a[160:163], a[128:131], v[16:19]\n"
"global_load_lds_dwordx4 v238, s[26:27] offset:2048 sc1\n"
"v_mfma_f32_16x16x32_bf16 v[16:19], a[164:167], a[132:135], v[16:19]\n"
"s_waitcnt lgkmcnt(4)\n"
"v_mfma_f32_16x16x32_bf16 v[20:23], a[160:163], a[136:139], v[20:23]\n"
"s_mov_b64 exec, 1\n"
"global_atomic_add v250, v252, s[30:31] offset:0 sc1\n"
"s_mov_b64 exec, -1\n"
"v_mfma_f32_16x16x32_bf16 v[20:23], a[164:167], a[140:143], v[20:23]\n"
"s_waitcnt lgkmcnt(2)\n"
"v_mfma_f32_16x16x32_bf16 v[24:27], a[160:163], a[144:147], v[24:27]\n"
"v_add_u32 v249, 49152, v248\n"
"v_mfma_f32_16x16x32_bf16 v[24:27], a[164:167], a[148:151], v[24:27]\n"
"s_waitcnt lgkmcnt(0)\n"
"v_mfma_f32_16x16x32_bf16 v[28:31], a[160:163], a[152:155], v[28:31]\n"
"global_load_dwordx4 a[160:163], v246, s[16:17] offset:-4096 sc1\n"
"v_mfma_f32_16x16x32_bf16 v[28:31], a[164:167], a[156:159], v[28:31]\n"
"s_waitcnt vmcnt(20)\n"
"v_mfma_f32_16x16x32_bf16 v[32:35], a[168:171], a[96:99], v[32:35]\n"
"global_load_dwordx4 a[164:167], v246, s[16:17] offset:-3072 sc1\n"
"v_mfma_f32_16x16x32_bf16 v[32:35], a[172:175], a[100:103], v[32:35]\n"
"global_store_dwordx4 v249, v[228:231], s[24:25] sc1\n"
"v_mfma_f32_16x16x32_bf16 v[36:39], a[168:171], a[104:107], v[36:39]\n"
"v_add_f32 v170, v234, v170\n"
"v_mfma_f32_16x16x32_bf16 v[36:39], a[172:175], a[108:111], v[36:39]\n"
"v_add_f32 v171, v235, v171\n"
"v_mfma_f32_16x16x32_bf16 v[40:43], a[168:171], a[112:115], v[40:43]\n"
"v_add_f32 v172, v232, v172\n"
"v_mfma_f32_16x16x32_bf16 v[40:43], a[172:175], a[116:119], v[40:43]\n"
"v_add_f32 v173, v233, v173\n"
"v_mfma_f32_16x16x32_bf16 v[44:47], a[168:171], a[120:123], v[44:47]\n"
"v_add_f32 v174, v234, v174\n"
"v_mfma_f32_16x16x32_bf16 v[44:47], a[172:175], a[124:127], v[44:47]\n"
"v_add_f32 v175, v235, v175\n"
"v_mfma_f32_16x16x32_bf16 v[48:51], a[168:171], a[128:131], v[48:51]\n"
"v_add_f32 v176, v232, v176\n"
"v_mfma_f32_16x16x32_bf16 v[48:51], a[172:175], a[132:135], v[48:51]\n"
"v_add_f32 v177, v233, v177\n"
"v_mfma_f32_16x16x32_bf16 v[52:55], a[168:171], a[136:139], v[52:55]\n"
"v_add_f32 v178, v234, v178\n"
"v_mfma_f32_16x16x32_bf16 v[52:55], a[172:175], a[140:143], v[52:55]\n"
"v_add_f32 v179, v235, v179\n"
"v_mfma_f32_16x16x32_bf16 v[56:59], a[168:171], a[144:147], v[56:59]\n"
"v_add_f32 v180, v232, v180\n"
"v_mfma_f32_16x16x32_bf16 v[56:59], a[172:175], a[148:151], v[56:59]\n"
"v_add_f32 v181, v233, v181\n"
"v_mfma_f32_16x16x32_bf16 v[60:63], a[168:171], a[152:155], v[60:63]\n"
"global_load_dwordx4 a[168:171], v246, s[16:17] offset:-2048 sc1\n"
"v_mfma_f32_16x16x32_bf16 v[60:63], a[172:175], a[156:159], v[60:63]\n"
"s_waitcnt vmcnt(21)\n"
"v_mfma_f32_16x16x32_bf16 v[64:67], a[176:179], a[96:99], v[64:67]\n"
"global_load_dwordx4 a[172:175], v246, s[16:17] offset:-1024 sc1\n"
"v_mfma_f32_16x16x32_bf16 v[64:67], a[180:183], a[100:103], v[64:67]\n"
"v_add_f32 v182, v234, v182\n"
"v_mfma_f32_16x16x32_bf16 v[68:71], a[176:179], a[104:107], v[68:71]\n"
"v_add_f32 v183, v235, v183\n"
"v_mfma_f32_16x16x32_bf16 v[68:71], a[180:183], a[108:111], v[68:71]\n"
"v_add_f32 v184, v232, v184\n"
"v_mfma_f32_16x16x32_bf16 v[72:75], a[176:179], a[112:115], v[72:75]\n"
"v_add_f32 v185, v233, v185\n"
"v_mfma_f32_16x16x32_bf16 v[72:75], a[180:183], a[116:119], v[72:75]\n"
"v_add_u32 v238, s66, v238\n"
"v_mfma_f32_16x16x32_bf16 v[76:79], a[176:179], a[120:123], v[76:79]\n"
"global_load_lds_dwordx4 v238, s[26:27] offset:3072 sc1\n"
"v_mfma_f32_16x16x32_bf16 v[76:79], a[180:183], a[124:127], v[76:79]\n"
"v_add_f32 v186, v234, v186\n"
"v_mfma_f32_16x16x32_bf16 v[80:83], a[176:179], a[128:131], v[80:83]\n"
"ds_read_b128 v[224:227], v247 offset:32768\n"
"v_mfma_f32_16x16x32_bf16 v[80:83], a[180:183], a[132:135], v[80:83]\n"
"v_add_f32 v187, v235, v187\n"
"v_mfma_f32_16x16x32_bf16 v[84:87], a[176:179], a[136:139], v[84:87]\n"
"ds_read_b128 v[228:231], v247 offset:33792\n"
"v_mfma_f32_16x16x32_bf16 v[84:87], a[180:183], a[140:143], v[84:87]\n"
"v_add_f32 v188, v232, v188\n"
"v_mfma_f32_16x16x32_bf16 v[88:91], a[176:179], a[144:147], v[88:91]\n"
"v_add_f32 v189, v233, v189\n"
"v_mfma_f32_16x16x32_bf16 v[88:91], a[180:183], a[148:151], v[88:91]\n"
"v_add_f32 v190, v234, v190\n"
"v_mfma_f32_16x16x32_bf16 v[92:95], a[176:179], a[152:155], v[92:95]\n"
"global_load_dwordx4 a[176:179], v246, s[16:17] offset:0 sc1\n"
"v_mfma_f32_16x16x32_bf16 v[92:95], a[180:183], a[156:159], v[92:95]\n"
"s_waitcnt vmcnt(20)\n"
"v_mfma_f32_16x16x32_bf16 v[96:99], a[184:187], a[96:99], v[96:99]\n"
"global_load_dwordx4 a[180:183], v246, s[16:17] offset:1024 sc1\n"
"v_mfma_f32_16x16x32_bf16 v[96:99], a[188:191], a[100:103], v[96:99]\n"
"v_add_f32 v191, v235, v191\n"
"v_mfma_f32_16x16x32_bf16 v[100:103], a[184:187], a[104:107], v[100:103]\n"
"s_add_u32 s64, s60, 73728\n"
"s_addc_u32 s65, s61, 0\n"
"v_mfma_f32_16x16x32_bf16 v[100:103], a[188:191], a[108:111], v[100:103]\n"
"v_and_b32 v235, s67, v237\n"
"v_lshlrev_b32 v234, 16, v237\n"
"v_mfma_f32_16x16x32_bf16 v[104:107], a[184:187], a[112:115], v[104:107]\n"
"v_add_u32 v249, 256, v248\n"
"v_and_b32 v233, s67, v236\n"
"v_mfma_f32_16x16x32_bf16 v[104:107], a[188:191], a[116:119], v[104:107]\n"
"s_waitcnt lgkmcnt(1)\n"
"global_store_dwordx4 v249, v[224:227], s[24:25] sc1\n"
"v_mfma_f32_16x16x32_bf16 v[108:111], a[184:187], a[120:123], v[108:111]\n"
"v_lshlrev_b32 v232, 16, v236\n"
"v_mfma_f32_16x16x32_bf16 v[108:111], a[188:191], a[124:127], v[108:111]\n"
"v_lshrrev_b32 v237, 4, v247\n"
"v_mfma_f32_16x16x32_bf16 v[112:115], a[184:187], a[128:131], v[112:115]\n"
"ds_read_b128 v[224:227], v247 offset:34816\n"
"v_and_b32 v237, 0x30, v237\n"
"v_mfma_f32_16x16x32_bf16 v[112:115], a[188:191], a[132:135], v[112:115]\n"
"global_load_dwordx2 v[236:237], v237, s[18:19] offset:0\n"
"v_mfma_f32_16x16x32_bf16 v[116:119], a[184:187], a[136:139], v[116:119]\n"
"v_add_f32 v192, v232, v192\n"
"v_mfma_f32_16x16x32_bf16 v[116:119], a[188:191], a[140:143], v[116:119]\n"
"v_add_f32 v193, v233, v193\n"
"v_mfma_f32_16x16x32_bf16 v[120:123], a[184:187], a[144:147], v[120:123]\n"
"v_add_f32 v194, v234, v194\n"
"v_mfma_f32_16x16x32_bf16 v[120:123], a[188:191], a[148:151], v[120:123]\n"
"v_add_f32 v195, v235, v195\n"
"v_mfma_f32_16x16x32_bf16 v[124:127], a[184:187], a[152:155], v[124:127]\n"
"global_load_dword a184, v250, s[64:65]\n"
"global_load_dwordx4 a[184:187], v246, s[16:17] offset:2048 sc1\n"
"v_mfma_f32_16x16x32_bf16 v[124:127], a[188:191], a[156:159], v[124:127]\n"
"s_waitcnt vmcnt(22)\n"
"v_mfma_f32_16x16x32_bf16 v[128:131], a[192:195], a[96:99], v[128:131]\n"
"global_load_dwordx4 a[188:191], v246, s[16:17] offset:3072 sc1\n"
"v_mfma_f32_16x16x32_bf16 v[128:131], a[196:199], a[100:103], v[128:131]\n"
"v_add_f32 v196, v232, v196\n"
"v_mfma_f32_16x16x32_bf16 v[132:135], a[192:195], a[104:107], v[132:135]\n"
"v_add_u32 v246, 0x2000, v246\n"
"v_mfma_f32_16x16x32_bf16 v[132:135], a[196:199], a[108:111], v[132:135]\n"
"s_add_u32 m0, s42, 49152\n"
"v_add_u32 v238, 384, v248\n"
"v_mfma_f32_16x16x32_bf16 v[136:139], a[192:195], a[112:115], v[136:139]\n"
"global_load_lds_dwordx4 v238, s[26:27] offset:0 sc1\n"
"v_mfma_f32_16x16x32_bf16 v[136:139], a[196:199], a[116:119], v[136:139]\n"
"v_add_f32 v197, v233, v197\n"
"v_mfma_f32_16x16x32_bf16 v[140:143], a[192:195], a[120:123], v[140:143]\n"
"v_add_f32 v198, v234, v198\n"
"v_mfma_f32_16x16x32_bf16 v[140:143], a[196:199], a[124:127], v[140:143]\n"
"v_add_f32 v199, v235, v199\n"
"v_mfma_f32_16x16x32_bf16 v[144:147], a[192:195], a[128:131], v[144:147]\n"
"v_add_f32 v200, v232, v200\n"
"v_mfma_f32_16x16x32_bf16 v[144:147], a[196:199], a[132:135], v[144:147]\n"
"v_add_f32 v201, v233, v201\n"
"v_mfma_f32_16x16x32_bf16 v[148:151], a[192:195], a[136:139], v[148:151]\n"
"v_add_f32 v202, v234, v202\n"
"v_mfma_f32_16x16x32_bf16 v[148:151], a[196:199], a[140:143], v[148:151]\n"
"v_add_f32 v203, v235, v203\n"
"v_mfma_f32_16x16x32_bf16 v[152:155], a[192:195], a[144:147], v[152:155]\n"
"v_add_f32 v204, v232, v204\n"
"v_mfma_f32_16x16x32_bf16 v[152:155], a[196:199], a[148:151], v[152:155]\n"
"v_add_f32 v205, v233, v205\n"
"v_mfma_f32_16x16x32_bf16 v[156:159], a[192:195], a[152:155], v[156:159]\n"
"global_load_dwordx4 a[192:195], v246, s[16:17] offset:-4096 sc1\n"
"v_mfma_f32_16x16x32_bf16 v[156:159], a[196:199], a[156:159], v[156:159]\n"
"s_waitcnt vmcnt(23)\n"
"v_mfma_f32_16x16x32_bf16 v[160:163], a[200:203], a[96:99], v[160:163]\n"
"global_load_dwordx4 a[196:199], v246, s[16:17] offset:-3072 sc1\n"
"v_mfma_f32_16x16x32_bf16 v[160:163], a[204:207], a[100:103], v[160:163]\n"
"v_add_f32 v206, v234, v206\n"
"v_mfma_f32_16x16x32_bf16 v[164:167], a[200:203], a[104:107], v[164:167]\n"
"v_add_f32 v207, v235, v207\n"
"v_mfma_f32_16x16x32_bf16 v[164:167], a[204:207], a[108:111], v[164:167]\n"
"v_add_f32 v208, v232, v208\n"
"v_mfma_f32_16x16x32_bf16 v[168:171], a[200:203], a[112:115], v[168:171]\n"
"v_add_f32 v209, v233, v209\n"
"v_mfma_f32_16x16x32_bf16 v[168:171], a[204:207], a[116:119], v[168:171]\n"
"v_add_f32 v210, v234, v210\n"
"v_mfma_f32_16x16x32_bf16 v[172:175], a[200:203], a[120:123], v[172:175]\n"
"v_add_f32 v211, v235, v211\n"
"v_mfma_f32_16x16x32_bf16 v[172:175], a[204:207], a[124:127], v[172:175]\n"
"v_add_f32 v212, v232, v212\n"
"v_mfma_f32_16x16x32_bf16 v[176:179], a[200:203], a[128:131], v[176:179]\n"
"v_add_f32 v213, v233, v213\n"
"v_mfma_f32_16x16x32_bf16 v[176:179], a[204:207], a[132:135], v[176:179]\n"
"v_add_f32 v214, v234, v214\n"
"v_mfma_f32_16x16x32_bf16 v[180:183], a[200:203], a[136:139], v[180:183]\n"
"v_add_f32 v215, v235, v215\n"
"v_mfma_f32_16x16x32_bf16 v[180:183], a[204:207], a[140:143], v[180:183]\n"
"v_add_f32 v216, v232, v216\n"
"v_mfma_f32_16x16x32_bf16 v[184:187], a[200:203], a[144:147], v[184:187]\n"
"v_add_u32 v249, 16640, v248\n"
"v_mfma_f32_16x16x32_bf16 v[184:187], a[204:207], a[148:151], v[184:187]\n"
"s_waitcnt lgkmcnt(1)\n"
"global_store_dwordx4 v249, v[228:231], s[24:25] sc1\n"
"v_mfma_f32_16x16x32_bf16 v[188:191], a[200:203], a[152:155], v[188:191]\n"
"global_load_dwordx4 a[200:203], v246, s[16:17] offset:-2048 sc1\n"
"v_mfma_f32_16x16x32_bf16 v[188:191], a[204:207], a[156:159], v[188:191]\n"
"s_waitcnt vmcnt(22)\n"
"v_mfma_f32_16x16x32_bf16 v[192:195], a[208:211], a[96:99], v[192:195]\n"
"global_load_dwordx4 a[204:207], v246, s[16:17] offset:-1024 sc1\n"
"v_mfma_f32_16x16x32_bf16 v[192:195], a[212:215], a[100:103], v[192:195]\n"
"ds_read_b128 v[228:231], v247 offset:35840\n"
"v_mfma_f32_16x16x32_bf16 v[196:199], a[208:211], a[104:107], v[196:199]\n"
"v_add_u32 v238, s66, v238\n"
"v_mfma_f32_16x16x32_bf16 v[196:199], a[212:215], a[108:111], v[196:199]\n"
"global_load_lds_dwordx4 v238, s[26:27] offset:1024 sc1\n"
"v_mfma_f32_16x16x32_bf16 v[200:203], a[208:211], a[112:115], v[200:203]\n"
"v_mfma_f32_16x16x32_bf16 v[200:203], a[212:215], a[116:119], v[200:203]\n"
"v_mfma_f32_16x16x32_bf16 v[204:207], a[208:211], a[120:123], v[204:207]\n"
"v_mfma_f32_16x16x32_bf16 v[204:207], a[212:215], a[124:127], v[204:207]\n"
"v_mfma_f32_16x16x32_bf16 v[208:211], a[208:211], a[128:131], v[208:211]\n"
"v_mfma_f32_16x16x32_bf16 v[208:211], a[212:215], a[132:135], v[208:211]\n"
"v_mfma_f32_16x16x32_bf16 v[212:215], a[208:211], a[136:139], v[212:215]\n"
"v_mfma_f32_16x16x32_bf16 v[212:215], a[212:215], a[140:143], v[212:215]\n"
"v_mfma_f32_16x16x32_bf16 v[216:219], a[208:211], a[144:147], v[216:219]\n"
"v_mfma_f32_16x16x32_bf16 v[216:219], a[212:215], a[148:151], v[216:219]\n"
"v_mfma_f32_16x16x32_bf16 v[220:223], a[208:211], a[152:155], v[220:223]\n"
"global_load_dwordx4 a[208:211], v246, s[16:17] offset:0 sc1\n"
"v_mfma_f32_16x16x32_bf16 v[220:223], a[212:215], a[156:159], v[220:223]\n"
"s_waitcnt vmcnt(23)\n"
"v_mfma_f32_16x16x32_bf16 a[0:3], a[216:219], a[96:99], a[0:3]\n"
"global_load_dwordx4 a[212:215], v246, s[16:17] offset:1024 sc1\n"
"ds_read_b128 a[96:99], v242 offset:0\n"
"v_mfma_f32_16x16x32_bf16 a[0:3], a[220:223], a[100:103], a[0:3]\n"
"ds_read_b128 a[100:103], v243 offset:0\n"
"v_mfma_f32_16x16x32_bf16 a[4:7], a[216:219], a[104:107], a[4:7]\n"
"ds_read_b128 a[104:107], v242 offset:2048\n"
"v_mfma_f32_16x16x32_bf16 a[4:7], a[220:223], a[108:111], a[4:7]\n"
"ds_read_b128 a[108:111], v243 offset:2048\n"
"v_mfma_f32_16x16x32_bf16 a[8:11], a[216:219], a[112:115], a[8:11]\n"
"ds_read_b128 a[112:115], v242 offset:4096\n"
"v_mfma_f32_16x16x32_bf16 a[8:11], a[220:223], a[116:119], a[8:11]\n"
"ds_read_b128 a[116:119], v243 offset:4096\n"
"v_mfma_f32_16x16x32_bf16 a[12:15], a[216:219], a[120:123], a[12:15]\n"
"ds_read_b128 a[120:123], v242 offset:6144\n"
"v_mfma_f32_16x16x32_bf16 a[12:15], a[220:223], a[124:127], a[12:15]\n"
"ds_read_b128 a[124:127], v243 offset:6144\n"
"v_mfma_f32_16x16x32_bf16 a[16:19], a[216:219], a[128:131], a[16:19]\n"
"ds_read_b128 a[128:131], v242 offset:8192\n"
"v_mfma_f32_16x16x32_bf16 a[16:19], a[220:223], a[132:135], a[16:19]\n"
"ds_read_b128 a[132:135], v243 offset:8192\n"
"v_mfma_f32_16x16x32_bf16 a[20:23], a[216:219], a[136:139], a[20:23]\n"
"ds_read_b128 a[136:139], v242 offset:10240\n"
"v_mfma_f32_16x16x32_bf16 a[20:23], a[220:223], a[140:143], a[20:23]\n"
"ds_read_b128 a[140:143], v243 offset:10240\n"
"v_mfma_f32_16x16x32_bf16 a[24:27], a[216:219], a[144:147], a[24:27]\n"
"ds_read_b128 a[144:147], v242 offset:12288\n"
"v_mfma_f32_16x16x32_bf16 a[24:27], a[220:223], a[148:151], a[24:27]\n"
"ds_read_b128 a[148:151], v243 offset:12288\n"
"v_mfma_f32_16x16x32_bf16 a[28:31], a[216:219], a[152:155], a[28:31]\n"
"global_load_dwordx4 a[216:219], v246, s[16:17] offset:2048 sc1\n"
"ds_read_b128 a[152:155], v242 offset:14336\n"
"v_mfma_f32_16x16x32_bf16 a[28:31], a[220:223], a[156:159], a[28:31]\n"
"ds_read_b128 a[156:159], v243 offset:14336\n"
"s_waitcnt vmcnt(21) lgkmcnt(14)\n"
"v_mfma_f32_16x16x32_bf16 v[0:3], a[160:163], a[96:99], v[0:3]\n"
"global_load_dwordx4 a[220:223], v246, s[16:17] offset:3072 sc1\n"
"v_mfma_f32_16x16x32_bf16 v[0:3], a[164:167], a[100:103], v[0:3]\n"
"s_waitcnt lgkmcnt(12)\n"
"v_mfma_f32_16x16x32_bf16 v[4:7], a[160:163], a[104:107], v[4:7]\n"
"v_add_u32 v246, 0x2000, v246\n"
"v_mfma_f32_16x16x32_bf16 v[4:7], a[164:167], a[108:111], v[4:7]\n"
"s_waitcnt lgkmcnt(10)\n"
"v_mfma_f32_16x16x32_bf16 v[8:11], a[160:163], a[112:115], v[8:11]\n"
"v_add_u32 v249, 33024, v248\n"
"v_mfma_f32_16x16x32_bf16 v[8:11], a[164:167], a[116:119], v[8:11]\n"
"s_waitcnt lgkmcnt(8)\n"
"v_mfma_f32_16x16x32_bf16 v[12:15], a[160:163], a[120:123], v[12:15]\n"
"global_store_dwordx4 v249, v[224:227], s[24:25] sc1\n"
"v_mfma_f32_16x16x32_bf16 v[12:15], a[164:167], a[124:127], v[12:15]\n"
"s_waitcnt lgkmcnt(6)\n"
"v_mfma_f32_16x16x32_bf16 v[16:19], a[160:163], a[128:131], v[16:19]\n"
"v_add_u32 v238, s66, v238\n"
"v_mfma_f32_16x16x32_bf16 v[16:19], a[164:167], a[132:135], v[16:19]\n"
"s_waitcnt lgkmcnt(4)\n"
"v_mfma_f32_16x16x32_bf16 v[20:23], a[160:163], a[136:139], v[20:23]\n"
"global_load_lds_dwordx4 v238, s[26:27] offset:2048 sc1\n"
"v_mfma_f32_16x16x32_bf16 v[20:23], a[164:167], a[140:143], v[20:23]\n"
"s_waitcnt lgkmcnt(2)\n"
"v_mfma_f32_16x16x32_bf16 v[24:27], a[160:163], a[144:147], v[24:27]\n"
"global_load_dword v239, v250, s[34:35] sc1\n"
"v_mfma_f32_16x16x32_bf16 v[24:27], a[164:167], a[148:151], v[24:27]\n"
"s_waitcnt lgkmcnt(0)\n"
"v_mfma_f32_16x16x32_bf16 v[28:31], a[160:163], a[152:155], v[28:31]\n"
"global_load_dwordx4 a[160:163], v246, s[16:17] offset:-4096 sc1\n"
"v_mfma_f32_16x16x32_bf16 v[28:31], a[164:167], a[156:159], v[28:31]\n"
"s_waitcnt vmcnt(23)\n"
"v_mfma_f32_16x16x32_bf16 v[32:35], a[168:171], a[96:99], v[32:35]\n"
"global_load_dwordx4 a[164:167], v246, s[16:17] offset:-3072 sc1\n"
"v_mfma_f32_16x16x32_bf16 v[32:35], a[172:175], a[100:103], v[32:35]\n"
"v_add_f32 v217, v233, v217\n"
"v_mfma_f32_16x16x32_bf16 v[36:39], a[168:171], a[104:107], v[36:39]\n"
"v_add_f32 v218, v234, v218\n"
"v_mfma_f32_16x16x32_bf16 v[36:39], a[172:175], a[108:111], v[36:39]\n"
"v_add_f32 v219, v235, v219\n"
"v_mfma_f32_16x16x32_bf16 v[40:43], a[168:171], a[112:115], v[40:43]\n"
"v_add_f32 v220, v232, v220\n"
"v_mfma_f32_16x16x32_bf16 v[40:43], a[172:175], a[116:119], v[40:43]\n"
"v_add_f32 v221, v233, v221\n"
"v_mfma_f32_16x16x32_bf16 v[44:47], a[168:171], a[120:123], v[44:47]\n"
"v_add_f32 v222, v234, v222\n"
"v_mfma_f32_16x16x32_bf16 v[44:47], a[172:175], a[124:127], v[44:47]\n"
"v_add_f32 v223, v235, v223\n"
"v_mfma_f32_16x16x32_bf16 v[48:51], a[168:171], a[128:131], v[48:51]\n"
"v_mfma_f32_16x16x32_bf16 v[48:51], a[172:175], a[132:135], v[48:51]\n"
"v_mfma_f32_16x16x32_bf16 v[52:55], a[168:171], a[136:139], v[52:55]\n"
"v_mfma_f32_16x16x32_bf16 v[52:55], a[172:175], a[140:143], v[52:55]\n"
"v_mfma_f32_16x16x32_bf16 v[56:59], a[168:171], a[144:147], v[56:59]\n"
"v_mfma_f32_16x16x32_bf16 v[56:59], a[172:175], a[148:151], v[56:59]\n"
"v_mfma_f32_16x16x32_bf16 v[60:63], a[168:171], a[152:155], v[60:63]\n"
"global_load_dwordx4 a[168:171], v246, s[16:17] offset:-2048 sc1\n"
"v_mfma_f32_16x16x32_bf16 v[60:63], a[172:175], a[156:159], v[60:63]\n"
"s_waitcnt vmcnt(22)\n"
"v_mfma_f32_16x16x32_bf16 v[64:67], a[176:179], a[96:99], v[64:67]\n"
"global_load_dwordx4 a[172:175], v246, s[16:17] offset:-1024 sc1\n"
"v_mfma_f32_16x16x32_bf16 v[64:67], a[180:183], a[100:103], v[64:67]\n"
"v_mfma_f32_16x16x32_bf16 v[68:71], a[176:179], a[104:107], v[68:71]\n"
"v_mfma_f32_16x16x32_bf16 v[68:71], a[180:183], a[108:111], v[68:71]\n"
"v_mfma_f32_16x16x32_bf16 v[72:75], a[176:179], a[112:115], v[72:75]\n"
"v_mfma_f32_16x16x32_bf16 v[72:75], a[180:183], a[116:119], v[72:75]\n"
"v_mfma_f32_16x16x32_bf16 v[76:79], a[176:179], a[120:123], v[76:79]\n"
"v_mfma_f32_16x16x32_bf16 v[76:79], a[180:183], a[124:127], v[76:79]\n"
"v_add_u32 v238, s66, v238\n"
"v_mfma_f32_16x16x32_bf16 v[80:83], a[176:179], a[128:131], v[80:83]\n"
"global_load_lds_dwordx4 v238, s[26:27] offset:3072 sc1\n"
"v_mfma_f32_16x16x32_bf16 v[80:83], a[180:183], a[132:135], v[80:83]\n"
"v_mfma_f32_16x16x32_bf16 v[84:87], a[176:179], a[136:139], v[84:87]\n"
"s_waitcnt vmcnt(22)\n"
"v_and_b32 v235, s67, v237\n"
"v_lshlrev_b32 v234, 16, v237\n"
"v_mfma_f32_16x16x32_bf16 v[84:87], a[180:183], a[140:143], v[84:87]\n"
"v_and_b32 v233, s67, v236\n"
"v_lshlrev_b32 v232, 16, v236\n"
"v_mfma_f32_16x16x32_bf16 v[88:91], a[176:179], a[144:147], v[88:91]\n"
"v_add_u32 v249, 49408, v248\n"
"v_lshrrev_b32 v237, 4, v247\n"
"v_mfma_f32_16x16x32_bf16 v[88:91], a[180:183], a[148:151], v[88:91]\n"
"global_store_dwordx4 v249, v[228:231], s[24:25] sc1\n"
"v_mfma_f32_16x16x32_bf16 v[92:95], a[176:179], a[152:155], v[92:95]\n"
"global_load_dwordx4 a[176:179], v246, s[16:17] offset:0 sc1\n"
"v_and_b32 v237, 0x30, v237\n"
"v_mfma_f32_16x16x32_bf16 v[92:95], a[180:183], a[156:159], v[92:95]\n"
"global_load_dwordx2 v[236:237], v237, s[18:19] offset:8\n"
"s_waitcnt vmcnt(22)\n"
"v_mfma_f32_16x16x32_bf16 v[96:99], a[184:187], a[96:99], v[96:99]\n"
"global_load_dwordx4 a[180:183], v246, s[16:17] offset:1024 sc1\n"
"v_mfma_f32_16x16x32_bf16 v[96:99], a[188:191], a[100:103], v[96:99]\n"
"v_xor_b32 v247, 0x10000, v247\n"
"v_mfma_f32_16x16x32_bf16 v[100:103], a[184:187], a[104:107], v[100:103]\n"
"v_add_f32 v0, v232, v0\n"
"v_mfma_f32_16x16x32_bf16 v[100:103], a[188:191], a[108:111], v[100:103]\n"
"v_add_f32 v1, v233, v1\n"
"v_mfma_f32_16x16x32_bf16 v[104:107], a[184:187], a[112:115], v[104:107]\n"
"v_add_f32 v2, v234, v2\n"
"v_mfma_f32_16x16x32_bf16 v[104:107], a[188:191], a[116:119], v[104:107]\n"
"v_add_f32 v3, v235, v3\n"
"v_mfma_f32_16x16x32_bf16 v[108:111], a[184:187], a[120:123], v[108:111]\n"
"v_add_f32 v4, v232, v4\n"
"v_mfma_f32_16x16x32_bf16 v[108:111], a[188:191], a[124:127], v[108:111]\n"
"v_add_f32 v5, v233, v5\n"
"v_mfma_f32_16x16x32_bf16 v[112:115], a[184:187], a[128:131], v[112:115]\n"
"v_add_f32 v6, v234, v6\n"
"v_mfma_f32_16x16x32_bf16 v[112:115], a[188:191], a[132:135], v[112:115]\n"
"v_add_f32 v7, v235, v7\n"
"v_mfma_f32_16x16x32_bf16 v[116:119], a[184:187], a[136:139], v[116:119]\n"
"v_add_f32 v8, v232, v8\n"
"v_mfma_f32_16x16x32_bf16 v[116:119], a[188:191], a[140:143], v[116:119]\n"
"v_add_f32 v9, v233, v9\n"
"v_mfma_f32_16x16x32_bf16 v[120:123], a[184:187], a[144:147], v[120:123]\n"
"v_add_f32 v10, v234, v10\n"
"v_mfma_f32_16x16x32_bf16 v[120:123], a[188:191], a[148:151], v[120:123]\n"
"v_add_f32 v11, v235, v11\n"
"v_mfma_f32_16x16x32_bf16 v[124:127], a[184:187], a[152:155], v[124:127]\n"
"global_load_dwordx4 a[184:187], v246, s[16:17] offset:2048 sc1\n"
"v_mfma_f32_16x16x32_bf16 v[124:127], a[188:191], a[156:159], v[124:127]\n"
"s_waitcnt vmcnt(21)\n"
"v_mfma_f32_16x16x32_bf16 v[128:131], a[192:195], a[96:99], v[128:131]\n"
"global_load_dwordx4 a[188:191], v246, s[16:17] offset:3072 sc1\n"
"v_mfma_f32_16x16x32_bf16 v[128:131], a[196:199], a[100:103], v[128:131]\n"
"v_add_f32 v12, v232, v12\n"
"v_mfma_f32_16x16x32_bf16 v[132:135], a[192:195], a[104:107], v[132:135]\n"
"v_add_u32 v246, 0x2000, v246\n"
"v_mfma_f32_16x16x32_bf16 v[132:135], a[196:199], a[108:111], v[132:135]\n"
"v_add_f32 v13, v233, v13\n"
"v_mfma_f32_16x16x32_bf16 v[136:139], a[192:195], a[112:115], v[136:139]\n"
"v_add_f32 v14, v234, v14\n"
"v_mfma_f32_16x16x32_bf16 v[136:139], a[196:199], a[116:119], v[136:139]\n"
"v_add_f32 v15, v235, v15\n"
"v_mfma_f32_16x16x32_bf16 v[140:143], a[192:195], a[120:123], v[140:143]\n"
"v_add_f32 v16, v232, v16\n"
"v_mfma_f32_16x16x32_bf16 v[140:143], a[196:199], a[124:127], v[140:143]\n"
"v_add_f32 v17, v233, v17\n"
"v_mfma_f32_16x16x32_bf16 v[144:147], a[192:195], a[128:131], v[144:147]\n"
"ds_read_b128 v[224:227], v247 offset:0\n"
"v_mfma_f32_16x16x32_bf16 v[144:147], a[196:199], a[132:135], v[144:147]\n"
"s_waitcnt vmcnt(11)\n"
"v_readfirstlane_b32 s46, v239\n"
"v_mfma_f32_16x16x32_bf16 v[148:151], a[192:195], a[136:139], v[148:151]\n"
"ds_read_b128 v[228:231], v247 offset:1024\n"
"v_mfma_f32_16x16x32_bf16 v[148:151], a[196:199], a[140:143], v[148:151]\n"
"v_add_f32 v18, v234, v18\n"
"v_mfma_f32_16x16x32_bf16 v[152:155], a[192:195], a[144:147], v[152:155]\n"
"s_sub_u32 s46, s46, s44\n"
"v_mfma_f32_16x16x32_bf16 v[152:155], a[196:199], a[148:151], v[152:155]\n"
"s_cmp_ge_u32 s46, 113792\n"
"s_cbranch_scc0 .Lslow7_%=\n"
".Lback7_%=:\n"
"v_mfma_f32_16x16x32_bf16 v[156:159], a[192:195], a[152:155], v[156:159]\n"
"global_load_dwordx4 a[192:195], v246, s[16:17] offset:-4096 sc1\n"
"v_mfma_f32_16x16x32_bf16 v[156:159], a[196:199], a[156:159], v[156:159]\n"
"v_mfma_f32_16x16x32_bf16 v[160:163], a[200:203], a[96:99], v[160:163]\n"
"global_load_dwordx4 a[196:199], v246, s[16:17] offset:-3072 sc1\n"
"v_mfma_f32_16x16x32_bf16 v[160:163], a[204:207], a[100:103], v[160:163]\n"
"s_add_u32 m0, s42, 81920\n"
"v_add_u32 v238, 640, v248\n"
"v_mfma_f32_16x16x32_bf16 v[164:167], a[200:203], a[104:107], v[164:167]\n"
"global_load_lds_dwordx4 v238, s[26:27] offset:0 sc1\n"
"v_mfma_f32_16x16x32_bf16 v[164:167], a[204:207], a[108:111], v[164:167]\n"
"v_add_f32 v19, v235, v19\n"
"v_mfma_f32_16x16x32_bf16 v[168:171], a[200:203], a[112:115], v[168:171]\n"
"v_add_u32 v249, 512, v248\n"
"v_mfma_f32_16x16x32_bf16 v[168:171], a[204:207], a[116:119], v[168:171]\n"
"s_waitcnt lgkmcnt(1)\n"
"global_store_dwordx4 v249, v[224:227], s[24:25] sc1\n"
"v_mfma_f32_16x16x32_bf16 v[172:175], a[200:203], a[120:123], v[172:175]\n"
"v_add_f32 v20, v232, v20\n"
"v_mfma_f32_16x16x32_bf16 v[172:175], a[204:207], a[124:127], v[172:175]\n"
"v_add_f32 v21, v233, v21\n"
"v_mfma_f32_16x16x32_bf16 v[176:179], a[200:203], a[128:131], v[176:179]\n"
"ds_read_b128 v[224:227], v247 offset:2048\n"
"v_mfma_f32_16x16x32_bf16 v[176:179], a[204:207], a[132:135], v[176:179]\n"
"v_add_f32 v22, v234, v22\n"
"v_mfma_f32_16x16x32_bf16 v[180:183], a[200:203], a[136:139], v[180:183]\n"
"v_add_f32 v23, v235, v23\n"
"v_mfma_f32_16x16x32_bf16 v[180:183], a[204:207], a[140:143], v[180:183]\n"
"v_add_f32 v24, v232, v24\n"
"v_mfma_f32_16x16x32_bf16 v[184:187], a[200:203], a[144:147], v[184:187]\n"
"v_add_f32 v25, v233, v25\n"
"v_mfma_f32_16x16x32_bf16 v[184:187], a[204:207], a[148:151], v[184:187]\n"
"v_add_f32 v26, v234, v26\n"
"v_mfma_f32_16x16x32_bf16 v[188:191], a[200:203], a[152:155], v[188:191]\n"
"global_load_dwordx4 a[200:203], v246, s[16:17] offset:-2048 sc1\n"
"v_mfma_f32_16x16x32_bf16 v[188:191], a[204:207], a[156:159], v[188:191]\n"
"v_mfma_f32_16x16x32_bf16 v[192:195], a[208:211], a[96:99], v[192:195]\n"
"global_load_dwordx4 a[204:207], v246, s[16:17] offset:-1024 sc1\n"
"v_mfma_f32_16x16x32_bf16 v[192:195], a[212:215], a[100:103], v[192:195]\n"
"v_add_f32 v27, v235, v27\n"
"v_mfma_f32_16x16x32_bf16 v[196:199], a[208:211], a[104:107], v[196:199]\n"
"v_add_f32 v28, v232, v28\n"
"v_mfma_f32_16x16x32_bf16 v[196:199], a[212:215], a[108:111], v[196:199]\n"
"v_add_f32 v29, v233, v29\n"
"v_mfma_f32_16x16x32_bf16 v[200:203], a[208:211], a[112:115], v[200:203]\n"
"v_add_f32 v30, v234, v30\n"
"v_mfma_f32_16x16x32_bf16 v[200:203], a[212:215], a[116:119], v[200:203]\n"
"v_add_f32 v31, v235, v31\n"
"v_mfma_f32_16x16x32_bf16 v[204:207], a[208:211], a[120:123], v[204:207]\n"
"v_mfma_f32_16x16x32_bf16 v[204:207], a[212:215], a[124:127], v[204:207]\n"
"v_mfma_f32_16x16x32_bf16 v[208:211], a[208:211], a[128:131], v[208:211]\n"
"v_mfma_f32_16x16x32_bf16 v[208:211], a[212:215], a[132:135], v[208:211]\n"
"v_mfma_f32_16x16x32_bf16 v[212:215], a[208:211], a[136:139], v[212:215]\n"
"v_mfma_f32_16x16x32_bf16 v[212:215], a[212:215], a[140:143], v[212:215]\n"
"v_mfma_f32_16x16x32_bf16 v[216:219], a[208:211], a[144:147], v[216:219]\n"
"v_mfma_f32_16x16x32_bf16 v[216:219], a[212:215], a[148:151], v[216:219]\n"
"v_mfma_f32_16x16x32_bf16 v[220:223], a[208:211], a[152:155], v[220:223]\n"
"global_load_dwordx4 a[208:211], v246, s[16:17] offset:0 sc1\n"
"v_mfma_f32_16x16x32_bf16 v[220:223], a[212:215], a[156:159], v[220:223]\n"
"v_mfma_f32_16x16x32_bf16 a[0:3], a[216:219], a[96:99], a[0:3]\n"
"global_load_dwordx4 a[212:215], v246, s[16:17] offset:1024 sc1\n"
"ds_read_b128 a[96:99], v242 offset:32768\n"
"v_mfma_f32_16x16x32_bf16 a[0:3], a[220:223], a[100:103], a[0:3]\n"
"ds_read_b128 a[100:103], v243 offset:32768\n"
"v_mfma_f32_16x16x32_bf16 a[4:7], a[216:219], a[104:107], a[4:7]\n"
"ds_read_b128 a[104:107], v242 offset:34816\n"
"v_mfma_f32_16x16x32_bf16 a[4:7], a[220:223], a[108:111], a[4:7]\n"
"ds_read_b128 a[108:111], v243 offset:34816\n"
"v_mfma_f32_16x16x32_bf16 a[8:11], a[216:219], a[112:115], a[8:11]\n"
"ds_read_b128 a[112:115], v242 offset:36864\n"
"v_mfma_f32_16x16x32_bf16 a[8:11], a[220:223], a[116:119], a[8:11]\n"
"ds_read_b128 a[116:119], v243 offset:36864\n"
"v_mfma_f32_16x16x32_bf16 a[12:15], a[216:219], a[120:123], a[12:15]\n"
"ds_read_b128 a[120:123], v242 offset:38912\n"
"v_mfma_f32_16x16x32_bf16 a[12:15], a[220:223], a[124:127], a[12:15]\n"
"ds_read_b128 a[124:127], v243 offset:38912\n"
"v_mfma_f32_16x16x32_bf16 a[16:19], a[216:219], a[128:131], a[16:19]\n"
"ds_read_b128 a[128:131], v242 offset:40960\n"
"v_mfma_f32_16x16x32_bf16 a[16:19], a[220:223], a[132:135], a[16:19]\n"
"ds_read_b128 a[132:135], v243 offset:40960\n"
"v_mfma_f32_16x16x32_bf16 a[20:23], a[216:219], a[136:139], a[20:23]\n"
"ds_read_b128 a[136:139], v242 offset:43008\n"
"v_mfma_f32_16x16x32_bf16 a[20:23], a[220:223], a[140:143], a[20:23]\n"
"ds_read_b128 a[140:143], v243 offset:43008\n"
"v_mfma_f32_16x16x32_bf16 a[24:27], a[216:219], a[144:147], a[24:27]\n"
"ds_read_b128 a[144:147], v242 offset:45056\n"
"v_mfma_f32_16x16x32_bf16 a[24:27], a[220:223], a[148:151], a[24:27]\n"
"ds_read_b128 a[148:151], v243 offset:45056\n"
"v_mfma_f32_16x16x32_bf16 a[28:31], a[216:219], a[152:155], a[28:31]\n"
"global_load_dwordx4 a[216:219], v246, s[16:17] offset:2048 sc1\n"
"ds_read_b128 a[152:155], v242 offset:47104\n"
"v_mfma_f32_16x16x32_bf16 a[28:31], a[220:223], a[156:159], a[28:31]\n"
"ds_read_b128 a[156:159], v243 offset:47104\n"
"s_waitcnt vmcnt(18) lgkmcnt(14)\n"
"s_nop 0\n"
".Lpg18_%=:\n"
"s_cbranch_execz .Lpg22_%=\n"
"v_mfma_f32_16x16x32_bf16 v[0:3], a[160:163], a[96:99], v[0:3]\n"
"global_load_dwordx4 a[220:223], v246, s[16:17] offset:3072 sc1\n"
"v_mfma_f32_16x16x32_bf16 v[0:3], a[164:167], a[100:103], v[0:3]\n"
"s_waitcnt lgkmcnt(12)\n"
"v_mfma_f32_16x16x32_bf16 v[4:7], a[160:163], a[104:107], v[4:7]\n"
"v_add_u32 v246, 0x2000, v246\n"
"v_mfma_f32_16x16x32_bf16 v[4:7], a[164:167], a[108:111], v[4:7]\n"
"s_waitcnt lgkmcnt(10)\n"
"v_mfma_f32_16x16x32_bf16 v[8:11], a[160:163], a[112:115], v[8:11]\n"
"v_add_u32 v249, 16896, v248\n"
"v_mfma_f32_16x16x32_bf16 v[8:11], a[164:167], a[116:119], v[8:11]\n"
"s_waitcnt lgkmcnt(8)\n"
"v_mfma_f32_16x16x32_bf16 v[12:15], a[160:163], a[120:123], v[12:15]\n"
"global_store_dwordx4 v249, v[228:231], s[24:25] sc1\n"
"v_mfma_f32_16x16x32_bf16 v[12:15], a[164:167], a[124:127], v[12:15]\n"
"s_waitcnt lgkmcnt(6)\n"
"v_mfma_f32_16x16x32_bf16 v[16:19], a[160:163], a[128:131], v[16:19]\n"
"v_add_u32 v238, s66, v238\n"
"v_mfma_f32_16x16x32_bf16 v[16:19], a[164:167], a[132:135], v[16:19]\n"
"s_waitcnt lgkmcnt(4)\n"
"v_mfma_f32_16x16x32_bf16 v[20:23], a[160:163], a[136:139], v[20:23]\n"
"ds_read_b128 v[228:231], v247 offset:3072\n"
"v_mfma_f32_16x16x32_bf16 v[20:23], a[164:167], a[140:143], v[20:23]\n"
"s_waitcnt lgkmcnt(3)\n"
"v_mfma_f32_16x16x32_bf16 v[24:27], a[160:163], a[144:147], v[24:27]\n"
"global_load_lds_dwordx4 v238, s[26:27] offset:1024 sc1\n"
"v_mfma_f32_16x16x32_bf16 v[24:27], a[164:167], a[148:151], v[24:27]\n"
"s_waitcnt lgkmcnt(1)\n"
"v_mfma_f32_16x16x32_bf16 v[28:31], a[160:163], a[152:155], v[28:31]\n"
"global_load_dwordx4 a[160:163], v246, s[16:17] offset:-4096 sc1\n"
"v_mfma_f32_16x16x32_bf16 v[28:31], a[164:167], a[156:159], v[28:31]\n"
"s_waitcnt vmcnt(20)\n"
"v_mfma_f32_16x16x32_bf16 v[32:35], a[168:171], a[96:99], v[32:35]\n"
"global_load_dwordx4 a[164:167], v246, s[16:17] offset:-3072 sc1\n"
"v_mfma_f32_16x16x32_bf16 v[32:35], a[172:175], a[100:103], v[32:35]\n"
"v_mfma_f32_16x16x32_bf16 v[36:39], a[168:171], a[104:107], v[36:39]\n"
"v_mfma_f32_16x16x32_bf16 v[36:39], a[172:175], a[108:111], v[36:39]\n"
"v_mfma_f32_16x16x32_bf16 v[40:43], a[168:171], a[112:115], v[40:43]\n"
"v_mfma_f32_16x16x32_bf16 v[40:43], a[172:175], a[116:119], v[40:43]\n"
"v_mfma_f32_16x16x32_bf16 v[44:47], a[168:171], a[120:123], v[44:47]\n"
"v_mfma_f32_16x16x32_bf16 v[44:47], a[172:175], a[124:127], v[44:47]\n"
"v_mfma_f32_16x16x32_bf16 v[48:51], a[168:171], a[128:131], v[48:51]\n"
"v_mfma_f32_16x16x32_bf16 v[48:51], a[172:175], a[132:135], v[48:51]\n"
"v_mfma_f32_16x16x32_bf16 v[52:55], a[168:171], a[136:139], v[52:55]\n"
"v_mfma_f32_16x16x32_bf16 v[52:55], a[172:175], a[140:143], v[52:55]\n"
"v_mfma_f32_16x16x32_bf16 v[56:59], a[168:171], a[144:147], v[56:59]\n"
"v_mfma_f32_16x16x32_bf16 v[56:59], a[172:175], a[148:151], v[56:59]\n"
"v_mfma_f32_16x16x32_bf16 v[60:63], a[168:171], a[152:155], v[60:63]\n"
"global_load_dwordx4 a[168:171], v246, s[16:17] offset:-2048 sc1\n"
"v_mfma_f32_16x16x32_bf16 v[60:63], a[172:175], a[156:159], v[60:63]\n"
"s_waitcnt vmcnt(17)\n"
"v_mfma_f32_16x16x32_bf16 v[64:67], a[176:179], a[96:99], v[64:67]\n"
"global_load_dwordx4 a[172:175], v246, s[16:17] offset:-1024 sc1\n"
"v_and_b32 v235, s67, v237\n"
"v_mfma_f32_16x16x32_bf16 v[64:67], a[180:183], a[100:103], v[64:67]\n"
"v_lshlrev_b32 v234, 16, v237\n"
"v_and_b32 v233, s67, v236\n"
"v_mfma_f32_16x16x32_bf16 v[68:71], a[176:179], a[104:107], v[68:71]\n"
"v_lshlrev_b32 v232, 16, v236\n"
"v_mfma_f32_16x16x32_bf16 v[68:71], a[180:183], a[108:111], v[68:71]\n"
"v_lshrrev_b32 v237, 4, v247\n"
"v_mfma_f32_16x16x32_bf16 v[72:75], a[176:179], a[112:115], v[72:75]\n"
"v_add_u32 v249, 33280, v248\n"
"v_and_b32 v237, 0x30, v237\n"
"v_mfma_f32_16x16x32_bf16 v[72:75], a[180:183], a[116:119], v[72:75]\n"
"global_store_dwordx4 v249, v[224:227], s[24:25] sc1\n"
"v_mfma_f32_16x16x32_bf16 v[76:79], a[176:179], a[120:123], v[76:79]\n"
"global_load_dwordx2 v[236:237], v237, s[18:19] offset:64\n"
"v_mfma_f32_16x16x32_bf16 v[76:79], a[180:183], a[124:127], v[76:79]\n"
"v_mfma_f32_16x16x32_bf16 v[80:83], a[176:179], a[128:131], v[80:83]\n"
"v_add_f32 v32, v232, v32\n"
"v_mfma_f32_16x16x32_bf16 v[80:83], a[180:183], a[132:135], v[80:83]\n"
"v_add_u32 v238, s66, v238\n"
"v_mfma_f32_16x16x32_bf16 v[84:87], a[176:179], a[136:139], v[84:87]\n"
"global_load_lds_dwordx4 v238, s[26:27] offset:2048 sc1\n"
"v_mfma_f32_16x16x32_bf16 v[84:87], a[180:183], a[140:143], v[84:87]\n"
"v_add_f32 v33, v233, v33\n"
"v_mfma_f32_16x16x32_bf16 v[88:91], a[176:179], a[144:147], v[88:91]\n"
"v_add_f32 v34, v234, v34\n"
"v_mfma_f32_16x16x32_bf16 v[88:91], a[180:183], a[148:151], v[88:91]\n"
"v_add_f32 v35, v235, v35\n"
"v_mfma_f32_16x16x32_bf16 v[92:95], a[176:179], a[152:155], v[92:95]\n"
"global_load_dwordx4 a[176:179], v246, s[16:17] offset:0 sc1\n"
"v_mfma_f32_16x16x32_bf16 v[92:95], a[180:183], a[156:159], v[92:95]\n"
"s_waitcnt vmcnt(20)\n"
"v_mfma_f32_16x16x32_bf16 v[96:99], a[184:187], a[96:99], v[96:99]\n"
"global_load_dwordx4 a[180:183], v246, s[16:17] offset:1024 sc1\n"
"v_mfma_f32_16x16x32_bf16 v[96:99], a[188:191], a[100:103], v[96:99]\n"
"v_add_f32 v36, v232, v36\n"
"v_mfma_f32_16x16x32_bf16 v[100:103], a[184:187], a[104:107], v[100:103]\n"
"v_add_f32 v37, v233, v37\n"
"v_mfma_f32_16x16x32_bf16 v[100:103], a[188:191], a[108:111], v[100:103]\n"
"v_add_f32 v38, v234, v38\n"
"v_mfma_f32_16x16x32_bf16 v[104:107], a[184:187], a[112:115], v[104:107]\n"
"v_add_f32 v39, v235, v39\n"
"v_mfma_f32_16x16x32_bf16 v[104:107], a[188:191], a[116:119], v[104:107]\n"
"v_add_f32 v40, v232, v40\n"
"v_mfma_f32_16x16x32_bf16 v[108:111], a[184:187], a[120:123], v[108:111]\n"
"s_mov_b64 exec, 1\n"
"global_atomic_add v250, v252, s[30:31] offset:0 sc1\n"
"s_mov_b64 exec, -1\n"
"v_mfma_f32_16x16x32_bf16 v[108:111], a[188:191], a[124:127], v[108:111]\n"
"v_add_f32 v41, v233, v41\n"
"v_mfma_f32_16x16x32_bf16 v[112:115], a[184:187], a[128:131], v[112:115]\n"
"v_add_f32 v42, v234, v42\n"
"v_mfma_f32_16x16x32_bf16 v[112:115], a[188:191], a[132:135], v[112:115]\n"
"v_add_f32 v43, v235, v43\n"
"v_mfma_f32_16x16x32_bf16 v[116:119], a[184:187], a[136:139], v[116:119]\n"
"v_add_f32 v44, v232, v44\n"
"v_mfma_f32_16x16x32_bf16 v[116:119], a[188:191], a[140:143], v[116:119]\n"
"v_add_f32 v45, v233, v45\n"
"v_mfma_f32_16x16x32_bf16 v[120:123], a[184:187], a[144:147], v[120:123]\n"
"v_add_f32 v46, v234, v46\n"
"v_mfma_f32_16x16x32_bf16 v[120:123], a[188:191], a[148:151], v[120:123]\n"
"v_add_f32 v47, v235, v47\n"
"v_mfma_f32_16x16x32_bf16 v[124:127], a[184:187], a[152:155], v[124:127]\n"
"global_load_dwordx4 a[184:187], v246, s[16:17] offset:2048 sc1\n"
"v_mfma_f32_16x16x32_bf16 v[124:127], a[188:191], a[156:159], v[124:127]\n"
"s_waitcnt vmcnt(21)\n"
"v_mfma_f32_16x16x32_bf16 v[128:131], a[192:195], a[96:99], v[128:131]\n"
"global_load_dwordx4 a[188:191], v246, s[16:17] offset:3072 sc1\n"
"v_mfma_f32_16x16x32_bf16 v[128:131], a[196:199], a[100:103], v[128:131]\n"
"v_add_f32 v48, v232, v48\n"
"v_mfma_f32_16x16x32_bf16 v[132:135], a[192:195], a[104:107], v[132:135]\n"
"v_add_u32 v246, 0x2000, v246\n"
"v_mfma_f32_16x16x32_bf16 v[132:135], a[196:199], a[108:111], v[132:135]\n"
"v_add_f32 v49, v233, v49\n"
"v_mfma_f32_16x16x32_bf16 v[136:139], a[192:195], a[112:115], v[136:139]\n"
"v_add_f32 v50, v234, v50\n"
"v_mfma_f32_16x16x32_bf16 v[136:139], a[196:199], a[116:119], v[136:139]\n"
"v_add_f32 v51, v235, v51\n"
"v_mfma_f32_16x16x32_bf16 v[140:143], a[192:195], a[120:123], v[140:143]\n"
"v_add_f32 v52, v232, v52\n"
"v_mfma_f32_16x16x32_bf16 v[140:143], a[196:199], a[124:127], v[140:143]\n"
"v_add_u32 v238, s66, v238\n"
"v_mfma_f32_16x16x32_bf16 v[144:147], a[192:195], a[128:131], v[144:147]\n"
"global_load_lds_dwordx4 v238, s[26:27] offset:3072 sc1\n"
"v_mfma_f32_16x16x32_bf16 v[144:147], a[196:199], a[132:135], v[144:147]\n"
"v_add_f32 v53, v233, v53\n"
"v_mfma_f32_16x16x32_bf16 v[148:151], a[192:195], a[136:139], v[148:151]\n"
"v_add_f32 v54, v234, v54\n"
"v_mfma_f32_16x16x32_bf16 v[148:151], a[196:199], a[140:143], v[148:151]\n"
"v_add_f32 v55, v235, v55\n"
"v_mfma_f32_16x16x32_bf16 v[152:155], a[192:195], a[144:147], v[152:155]\n"
"v_add_u32 v249, 49664, v248\n"
"v_mfma_f32_16x16x32_bf16 v[152:155], a[196:199], a[148:151], v[152:155]\n"
"s_waitcnt lgkmcnt(0)\n"
"global_store_dwordx4 v249, v[228:231], s[24:25] sc1\n"
"v_mfma_f32_16x16x32_bf16 v[156:159], a[192:195], a[152:155], v[156:159]\n"
"global_load_dwordx4 a[192:195], v246, s[16:17] offset:-4096 sc1\n"
"v_mfma_f32_16x16x32_bf16 v[156:159], a[196:199], a[156:159], v[156:159]\n"
"s_waitcnt vmcnt(21)\n"
"v_mfma_f32_16x16x32_bf16 v[160:163], a[200:203], a[96:99], v[160:163]\n"
"global_load_dwordx4 a[196:199], v246, s[16:17] offset:-3072 sc1\n"
"v_mfma_f32_16x16x32_bf16 v[160:163], a[204:207], a[100:103], v[160:163]\n"
"v_add_f32 v56, v232, v56\n"
"v_mfma_f32_16x16x32_bf16 v[164:167], a[200:203], a[104:107], v[164:167]\n"
"v_add_f32 v57, v233, v57\n"
"v_mfma_f32_16x16x32_bf16 v[164:167], a[204:207], a[108:111], v[164:167]\n"
"v_add_f32 v58, v234, v58\n"
"v_mfma_f32_16x16x32_bf16 v[168:171], a[200:203], a[112:115], v[168:171]\n"
"v_add_f32 v59, v235, v59\n"
"v_mfma_f32_16x16x32_bf16 v[168:171], a[204:207], a[116:119], v[168:171]\n"
"v_add_f32 v60, v232, v60\n"
"v_mfma_f32_16x16x32_bf16 v[172:175], a[200:203], a[120:123], v[172:175]\n"
"v_add_f32 v61, v233, v61\n"
"v_mfma_f32_16x16x32_bf16 v[172:175], a[204:207], a[124:127], v[172:175]\n"
"v_add_f32 v62, v234, v62\n"
"v_mfma_f32_16x16x32_bf16 v[176:179], a[200:203], a[128:131], v[176:179]\n"
"v_add_f32 v63, v235, v63\n"
"v_mfma_f32_16x16x32_bf16 v[176:179], a[204:207], a[132:135], v[176:179]\n"
"s_add_u32 s64, s60, 77824\n"
"s_addc_u32 s65, s61, 0\n"
"v_mfma_f32_16x16x32_bf16 v[180:183], a[200:203], a[136:139], v[180:183]\n"
"v_mfma_f32_16x16x32_bf16 v[180:183], a[204:207], a[140:143], v[180:183]\n"
"v_mfma_f32_16x16x32_bf16 v[184:187], a[200:203], a[144:147], v[184:187]\n"
"v_mfma_f32_16x16x32_bf16 v[184:187], a[204:207], a[148:151], v[184:187]\n"
"v_mfma_f32_16x16x32_bf16 v[188:191], a[200:203], a[152:155], v[188:191]\n"
"global_load_dword a200, v250, s[64:65]\n"
"global_load_dwordx4 a[200:203], v246, s[16:17] offset:-2048 sc1\n"
"v_mfma_f32_16x16x32_bf16 v[188:191], a[204:207], a[156:159], v[188:191]\n"
"s_waitcnt vmcnt(22)\n"
"v_mfma_f32_16x16x32_bf16 v[192:195], a[208:211], a[96:99], v[192:195]\n"
"global_load_dwordx4 a[204:207], v246, s[16:17] offset:-1024 sc1\n"
"v_mfma_f32_16x16x32_bf16 v[192:195], a[212:215], a[100:103], v[192:195]\n"
"v_mfma_f32_16x16x32_bf16 v[196:199], a[208:211], a[104:107], v[196:199]\n"
"v_mfma_f32_16x16x32_bf16 v[196:199], a[212:215], a[108:111], v[196:199]\n"
"v_mfma_f32_16x16x32_bf16 v[200:203], a[208:211], a[112:115], v[200:203]\n"
"v_mfma_f32_16x16x32_bf16 v[200:203], a[212:215], a[116:119], v[200:203]\n"
"s_add_u32 m0, s42, 114688\n"
"v_add_u32 v238, 896, v248\n"
"v_mfma_f32_16x16x32_bf16 v[204:207], a[208:211], a[120:123], v[204:207]\n"
"global_load_lds_dwordx4 v238, s[26:27] offset:0 sc1\n"
"v_mfma_f32_16x16x32_bf16 v[204:207], a[212:215], a[124:127], v[204:207]\n"
"v_mfma_f32_16x16x32_bf16 v[208:211], a[208:211], a[128:131], v[208:211]\n"
"ds_read_b128 v[224:227], v247 offset:32768\n"
"v_mfma_f32_16x16x32_bf16 v[208:211], a[212:215], a[132:135], v[208:211]\n"
"v_mfma_f32_16x16x32_bf16 v[212:215], a[208:211], a[136:139], v[212:215]\n"
"ds_read_b128 v[228:231], v247 offset:33792\n"
"v_mfma_f32_16x16x32_bf16 v[212:215], a[212:215], a[140:143], v[212:215]\n"
"v_mfma_f32_16x16x32_bf16 v[216:219], a[208:211], a[144:147], v[216:219]\n"
"v_mfma_f32_16x16x32_bf16 v[216:219], a[212:215], a[148:151], v[216:219]\n"
"s_barrier\n"
"v_mfma_f32_16x16x32_bf16 v[220:223], a[208:211], a[152:155], v[220:223]\n"
"global_load_dwordx4 a[208:211], v246, s[16:17] offset:0 sc1\n"
"v_mfma_f32_16x16x32_bf16 v[220:223], a[212:215], a[156:159], v[220:223]\n"
"s_waitcnt vmcnt(23)\n"
"v_mfma_f32_16x16x32_bf16 a[0:3], a[216:219], a[96:99], a[0:3]\n"
"global_load_dwordx4 a[212:215], v246, s[16:17] offset:1024 sc1\n"
"ds_read_b128 a[96:99], v240 offset:16384\n"
"v_mfma_f32_16x16x32_bf16 a[0:3], a[220:223], a[100:103], a[0:3]\n"
"ds_read_b128 a[100:103], v241 offset:16384\n"
"v_mfma_f32_16x16x32_bf16 a[4:7], a[216:219], a[104:107], a[4:7]\n"
"ds_read_b128 a[104:107], v240 offset:18432\n"
"v_mfma_f32_16x16x32_bf16 a[4:7], a[220:223], a[108:111], a[4:7]\n"
"ds_read_b128 a[108:111], v241 offset:18432\n"
"v_mfma_f32_16x16x32_bf16 a[8:11], a[216:219], a[112:115], a[8:11]\n"
"ds_read_b128 a[112:115], v240 offset:20480\n"
"v_mfma_f32_16x16x32_bf16 a[8:11], a[220:223], a[116:119], a[8:11]\n"
"ds_read_b128 a[116:119], v241 offset:20480\n"
"v_mfma_f32_16x16x32_bf16 a[12:15], a[216:219], a[120:123], a[12:15]\n"
"ds_read_b128 a[120:123], v240 offset:22528\n"
"v_mfma_f32_16x16x32_bf16 a[12:15], a[220:223], a[124:127], a[12:15]\n"
"ds_read_b128 a[124:127], v241 offset:22528\n"
"v_mfma_f32_16x16x32_bf16 a[16:19], a[216:219], a[128:131], a[16:19]\n"
"ds_read_b128 a[128:131], v240 offset:24576\n"
"v_mfma_f32_16x16x32_bf16 a[16:19], a[220:223], a[132:135], a[16:19]\n"
"ds_read_b128 a[132:135], v241 offset:24576\n"
"v_mfma_f32_16x16x32_bf16 a[20:23], a[216:219], a[136:139], a[20:23]\n"
"ds_read_b128 a[136:139], v240 offset:26624\n"
"v_mfma_f32_16x16x32_bf16 a[20:23], a[220:223], a[140:143], a[20:23]\n"
"ds_read_b128 a[140:143], v241 offset:26624\n"
"v_mfma_f32_16x16x32_bf16 a[24:27], a[216:219], a[144:147], a[24:27]\n"
"ds_read_b128 a[144:147], v240 offset:28672\n"
"v_mfma_f32_16x16x32_bf16 a[24:27], a[220:223], a[148:151], a[24:27]\n"
"ds_read_b128 a[148:151], v241 offset:28672\n"
"v_mfma_f32_16x16x32_bf16 a[28:31], a[216:219], a[152:155], a[28:31]\n"
"global_load_dwordx4 a[216:219], v246, s[16:17] offset:2048 sc1\n"
"ds_read_b128 a[152:155], v240 offset:30720\n"
"v_mfma_f32_16x16x32_bf16 a[28:31], a[220:223], a[156:159], a[28:31]\n"
"ds_read_b128 a[156:159], v241 offset:30720\n"
"s_waitcnt vmcnt(21) lgkmcnt(14)\n"
"v_mfma_f32_16x16x32_bf16 v[0:3], a[160:163], a[96:99], v[0:3]\n"
"global_load_dwordx4 a[220:223], v246, s[16:17] offset:3072 sc1\n"
"v_mfma_f32_16x16x32_bf16 v[0:3], a[164:167], a[100:103], v[0:3]\n"
"s_waitcnt lgkmcnt(12)\n"
"v_mfma_f32_16x16x32_bf16 v[4:7], a[160:163], a[104:107], v[4:7]\n"
"v_add_u32 v246, 0x2000, v246\n"
"v_mfma_f32_16x16x32_bf16 v[4:7], a[164:167], a[108:111], v[4:7]\n"
"s_waitcnt lgkmcnt(10)\n"
"v_mfma_f32_16x16x32_bf16 v[8:11], a[160:163], a[112:115], v[8:11]\n"
"v_add_u32 v249, 768, v248\n"
"v_mfma_f32_16x16x32_bf16 v[8:11], a[164:167], a[116:119], v[8:11]\n"
"s_waitcnt lgkmcnt(8)\n"
"v_mfma_f32_16x16x32_bf16 v[12:15], a[160:163], a[120:123], v[12:15]\n"
"global_store_dwordx4 v249, v[224:227], s[24:25] sc1\n"
"v_mfma_f32_16x16x32_bf16 v[12:15], a[164:167], a[124:127], v[12:15]\n"
"s_waitcnt lgkmcnt(6)\n"
"v_mfma_f32_16x16x32_bf16 v[16:19], a[160:163], a[128:131], v[16:19]\n"
"v_add_u32 v238, s66, v238\n"
"v_mfma_f32_16x16x32_bf16 v[16:19], a[164:167], a[132:135], v[16:19]\n"
"s_waitcnt lgkmcnt(4)\n"
"v_mfma_f32_16x16x32_bf16 v[20:23], a[160:163], a[136:139], v[20:23]\n"
"ds_read_b128 v[224:227], v247 offset:34816\n"
"v_mfma_f32_16x16x32_bf16 v[20:23], a[164:167], a[140:143], v[20:23]\n"
"s_waitcnt lgkmcnt(3)\n"
"v_mfma_f32_16x16x32_bf16 v[24:27], a[160:163], a[144:147], v[24:27]\n"
"global_load_lds_dwordx4 v238, s[26:27] offset:1024 sc1\n"
"v_mfma_f32_16x16x32_bf16 v[24:27], a[164:167], a[148:151], v[24:27]\n"
"s_waitcnt lgkmcnt(1)\n"
"v_mfma_f32_16x16x32_bf16 v[28:31], a[160:163], a[152:155], v[28:31]\n"
"global_load_dwordx4 a[160:163], v246, s[16:17] offset:-4096 sc1\n"
"v_mfma_f32_16x16x32_bf16 v[28:31], a[164:167], a[156:159], v[28:31]\n"
"s_waitcnt vmcnt(23)\n"
"v_mfma_f32_16x16x32_bf16 v[32:35], a[168:171], a[96:99], v[32:35]\n"
"global_load_dwordx4 a[164:167], v246, s[16:17] offset:-3072 sc1\n"
"v_mfma_f32_16x16x32_bf16 v[32:35], a[172:175], a[100:103], v[32:35]\n"
"global_load_dword v239, v250, s[34:35] sc1\n"
"v_mfma_f32_16x16x32_bf16 v[36:39], a[168:171], a[104:107], v[36:39]\n"
"v_mfma_f32_16x16x32_bf16 v[36:39], a[172:175], a[108:111], v[36:39]\n"
"v_mfma_f32_16x16x32_bf16 v[40:43], a[168:171], a[112:115], v[40:43]\n"
"v_mfma_f32_16x16x32_bf16 v[40:43], a[172:175], a[116:119], v[40:43]\n"
"v_mfma_f32_16x16x32_bf16 v[44:47], a[168:171], a[120:123], v[44:47]\n"
"v_mfma_f32_16x16x32_bf16 v[44:47], a[172:175], a[124:127], v[44:47]\n"
"s_waitcnt vmcnt(23)\n"
"v_and_b32 v235, s67, v237\n"
"v_lshlrev_b32 v234, 16, v237\n"
"v_mfma_f32_16x16x32_bf16 v[48:51], a[168:171], a[128:131], v[48:51]\n"
"v_and_b32 v233, s67, v236\n"
"v_lshlrev_b32 v232, 16, v236\n"
"v_mfma_f32_16x16x32_bf16 v[48:51], a[172:175], a[132:135], v[48:51]\n"
"v_lshrrev_b32 v237, 4, v247\n"
"v_mfma_f32_16x16x32_bf16 v[52:55], a[168:171], a[136:139], v[52:55]\n"
"v_and_b32 v237, 0x30, v237\n"
"v_mfma_f32_16x16x32_bf16 v[52:55], a[172:175], a[140:143], v[52:55]\n"
"global_load_dwordx2 v[236:237], v237, s[18:19] offset:72\n"
"v_mfma_f32_16x16x32_bf16 v[56:59], a[168:171], a[144:147], v[56:59]\n"
"v_add_u32 v249, 17152, v248\n"
"v_mfma_f32_16x16x32_bf16 v[56:59], a[172:175], a[148:151], v[56:59]\n"
"global_store_dwordx4 v249, v[228:231], s[24:25] sc1\n"
"v_mfma_f32_16x16x32_bf16 v[60:63], a[168:171], a[152:155], v[60:63]\n"
"global_load_dwordx4 a[168:171], v246, s[16:17] offset:-2048 sc1\n"
"v_mfma_f32_16x16x32_bf16 v[60:63], a[172:175], a[156:159], v[60:63]\n"
"s_waitcnt vmcnt(23)\n"
"v_mfma_f32_16x16x32_bf16 v[64:67], a[176:179], a[96:99], v[64:67]\n"
"global_load_dwordx4 a[172:175], v246, s[16:17] offset:-1024 sc1\n"
"v_mfma_f32_16x16x32_bf16 v[64:67], a[180:183], a[100:103], v[64:67]\n"
"ds_read_b128 v[228:231], v247 offset:35840\n"
"v_mfma_f32_16x16x32_bf16 v[68:71], a[176:179], a[104:107], v[68:71]\n"
"v_mfma_f32_16x16x32_bf16 v[68:71], a[180:183], a[108:111], v[68:71]\n"
"v_mfma_f32_16x16x32_bf16 v[72:75], a[176:179], a[112:115], v[72:75]\n"
"v_mfma_f32_16x16x32_bf16 v[72:75], a[180:183], a[116:119], v[72:75]\n"
"v_mfma_f32_16x16x32_bf16 v[76:79], a[176:179], a[120:123], v[76:79]\n"
"v_mfma_f32_16x16x32_bf16 v[76:79], a[180:183], a[124:127], v[76:79]\n"
"v_mfma_f32_16x16x32_bf16 v[80:83], a[176:179], a[128:131], v[80:83]\n"
"v_mfma_f32_16x16x32_bf16 v[80:83], a[180:183], a[132:135], v[80:83]\n"
"v_add_u32 v238, s66, v238\n"
"v_mfma_f32_16x16x32_bf16 v[84:87], a[176:179], a[136:139], v[84:87]\n"
"global_load_lds_dwordx4 v238, s[26:27] offset:2048 sc1\n"
"v_mfma_f32_16x16x32_bf16 v[84:87], a[180:183], a[140:143], v[84:87]\n"
"v_mfma_f32_16x16x32_bf16 v[88:91], a[176:179], a[144:147], v[88:91]\n"
"v_mfma_f32_16x16x32_bf16 v[88:91], a[180:183], a[148:151], v[88:91]\n"
"v_mfma_f32_16x16x32_bf16 v[92:95], a[176:179], a[152:155], v[92:95]\n"
"global_load_dwordx4 a[176:179], v246, s[16:17] offset:0 sc1\n"
"v_mfma_f32_16x16x32_bf16 v[92:95], a[180:183], a[156:159], v[92:95]\n"
"s_waitcnt vmcnt(23)\n"
"v_mfma_f32_16x16x32_bf16 v[96:99], a[184:187], a[96:99], v[96:99]\n"
"global_load_dwordx4 a[180:183], v246, s[16:17] offset:1024 sc1\n"
"v_mfma_f32_16x16x32_bf16 v[96:99], a[188:191], a[100:103], v[96:99]\n"
"v_mfma_f32_16x16x32_bf16 v[100:103], a[184:187], a[104:107], v[100:103]\n"
"v_mfma_f32_16x16x32_bf16 v[100:103], a[188:191], a[108:111], v[100:103]\n"
"v_mfma_f32_16x16x32_bf16 v[104:107], a[184:187], a[112:115], v[104:107]\n"
"v_mfma_f32_16x16x32_bf16 v[104:107], a[188:191], a[116:119], v[104:107]\n"
"v_mfma_f32_16x16x32_bf16 v[108:111], a[184:187], a[120:123], v[108:111]\n"
"v_mfma_f32_16x16x32_bf16 v[108:111], a[188:191], a[124:127], v[108:111]\n"
"v_mfma_f32_16x16x32_bf16 v[112:115], a[184:187], a[128:131], v[112:115]\n"
"v_add_f32 v64, v232, v64\n"
"v_mfma_f32_16x16x32_bf16 v[112:115], a[188:191], a[132:135], v[112:115]\n"
"v_add_f32 v65, v233, v65\n"
"v_mfma_f32_16x16x32_bf16 v[116:119], a[184:187], a[136:139], v[116:119]\n"
"v_add_f32 v66, v234, v66\n"
"v_mfma_f32_16x16x32_bf16 v[116:119], a[188:191], a[140:143], v[116:119]\n"
"v_add_f32 v67, v235, v67\n"
"v_mfma_f32_16x16x32_bf16 v[120:123], a[184:187], a[144:147], v[120:123]\n"
"v_add_f32 v68, v232, v68\n"
"v_mfma_f32_16x16x32_bf16 v[120:123], a[188:191], a[148:151], v[120:123]\n"
"v_add_f32 v69, v233, v69\n"
"v_mfma_f32_16x16x32_bf16 v[124:127], a[184:187], a[152:155], v[124:127]\n"
"global_load_dwordx4 a[184:187], v246, s[16:17] offset:2048 sc1\n"
"v_mfma_f32_16x16x32_bf16 v[124:127], a[188:191], a[156:159], v[124:127]\n"
"s_waitcnt vmcnt(21)\n"
"v_mfma_f32_16x16x32_bf16 v[128:131], a[192:195], a[96:99], v[128:131]\n"
"global_load_dwordx4 a[188:191], v246, s[16:17] offset:3072 sc1\n"
"v_mfma_f32_16x16x32_bf16 v[128:131], a[196:199], a[100:103], v[128:131]\n"
"v_add_f32 v70, v234, v70\n"
"v_mfma_f32_16x16x32_bf16 v[132:135], a[192:195], a[104:107], v[132:135]\n"
"v_add_u32 v246, 0x2000, v246\n"
"v_mfma_f32_16x16x32_bf16 v[132:135], a[196:199], a[108:111], v[132:135]\n"
"v_add_f32 v71, v235, v71\n"
"v_mfma_f32_16x16x32_bf16 v[136:139], a[192:195], a[112:115], v[136:139]\n"
"v_add_u32 v249, 33536, v248\n"
"v_mfma_f32_16x16x32_bf16 v[136:139], a[196:199], a[116:119], v[136:139]\n"
"s_waitcnt lgkmcnt(1)\n"
"global_store_dwordx4 v249, v[224:227], s[24:25] sc1\n"
"v_mfma_f32_16x16x32_bf16 v[140:143], a[192:195], a[120:123], v[140:143]\n"
"v_add_f32 v72, v232, v72\n"
"v_mfma_f32_16x16x32_bf16 v[140:143], a[196:199], a[124:127], v[140:143]\n"
"v_add_u32 v238, s66, v238\n"
"v_mfma_f32_16x16x32_bf16 v[144:147], a[192:195], a[128:131], v[144:147]\n"
"global_load_lds_dwordx4 v238, s[26:27] offset:3072 sc1\n"
"v_mfma_f32_16x16x32_bf16 v[144:147], a[196:199], a[132:135], v[144:147]\n"
"v_add_f32 v73, v233, v73\n"
"v_mfma_f32_16x16x32_bf16 v[148:151], a[192:195], a[136:139], v[148:151]\n"
"v_add_f32 v74, v234, v74\n"
"v_mfma_f32_16x16x32_bf16 v[148:151], a[196:199], a[140:143], v[148:151]\n"
"v_add_f32 v75, v235, v75\n"
"v_mfma_f32_16x16x32_bf16 v[152:155], a[192:195], a[144:147], v[152:155]\n"
"v_add_f32 v76, v232, v76\n"
"v_mfma_f32_16x16x32_bf16 v[152:155], a[196:199], a[148:151], v[152:155]\n"
"v_add_f32 v77, v233, v77\n"
"v_mfma_f32_16x16x32_bf16 v[156:159], a[192:195], a[152:155], v[156:159]\n"
"global_load_dwordx4 a[192:195], v246, s[16:17] offset:-4096 sc1\n"
"v_mfma_f32_16x16x32_bf16 v[156:159], a[196:199], a[156:159], v[156:159]\n"
"s_waitcnt vmcnt(22)\n"
"v_mfma_f32_16x16x32_bf16 v[160:163], a[200:203], a[96:99], v[160:163]\n"
"global_load_dwordx4 a[196:199], v246, s[16:17] offset:-3072 sc1\n"
"v_mfma_f32_16x16x32_bf16 v[160:163], a[204:207], a[100:103], v[160:163]\n"
"s_waitcnt vmcnt(13)\n"
"v_readfirstlane_b32 s46, v239\n"
"v_mfma_f32_16x16x32_bf16 v[164:167], a[200:203], a[104:107], v[164:167]\n"
"v_add_f32 v78, v234, v78\n"
"v_mfma_f32_16x16x32_bf16 v[164:167], a[204:207], a[108:111], v[164:167]\n"
"v_add_f32 v79, v235, v79\n"
"v_mfma_f32_16x16x32_bf16 v[168:171], a[200:203], a[112:115], v[168:171]\n"
"s_sub_u32 s46, s46, s44\n"
"v_mfma_f32_16x16x32_bf16 v[168:171], a[204:207], a[116:119], v[168:171]\n"
"s_cmp_ge_u32 s46, 130048\n"
"s_cbranch_scc0 .Lslow8_%=\n"
".Lback8_%=:\n"
"v_mfma_f32_16x16x32_bf16 v[172:175], a[200:203], a[120:123], v[172:175]\n"
"s_add_u32 m0, s42, 0\n"
"v_add_u32 v238, 0, v248\n"
"v_mfma_f32_16x16x32_bf16 v[172:175], a[204:207], a[124:127], v[172:175]\n"
"global_load_lds_dwordx4 v238, s[26:27] offset:0 sc1\n"
"v_mfma_f32_16x16x32_bf16 v[176:179], a[200:203], a[128:131], v[176:179]\n"
"v_add_f32 v80, v232, v80\n"
"v_mfma_f32_16x16x32_bf16 v[176:179], a[204:207], a[132:135], v[176:179]\n"
"v_add_f32 v81, v233, v81\n"
"v_mfma_f32_16x16x32_bf16 v[180:183], a[200:203], a[136:139], v[180:183]\n"
"v_add_f32 v82, v234, v82\n"
"v_mfma_f32_16x16x32_bf16 v[180:183], a[204:207], a[140:143], v[180:183]\n"
"v_add_f32 v83, v235, v83\n"
"v_mfma_f32_16x16x32_bf16 v[184:187], a[200:203], a[144:147], v[184:187]\n"
"v_add_f32 v84, v232, v84\n"
"v_mfma_f32_16x16x32_bf16 v[184:187], a[204:207], a[148:151], v[184:187]\n"
"v_add_f32 v85, v233, v85\n"
"v_mfma_f32_16x16x32_bf16 v[188:191], a[200:203], a[152:155], v[188:191]\n"
"global_load_dwordx4 a[200:203], v246, s[16:17] offset:-2048 sc1\n"
"v_mfma_f32_16x16x32_bf16 v[188:191], a[204:207], a[156:159], v[188:191]\n"
"v_mfma_f32_16x16x32_bf16 v[192:195], a[208:211], a[96:99], v[192:195]\n"
"global_load_dwordx4 a[204:207], v246, s[16:17] offset:-1024 sc1\n"
"v_mfma_f32_16x16x32_bf16 v[192:195], a[212:215], a[100:103], v[192:195]\n"
"v_add_f32 v86, v234, v86\n"
"v_mfma_f32_16x16x32_bf16 v[196:199], a[208:211], a[104:107], v[196:199]\n"
"v_add_f32 v87, v235, v87\n"
"v_mfma_f32_16x16x32_bf16 v[196:199], a[212:215], a[108:111], v[196:199]\n"
"v_add_f32 v88, v232, v88\n"
"v_mfma_f32_16x16x32_bf16 v[200:203], a[208:211], a[112:115], v[200:203]\n"
"v_add_f32 v89, v233, v89\n"
"v_mfma_f32_16x16x32_bf16 v[200:203], a[212:215], a[116:119], v[200:203]\n"
"v_add_f32 v90, v234, v90\n"
"v_mfma_f32_16x16x32_bf16 v[204:207], a[208:211], a[120:123], v[204:207]\n"
"v_add_f32 v91, v235, v91\n"
"v_mfma_f32_16x16x32_bf16 v[204:207], a[212:215], a[124:127], v[204:207]\n"
"v_add_f32 v92, v232, v92\n"
"v_mfma_f32_16x16x32_bf16 v[208:211], a[208:211], a[128:131], v[208:211]\n"
"v_add_f32 v93, v233, v93\n"
"v_mfma_f32_16x16x32_bf16 v[208:211], a[212:215], a[132:135], v[208:211]\n"
"v_add_f32 v94, v234, v94\n"
"v_mfma_f32_16x16x32_bf16 v[212:215], a[208:211], a[136:139], v[212:215]\n"
"v_add_f32 v95, v235, v95\n"
"v_mfma_f32_16x16x32_bf16 v[212:215], a[212:215], a[140:143], v[212:215]\n"
"v_mfma_f32_16x16x32_bf16 v[216:219], a[208:211], a[144:147], v[216:219]\n"
"v_add_u32 v249, 49920, v248\n"
"v_mfma_f32_16x16x32_bf16 v[216:219], a[212:215], a[148:151], v[216:219]\n"
"s_waitcnt lgkmcnt(0)\n"
"global_store_dwordx4 v249, v[228:231], s[24:25] sc1\n"
"v_mfma_f32_16x16x32_bf16 v[220:223], a[208:211], a[152:155], v[220:223]\n"
"global_load_dwordx4 a[208:211], v246, s[16:17] offset:0 sc1\n"
"v_mfma_f32_16x16x32_bf16 v[220:223], a[212:215], a[156:159], v[220:223]\n"
"v_mfma_f32_16x16x32_bf16 a[0:3], a[216:219], a[96:99], a[0:3]\n"
"global_load_dwordx4 a[212:215], v246, s[16:17] offset:1024 sc1\n"
"ds_read_b128 a[96:99], v240 offset:49152\n"
"v_mfma_f32_16x16x32_bf16 a[0:3], a[220:223], a[100:103], a[0:3]\n"
"ds_read_b128 a[100:103], v241 offset:49152\n"
"v_mfma_f32_16x16x32_bf16 a[4:7], a[216:219], a[104:107], a[4:7]\n"
"ds_read_b128 a[104:107], v240 offset:51200\n"
"v_mfma_f32_16x16x32_bf16 a[4:7], a[220:223], a[108:111], a[4:7]\n"
"ds_read_b128 a[108:111], v241 offset:51200\n"
"v_mfma_f32_16x16x32_bf16 a[8:11], a[216:219], a[112:115], a[8:11]\n"
"ds_read_b128 a[112:115], v240 offset:53248\n"
"v_mfma_f32_16x16x32_bf16 a[8:11], a[220:223], a[116:119], a[8:11]\n"
"ds_read_b128 a[116:119], v241 offset:53248\n"
"v_mfma_f32_16x16x32_bf16 a[12:15], a[216:219], a[120:123], a[12:15]\n"
"ds_read_b128 a[120:123], v240 offset:55296\n"
"v_mfma_f32_16x16x32_bf16 a[12:15], a[220:223], a[124:127], a[12:15]\n"
"ds_read_b128 a[124:127], v241 offset:55296\n"
"v_mfma_f32_16x16x32_bf16 a[16:19], a[216:219], a[128:131], a[16:19]\n"
"ds_read_b128 a[128:131], v240 offset:57344\n"
"v_mfma_f32_16x16x32_bf16 a[16:19], a[220:223], a[132:135], a[16:19]\n"
"ds_read_b128 a[132:135], v241 offset:57344\n"
"v_mfma_f32_16x16x32_bf16 a[20:23], a[216:219], a[136:139], a[20:23]\n"
"ds_read_b128 a[136:139], v240 offset:59392\n"
"v_mfma_f32_16x16x32_bf16 a[20:23], a[220:223], a[140:143], a[20:23]\n"
"ds_read_b128 a[140:143], v241 offset:59392\n"
"v_mfma_f32_16x16x32_bf16 a[24:27], a[216:219], a[144:147], a[24:27]\n"
"ds_read_b128 a[144:147], v240 offset:61440\n"
"v_mfma_f32_16x16x32_bf16 a[24:27], a[220:223], a[148:151], a[24:27]\n"
"ds_read_b128 a[148:151], v241 offset:61440\n"
"v_mfma_f32_16x16x32_bf16 a[28:31], a[216:219], a[152:155], a[28:31]\n"
"global_load_dwordx4 a[216:219], v246, s[16:17] offset:2048 sc1\n"
"ds_read_b128 a[152:155], v240 offset:63488\n"
"v_mfma_f32_16x16x32_bf16 a[28:31], a[220:223], a[156:159], a[28:31]\n"
"ds_read_b128 a[156:159], v241 offset:63488\n"
"s_waitcnt lgkmcnt(14)\n"
"v_mfma_f32_16x16x32_bf16 v[0:3], a[160:163], a[96:99], v[0:3]\n"
"global_load_dwordx4 a[220:223], v246, s[16:17] offset:3072 sc1\n"
"v_mfma_f32_16x16x32_bf16 v[0:3], a[164:167], a[100:103], v[0:3]\n"
"s_waitcnt lgkmcnt(12)\n"
"v_mfma_f32_16x16x32_bf16 v[4:7], a[160:163], a[104:107], v[4:7]\n"
"v_add_u32 v246, 0x2000, v246\n"
"v_mfma_f32_16x16x32_bf16 v[4:7], a[164:167], a[108:111], v[4:7]\n"
"s_waitcnt lgkmcnt(10)\n"
"v_mfma_f32_16x16x32_bf16 v[8:11], a[160:163], a[112:115], v[8:11]\n"
"v_xor_b32 v247, 0x10000, v247\n"
"v_mfma_f32_16x16x32_bf16 v[8:11], a[164:167], a[116:119], v[8:11]\n"
"s_waitcnt lgkmcnt(8)\n"
"v_mfma_f32_16x16x32_bf16 v[12:15], a[160:163], a[120:123], v[12:15]\n"
"v_add_u32 v238, s66, v238\n"
"v_mfma_f32_16x16x32_bf16 v[12:15], a[164:167], a[124:127], v[12:15]\n"
"s_waitcnt lgkmcnt(6)\n"
"v_mfma_f32_16x16x32_bf16 v[16:19], a[160:163], a[128:131], v[16:19]\n"
"global_load_lds_dwordx4 v238, s[26:27] offset:1024 sc1\n"
"v_mfma_f32_16x16x32_bf16 v[16:19], a[164:167], a[132:135], v[16:19]\n"
"s_waitcnt lgkmcnt(4)\n"
"v_mfma_f32_16x16x32_bf16 v[20:23], a[160:163], a[136:139], v[20:23]\n"
"v_mfma_f32_16x16x32_bf16 v[20:23], a[164:167], a[140:143], v[20:23]\n"
"s_waitcnt lgkmcnt(2)\n"
"v_mfma_f32_16x16x32_bf16 v[24:27], a[160:163], a[144:147], v[24:27]\n"
"s_waitcnt vmcnt(21)\n"
"v_and_b32 v235, s67, v237\n"
"v_lshlrev_b32 v234, 16, v237\n"
"v_mfma_f32_16x16x32_bf16 v[24:27], a[164:167], a[148:151], v[24:27]\n"
"v_and_b32 v233, s67, v236\n"
"s_waitcnt lgkmcnt(0)\n"
"v_mfma_f32_16x16x32_bf16 v[28:31], a[160:163], a[152:155], v[28:31]\n"
"global_load_dwordx4 a[160:163], v246, s[16:17] offset:-4096 sc1\n"
"v_lshlrev_b32 v232, 16, v236\n"
"v_mfma_f32_16x16x32_bf16 v[28:31], a[164:167], a[156:159], v[28:31]\n"
"v_lshrrev_b32 v237, 4, v247\n"
"s_waitcnt vmcnt(19)\n"
"v_mfma_f32_16x16x32_bf16 v[32:35], a[168:171], a[96:99], v[32:35]\n"
"global_load_dwordx4 a[164:167], v246, s[16:17] offset:-3072 sc1\n"
"v_and_b32 v237, 0x30, v237\n"
"v_mfma_f32_16x16x32_bf16 v[32:35], a[172:175], a[100:103], v[32:35]\n"
"global_load_dwordx2 v[236:237], v237, s[18:19] offset:200\n"
"v_mfma_f32_16x16x32_bf16 v[36:39], a[168:171], a[104:107], v[36:39]\n"
"v_add_f32 v96, v232, v96\n"
"v_mfma_f32_16x16x32_bf16 v[36:39], a[172:175], a[108:111], v[36:39]\n"
"v_add_f32 v97, v233, v97\n"
"v_mfma_f32_16x16x32_bf16 v[40:43], a[168:171], a[112:115], v[40:43]\n"
"v_add_f32 v98, v234, v98\n"
"v_mfma_f32_16x16x32_bf16 v[40:43], a[172:175], a[116:119], v[40:43]\n"
"v_add_f32 v99, v235, v99\n"
"v_mfma_f32_16x16x32_bf16 v[44:47], a[168:171], a[120:123], v[44:47]\n"
"v_add_f32 v100, v232, v100\n"
"v_mfma_f32_16x16x32_bf16 v[44:47], a[172:175], a[124:127], v[44:47]\n"
"v_add_f32 v101, v233, v101\n"
"v_mfma_f32_16x16x32_bf16 v[48:51], a[168:171], a[128:131], v[48:51]\n"
"v_add_f32 v102, v234, v102\n"
"v_mfma_f32_16x16x32_bf16 v[48:51], a[172:175], a[132:135], v[48:51]\n"
"v_add_f32 v103, v235, v103\n"
"v_mfma_f32_16x16x32_bf16 v[52:55], a[168:171], a[136:139], v[52:55]\n"
"v_add_f32 v104, v232, v104\n"
"v_mfma_f32_16x16x32_bf16 v[52:55], a[172:175], a[140:143], v[52:55]\n"
"v_add_f32 v105, v233, v105\n"
"v_mfma_f32_16x16x32_bf16 v[56:59], a[168:171], a[144:147], v[56:59]\n"
"v_add_f32 v106, v234, v106\n"
"v_mfma_f32_16x16x32_bf16 v[56:59], a[172:175], a[148:151], v[56:59]\n"
"v_add_f32 v107, v235, v107\n"
"v_mfma_f32_16x16x32_bf16 v[60:63], a[168:171], a[152:155], v[60:63]\n"
"global_load_dwordx4 a[168:171], v246, s[16:17] offset:-2048 sc1\n"
"v_mfma_f32_16x16x32_bf16 v[60:63], a[172:175], a[156:159], v[60:63]\n"
"s_waitcnt vmcnt(19)\n"
"v_mfma_f32_16x16x32_bf16 v[64:67], a[176:179], a[96:99], v[64:67]\n"
"global_load_dwordx4 a[172:175], v246, s[16:17] offset:-1024 sc1\n"
"v_mfma_f32_16x16x32_bf16 v[64:67], a[180:183], a[100:103], v[64:67]\n"
"v_add_f32 v108, v232, v108\n"
"v_mfma_f32_16x16x32_bf16 v[68:71], a[176:179], a[104:107], v[68:71]\n"
"v_add_f32 v109, v233, v109\n"
"v_mfma_f32_16x16x32_bf16 v[68:71], a[180:183], a[108:111], v[68:71]\n"
"v_add_f32 v110, v234, v110\n"
"v_mfma_f32_16x16x32_bf16 v[72:75], a[176:179], a[112:115], v[72:75]\n"
"v_add_f32 v111, v235, v111\n"
"v_mfma_f32_16x16x32_bf16 v[72:75], a[180:183], a[116:119], v[72:75]\n"
"v_add_u32 v238, s66, v238\n"
"v_mfma_f32_16x16x32_bf16 v[76:79], a[176:179], a[120:123], v[76:79]\n"
"global_load_lds_dwordx4 v238, s[26:27] offset:2048 sc1\n"
"v_mfma_f32_16x16x32_bf16 v[76:79], a[180:183], a[124:127], v[76:79]\n"
"v_add_f32 v112, v232, v112\n"
"v_mfma_f32_16x16x32_bf16 v[80:83], a[176:179], a[128:131], v[80:83]\n"
"v_add_f32 v113, v233, v113\n"
"v_mfma_f32_16x16x32_bf16 v[80:83], a[180:183], a[132:135], v[80:83]\n"
"v_add_f32 v114, v234, v114\n"
"v_mfma_f32_16x16x32_bf16 v[84:87], a[176:179], a[136:139], v[84:87]\n"
"v_add_f32 v115, v235, v115\n"
"v_mfma_f32_16x16x32_bf16 v[84:87], a[180:183], a[140:143], v[84:87]\n"
"v_add_f32 v116, v232, v116\n"
"v_mfma_f32_16x16x32_bf16 v[88:91], a[176:179], a[144:147], v[88:91]\n"
"v_mfma_f32_16x16x32_bf16 v[88:91], a[180:183], a[148:151], v[88:91]\n"
"v_mfma_f32_16x16x32_bf16 v[92:95], a[176:179], a[152:155], v[92:95]\n"
"global_load_dwordx4 a[176:179], v246, s[16:17] offset:0 sc1\n"
"v_mfma_f32_16x16x32_bf16 v[92:95], a[180:183], a[156:159], v[92:95]\n"
"s_waitcnt vmcnt(20)\n"
"v_mfma_f32_16x16x32_bf16 v[96:99], a[184:187], a[96:99], v[96:99]\n"
"global_load_dwordx4 a[180:183], v246, s[16:17] offset:1024 sc1\n"
"v_mfma_f32_16x16x32_bf16 v[96:99], a[188:191], a[100:103], v[96:99]\n"
"v_mfma_f32_16x16x32_bf16 v[100:103], a[184:187], a[104:107], v[100:103]\n"
"v_mfma_f32_16x16x32_bf16 v[100:103], a[188:191], a[108:111], v[100:103]\n"
"v_mfma_f32_16x16x32_bf16 v[104:107], a[184:187], a[112:115], v[104:107]\n"
"v_mfma_f32_16x16x32_bf16 v[104:107], a[188:191], a[116:119], v[104:107]\n"
"v_mfma_f32_16x16x32_bf16 v[108:111], a[184:187], a[120:123], v[108:111]\n"
"v_mfma_f32_16x16x32_bf16 v[108:111], a[188:191], a[124:127], v[108:111]\n"
"v_mfma_f32_16x16x32_bf16 v[112:115], a[184:187], a[128:131], v[112:115]\n"
"s_nop 0\n"
".Lpg19_%=:\n"
"s_cbranch_execz .Lretb_%=\n"
"v_mfma_f32_16x16x32_bf16 v[112:115], a[188:191], a[132:135], v[112:115]\n"
"v_mfma_f32_16x16x32_bf16 v[116:119], a[184:187], a[136:139], v[116:119]\n"
"v_mfma_f32_16x16x32_bf16 v[116:119], a[188:191], a[140:143], v[116:119]\n"
"v_mfma_f32_16x16x32_bf16 v[120:123], a[184:187], a[144:147], v[120:123]\n"
"v_mfma_f32_16x16x32_bf16 v[120:123], a[188:191], a[148:151], v[120:123]\n"
"v_mfma_f32_16x16x32_bf16 v[124:127], a[184:187], a[152:155], v[124:127]\n"
"global_load_dwordx4 a[184:187], v246, s[16:17] offset:2048 sc1\n"
"v_mfma_f32_16x16x32_bf16 v[124:127], a[188:191], a[156:159], v[124:127]\n"
"s_waitcnt vmcnt(18)\n"
"v_mfma_f32_16x16x32_bf16 v[128:131], a[192:195], a[96:99], v[128:131]\n"
"global_load_dwordx4 a[188:191], v246, s[16:17] offset:3072 sc1\n"
"v_mfma_f32_16x16x32_bf16 v[128:131], a[196:199], a[100:103], v[128:131]\n"
"v_mfma_f32_16x16x32_bf16 v[132:135], a[192:195], a[104:107], v[132:135]\n"
"v_add_u32 v246, 0x2000, v246\n"
"v_mfma_f32_16x16x32_bf16 v[132:135], a[196:199], a[108:111], v[132:135]\n"
"v_add_u32 v238, s66, v238\n"
"v_mfma_f32_16x16x32_bf16 v[136:139], a[192:195], a[112:115], v[136:139]\n"
"global_load_lds_dwordx4 v238, s[26:27] offset:3072 sc1\n"
"v_mfma_f32_16x16x32_bf16 v[136:139], a[196:199], a[116:119], v[136:139]\n"
"v_mfma_f32_16x16x32_bf16 v[140:143], a[192:195], a[120:123], v[140:143]\n"
"v_mfma_f32_16x16x32_bf16 v[140:143], a[196:199], a[124:127], v[140:143]\n"
"v_mfma_f32_16x16x32_bf16 v[144:147], a[192:195], a[128:131], v[144:147]\n"
"v_add_f32 v117, v233, v117\n"
"v_mfma_f32_16x16x32_bf16 v[144:147], a[196:199], a[132:135], v[144:147]\n"
"v_add_f32 v118, v234, v118\n"
"v_mfma_f32_16x16x32_bf16 v[148:151], a[192:195], a[136:139], v[148:151]\n"
"v_add_f32 v119, v235, v119\n"
"v_mfma_f32_16x16x32_bf16 v[148:151], a[196:199], a[140:143], v[148:151]\n"
"v_add_f32 v120, v232, v120\n"
"v_mfma_f32_16x16x32_bf16 v[152:155], a[192:195], a[144:147], v[152:155]\n"
"v_add_f32 v121, v233, v121\n"
"v_mfma_f32_16x16x32_bf16 v[152:155], a[196:199], a[148:151], v[152:155]\n"
"v_add_f32 v122, v234, v122\n"
"v_mfma_f32_16x16x32_bf16 v[156:159], a[192:195], a[152:155], v[156:159]\n"
"global_load_dwordx4 a[192:195], v246, s[16:17] offset:-4096 sc1\n"
"v_mfma_f32_16x16x32_bf16 v[156:159], a[196:199], a[156:159], v[156:159]\n"
"s_waitcnt vmcnt(18)\n"
"v_mfma_f32_16x16x32_bf16 v[160:163], a[200:203], a[96:99], v[160:163]\n"
"global_load_dwordx4 a[196:199], v246, s[16:17] offset:-3072 sc1\n"
"v_mfma_f32_16x16x32_bf16 v[160:163], a[204:207], a[100:103], v[160:163]\n"
"v_add_f32 v123, v235, v123\n"
"v_mfma_f32_16x16x32_bf16 v[164:167], a[200:203], a[104:107], v[164:167]\n"
"v_add_f32 v124, v232, v124\n"
"v_mfma_f32_16x16x32_bf16 v[164:167], a[204:207], a[108:111], v[164:167]\n"
"v_add_f32 v125, v233, v125\n"
"v_mfma_f32_16x16x32_bf16 v[168:171], a[200:203], a[112:115], v[168:171]\n"
"v_add_f32 v126, v234, v126\n"
"v_mfma_f32_16x16x32_bf16 v[168:171], a[204:207], a[116:119], v[168:171]\n"
"v_add_f32 v127, v235, v127\n"
"v_mfma_f32_16x16x32_bf16 v[172:175], a[200:203], a[120:123], v[172:175]\n"
"v_mfma_f32_16x16x32_bf16 v[172:175], a[204:207], a[124:127], v[172:175]\n"
"v_mfma_f32_16x16x32_bf16 v[176:179], a[200:203], a[128:131], v[176:179]\n"
"v_mfma_f32_16x16x32_bf16 v[176:179], a[204:207], a[132:135], v[176:179]\n"
"v_mfma_f32_16x16x32_bf16 v[180:183], a[200:203], a[136:139], v[180:183]\n"
"v_mfma_f32_16x16x32_bf16 v[180:183], a[204:207], a[140:143], v[180:183]\n"
"v_mfma_f32_16x16x32_bf16 v[184:187], a[200:203], a[144:147], v[184:187]\n"
"v_mfma_f32_16x16x32_bf16 v[184:187], a[204:207], a[148:151], v[184:187]\n"
"v_mfma_f32_16x16x32_bf16 v[188:191], a[200:203], a[152:155], v[188:191]\n"
"global_load_dwordx4 a[200:203], v246, s[16:17] offset:-2048 sc1\n"
"v_mfma_f32_16x16x32_bf16 v[188:191], a[204:207], a[156:159], v[188:191]\n"
"s_waitcnt vmcnt(17)\n"
"v_mfma_f32_16x16x32_bf16 v[192:195], a[208:211], a[96:99], v[192:195]\n"
"global_load_dwordx4 a[204:207], v246, s[16:17] offset:-1024 sc1\n"
"v_mfma_f32_16x16x32_bf16 v[192:195], a[212:215], a[100:103], v[192:195]\n"
"s_add_u32 m0, s42, 32768\n"
"v_add_u32 v238, 256, v248\n"
"v_mfma_f32_16x16x32_bf16 v[196:199], a[208:211], a[104:107], v[196:199]\n"
"global_load_lds_dwordx4 v238, s[26:27] offset:0 sc1\n"
"v_mfma_f32_16x16x32_bf16 v[196:199], a[212:215], a[108:111], v[196:199]\n"
"v_mfma_f32_16x16x32_bf16 v[200:203], a[208:211], a[112:115], v[200:203]\n"
"s_add_u32 s64, s60, 81920\n"
"s_addc_u32 s65, s61, 0\n"
"v_mfma_f32_16x16x32_bf16 v[200:203], a[212:215], a[116:119], v[200:203]\n"
"v_mfma_f32_16x16x32_bf16 v[204:207], a[208:211], a[120:123], v[204:207]\n"
"v_mfma_f32_16x16x32_bf16 v[204:207], a[212:215], a[124:127], v[204:207]\n"
"v_mfma_f32_16x16x32_bf16 v[208:211], a[208:211], a[128:131], v[208:211]\n"
"v_mfma_f32_16x16x32_bf16 v[208:211], a[212:215], a[132:135], v[208:211]\n"
"v_mfma_f32_16x16x32_bf16 v[212:215], a[208:211], a[136:139], v[212:215]\n"
"v_mfma_f32_16x16x32_bf16 v[212:215], a[212:215], a[140:143], v[212:215]\n"
"v_mfma_f32_16x16x32_bf16 v[216:219], a[208:211], a[144:147], v[216:219]\n"
"s_mov_b64 exec, 1\n"
"global_atomic_add v250, v252, s[30:31] offset:0 sc1\n"
"s_mov_b64 exec, -1\n"
"v_mfma_f32_16x16x32_bf16 v[216:219], a[212:215], a[148:151], v[216:219]\n"
"s_barrier\n"
"v_mfma_f32_16x16x32_bf16 v[220:223], a[208:211], a[152:155], v[220:223]\n"
"global_load_dwordx4 a[208:211], v246, s[16:17] offset:0 sc1\n"
"v_mfma_f32_16x16x32_bf16 v[220:223], a[212:215], a[156:159], v[220:223]\n"
"s_waitcnt vmcnt(19)\n"
"v_mfma_f32_16x16x32_bf16 a[0:3], a[216:219], a[96:99], a[0:3]\n"
"global_load_dwordx4 a[212:215], v246, s[16:17] offset:1024 sc1\n"
"ds_read_b128 a[96:99], v242 offset:16384\n"
"v_mfma_f32_16x16x32_bf16 a[0:3], a[220:223], a[100:103], a[0:3]\n"
"ds_read_b128 a[100:103], v243 offset:16384\n"
"v_mfma_f32_16x16x32_bf16 a[4:7], a[216:219], a[104:107], a[4:7]\n"
"ds_read_b128 a[104:107], v242 offset:18432\n"
"v_mfma_f32_16x16x32_bf16 a[4:7], a[220:223], a[108:111], a[4:7]\n"
"ds_read_b128 a[108:111], v243 offset:18432\n"
"v_mfma_f32_16x16x32_bf16 a[8:11], a[216:219], a[112:115], a[8:11]\n"
"ds_read_b128 a[112:115], v242 offset:20480\n"
"v_mfma_f32_16x16x32_bf16 a[8:11], a[220:223], a[116:119], a[8:11]\n"
"ds_read_b128 a[116:119], v243 offset:20480\n"
"v_mfma_f32_16x16x32_bf16 a[12:15], a[216:219], a[120:123], a[12:15]\n"
"ds_read_b128 a[120:123], v242 offset:22528\n"
"v_mfma_f32_16x16x32_bf16 a[12:15], a[220:223], a[124:127], a[12:15]\n"
"ds_read_b128 a[124:127], v243 offset:22528\n"
"v_mfma_f32_16x16x32_bf16 a[16:19], a[216:219], a[128:131], a[16:19]\n"
"ds_read_b128 a[128:131], v242 offset:24576\n"
"v_mfma_f32_16x16x32_bf16 a[16:19], a[220:223], a[132:135], a[16:19]\n"
"ds_read_b128 a[132:135], v243 offset:24576\n"
"v_mfma_f32_16x16x32_bf16 a[20:23], a[216:219], a[136:139], a[20:23]\n"
"ds_read_b128 a[136:139], v242 offset:26624\n"
"v_mfma_f32_16x16x32_bf16 a[20:23], a[220:223], a[140:143], a[20:23]\n"
"ds_read_b128 a[140:143], v243 offset:26624\n"
"v_mfma_f32_16x16x32_bf16 a[24:27], a[216:219], a[144:147], a[24:27]\n"
"ds_read_b128 a[144:147], v242 offset:28672\n"
"v_mfma_f32_16x16x32_bf16 a[24:27], a[220:223], a[148:151], a[24:27]\n"
"ds_read_b128 a[148:151], v243 offset:28672\n"
"v_mfma_f32_16x16x32_bf16 a[28:31], a[216:219], a[152:155], a[28:31]\n"
"global_load_dword a216, v250, s[64:65]\n"
"global_load_dwordx4 a[216:219], v246, s[16:17] offset:2048 sc1\n"
"ds_read_b128 a[152:155], v242 offset:30720\n"
"v_mfma_f32_16x16x32_bf16 a[28:31], a[220:223], a[156:159], a[28:31]\n"
"ds_read_b128 a[156:159], v243 offset:30720\n"
"s_waitcnt vmcnt(19) lgkmcnt(14)\n"
"v_mfma_f32_16x16x32_bf16 v[0:3], a[160:163], a[96:99], v[0:3]\n"
"global_load_dwordx4 a[220:223], v246, s[16:17] offset:3072 sc1\n"
"v_mfma_f32_16x16x32_bf16 v[0:3], a[164:167], a[100:103], v[0:3]\n"
"s_waitcnt lgkmcnt(12)\n"
"v_mfma_f32_16x16x32_bf16 v[4:7], a[160:163], a[104:107], v[4:7]\n"
"v_add_u32 v246, 0x2000, v246\n"
"s_waitcnt vmcnt(19)\n"
"v_and_b32 v235, s67, v237\n"
"v_mfma_f32_16x16x32_bf16 v[4:7], a[164:167], a[108:111], v[4:7]\n"
"v_lshlrev_b32 v234, 16, v237\n"
"s_waitcnt lgkmcnt(10)\n"
"v_mfma_f32_16x16x32_bf16 v[8:11], a[160:163], a[112:115], v[8:11]\n"
"v_add_u32 v238, s66, v238\n"
"v_and_b32 v233, s67, v236\n"
"v_mfma_f32_16x16x32_bf16 v[8:11], a[164:167], a[116:119], v[8:11]\n"
"v_lshlrev_b32 v232, 16, v236\n"
"s_waitcnt lgkmcnt(8)\n"
"v_mfma_f32_16x16x32_bf16 v[12:15], a[160:163], a[120:123], v[12:15]\n"
"global_load_lds_dwordx4 v238, s[26:27] offset:1024 sc1\n"
"v_mfma_f32_16x16x32_bf16 v[12:15], a[164:167], a[124:127], v[12:15]\n"
"s_waitcnt lgkmcnt(6)\n"
"v_mfma_f32_16x16x32_bf16 v[16:19], a[160:163], a[128:131], v[16:19]\n"
"v_accvgpr_read_b32 v249, a0\n"
"v_mfma_f32_16x16x32_bf16 v[16:19], a[164:167], a[132:135], v[16:19]\n"
"v_add_f32 v249, v232, v249\n"
"s_waitcnt lgkmcnt(4)\n"
"v_mfma_f32_16x16x32_bf16 v[20:23], a[160:163], a[136:139], v[20:23]\n"
"v_accvgpr_write_b32 a0, v249\n"
"v_mfma_f32_16x16x32_bf16 v[20:23], a[164:167], a[140:143], v[20:23]\n"
"s_waitcnt lgkmcnt(2)\n"
"v_mfma_f32_16x16x32_bf16 v[24:27], a[160:163], a[144:147], v[24:27]\n"
"v_accvgpr_read_b32 v249, a1\n"
"v_mfma_f32_16x16x32_bf16 v[24:27], a[164:167], a[148:151], v[24:27]\n"
"v_add_f32 v249, v233, v249\n"
"s_waitcnt lgkmcnt(0)\n"
"v_mfma_f32_16x16x32_bf16 v[28:31], a[160:163], a[152:155], v[28:31]\n"
"global_load_dwordx4 a[160:163], v246, s[16:17] offset:-4096 sc1\n"
"v_accvgpr_write_b32 a1, v249\n"
"v_mfma_f32_16x16x32_bf16 v[28:31], a[164:167], a[156:159], v[28:31]\n"
"s_waitcnt vmcnt(19)\n"
"v_mfma_f32_16x16x32_bf16 v[32:35], a[168:171], a[96:99], v[32:35]\n"
"global_load_dwordx4 a[164:167], v246, s[16:17] offset:-3072 sc1\n"
"v_mfma_f32_16x16x32_bf16 v[32:35], a[172:175], a[100:103], v[32:35]\n"
"v_accvgpr_read_b32 v249, a2\n"
"v_mfma_f32_16x16x32_bf16 v[36:39], a[168:171], a[104:107], v[36:39]\n"
"v_add_f32 v249, v234, v249\n"
"v_mfma_f32_16x16x32_bf16 v[36:39], a[172:175], a[108:111], v[36:39]\n"
"v_accvgpr_write_b32 a2, v249\n"
"v_mfma_f32_16x16x32_bf16 v[40:43], a[168:171], a[112:115], v[40:43]\n"
"v_accvgpr_read_b32 v249, a3\n"
"v_mfma_f32_16x16x32_bf16 v[40:43], a[172:175], a[116:119], v[40:43]\n"
"v_add_f32 v249, v235, v249\n"
"v_mfma_f32_16x16x32_bf16 v[44:47], a[168:171], a[120:123], v[44:47]\n"
"v_accvgpr_write_b32 a3, v249\n"
"v_mfma_f32_16x16x32_bf16 v[44:47], a[172:175], a[124:127], v[44:47]\n"
"v_accvgpr_read_b32 v249, a4\n"
"v_mfma_f32_16x16x32_bf16 v[48:51], a[168:171], a[128:131], v[48:51]\n"
"v_add_f32 v249, v232, v249\n"
"v_mfma_f32_16x16x32_bf16 v[48:51], a[172:175], a[132:135], v[48:51]\n"
"v_accvgpr_write_b32 a4, v249\n"
"v_mfma_f32_16x16x32_bf16 v[52:55], a[168:171], a[136:139], v[52:55]\n"
"v_accvgpr_read_b32 v249, a5\n"
"v_mfma_f32_16x16x32_bf16 v[52:55], a[172:175], a[140:143], v[52:55]\n"
"v_add_f32 v249, v233, v249\n"
"v_mfma_f32_16x16x32_bf16 v[56:59], a[168:171], a[144:147], v[56:59]\n"
"v_accvgpr_write_b32 a5, v249\n"
"v_mfma_f32_16x16x32_bf16 v[56:59], a[172:175], a[148:151], v[56:59]\n"
"v_accvgpr_read_b32 v249, a6\n"
"v_mfma_f32_16x16x32_bf16 v[60:63], a[168:171], a[152:155], v[60:63]\n"
"global_load_dwordx4 a[168:171], v246, s[16:17] offset:-2048 sc1\n"
"v_add_f32 v249, v234, v249\n"
"v_mfma_f32_16x16x32_bf16 v[60:63], a[172:175], a[156:159], v[60:63]\n"
"v_accvgpr_write_b32 a6, v249\n"
"s_waitcnt vmcnt(18)\n"
"v_mfma_f32_16x16x32_bf16 v[64:67], a[176:179], a[96:99], v[64:67]\n"
"global_load_dwordx4 a[172:175], v246, s[16:17] offset:-1024 sc1\n"
"v_mfma_f32_16x16x32_bf16 v[64:67], a[180:183], a[100:103], v[64:67]\n"
"v_accvgpr_read_b32 v249, a7\n"
"v_mfma_f32_16x16x32_bf16 v[68:71], a[176:179], a[104:107], v[68:71]\n"
"v_add_f32 v249, v235, v249\n"
"v_mfma_f32_16x16x32_bf16 v[68:71], a[180:183], a[108:111], v[68:71]\n"
"v_add_u32 v238, s66, v238\n"
"v_accvgpr_write_b32 a7, v249\n"
"v_mfma_f32_16x16x32_bf16 v[72:75], a[176:179], a[112:115], v[72:75]\n"
"global_load_lds_dwordx4 v238, s[26:27] offset:2048 sc1\n"
"v_mfma_f32_16x16x32_bf16 v[72:75], a[180:183], a[116:119], v[72:75]\n"
"v_accvgpr_read_b32 v249, a8\n"
"v_mfma_f32_16x16x32_bf16 v[76:79], a[176:179], a[120:123], v[76:79]\n"
"v_add_f32 v249, v232, v249\n"
"v_mfma_f32_16x16x32_bf16 v[76:79], a[180:183], a[124:127], v[76:79]\n"
"v_accvgpr_write_b32 a8, v249\n"
"v_mfma_f32_16x16x32_bf16 v[80:83], a[176:179], a[128:131], v[80:83]\n"
"v_accvgpr_read_b32 v249, a9\n"
"v_mfma_f32_16x16x32_bf16 v[80:83], a[180:183], a[132:135], v[80:83]\n"
"v_add_f32 v249, v233, v249\n"
"v_mfma_f32_16x16x32_bf16 v[84:87], a[176:179], a[136:139], v[84:87]\n"
"v_accvgpr_write_b32 a9, v249\n"
"v_mfma_f32_16x16x32_bf16 v[84:87], a[180:183], a[140:143], v[84:87]\n"
"v_accvgpr_read_b32 v249, a10\n"
"v_mfma_f32_16x16x32_bf16 v[88:91], a[176:179], a[144:147], v[88:91]\n"
"v_add_f32 v249, v234, v249\n"
"v_mfma_f32_16x16x32_bf16 v[88:91], a[180:183], a[148:151], v[88:91]\n"
"v_accvgpr_write_b32 a10, v249\n"
"v_mfma_f32_16x16x32_bf16 v[92:95], a[176:179], a[152:155], v[92:95]\n"
"global_load_dwordx4 a[176:179], v246, s[16:17] offset:0 sc1\n"
"v_mfma_f32_16x16x32_bf16 v[92:95], a[180:183], a[156:159], v[92:95]\n"
"s_waitcnt vmcnt(19)\n"
"v_mfma_f32_16x16x32_bf16 v[96:99], a[184:187], a[96:99], v[96:99]\n"
"global_load_dwordx4 a[180:183], v246, s[16:17] offset:1024 sc1\n"
"v_mfma_f32_16x16x32_bf16 v[96:99], a[188:191], a[100:103], v[96:99]\n"
"v_accvgpr_read_b32 v249, a11\n"
"v_mfma_f32_16x16x32_bf16 v[100:103], a[184:187], a[104:107], v[100:103]\n"
"global_load_dword v239, v250, s[34:35] sc1\n"
"v_add_f32 v249, v235, v249\n"
"v_mfma_f32_16x16x32_bf16 v[100:103], a[188:191], a[108:111], v[100:103]\n"
"v_accvgpr_write_b32 a11, v249\n"
"v_mfma_f32_16x16x32_bf16 v[104:107], a[184:187], a[112:115], v[104:107]\n"
"v_accvgpr_read_b32 v249, a12\n"
"v_mfma_f32_16x16x32_bf16 v[104:107], a[188:191], a[116:119], v[104:107]\n"
"v_add_f32 v249, v232, v249\n"
"v_mfma_f32_16x16x32_bf16 v[108:111], a[184:187], a[120:123], v[108:111]\n"
"v_accvgpr_write_b32 a12, v249\n"
"v_mfma_f32_16x16x32_bf16 v[108:111], a[188:191], a[124:127], v[108:111]\n"
"v_accvgpr_read_b32 v249, a13\n"
"v_mfma_f32_16x16x32_bf16 v[112:115], a[184:187], a[128:131], v[112:115]\n"
"v_add_f32 v249, v233, v249\n"
"v_mfma_f32_16x16x32_bf16 v[112:115], a[188:191], a[132:135], v[112:115]\n"
"v_accvgpr_write_b32 a13, v249\n"
"v_mfma_f32_16x16x32_bf16 v[116:119], a[184:187], a[136:139], v[116:119]\n"
"v_accvgpr_read_b32 v249, a14\n"
"v_mfma_f32_16x16x32_bf16 v[116:119], a[188:191], a[140:143], v[116:119]\n"
"v_add_f32 v249, v234, v249\n"
"v_mfma_f32_16x16x32_bf16 v[120:123], a[184:187], a[144:147], v[120:123]\n"
"v_accvgpr_write_b32 a14, v249\n"
"v_mfma_f32_16x16x32_bf16 v[120:123], a[188:191], a[148:151], v[120:123]\n"
"v_accvgpr_read_b32 v249, a15\n"
"v_mfma_f32_16x16x32_bf16 v[124:127], a[184:187], a[152:155], v[124:127]\n"
"global_load_dwordx4 a[184:187], v246, s[16:17] offset:2048 sc1\n"
"v_add_f32 v249, v235, v249\n"
"v_mfma_f32_16x16x32_bf16 v[124:127], a[188:191], a[156:159], v[124:127]\n"
"v_accvgpr_write_b32 a15, v249\n"
"s_waitcnt vmcnt(19)\n"
"v_mfma_f32_16x16x32_bf16 v[128:131], a[192:195], a[96:99], v[128:131]\n"
"global_load_dwordx4 a[188:191], v246, s[16:17] offset:3072 sc1\n"
"v_mfma_f32_16x16x32_bf16 v[128:131], a[196:199], a[100:103], v[128:131]\n"
"v_add_u32 v238, s66, v238\n"
"v_mfma_f32_16x16x32_bf16 v[132:135], a[192:195], a[104:107], v[132:135]\n"
"v_add_u32 v246, 0x2000, v246\n"
"v_mfma_f32_16x16x32_bf16 v[132:135], a[196:199], a[108:111], v[132:135]\n"
"global_load_lds_dwordx4 v238, s[26:27] offset:3072 sc1\n"
"v_mfma_f32_16x16x32_bf16 v[136:139], a[192:195], a[112:115], v[136:139]\n"
"v_accvgpr_read_b32 v249, a16\n"
"v_mfma_f32_16x16x32_bf16 v[136:139], a[196:199], a[116:119], v[136:139]\n"
"v_add_f32 v249, v232, v249\n"
"v_mfma_f32_16x16x32_bf16 v[140:143], a[192:195], a[120:123], v[140:143]\n"
"v_accvgpr_write_b32 a16, v249\n"
"v_mfma_f32_16x16x32_bf16 v[140:143], a[196:199], a[124:127], v[140:143]\n"
"v_accvgpr_read_b32 v249, a17\n"
"v_mfma_f32_16x16x32_bf16 v[144:147], a[192:195], a[128:131], v[144:147]\n"
"v_add_f32 v249, v233, v249\n"
"v_mfma_f32_16x16x32_bf16 v[144:147], a[196:199], a[132:135], v[144:147]\n"
"v_accvgpr_write_b32 a17, v249\n"
"v_mfma_f32_16x16x32_bf16 v[148:151], a[192:195], a[136:139], v[148:151]\n"
"v_accvgpr_read_b32 v249, a18\n"
"v_mfma_f32_16x16x32_bf16 v[148:151], a[196:199], a[140:143], v[148:151]\n"
"v_add_f32 v249, v234, v249\n"
"v_mfma_f32_16x16x32_bf16 v[152:155], a[192:195], a[144:147], v[152:155]\n"
"v_accvgpr_write_b32 a18, v249\n"
"v_mfma_f32_16x16x32_bf16 v[152:155], a[196:199], a[148:151], v[152:155]\n"
"v_accvgpr_read_b32 v249, a19\n"
"v_mfma_f32_16x16x32_bf16 v[156:159], a[192:195], a[152:155], v[156:159]\n"
"global_load_dwordx4 a[192:195], v246, s[16:17] offset:-4096 sc1\n"
"v_add_f32 v249, v235, v249\n"
"v_mfma_f32_16x16x32_bf16 v[156:159], a[196:199], a[156:159], v[156:159]\n"
"v_accvgpr_write_b32 a19, v249\n"
"s_waitcnt vmcnt(20)\n"
"v_mfma_f32_16x16x32_bf16 v[160:163], a[200:203], a[96:99], v[160:163]\n"
"global_load_dwordx4 a[196:199], v246, s[16:17] offset:-3072 sc1\n"
"v_mfma_f32_16x16x32_bf16 v[160:163], a[204:207], a[100:103], v[160:163]\n"
"v_accvgpr_read_b32 v249, a20\n"
"v_mfma_f32_16x16x32_bf16 v[164:167], a[200:203], a[104:107], v[164:167]\n"
"v_add_f32 v249, v232, v249\n"
"v_mfma_f32_16x16x32_bf16 v[164:167], a[204:207], a[108:111], v[164:167]\n"
"v_accvgpr_write_b32 a20, v249\n"
"v_mfma_f32_16x16x32_bf16 v[168:171], a[200:203], a[112:115], v[168:171]\n"
"v_accvgpr_read_b32 v249, a21\n"
"v_mfma_f32_16x16x32_bf16 v[168:171], a[204:207], a[116:119], v[168:171]\n"
"v_add_f32 v249, v233, v249\n"
"v_mfma_f32_16x16x32_bf16 v[172:175], a[200:203], a[120:123], v[172:175]\n"
"v_accvgpr_write_b32 a21, v249\n"
"v_mfma_f32_16x16x32_bf16 v[172:175], a[204:207], a[124:127], v[172:175]\n"
"v_accvgpr_read_b32 v249, a22\n"
"v_mfma_f32_16x16x32_bf16 v[176:179], a[200:203], a[128:131], v[176:179]\n"
"v_add_f32 v249, v234, v249\n"
"v_mfma_f32_16x16x32_bf16 v[176:179], a[204:207], a[132:135], v[176:179]\n"
"v_accvgpr_write_b32 a22, v249\n"
"v_mfma_f32_16x16x32_bf16 v[180:183], a[200:203], a[136:139], v[180:183]\n"
"v_accvgpr_read_b32 v249, a23\n"
"v_mfma_f32_16x16x32_bf16 v[180:183], a[204:207], a[140:143], v[180:183]\n"
"v_add_f32 v249, v235, v249\n"
"v_mfma_f32_16x16x32_bf16 v[184:187], a[200:203], a[144:147], v[184:187]\n"
"v_accvgpr_write_b32 a23, v249\n"
"v_mfma_f32_16x16x32_bf16 v[184:187], a[204:207], a[148:151], v[184:187]\n"
"v_accvgpr_read_b32 v249, a24\n"
"v_mfma_f32_16x16x32_bf16 v[188:191], a[200:203], a[152:155], v[188:191]\n"
"global_load_dwordx4 a[200:203], v246, s[16:17] offset:-2048 sc1\n"
"v_add_f32 v249, v232, v249\n"
"v_mfma_f32_16x16x32_bf16 v[188:191], a[204:207], a[156:159], v[188:191]\n"
"v_accvgpr_write_b32 a24, v249\n"
"s_waitcnt vmcnt(18)\n"
"v_mfma_f32_16x16x32_bf16 v[192:195], a[208:211], a[96:99], v[192:195]\n"
"global_load_dwordx4 a[204:207], v246, s[16:17] offset:-1024 sc1\n"
"v_mfma_f32_16x16x32_bf16 v[192:195], a[212:215], a[100:103], v[192:195]\n"
"v_accvgpr_read_b32 v249, a25\n"
"v_mfma_f32_16x16x32_bf16 v[196:199], a[208:211], a[104:107], v[196:199]\n"
"v_add_f32 v249, v233, v249\n"
"v_mfma_f32_16x16x32_bf16 v[196:199], a[212:215], a[108:111], v[196:199]\n"
"v_accvgpr_write_b32 a25, v249\n"
"v_mfma_f32_16x16x32_bf16 v[200:203], a[208:211], a[112:115], v[200:203]\n"
"v_accvgpr_read_b32 v249, a26\n"
"v_mfma_f32_16x16x32_bf16 v[200:203], a[212:215], a[116:119], v[200:203]\n"
"v_add_f32 v249, v234, v249\n"
"v_mfma_f32_16x16x32_bf16 v[204:207], a[208:211], a[120:123], v[204:207]\n"
"v_accvgpr_write_b32 a26, v249\n"
"v_mfma_f32_16x16x32_bf16 v[204:207], a[212:215], a[124:127], v[204:207]\n"
"v_mfma_f32_16x16x32_bf16 v[208:211], a[208:211], a[128:131], v[208:211]\n"
"v_mfma_f32_16x16x32_bf16 v[208:211], a[212:215], a[132:135], v[208:211]\n"
"v_mfma_f32_16x16x32_bf16 v[212:215], a[208:211], a[136:139], v[212:215]\n"
"v_mfma_f32_16x16x32_bf16 v[212:215], a[212:215], a[140:143], v[212:215]\n"
"v_mfma_f32_16x16x32_bf16 v[216:219], a[208:211], a[144:147], v[216:219]\n"
"v_mfma_f32_16x16x32_bf16 v[216:219], a[212:215], a[148:151], v[216:219]\n"
"v_mfma_f32_16x16x32_bf16 v[220:223], a[208:211], a[152:155], v[220:223]\n"
"global_load_dwordx4 a[208:211], v246, s[16:17] offset:0 sc1\n"
"v_mfma_f32_16x16x32_bf16 v[220:223], a[212:215], a[156:159], v[220:223]\n"
"s_waitcnt vmcnt(17)\n"
"v_mfma_f32_16x16x32_bf16 a[0:3], a[216:219], a[96:99], a[0:3]\n"
"global_load_dwordx4 a[212:215], v246, s[16:17] offset:1024 sc1\n"
"ds_read_b128 a[96:99], v242 offset:49152\n"
"v_mfma_f32_16x16x32_bf16 a[0:3], a[220:223], a[100:103], a[0:3]\n"
"ds_read_b128 a[100:103], v243 offset:49152\n"
"v_mfma_f32_16x16x32_bf16 a[4:7], a[216:219], a[104:107], a[4:7]\n"
"ds_read_b128 a[104:107], v242 offset:51200\n"
"v_mfma_f32_16x16x32_bf16 a[4:7], a[220:223], a[108:111], a[4:7]\n"
"ds_read_b128 a[108:111], v243 offset:51200\n"
"v_mfma_f32_16x16x32_bf16 a[8:11], a[216:219], a[112:115], a[8:11]\n"
"ds_read_b128 a[112:115], v242 offset:53248\n"
"v_mfma_f32_16x16x32_bf16 a[8:11], a[220:223], a[116:119], a[8:11]\n"
"ds_read_b128 a[116:119], v243 offset:53248\n"
"v_mfma_f32_16x16x32_bf16 a[12:15], a[216:219], a[120:123], a[12:15]\n"
"ds_read_b128 a[120:123], v242 offset:55296\n"
"v_mfma_f32_16x16x32_bf16 a[12:15], a[220:223], a[124:127], a[12:15]\n"
"ds_read_b128 a[124:127], v243 offset:55296\n"
"v_mfma_f32_16x16x32_bf16 a[16:19], a[216:219], a[128:131], a[16:19]\n"
"ds_read_b128 a[128:131], v242 offset:57344\n"
"v_mfma_f32_16x16x32_bf16 a[16:19], a[220:223], a[132:135], a[16:19]\n"
"ds_read_b128 a[132:135], v243 offset:57344\n"
"v_mfma_f32_16x16x32_bf16 a[20:23], a[216:219], a[136:139], a[20:23]\n"
"ds_read_b128 a[136:139], v242 offset:59392\n"
"v_mfma_f32_16x16x32_bf16 a[20:23], a[220:223], a[140:143], a[20:23]\n"
"ds_read_b128 a[140:143], v243 offset:59392\n"
"v_mfma_f32_16x16x32_bf16 a[24:27], a[216:219], a[144:147], a[24:27]\n"
"ds_read_b128 a[144:147], v242 offset:61440\n"
"v_mfma_f32_16x16x32_bf16 a[24:27], a[220:223], a[148:151], a[24:27]\n"
"ds_read_b128 a[148:151], v243 offset:61440\n"
"v_mfma_f32_16x16x32_bf16 a[28:31], a[216:219], a[152:155], a[28:31]\n"
"global_load_dwordx4 a[216:219], v246, s[16:17] offset:2048 sc1\n"
"ds_read_b128 a[152:155], v242 offset:63488\n"
"v_mfma_f32_16x16x32_bf16 a[28:31], a[220:223], a[156:159], a[28:31]\n"
"ds_read_b128 a[156:159], v243 offset:63488\n"
"s_waitcnt vmcnt(16) lgkmcnt(14)\n"
"v_mfma_f32_16x16x32_bf16 v[0:3], a[160:163], a[96:99], v[0:3]\n"
"global_load_dwordx4 a[220:223], v246, s[16:17] offset:3072 sc1\n"
"v_mfma_f32_16x16x32_bf16 v[0:3], a[164:167], a[100:103], v[0:3]\n"
"s_waitcnt lgkmcnt(12)\n"
"v_mfma_f32_16x16x32_bf16 v[4:7], a[160:163], a[104:107], v[4:7]\n"
"v_add_u32 v246, 0x2000, v246\n"
"v_mfma_f32_16x16x32_bf16 v[4:7], a[164:167], a[108:111], v[4:7]\n"
"s_waitcnt lgkmcnt(10)\n"
"v_mfma_f32_16x16x32_bf16 v[8:11], a[160:163], a[112:115], v[8:11]\n"
"s_waitcnt vmcnt(11)\n"
"v_readfirstlane_b32 s46, v239\n"
"v_mfma_f32_16x16x32_bf16 v[8:11], a[164:167], a[116:119], v[8:11]\n"
"s_waitcnt lgkmcnt(8)\n"
"v_mfma_f32_16x16x32_bf16 v[12:15], a[160:163], a[120:123], v[12:15]\n"
"v_mfma_f32_16x16x32_bf16 v[12:15], a[164:167], a[124:127], v[12:15]\n"
"s_waitcnt lgkmcnt(6)\n"
"v_mfma_f32_16x16x32_bf16 v[16:19], a[160:163], a[128:131], v[16:19]\n"
"s_sub_u32 s46, s46, s44\n"
"v_mfma_f32_16x16x32_bf16 v[16:19], a[164:167], a[132:135], v[16:19]\n"
"s_waitcnt lgkmcnt(4)\n"
"v_mfma_f32_16x16x32_bf16 v[20:23], a[160:163], a[136:139], v[20:23]\n"
"s_cmp_ge_u32 s46, 146304\n"
"s_cbranch_scc0 .Lslow9_%=\n"
".Lback9_%=:\n"
"v_mfma_f32_16x16x32_bf16 v[20:23], a[164:167], a[140:143], v[20:23]\n"
"s_waitcnt lgkmcnt(2)\n"
"v_mfma_f32_16x16x32_bf16 v[24:27], a[160:163], a[144:147], v[24:27]\n"
"s_add_u32 m0, s42, 65536\n"
"v_add_u32 v238, 512, v248\n"
"v_mfma_f32_16x16x32_bf16 v[24:27], a[164:167], a[148:151], v[24:27]\n"
"s_waitcnt lgkmcnt(0)\n"
"v_mfma_f32_16x16x32_bf16 v[28:31], a[160:163], a[152:155], v[28:31]\n"
"global_load_dwordx4 a[160:163], v246, s[16:17] offset:-4096 sc1\n"
"v_mfma_f32_16x16x32_bf16 v[28:31], a[164:167], a[156:159], v[28:31]\n"
"v_mfma_f32_16x16x32_bf16 v[32:35], a[168:171], a[96:99], v[32:35]\n"
"global_load_dwordx4 a[164:167], v246, s[16:17] offset:-3072 sc1\n"
"v_mfma_f32_16x16x32_bf16 v[32:35], a[172:175], a[100:103], v[32:35]\n"
"global_load_lds_dwordx4 v238, s[26:27] offset:0 sc1\n"
"v_mfma_f32_16x16x32_bf16 v[36:39], a[168:171], a[104:107], v[36:39]\n"
"v_accvgpr_read_b32 v249, a27\n"
"v_mfma_f32_16x16x32_bf16 v[36:39], a[172:175], a[108:111], v[36:39]\n"
"v_add_f32 v249, v235, v249\n"
"v_mfma_f32_16x16x32_bf16 v[40:43], a[168:171], a[112:115], v[40:43]\n"
"v_accvgpr_write_b32 a27, v249\n"
"v_mfma_f32_16x16x32_bf16 v[40:43], a[172:175], a[116:119], v[40:43]\n"
"v_accvgpr_read_b32 v249, a28\n"
"v_mfma_f32_16x16x32_bf16 v[44:47], a[168:171], a[120:123], v[44:47]\n"
"v_add_f32 v249, v232, v249\n"
"v_mfma_f32_16x16x32_bf16 v[44:47], a[172:175], a[124:127], v[44:47]\n"
"v_accvgpr_write_b32 a28, v249\n"
"v_mfma_f32_16x16x32_bf16 v[48:51], a[168:171], a[128:131], v[48:51]\n"
"v_accvgpr_read_b32 v249, a29\n"
"v_mfma_f32_16x16x32_bf16 v[48:51], a[172:175], a[132:135], v[48:51]\n"
"v_add_f32 v249, v233, v249\n"
"v_mfma_f32_16x16x32_bf16 v[52:55], a[168:171], a[136:139], v[52:55]\n"
"v_accvgpr_write_b32 a29, v249\n"
"v_mfma_f32_16x16x32_bf16 v[52:55], a[172:175], a[140:143], v[52:55]\n"
"v_accvgpr_read_b32 v249, a30\n"
"v_mfma_f32_16x16x32_bf16 v[56:59], a[168:171], a[144:147], v[56:59]\n"
"v_add_f32 v249, v234, v249\n"
"v_mfma_f32_16x16x32_bf16 v[56:59], a[172:175], a[148:151], v[56:59]\n"
"v_accvgpr_write_b32 a30, v249\n"
"v_mfma_f32_16x16x32_bf16 v[60:63], a[168:171], a[152:155], v[60:63]\n"
"global_load_dwordx4 a[168:171], v246, s[16:17] offset:-2048 sc1\n"
"v_mfma_f32_16x16x32_bf16 v[60:63], a[172:175], a[156:159], v[60:63]\n"
"v_mfma_f32_16x16x32_bf16 v[64:67], a[176:179], a[96:99], v[64:67]\n"
"global_load_dwordx4 a[172:175], v246, s[16:17] offset:-1024 sc1\n"
"v_mfma_f32_16x16x32_bf16 v[64:67], a[180:183], a[100:103], v[64:67]\n"
"v_accvgpr_read_b32 v249, a31\n"
"v_mfma_f32_16x16x32_bf16 v[68:71], a[176:179], a[104:107], v[68:71]\n"
"v_add_f32 v249, v235, v249\n"
"v_mfma_f32_16x16x32_bf16 v[68:71], a[180:183], a[108:111], v[68:71]\n"
"v_add_u32 v238, s66, v238\n"
"v_accvgpr_write_b32 a31, v249\n"
"v_mfma_f32_16x16x32_bf16 v[72:75], a[176:179], a[112:115], v[72:75]\n"
"global_load_lds_dwordx4 v238, s[26:27] offset:1024 sc1\n"
"v_mfma_f32_16x16x32_bf16 v[72:75], a[180:183], a[116:119], v[72:75]\n"
"v_mfma_f32_16x16x32_bf16 v[76:79], a[176:179], a[120:123], v[76:79]\n"
"v_mfma_f32_16x16x32_bf16 v[76:79], a[180:183], a[124:127], v[76:79]\n"
"v_mfma_f32_16x16x32_bf16 v[80:83], a[176:179], a[128:131], v[80:83]\n"
"v_mfma_f32_16x16x32_bf16 v[80:83], a[180:183], a[132:135], v[80:83]\n"
"v_mfma_f32_16x16x32_bf16 v[84:87], a[176:179], a[136:139], v[84:87]\n"
"v_mfma_f32_16x16x32_bf16 v[84:87], a[180:183], a[140:143], v[84:87]\n"
"v_mfma_f32_16x16x32_bf16 v[88:91], a[176:179], a[144:147], v[88:91]\n"
"v_mfma_f32_16x16x32_bf16 v[88:91], a[180:183], a[148:151], v[88:91]\n"
"v_mfma_f32_16x16x32_bf16 v[92:95], a[176:179], a[152:155], v[92:95]\n"
"global_load_dwordx4 a[176:179], v246, s[16:17] offset:0 sc1\n"
"v_mfma_f32_16x16x32_bf16 v[92:95], a[180:183], a[156:159], v[92:95]\n"
"s_waitcnt vmcnt(16)\n"
"v_mfma_f32_16x16x32_bf16 v[96:99], a[184:187], a[96:99], v[96:99]\n"
"global_load_dwordx4 a[180:183], v246, s[16:17] offset:1024 sc1\n"
"v_mfma_f32_16x16x32_bf16 v[96:99], a[188:191], a[100:103], v[96:99]\n"
"v_mfma_f32_16x16x32_bf16 v[100:103], a[184:187], a[104:107], v[100:103]\n"
"v_mfma_f32_16x16x32_bf16 v[100:103], a[188:191], a[108:111], v[100:103]\n"
"v_mfma_f32_16x16x32_bf16 v[104:107], a[184:187], a[112:115], v[104:107]\n"
"v_mfma_f32_16x16x32_bf16 v[104:107], a[188:191], a[116:119], v[104:107]\n"
"v_mfma_f32_16x16x32_bf16 v[108:111], a[184:187], a[120:123], v[108:111]\n"
"v_add_u32 v238, s66, v238\n"
"v_mfma_f32_16x16x32_bf16 v[108:111], a[188:191], a[124:127], v[108:111]\n"
"global_load_lds_dwordx4 v238, s[26:27] offset:2048 sc1\n"
"v_mfma_f32_16x16x32_bf16 v[112:115], a[184:187], a[128:131], v[112:115]\n"
"v_mfma_f32_16x16x32_bf16 v[112:115], a[188:191], a[132:135], v[112:115]\n"
"v_mfma_f32_16x16x32_bf16 v[116:119], a[184:187], a[136:139], v[116:119]\n"
"v_mfma_f32_16x16x32_bf16 v[116:119], a[188:191], a[140:143], v[116:119]\n"
"v_mfma_f32_16x16x32_bf16 v[120:123], a[184:187], a[144:147], v[120:123]\n"
"v_mfma_f32_16x16x32_bf16 v[120:123], a[188:191], a[148:151], v[120:123]\n"
"v_mfma_f32_16x16x32_bf16 v[124:127], a[184:187], a[152:155], v[124:127]\n"
"global_load_dwordx4 a[184:187], v246, s[16:17] offset:2048 sc1\n"
"v_mfma_f32_16x16x32_bf16 v[124:127], a[188:191], a[156:159], v[124:127]\n"
"s_waitcnt vmcnt(16)\n"
"v_mfma_f32_16x16x32_bf16 v[128:131], a[192:195], a[96:99], v[128:131]\n"
"global_load_dwordx4 a[188:191], v246, s[16:17] offset:3072 sc1\n"
"v_mfma_f32_16x16x32_bf16 v[128:131], a[196:199], a[100:103], v[128:131]\n"
"v_mfma_f32_16x16x32_bf16 v[132:135], a[192:195], a[104:107], v[132:135]\n"
"v_add_u32 v246, 0x2000, v246\n"
"v_mfma_f32_16x16x32_bf16 v[132:135], a[196:199], a[108:111], v[132:135]\n"
"v_mfma_f32_16x16x32_bf16 v[136:139], a[192:195], a[112:115], v[136:139]\n"
"v_mfma_f32_16x16x32_bf16 v[136:139], a[196:199], a[116:119], v[136:139]\n"
"v_mfma_f32_16x16x32_bf16 v[140:143], a[192:195], a[120:123], v[140:143]\n"
"v_mfma_f32_16x16x32_bf16 v[140:143], a[196:199], a[124:127], v[140:143]\n"
"v_mfma_f32_16x16x32_bf16 v[144:147], a[192:195], a[128:131], v[144:147]\n"
"v_mfma_f32_16x16x32_bf16 v[144:147], a[196:199], a[132:135], v[144:147]\n"
"v_add_u32 v238, s66, v238\n"
"v_mfma_f32_16x16x32_bf16 v[148:151], a[192:195], a[136:139], v[148:151]\n"
"global_load_lds_dwordx4 v238, s[26:27] offset:3072 sc1\n"
"v_mfma_f32_16x16x32_bf16 v[148:151], a[196:199], a[140:143], v[148:151]\n"
"v_mfma_f32_16x16x32_bf16 v[152:155], a[192:195], a[144:147], v[152:155]\n"
"v_mfma_f32_16x16x32_bf16 v[152:155], a[196:199], a[148:151], v[152:155]\n"
"v_mfma_f32_16x16x32_bf16 v[156:159], a[192:195], a[152:155], v[156:159]\n"
"global_load_dwordx4 a[192:195], v246, s[16:17] offset:-4096 sc1\n"
"v_mfma_f32_16x16x32_bf16 v[156:159], a[196:199], a[156:159], v[156:159]\n"
"s_waitcnt vmcnt(17)\n"
"v_mfma_f32_16x16x32_bf16 v[160:163], a[200:203], a[96:99], v[160:163]\n"
"global_load_dwordx4 a[196:199], v246, s[16:17] offset:-3072 sc1\n"
"v_mfma_f32_16x16x32_bf16 v[160:163], a[204:207], a[100:103], v[160:163]\n"
"v_mfma_f32_16x16x32_bf16 v[164:167], a[200:203], a[104:107], v[164:167]\n"
"v_mfma_f32_16x16x32_bf16 v[164:167], a[204:207], a[108:111], v[164:167]\n"
"v_mfma_f32_16x16x32_bf16 v[168:171], a[200:203], a[112:115], v[168:171]\n"
"v_mfma_f32_16x16x32_bf16 v[168:171], a[204:207], a[116:119], v[168:171]\n"
"v_mfma_f32_16x16x32_bf16 v[172:175], a[200:203], a[120:123], v[172:175]\n"
"v_mfma_f32_16x16x32_bf16 v[172:175], a[204:207], a[124:127], v[172:175]\n"
"v_mfma_f32_16x16x32_bf16 v[176:179], a[200:203], a[128:131], v[176:179]\n"
"v_mfma_f32_16x16x32_bf16 v[176:179], a[204:207], a[132:135], v[176:179]\n"
"v_mfma_f32_16x16x32_bf16 v[180:183], a[200:203], a[136:139], v[180:183]\n"
"v_mfma_f32_16x16x32_bf16 v[180:183], a[204:207], a[140:143], v[180:183]\n"
"v_mfma_f32_16x16x32_bf16 v[184:187], a[200:203], a[144:147], v[184:187]\n"
"s_add_u32 m0, s42, 98304\n"
"v_add_u32 v238, 768, v248\n"
"v_mfma_f32_16x16x32_bf16 v[184:187], a[204:207], a[148:151], v[184:187]\n"
"global_load_lds_dwordx4 v238, s[26:27] offset:0 sc1\n"
"v_mfma_f32_16x16x32_bf16 v[188:191], a[200:203], a[152:155], v[188:191]\n"
"global_load_dwordx4 a[200:203], v246, s[16:17] offset:-2048 sc1\n"
"v_mfma_f32_16x16x32_bf16 v[188:191], a[204:207], a[156:159], v[188:191]\n"
"s_waitcnt vmcnt(18)\n"
"v_mfma_f32_16x16x32_bf16 v[192:195], a[208:211], a[96:99], v[192:195]\n"
"global_load_dwordx4 a[204:207], v246, s[16:17] offset:-1024 sc1\n"
"v_mfma_f32_16x16x32_bf16 v[192:195], a[212:215], a[100:103], v[192:195]\n"
"v_mfma_f32_16x16x32_bf16 v[196:199], a[208:211], a[104:107], v[196:199]\n"
"v_mfma_f32_16x16x32_bf16 v[196:199], a[212:215], a[108:111], v[196:199]\n"
"v_mfma_f32_16x16x32_bf16 v[200:203], a[208:211], a[112:115], v[200:203]\n"
"v_mfma_f32_16x16x32_bf16 v[200:203], a[212:215], a[116:119], v[200:203]\n"
"v_mfma_f32_16x16x32_bf16 v[204:207], a[208:211], a[120:123], v[204:207]\n"
"v_mfma_f32_16x16x32_bf16 v[204:207], a[212:215], a[124:127], v[204:207]\n"
"v_mfma_f32_16x16x32_bf16 v[208:211], a[208:211], a[128:131], v[208:211]\n"
"s_nop 0\n"
".Lpg20_%=:\n"
"s_cbranch_execz .Lretb_%=\n"
"v_mfma_f32_16x16x32_bf16 v[208:211], a[212:215], a[132:135], v[208:211]\n"
"v_mfma_f32_16x16x32_bf16 v[212:215], a[208:211], a[136:139], v[212:215]\n"
"v_mfma_f32_16x16x32_bf16 v[212:215], a[212:215], a[140:143], v[212:215]\n"
"v_mfma_f32_16x16x32_bf16 v[216:219], a[208:211], a[144:147], v[216:219]\n"
"v_mfma_f32_16x16x32_bf16 v[216:219], a[212:215], a[148:151], v[216:219]\n"
"s_barrier\n"
"v_mfma_f32_16x16x32_bf16 v[220:223], a[208:211], a[152:155], v[220:223]\n"
"global_load_dwordx4 a[208:211], v246, s[16:17] offset:0 sc1\n"
"v_mfma_f32_16x16x32_bf16 v[220:223], a[212:215], a[156:159], v[220:223]\n"
"s_waitcnt vmcnt(18)\n"
"v_mfma_f32_16x16x32_bf16 a[0:3], a[216:219], a[96:99], a[0:3]\n"
"global_load_dwordx4 a[212:215], v246, s[16:17] offset:1024 sc1\n"
"ds_read_b128 a[96:99], v240 offset:0\n"
"v_mfma_f32_16x16x32_bf16 a[0:3], a[220:223], a[100:103], a[0:3]\n"
"ds_read_b128 a[100:103], v241 offset:0\n"
"v_mfma_f32_16x16x32_bf16 a[4:7], a[216:219], a[104:107], a[4:7]\n"
"ds_read_b128 a[104:107], v240 offset:2048\n"
"v_mfma_f32_16x16x32_bf16 a[4:7], a[220:223], a[108:111], a[4:7]\n"
"ds_read_b128 a[108:111], v241 offset:2048\n"
"v_mfma_f32_16x16x32_bf16 a[8:11], a[216:219], a[112:115], a[8:11]\n"
"ds_read_b128 a[112:115], v240 offset:4096\n"
"v_mfma_f32_16x16x32_bf16 a[8:11], a[220:223], a[116:119], a[8:11]\n"
"ds_read_b128 a[116:119], v241 offset:4096\n"
"v_mfma_f32_16x16x32_bf16 a[12:15], a[216:219], a[120:123], a[12:15]\n"
"ds_read_b128 a[120:123], v240 offset:6144\n"
"v_mfma_f32_16x16x32_bf16 a[12:15], a[220:223], a[124:127], a[12:15]\n"
"ds_read_b128 a[124:127], v241 offset:6144\n"
"v_mfma_f32_16x16x32_bf16 a[16:19], a[216:219], a[128:131], a[16:19]\n"
"ds_read_b128 a[128:131], v240 offset:8192\n"
"v_mfma_f32_16x16x32_bf16 a[16:19], a[220:223], a[132:135], a[16:19]\n"
"ds_read_b128 a[132:135], v241 offset:8192\n"
"v_mfma_f32_16x16x32_bf16 a[20:23], a[216:219], a[136:139], a[20:23]\n"
"ds_read_b128 a[136:139], v240 offset:10240\n"
"v_mfma_f32_16x16x32_bf16 a[20:23], a[220:223], a[140:143], a[20:23]\n"
"ds_read_b128 a[140:143], v241 offset:10240\n"
"v_mfma_f32_16x16x32_bf16 a[24:27], a[216:219], a[144:147], a[24:27]\n"
"ds_read_b128 a[144:147], v240 offset:12288\n"
"v_mfma_f32_16x16x32_bf16 a[24:27], a[220:223], a[148:151], a[24:27]\n"
"ds_read_b128 a[148:151], v241 offset:12288\n"
"v_mfma_f32_16x16x32_bf16 a[28:31], a[216:219], a[152:155], a[28:31]\n"
"global_load_dwordx4 a[216:219], v246, s[16:17] offset:2048 sc1\n"
"ds_read_b128 a[152:155], v240 offset:14336\n"
"v_mfma_f32_16x16x32_bf16 a[28:31], a[220:223], a[156:159], a[28:31]\n"
"ds_read_b128 a[156:159], v241 offset:14336\n"
"s_waitcnt vmcnt(18) lgkmcnt(14)\n"
"v_mfma_f32_16x16x32_bf16 v[0:3], a[160:163], a[96:99], v[0:3]\n"
"global_load_dwordx4 a[220:223], v246, s[16:17] offset:3072 sc1\n"
"v_mfma_f32_16x16x32_bf16 v[0:3], a[164:167], a[100:103], v[0:3]\n"
"s_waitcnt lgkmcnt(12)\n"
"v_mfma_f32_16x16x32_bf16 v[4:7], a[160:163], a[104:107], v[4:7]\n"
"v_add_u32 v246, 0x2000, v246\n"
"v_mfma_f32_16x16x32_bf16 v[4:7], a[164:167], a[108:111], v[4:7]\n"
"s_waitcnt lgkmcnt(10)\n"
"v_mfma_f32_16x16x32_bf16 v[8:11], a[160:163], a[112:115], v[8:11]\n"
"v_add_u32 v238, s66, v238\n"
"v_mfma_f32_16x16x32_bf16 v[8:11], a[164:167], a[116:119], v[8:11]\n"
"s_waitcnt lgkmcnt(8)\n"
"v_mfma_f32_16x16x32_bf16 v[12:15], a[160:163], a[120:123], v[12:15]\n"
"global_load_lds_dwordx4 v238, s[26:27] offset:1024 sc1\n"
"v_mfma_f32_16x16x32_bf16 v[12:15], a[164:167], a[124:127], v[12:15]\n"
"s_waitcnt lgkmcnt(6)\n"
"v_mfma_f32_16x16x32_bf16 v[16:19], a[160:163], a[128:131], v[16:19]\n"
"v_mfma_f32_16x16x32_bf16 v[16:19], a[164:167], a[132:135], v[16:19]\n"
"s_waitcnt lgkmcnt(4)\n"
"v_mfma_f32_16x16x32_bf16 v[20:23], a[160:163], a[136:139], v[20:23]\n"
"v_mfma_f32_16x16x32_bf16 v[20:23], a[164:167], a[140:143], v[20:23]\n"
"s_waitcnt lgkmcnt(2)\n"
"v_mfma_f32_16x16x32_bf16 v[24:27], a[160:163], a[144:147], v[24:27]\n"
"v_mfma_f32_16x16x32_bf16 v[24:27], a[164:167], a[148:151], v[24:27]\n"
"s_waitcnt lgkmcnt(0)\n"
"v_mfma_f32_16x16x32_bf16 v[28:31], a[160:163], a[152:155], v[28:31]\n"
"global_load_dwordx4 a[160:163], v246, s[16:17] offset:-4096 sc1\n"
"v_mfma_f32_16x16x32_bf16 v[28:31], a[164:167], a[156:159], v[28:31]\n"
"s_waitcnt vmcnt(18)\n"
"v_mfma_f32_16x16x32_bf16 v[32:35], a[168:171], a[96:99], v[32:35]\n"
"global_load_dwordx4 a[164:167], v246, s[16:17] offset:-3072 sc1\n"
"v_mfma_f32_16x16x32_bf16 v[32:35], a[172:175], a[100:103], v[32:35]\n"
"v_mfma_f32_16x16x32_bf16 v[36:39], a[168:171], a[104:107], v[36:39]\n"
"v_mfma_f32_16x16x32_bf16 v[36:39], a[172:175], a[108:111], v[36:39]\n"
"v_mfma_f32_16x16x32_bf16 v[40:43], a[168:171], a[112:115], v[40:43]\n"
"s_add_u32 s64, s60, 86016\n"
"s_addc_u32 s65, s61, 0\n"
"v_mfma_f32_16x16x32_bf16 v[40:43], a[172:175], a[116:119], v[40:43]\n"
"v_mfma_f32_16x16x32_bf16 v[44:47], a[168:171], a[120:123], v[44:47]\n"
"v_mfma_f32_16x16x32_bf16 v[44:47], a[172:175], a[124:127], v[44:47]\n"
"v_mfma_f32_16x16x32_bf16 v[48:51], a[168:171], a[128:131], v[48:51]\n"
"v_add_u32 v238, s66, v238\n"
"v_mfma_f32_16x16x32_bf16 v[48:51], a[172:175], a[132:135], v[48:51]\n"
"global_load_lds_dwordx4 v238, s[26:27] offset:2048 sc1\n"
"v_mfma_f32_16x16x32_bf16 v[52:55], a[168:171], a[136:139], v[52:55]\n"
"v_mfma_f32_16x16x32_bf16 v[52:55], a[172:175], a[140:143], v[52:55]\n"
"v_mfma_f32_16x16x32_bf16 v[56:59], a[168:171], a[144:147], v[56:59]\n"
"v_mfma_f32_16x16x32_bf16 v[56:59], a[172:175], a[148:151], v[56:59]\n"
"v_mfma_f32_16x16x32_bf16 v[60:63], a[168:171], a[152:155], v[60:63]\n"
"global_load_dwordx4 a[168:171], v246, s[16:17] offset:-2048 sc1\n"
"v_mfma_f32_16x16x32_bf16 v[60:63], a[172:175], a[156:159], v[60:63]\n"
"s_waitcnt vmcnt(18)\n"
"v_mfma_f32_16x16x32_bf16 v[64:67], a[176:179], a[96:99], v[64:67]\n"
"global_load_dwordx4 a[172:175], v246, s[16:17] offset:-1024 sc1\n"
"v_mfma_f32_16x16x32_bf16 v[64:67], a[180:183], a[100:103], v[64:67]\n"
"v_mfma_f32_16x16x32_bf16 v[68:71], a[176:179], a[104:107], v[68:71]\n"
"v_mfma_f32_16x16x32_bf16 v[68:71], a[180:183], a[108:111], v[68:71]\n"
"v_mfma_f32_16x16x32_bf16 v[72:75], a[176:179], a[112:115], v[72:75]\n"
"v_mfma_f32_16x16x32_bf16 v[72:75], a[180:183], a[116:119], v[72:75]\n"
"v_mfma_f32_16x16x32_bf16 v[76:79], a[176:179], a[120:123], v[76:79]\n"
"v_mfma_f32_16x16x32_bf16 v[76:79], a[180:183], a[124:127], v[76:79]\n"
"v_mfma_f32_16x16x32_bf16 v[80:83], a[176:179], a[128:131], v[80:83]\n"
"v_mfma_f32_16x16x32_bf16 v[80:83], a[180:183], a[132:135], v[80:83]\n"
"v_mfma_f32_16x16x32_bf16 v[84:87], a[176:179], a[136:139], v[84:87]\n"
"v_mfma_f32_16x16x32_bf16 v[84:87], a[180:183], a[140:143], v[84:87]\n"
"v_add_u32 v238, s66, v238\n"
"v_mfma_f32_16x16x32_bf16 v[88:91], a[176:179], a[144:147], v[88:91]\n"
"global_load_lds_dwordx4 v238, s[26:27] offset:3072 sc1\n"
"v_mfma_f32_16x16x32_bf16 v[88:91], a[180:183], a[148:151], v[88:91]\n"
"v_mfma_f32_16x16x32_bf16 v[92:95], a[176:179], a[152:155], v[92:95]\n"
"global_load_dword a176, v250, s[64:65]\n"
"global_load_dwordx4 a[176:179], v246, s[16:17] offset:0 sc1\n"
"v_mfma_f32_16x16x32_bf16 v[92:95], a[180:183], a[156:159], v[92:95]\n"
"s_waitcnt vmcnt(19)\n"
"v_mfma_f32_16x16x32_bf16 v[96:99], a[184:187], a[96:99], v[96:99]\n"
"global_load_dwordx4 a[180:183], v246, s[16:17] offset:1024 sc1\n"
"ds_read_b128 a[96:99], v240 offset:32768\n"
"v_mfma_f32_16x16x32_bf16 v[96:99], a[188:191], a[100:103], v[96:99]\n"
"ds_read_b128 a[100:103], v241 offset:32768\n"
"v_mfma_f32_16x16x32_bf16 v[100:103], a[184:187], a[104:107], v[100:103]\n"
"ds_read_b128 a[104:107], v240 offset:34816\n"
"v_mfma_f32_16x16x32_bf16 v[100:103], a[188:191], a[108:111], v[100:103]\n"
"ds_read_b128 a[108:111], v241 offset:34816\n"
"v_mfma_f32_16x16x32_bf16 v[104:107], a[184:187], a[112:115], v[104:107]\n"
"ds_read_b128 a[112:115], v240 offset:36864\n"
"v_mfma_f32_16x16x32_bf16 v[104:107], a[188:191], a[116:119], v[104:107]\n"
"ds_read_b128 a[116:119], v241 offset:36864\n"
"v_mfma_f32_16x16x32_bf16 v[108:111], a[184:187], a[120:123], v[108:111]\n"
"ds_read_b128 a[120:123], v240 offset:38912\n"
"v_mfma_f32_16x16x32_bf16 v[108:111], a[188:191], a[124:127], v[108:111]\n"
"ds_read_b128 a[124:127], v241 offset:38912\n"
"v_mfma_f32_16x16x32_bf16 v[112:115], a[184:187], a[128:131], v[112:115]\n"
"ds_read_b128 a[128:131], v240 offset:40960\n"
"v_mfma_f32_16x16x32_bf16 v[112:115], a[188:191], a[132:135], v[112:115]\n"
"ds_read_b128 a[132:135], v241 offset:40960\n"
"v_mfma_f32_16x16x32_bf16 v[116:119], a[184:187], a[136:139], v[116:119]\n"
"ds_read_b128 a[136:139], v240 offset:43008\n"
"v_mfma_f32_16x16x32_bf16 v[116:119], a[188:191], a[140:143], v[116:119]\n"
"ds_read_b128 a[140:143], v241 offset:43008\n"
"v_mfma_f32_16x16x32_bf16 v[120:123], a[184:187], a[144:147], v[120:123]\n"
"ds_read_b128 a[144:147], v240 offset:45056\n"
"v_mfma_f32_16x16x32_bf16 v[120:123], a[188:191], a[148:151], v[120:123]\n"
"ds_read_b128 a[148:151], v241 offset:45056\n"
"v_mfma_f32_16x16x32_bf16 v[124:127], a[184:187], a[152:155], v[124:127]\n"
"global_load_dwordx4 a[184:187], v246, s[16:17] offset:2048 sc1\n"
"ds_read_b128 a[152:155], v240 offset:47104\n"
"v_mfma_f32_16x16x32_bf16 v[124:127], a[188:191], a[156:159], v[124:127]\n"
"ds_read_b128 a[156:159], v241 offset:47104\n"
"s_waitcnt vmcnt(18) lgkmcnt(14)\n"
"v_mfma_f32_16x16x32_bf16 v[0:3], a[192:195], a[96:99], v[0:3]\n"
"global_load_dwordx4 a[188:191], v246, s[16:17] offset:3072 sc1\n"
"v_mfma_f32_16x16x32_bf16 v[0:3], a[196:199], a[100:103], v[0:3]\n"
"s_waitcnt lgkmcnt(12)\n"
"v_mfma_f32_16x16x32_bf16 v[4:7], a[192:195], a[104:107], v[4:7]\n"
"v_add_u32 v246, 0x2000, v246\n"
"v_mfma_f32_16x16x32_bf16 v[4:7], a[196:199], a[108:111], v[4:7]\n"
"s_waitcnt lgkmcnt(10)\n"
"v_mfma_f32_16x16x32_bf16 v[8:11], a[192:195], a[112:115], v[8:11]\n"
"v_mfma_f32_16x16x32_bf16 v[8:11], a[196:199], a[116:119], v[8:11]\n"
"s_waitcnt lgkmcnt(8)\n"
"v_mfma_f32_16x16x32_bf16 v[12:15], a[192:195], a[120:123], v[12:15]\n"
"v_mfma_f32_16x16x32_bf16 v[12:15], a[196:199], a[124:127], v[12:15]\n"
"s_waitcnt lgkmcnt(6)\n"
"v_mfma_f32_16x16x32_bf16 v[16:19], a[192:195], a[128:131], v[16:19]\n"
"v_mfma_f32_16x16x32_bf16 v[16:19], a[196:199], a[132:135], v[16:19]\n"
"s_waitcnt lgkmcnt(4)\n"
"v_mfma_f32_16x16x32_bf16 v[20:23], a[192:195], a[136:139], v[20:23]\n"
"v_mfma_f32_16x16x32_bf16 v[20:23], a[196:199], a[140:143], v[20:23]\n"
"s_waitcnt lgkmcnt(2)\n"
"v_mfma_f32_16x16x32_bf16 v[24:27], a[192:195], a[144:147], v[24:27]\n"
"v_mfma_f32_16x16x32_bf16 v[24:27], a[196:199], a[148:151], v[24:27]\n"
"s_waitcnt lgkmcnt(0)\n"
"v_mfma_f32_16x16x32_bf16 v[28:31], a[192:195], a[152:155], v[28:31]\n"
"global_load_dwordx4 a[192:195], v246, s[16:17] offset:-4096 sc1\n"
"v_mfma_f32_16x16x32_bf16 v[28:31], a[196:199], a[156:159], v[28:31]\n"
"s_waitcnt vmcnt(17)\n"
"v_mfma_f32_16x16x32_bf16 v[32:35], a[200:203], a[96:99], v[32:35]\n"
"global_load_dwordx4 a[196:199], v246, s[16:17] offset:-3072 sc1\n"
"v_mfma_f32_16x16x32_bf16 v[32:35], a[204:207], a[100:103], v[32:35]\n"
"v_mfma_f32_16x16x32_bf16 v[36:39], a[200:203], a[104:107], v[36:39]\n"
"v_mfma_f32_16x16x32_bf16 v[36:39], a[204:207], a[108:111], v[36:39]\n"
"v_mfma_f32_16x16x32_bf16 v[40:43], a[200:203], a[112:115], v[40:43]\n"
"v_mfma_f32_16x16x32_bf16 v[40:43], a[204:207], a[116:119], v[40:43]\n"
"v_mfma_f32_16x16x32_bf16 v[44:47], a[200:203], a[120:123], v[44:47]\n"
"v_mfma_f32_16x16x32_bf16 v[44:47], a[204:207], a[124:127], v[44:47]\n"
"v_mfma_f32_16x16x32_bf16 v[48:51], a[200:203], a[128:131], v[48:51]\n"
"v_mfma_f32_16x16x32_bf16 v[48:51], a[204:207], a[132:135], v[48:51]\n"
"v_mfma_f32_16x16x32_bf16 v[52:55], a[200:203], a[136:139], v[52:55]\n"
"v_mfma_f32_16x16x32_bf16 v[52:55], a[204:207], a[140:143], v[52:55]\n"
"v_mfma_f32_16x16x32_bf16 v[56:59], a[200:203], a[144:147], v[56:59]\n"
"v_mfma_f32_16x16x32_bf16 v[56:59], a[204:207], a[148:151], v[56:59]\n"
"v_mfma_f32_16x16x32_bf16 v[60:63], a[200:203], a[152:155], v[60:63]\n"
"global_load_dwordx4 a[200:203], v246, s[16:17] offset:-2048 sc1\n"
"v_mfma_f32_16x16x32_bf16 v[60:63], a[204:207], a[156:159], v[60:63]\n"
"s_waitcnt vmcnt(17)\n"
"v_mfma_f32_16x16x32_bf16 v[64:67], a[208:211], a[96:99], v[64:67]\n"
"global_load_dwordx4 a[204:207], v246, s[16:17] offset:-1024 sc1\n"
"v_mfma_f32_16x16x32_bf16 v[64:67], a[212:215], a[100:103], v[64:67]\n"
"v_mfma_f32_16x16x32_bf16 v[68:71], a[208:211], a[104:107], v[68:71]\n"
"v_mfma_f32_16x16x32_bf16 v[68:71], a[212:215], a[108:111], v[68:71]\n"
"v_mfma_f32_16x16x32_bf16 v[72:75], a[208:211], a[112:115], v[72:75]\n"
"v_mfma_f32_16x16x32_bf16 v[72:75], a[212:215], a[116:119], v[72:75]\n"
"v_mfma_f32_16x16x32_bf16 v[76:79], a[208:211], a[120:123], v[76:79]\n"
"v_mfma_f32_16x16x32_bf16 v[76:79], a[212:215], a[124:127], v[76:79]\n"
"v_mfma_f32_16x16x32_bf16 v[80:83], a[208:211], a[128:131], v[80:83]\n"
"v_mfma_f32_16x16x32_bf16 v[80:83], a[212:215], a[132:135], v[80:83]\n"
"v_mfma_f32_16x16x32_bf16 v[84:87], a[208:211], a[136:139], v[84:87]\n"
"v_mfma_f32_16x16x32_bf16 v[84:87], a[212:215], a[140:143], v[84:87]\n"
"v_mfma_f32_16x16x32_bf16 v[88:91], a[208:211], a[144:147], v[88:91]\n"
"v_mfma_f32_16x16x32_bf16 v[88:91], a[212:215], a[148:151], v[88:91]\n"
"s_waitcnt vmcnt(9)\n"
"s_barrier\n"
"s_mov_b64 exec, 1\n"
"global_atomic_add v250, v252, s[30:31] offset:0 sc1\n"
"s_mov_b64 exec, -1\n"
"v_mfma_f32_16x16x32_bf16 v[92:95], a[208:211], a[152:155], v[92:95]\n"
"global_load_dwordx4 a[208:211], v246, s[16:17] offset:0 sc1\n"
"v_mfma_f32_16x16x32_bf16 v[92:95], a[212:215], a[156:159], v[92:95]\n"
"v_mfma_f32_16x16x32_bf16 v[96:99], a[216:219], a[96:99], v[96:99]\n"
"global_load_dwordx4 a[212:215], v246, s[16:17] offset:1024 sc1\n"
"ds_read_b128 a[96:99], v242 offset:0\n"
"v_mfma_f32_16x16x32_bf16 v[96:99], a[220:223], a[100:103], v[96:99]\n"
"ds_read_b128 a[100:103], v243 offset:0\n"
"v_mfma_f32_16x16x32_bf16 v[100:103], a[216:219], a[104:107], v[100:103]\n"
"ds_read_b128 a[104:107], v242 offset:2048\n"
"v_mfma_f32_16x16x32_bf16 v[100:103], a[220:223], a[108:111], v[100:103]\n"
"ds_read_b128 a[108:111], v243 offset:2048\n"
"v_mfma_f32_16x16x32_bf16 v[104:107], a[216:219], a[112:115], v[104:107]\n"
"ds_read_b128 a[112:115], v242 offset:4096\n"
"v_mfma_f32_16x16x32_bf16 v[104:107], a[220:223], a[116:119], v[104:107]\n"
"ds_read_b128 a[116:119], v243 offset:4096\n"
"v_mfma_f32_16x16x32_bf16 v[108:111], a[216:219], a[120:123], v[108:111]\n"
"ds_read_b128 a[120:123], v242 offset:6144\n"
"v_mfma_f32_16x16x32_bf16 v[108:111], a[220:223], a[124:127], v[108:111]\n"
"ds_read_b128 a[124:127], v243 offset:6144\n"
"v_mfma_f32_16x16x32_bf16 v[112:115], a[216:219], a[128:131], v[112:115]\n"
"ds_read_b128 a[128:131], v242 offset:8192\n"
"v_mfma_f32_16x16x32_bf16 v[112:115], a[220:223], a[132:135], v[112:115]\n"
"ds_read_b128 a[132:135], v243 offset:8192\n"
"v_mfma_f32_16x16x32_bf16 v[116:119], a[216:219], a[136:139], v[116:119]\n"
"ds_read_b128 a[136:139], v242 offset:10240\n"
"v_mfma_f32_16x16x32_bf16 v[116:119], a[220:223], a[140:143], v[116:119]\n"
"ds_read_b128 a[140:143], v243 offset:10240\n"
"v_mfma_f32_16x16x32_bf16 v[120:123], a[216:219], a[144:147], v[120:123]\n"
"ds_read_b128 a[144:147], v242 offset:12288\n"
"v_mfma_f32_16x16x32_bf16 v[120:123], a[220:223], a[148:151], v[120:123]\n"
"ds_read_b128 a[148:151], v243 offset:12288\n"
"v_mfma_f32_16x16x32_bf16 v[124:127], a[216:219], a[152:155], v[124:127]\n"
"global_load_dwordx4 a[216:219], v246, s[16:17] offset:2048 sc1\n"
"ds_read_b128 a[152:155], v242 offset:14336\n"
"v_mfma_f32_16x16x32_bf16 v[124:127], a[220:223], a[156:159], v[124:127]\n"
"ds_read_b128 a[156:159], v243 offset:14336\n"
"s_waitcnt lgkmcnt(14)\n"
"v_mfma_f32_16x16x32_bf16 v[0:3], a[160:163], a[96:99], v[0:3]\n"
"global_load_dwordx4 a[220:223], v246, s[16:17] offset:3072 sc1\n"
"v_mfma_f32_16x16x32_bf16 v[0:3], a[164:167], a[100:103], v[0:3]\n"
"s_waitcnt lgkmcnt(12)\n"
"v_mfma_f32_16x16x32_bf16 v[4:7], a[160:163], a[104:107], v[4:7]\n"
"v_add_u32 v246, 0x2000, v246\n"
"v_mfma_f32_16x16x32_bf16 v[4:7], a[164:167], a[108:111], v[4:7]\n"
"s_waitcnt lgkmcnt(10)\n"
"v_mfma_f32_16x16x32_bf16 v[8:11], a[160:163], a[112:115], v[8:11]\n"
"v_mfma_f32_16x16x32_bf16 v[8:11], a[164:167], a[116:119], v[8:11]\n"
"s_waitcnt lgkmcnt(8)\n"
"v_mfma_f32_16x16x32_bf16 v[12:15], a[160:163], a[120:123], v[12:15]\n"
"v_mfma_f32_16x16x32_bf16 v[12:15], a[164:167], a[124:127], v[12:15]\n"
"s_waitcnt lgkmcnt(6)\n"
"v_mfma_f32_16x16x32_bf16 v[16:19], a[160:163], a[128:131], v[16:19]\n"
"v_mfma_f32_16x16x32_bf16 v[16:19], a[164:167], a[132:135], v[16:19]\n"
"s_waitcnt lgkmcnt(4)\n"
"v_mfma_f32_16x16x32_bf16 v[20:23], a[160:163], a[136:139], v[20:23]\n"
"v_mfma_f32_16x16x32_bf16 v[20:23], a[164:167], a[140:143], v[20:23]\n"
"s_waitcnt lgkmcnt(2)\n"
"v_mfma_f32_16x16x32_bf16 v[24:27], a[160:163], a[144:147], v[24:27]\n"
"v_mfma_f32_16x16x32_bf16 v[24:27], a[164:167], a[148:151], v[24:27]\n"
"s_waitcnt lgkmcnt(0)\n"
"v_mfma_f32_16x16x32_bf16 v[28:31], a[160:163], a[152:155], v[28:31]\n"
"global_load_dwordx4 a[160:163], v246, s[16:17] offset:-4096 sc1\n"
"v_mfma_f32_16x16x32_bf16 v[28:31], a[164:167], a[156:159], v[28:31]\n"
"v_mfma_f32_16x16x32_bf16 v[32:35], a[168:171], a[96:99], v[32:35]\n"
"global_load_dwordx4 a[164:167], v246, s[16:17] offset:-3072 sc1\n"
"v_mfma_f32_16x16x32_bf16 v[32:35], a[172:175], a[100:103], v[32:35]\n"
"v_mfma_f32_16x16x32_bf16 v[36:39], a[168:171], a[104:107], v[36:39]\n"
"v_mfma_f32_16x16x32_bf16 v[36:39], a[172:175], a[108:111], v[36:39]\n"
"v_mfma_f32_16x16x32_bf16 v[40:43], a[168:171], a[112:115], v[40:43]\n"
"v_mfma_f32_16x16x32_bf16 v[40:43], a[172:175], a[116:119], v[40:43]\n"
"v_mfma_f32_16x16x32_bf16 v[44:47], a[168:171], a[120:123], v[44:47]\n"
"v_mfma_f32_16x16x32_bf16 v[44:47], a[172:175], a[124:127], v[44:47]\n"
"v_mfma_f32_16x16x32_bf16 v[48:51], a[168:171], a[128:131], v[48:51]\n"
"v_mfma_f32_16x16x32_bf16 v[48:51], a[172:175], a[132:135], v[48:51]\n"
"v_mfma_f32_16x16x32_bf16 v[52:55], a[168:171], a[136:139], v[52:55]\n"
"v_mfma_f32_16x16x32_bf16 v[52:55], a[172:175], a[140:143], v[52:55]\n"
"v_mfma_f32_16x16x32_bf16 v[56:59], a[168:171], a[144:147], v[56:59]\n"
"v_mfma_f32_16x16x32_bf16 v[56:59], a[172:175], a[148:151], v[56:59]\n"
"v_mfma_f32_16x16x32_bf16 v[60:63], a[168:171], a[152:155], v[60:63]\n"
"global_load_dwordx4 a[168:171], v246, s[16:17] offset:-2048 sc1\n"
"v_mfma_f32_16x16x32_bf16 v[60:63], a[172:175], a[156:159], v[60:63]\n"
"s_waitcnt vmcnt(14)\n"
"v_mfma_f32_16x16x32_bf16 v[64:67], a[176:179], a[96:99], v[64:67]\n"
"global_load_dwordx4 a[172:175], v246, s[16:17] offset:-1024 sc1\n"
"v_mfma_f32_16x16x32_bf16 v[64:67], a[180:183], a[100:103], v[64:67]\n"
"v_mfma_f32_16x16x32_bf16 v[68:71], a[176:179], a[104:107], v[68:71]\n"
"v_mfma_f32_16x16x32_bf16 v[68:71], a[180:183], a[108:111], v[68:71]\n"
"v_mfma_f32_16x16x32_bf16 v[72:75], a[176:179], a[112:115], v[72:75]\n"
"v_mfma_f32_16x16x32_bf16 v[72:75], a[180:183], a[116:119], v[72:75]\n"
"v_mfma_f32_16x16x32_bf16 v[76:79], a[176:179], a[120:123], v[76:79]\n"
"v_mfma_f32_16x16x32_bf16 v[76:79], a[180:183], a[124:127], v[76:79]\n"
"v_mfma_f32_16x16x32_bf16 v[80:83], a[176:179], a[128:131], v[80:83]\n"
"v_mfma_f32_16x16x32_bf16 v[80:83], a[180:183], a[132:135], v[80:83]\n"
"v_mfma_f32_16x16x32_bf16 v[84:87], a[176:179], a[136:139], v[84:87]\n"
"v_mfma_f32_16x16x32_bf16 v[84:87], a[180:183], a[140:143], v[84:87]\n"
"v_mfma_f32_16x16x32_bf16 v[88:91], a[176:179], a[144:147], v[88:91]\n"
"v_mfma_f32_16x16x32_bf16 v[88:91], a[180:183], a[148:151], v[88:91]\n"
"v_mfma_f32_16x16x32_bf16 v[92:95], a[176:179], a[152:155], v[92:95]\n"
"global_load_dwordx4 a[176:179], v246, s[16:17] offset:0 sc1\n"
"v_mfma_f32_16x16x32_bf16 v[92:95], a[180:183], a[156:159], v[92:95]\n"
"s_waitcnt vmcnt(14)\n"
"v_mfma_f32_16x16x32_bf16 v[96:99], a[184:187], a[96:99], v[96:99]\n"
"global_load_dwordx4 a[180:183], v246, s[16:17] offset:1024 sc1\n"
"ds_read_b128 a[96:99], v242 offset:32768\n"
"v_mfma_f32_16x16x32_bf16 v[96:99], a[188:191], a[100:103], v[96:99]\n"
"ds_read_b128 a[100:103], v243 offset:32768\n"
"v_mfma_f32_16x16x32_bf16 v[100:103], a[184:187], a[104:107], v[100:103]\n"
"ds_read_b128 a[104:107], v242 offset:34816\n"
"v_mfma_f32_16x16x32_bf16 v[100:103], a[188:191], a[108:111], v[100:103]\n"
"ds_read_b128 a[108:111], v243 offset:34816\n"
"v_mfma_f32_16x16x32_bf16 v[104:107], a[184:187], a[112:115], v[104:107]\n"
"ds_read_b128 a[112:115], v242 offset:36864\n"
"v_mfma_f32_16x16x32_bf16 v[104:107], a[188:191], a[116:119], v[104:107]\n"
"ds_read_b128 a[116:119], v243 offset:36864\n"
"v_mfma_f32_16x16x32_bf16 v[108:111], a[184:187], a[120:123], v[108:111]\n"
"ds_read_b128 a[120:123], v242 offset:38912\n"
"v_mfma_f32_16x16x32_bf16 v[108:111], a[188:191], a[124:127], v[108:111]\n"
"ds_read_b128 a[124:127], v243 offset:38912\n"
"v_mfma_f32_16x16x32_bf16 v[112:115], a[184:187], a[128:131], v[112:115]\n"
"ds_read_b128 a[128:131], v242 offset:40960\n"
"v_mfma_f32_16x16x32_bf16 v[112:115], a[188:191], a[132:135], v[112:115]\n"
"ds_read_b128 a[132:135], v243 offset:40960\n"
"v_mfma_f32_16x16x32_bf16 v[116:119], a[184:187], a[136:139], v[116:119]\n"
"ds_read_b128 a[136:139], v242 offset:43008\n"
"v_mfma_f32_16x16x32_bf16 v[116:119], a[188:191], a[140:143], v[116:119]\n"
"ds_read_b128 a[140:143], v243 offset:43008\n"
"v_mfma_f32_16x16x32_bf16 v[120:123], a[184:187], a[144:147], v[120:123]\n"
"ds_read_b128 a[144:147], v242 offset:45056\n"
"v_mfma_f32_16x16x32_bf16 v[120:123], a[188:191], a[148:151], v[120:123]\n"
"ds_read_b128 a[148:151], v243 offset:45056\n"
"v_mfma_f32_16x16x32_bf16 v[124:127], a[184:187], a[152:155], v[124:127]\n"
"global_load_dwordx4 a[184:187], v246, s[16:17] offset:2048 sc1\n"
"ds_read_b128 a[152:155], v242 offset:47104\n"
"v_mfma_f32_16x16x32_bf16 v[124:127], a[188:191], a[156:159], v[124:127]\n"
"ds_read_b128 a[156:159], v243 offset:47104\n"
"s_waitcnt vmcnt(14) lgkmcnt(14)\n"
"v_mfma_f32_16x16x32_bf16 v[0:3], a[192:195], a[96:99], v[0:3]\n"
"global_load_dwordx4 a[188:191], v246, s[16:17] offset:3072 sc1\n"
"v_mfma_f32_16x16x32_bf16 v[0:3], a[196:199], a[100:103], v[0:3]\n"
"s_waitcnt lgkmcnt(12)\n"
"v_mfma_f32_16x16x32_bf16 v[4:7], a[192:195], a[104:107], v[4:7]\n"
"v_add_u32 v246, 0x2000, v246\n"
"v_mfma_f32_16x16x32_bf16 v[4:7], a[196:199], a[108:111], v[4:7]\n"
"s_waitcnt lgkmcnt(10)\n"
"v_mfma_f32_16x16x32_bf16 v[8:11], a[192:195], a[112:115], v[8:11]\n"
"global_load_dword v239, v250, s[34:35] sc1\n"
"v_mfma_f32_16x16x32_bf16 v[8:11], a[196:199], a[116:119], v[8:11]\n"
"s_waitcnt lgkmcnt(8)\n"
"v_mfma_f32_16x16x32_bf16 v[12:15], a[192:195], a[120:123], v[12:15]\n"
"s_add_u32 s64, s60, 90112\n"
"s_addc_u32 s65, s61, 0\n"
"v_mfma_f32_16x16x32_bf16 v[12:15], a[196:199], a[124:127], v[12:15]\n"
"s_waitcnt lgkmcnt(6)\n"
"v_mfma_f32_16x16x32_bf16 v[16:19], a[192:195], a[128:131], v[16:19]\n"
"v_mfma_f32_16x16x32_bf16 v[16:19], a[196:199], a[132:135], v[16:19]\n"
"s_waitcnt lgkmcnt(4)\n"
"v_mfma_f32_16x16x32_bf16 v[20:23], a[192:195], a[136:139], v[20:23]\n"
"v_mfma_f32_16x16x32_bf16 v[20:23], a[196:199], a[140:143], v[20:23]\n"
"s_waitcnt lgkmcnt(2)\n"
"v_mfma_f32_16x16x32_bf16 v[24:27], a[192:195], a[144:147], v[24:27]\n"
"v_mfma_f32_16x16x32_bf16 v[24:27], a[196:199], a[148:151], v[24:27]\n"
"s_waitcnt lgkmcnt(0)\n"
"v_mfma_f32_16x16x32_bf16 v[28:31], a[192:195], a[152:155], v[28:31]\n"
"global_load_dword a192, v250, s[64:65]\n"
"global_load_dwordx4 a[192:195], v246, s[16:17] offset:-4096 sc1\n"
"v_mfma_f32_16x16x32_bf16 v[28:31], a[196:199], a[156:159], v[28:31]\n"
"s_waitcnt vmcnt(16)\n"
"v_mfma_f32_16x16x32_bf16 v[32:35], a[200:203], a[96:99], v[32:35]\n"
"global_load_dwordx4 a[196:199], v246, s[16:17] offset:-3072 sc1\n"
"v_mfma_f32_16x16x32_bf16 v[32:35], a[204:207], a[100:103], v[32:35]\n"
"v_mfma_f32_16x16x32_bf16 v[36:39], a[200:203], a[104:107], v[36:39]\n"
"v_mfma_f32_16x16x32_bf16 v[36:39], a[204:207], a[108:111], v[36:39]\n"
"v_mfma_f32_16x16x32_bf16 v[40:43], a[200:203], a[112:115], v[40:43]\n"
"v_mfma_f32_16x16x32_bf16 v[40:43], a[204:207], a[116:119], v[40:43]\n"
"v_mfma_f32_16x16x32_bf16 v[44:47], a[200:203], a[120:123], v[44:47]\n"
"v_bfe_u32 v236, v247, 4, 4\n"
"s_mul_i32 s48, %1, 28672\n"
"v_mfma_f32_16x16x32_bf16 v[44:47], a[204:207], a[124:127], v[44:47]\n"
"v_bfe_u32 v249, v247, 8, 2\n"
"v_add_u32 v234, s48, v247\n"
"v_mfma_f32_16x16x32_bf16 v[48:51], a[200:203], a[128:131], v[48:51]\n"
"v_lshl_add_u32 v236, v236, 2, v249\n"
"v_mfma_f32_16x16x32_bf16 v[48:51], a[204:207], a[132:135], v[48:51]\n"
"v_lshl_add_u32 v236, v236, 4, s43\n"
"v_mfma_f32_16x16x32_bf16 v[52:55], a[200:203], a[136:139], v[52:55]\n"
"v_mfma_f32_16x16x32_bf16 v[52:55], a[204:207], a[140:143], v[52:55]\n"
"v_mfma_f32_16x16x32_bf16 v[56:59], a[200:203], a[144:147], v[56:59]\n"
"v_mfma_f32_16x16x32_bf16 v[56:59], a[204:207], a[148:151], v[56:59]\n"
"v_mfma_f32_16x16x32_bf16 v[60:63], a[200:203], a[152:155], v[60:63]\n"
"global_load_dwordx4 a[200:203], v246, s[16:17] offset:-2048 sc1\n"
"v_mfma_f32_16x16x32_bf16 v[60:63], a[204:207], a[156:159], v[60:63]\n"
"s_waitcnt vmcnt(15)\n"
"v_mfma_f32_16x16x32_bf16 v[64:67], a[208:211], a[96:99], v[64:67]\n"
"global_load_dwordx4 a[204:207], v246, s[16:17] offset:-1024 sc1\n"
"v_mfma_f32_16x16x32_bf16 v[64:67], a[212:215], a[100:103], v[64:67]\n"
"v_mfma_f32_16x16x32_bf16 v[68:71], a[208:211], a[104:107], v[68:71]\n"
"v_mfma_f32_16x16x32_bf16 v[68:71], a[212:215], a[108:111], v[68:71]\n"
"v_mfma_f32_16x16x32_bf16 v[72:75], a[208:211], a[112:115], v[72:75]\n"
"v_mfma_f32_16x16x32_bf16 v[72:75], a[212:215], a[116:119], v[72:75]\n"
"v_mfma_f32_16x16x32_bf16 v[76:79], a[208:211], a[120:123], v[76:79]\n"
"v_mfma_f32_16x16x32_bf16 v[76:79], a[212:215], a[124:127], v[76:79]\n"
"v_mfma_f32_16x16x32_bf16 v[80:83], a[208:211], a[128:131], v[80:83]\n"
"v_mfma_f32_16x16x32_bf16 v[80:83], a[212:215], a[132:135], v[80:83]\n"
"v_mfma_f32_16x16x32_bf16 v[84:87], a[208:211], a[136:139], v[84:87]\n"
"v_mfma_f32_16x16x32_bf16 v[84:87], a[212:215], a[140:143], v[84:87]\n"
"v_mfma_f32_16x16x32_bf16 v[88:91], a[208:211], a[144:147], v[88:91]\n"
"v_mfma_f32_16x16x32_bf16 v[88:91], a[212:215], a[148:151], v[88:91]\n"
"v_mfma_f32_16x16x32_bf16 v[92:95], a[208:211], a[152:155], v[92:95]\n"
"global_load_dwordx4 a[208:211], v246, s[16:17] offset:0 sc1\n"
"v_mfma_f32_16x16x32_bf16 v[92:95], a[212:215], a[156:159], v[92:95]\n"
"s_waitcnt vmcnt(15)\n"
"v_mfma_f32_16x16x32_bf16 v[96:99], a[216:219], a[96:99], v[96:99]\n"
"global_load_dwordx4 a[212:215], v246, s[16:17] offset:1024 sc1\n"
"ds_read_b128 a[96:99], v240 offset:0\n"
"v_mfma_f32_16x16x32_bf16 v[96:99], a[220:223], a[100:103], v[96:99]\n"
"ds_read_b128 a[100:103], v241 offset:0\n"
"v_mfma_f32_16x16x32_bf16 v[100:103], a[216:219], a[104:107], v[100:103]\n"
"ds_read_b128 a[104:107], v240 offset:2048\n"
"v_mfma_f32_16x16x32_bf16 v[100:103], a[220:223], a[108:111], v[100:103]\n"
"ds_read_b128 a[108:111], v241 offset:2048\n"
"v_mfma_f32_16x16x32_bf16 v[104:107], a[216:219], a[112:115], v[104:107]\n"
"ds_read_b128 a[112:115], v240 offset:4096\n"
"v_mfma_f32_16x16x32_bf16 v[104:107], a[220:223], a[116:119], v[104:107]\n"
"ds_read_b128 a[116:119], v241 offset:4096\n"
"v_mfma_f32_16x16x32_bf16 v[108:111], a[216:219], a[120:123], v[108:111]\n"
"ds_read_b128 a[120:123], v240 offset:6144\n"
"v_mfma_f32_16x16x32_bf16 v[108:111], a[220:223], a[124:127], v[108:111]\n"
"ds_read_b128 a[124:127], v241 offset:6144\n"
"v_mfma_f32_16x16x32_bf16 v[112:115], a[216:219], a[128:131], v[112:115]\n"
"ds_read_b128 a[128:131], v240 offset:8192\n"
"v_mfma_f32_16x16x32_bf16 v[112:115], a[220:223], a[132:135], v[112:115]\n"
"ds_read_b128 a[132:135], v241 offset:8192\n"
"v_mfma_f32_16x16x32_bf16 v[116:119], a[216:219], a[136:139], v[116:119]\n"
"ds_read_b128 a[136:139], v240 offset:10240\n"
"v_mfma_f32_16x16x32_bf16 v[116:119], a[220:223], a[140:143], v[116:119]\n"
"ds_read_b128 a[140:143], v241 offset:10240\n"
"v_mfma_f32_16x16x32_bf16 v[120:123], a[216:219], a[144:147], v[120:123]\n"
"ds_read_b128 a[144:147], v240 offset:12288\n"
"v_mfma_f32_16x16x32_bf16 v[120:123], a[220:223], a[148:151], v[120:123]\n"
"ds_read_b128 a[148:151], v241 offset:12288\n"
"v_mfma_f32_16x16x32_bf16 v[124:127], a[216:219], a[152:155], v[124:127]\n"
"global_load_dwordx4 a[216:219], v246, s[16:17] offset:2048 sc1\n"
"ds_read_b128 a[152:155], v240 offset:14336\n"
"v_mfma_f32_16x16x32_bf16 v[124:127], a[220:223], a[156:159], v[124:127]\n"
"ds_read_b128 a[156:159], v241 offset:14336\n"
"s_waitcnt vmcnt(15) lgkmcnt(14)\n"
"v_mfma_f32_16x16x32_bf16 v[128:131], a[160:163], a[96:99], v[128:131]\n"
"global_load_dwordx4 a[220:223], v246, s[16:17] offset:3072 sc1\n"
"v_mfma_f32_16x16x32_bf16 v[128:131], a[164:167], a[100:103], v[128:131]\n"
"s_waitcnt lgkmcnt(12)\n"
"v_mfma_f32_16x16x32_bf16 v[132:135], a[160:163], a[104:107], v[132:135]\n"
"v_add_u32 v246, 0x2000, v246\n"
"v_mfma_f32_16x16x32_bf16 v[224:227], v[252:255], a[32:35], 0\n"
"v_mfma_f32_16x16x32_bf16 v[132:135], a[164:167], a[108:111], v[132:135]\n"
"s_waitcnt lgkmcnt(10)\n"
"v_mfma_f32_16x16x32_bf16 v[136:139], a[160:163], a[112:115], v[136:139]\n"
"v_mfma_f32_16x16x32_bf16 v[136:139], a[164:167], a[116:119], v[136:139]\n"
"s_waitcnt lgkmcnt(8)\n"
"v_mfma_f32_16x16x32_bf16 v[140:143], a[160:163], a[120:123], v[140:143]\n"
"v_mfma_f32_16x16x32_bf16 v[140:143], a[164:167], a[124:127], v[140:143]\n"
"s_nop 0\n"
"v_mul_f32 v224, v0, v224\n"
"s_waitcnt lgkmcnt(6)\n"
"v_mfma_f32_16x16x32_bf16 v[144:147], a[160:163], a[128:131], v[144:147]\n"
"s_waitcnt vmcnt(9)\n"
"v_readfirstlane_b32 s46, v239\n"
"v_mul_f32 v225, v1, v225\n"
"v_mfma_f32_16x16x32_bf16 v[144:147], a[164:167], a[132:135], v[144:147]\n"
"v_mfma_f32_16x16x32_bf16 v[228:231], v[250:253], a[32:35], 0\n"
"s_waitcnt lgkmcnt(4)\n"
"v_mfma_f32_16x16x32_bf16 v[148:151], a[160:163], a[136:139], v[148:151]\n"
"v_mul_f32 v226, v2, v226\n"
"v_mul_f32 v227, v3, v227\n"
"v_mfma_f32_16x16x32_bf16 v[148:151], a[164:167], a[140:143], v[148:151]\n"
"v_cvt_pk_bf16_f32 v224, v224, v225\n"
"s_waitcnt lgkmcnt(2)\n"
"v_mfma_f32_16x16x32_bf16 v[152:155], a[160:163], a[144:147], v[152:155]\n"
"s_sub_u32 s46, s46, s44\n"
"v_cvt_pk_bf16_f32 v225, v226, v227\n"
"v_mfma_f32_16x16x32_bf16 v[152:155], a[164:167], a[148:151], v[152:155]\n"
"ds_write_b64 v244, v[224:225] offset:16384\n"
"s_waitcnt lgkmcnt(1)\n"
"v_mfma_f32_16x16x32_bf16 v[156:159], a[160:163], a[152:155], v[156:159]\n"
"global_load_dwordx4 a[160:163], v246, s[16:17] offset:-4096 sc1\n"
"v_mul_f32 v228, v32, v228\n"
"v_mfma_f32_16x16x32_bf16 v[156:159], a[164:167], a[156:159], v[156:159]\n"
"v_mul_f32 v229, v33, v229\n"
"v_mfma_f32_16x16x32_bf16 v[160:163], a[168:171], a[96:99], v[160:163]\n"
"global_load_dwordx4 a[164:167], v246, s[16:17] offset:-3072 sc1\n"
"v_mfma_f32_16x16x32_bf16 v[224:227], v[252:255], a[64:67], 0\n"
"v_mfma_f32_16x16x32_bf16 v[160:163], a[172:175], a[100:103], v[160:163]\n"
"s_cmp_ge_u32 s46, 162560\n"
"s_cbranch_scc0 .Lslow10_%=\n"
".Lback10_%=:\n"
"v_mfma_f32_16x16x32_bf16 v[164:167], a[168:171], a[104:107], v[164:167]\n"
"v_mul_f32 v230, v34, v230\n"
"v_mul_f32 v231, v35, v231\n"
"v_mfma_f32_16x16x32_bf16 v[164:167], a[172:175], a[108:111], v[164:167]\n"
"v_cvt_pk_bf16_f32 v228, v228, v229\n"
"v_cvt_pk_bf16_f32 v229, v230, v231\n"
"v_mfma_f32_16x16x32_bf16 v[168:171], a[168:171], a[112:115], v[168:171]\n"
"ds_write_b64 v244, v[228:229] offset:16392\n"
"v_mul_f32 v224, v64, v224\n"
"v_mfma_f32_16x16x32_bf16 v[168:171], a[172:175], a[116:119], v[168:171]\n"
"v_mul_f32 v225, v65, v225\n"
"v_mfma_f32_16x16x32_bf16 v[228:231], v[250:253], a[64:67], 0\n"
"v_mfma_f32_16x16x32_bf16 v[172:175], a[168:171], a[120:123], v[172:175]\n"
"v_mul_f32 v226, v66, v226\n"
"v_mul_f32 v227, v67, v227\n"
"v_mfma_f32_16x16x32_bf16 v[172:175], a[172:175], a[124:127], v[172:175]\n"
"v_cvt_pk_bf16_f32 v224, v224, v225\n"
"v_cvt_pk_bf16_f32 v225, v226, v227\n"
"v_mfma_f32_16x16x32_bf16 v[176:179], a[168:171], a[128:131], v[176:179]\n"
"ds_write_b64 v245, v[224:225] offset:16384\n"
"v_mfma_f32_16x16x32_bf16 v[176:179], a[172:175], a[132:135], v[176:179]\n"
"v_mul_f32 v228, v96, v228\n"
"v_mul_f32 v229, v97, v229\n"
"v_mfma_f32_16x16x32_bf16 v[180:183], a[168:171], a[136:139], v[180:183]\n"
"v_mfma_f32_16x16x32_bf16 v[224:227], v[252:255], a[36:39], 0\n"
"v_mfma_f32_16x16x32_bf16 v[180:183], a[172:175], a[140:143], v[180:183]\n"
"v_mul_f32 v230, v98, v230\n"
"v_mfma_f32_16x16x32_bf16 v[184:187], a[168:171], a[144:147], v[184:187]\n"
"v_mul_f32 v231, v99, v231\n"
"v_cvt_pk_bf16_f32 v228, v228, v229\n"
"v_mfma_f32_16x16x32_bf16 v[184:187], a[172:175], a[148:151], v[184:187]\n"
"v_cvt_pk_bf16_f32 v229, v230, v231\n"
"v_mfma_f32_16x16x32_bf16 v[188:191], a[168:171], a[152:155], v[188:191]\n"
"global_load_dwordx4 a[168:171], v246, s[16:17] offset:-2048 sc1\n"
"ds_write_b64 v245, v[228:229] offset:16392\n"
"v_mfma_f32_16x16x32_bf16 v[188:191], a[172:175], a[156:159], v[188:191]\n"
"ds_read_b128 a[32:35], v234 offset:16384\n"
"v_mfma_f32_16x16x32_bf16 v[192:195], a[176:179], a[96:99], v[192:195]\n"
"global_load_dwordx4 a[172:175], v246, s[16:17] offset:-1024 sc1\n"
".Lpg21_%=:\n"
"s_cbranch_execz .Lretb_%=\n"
"ds_read_b128 a[64:67], v234 offset:17408\n"
"v_mfma_f32_16x16x32_bf16 v[192:195], a[180:183], a[100:103], v[192:195]\n"
"v_mul_f32 v224, v4, v224\n"
"v_mul_f32 v225, v5, v225\n"
"v_mfma_f32_16x16x32_bf16 v[196:199], a[176:179], a[104:107], v[196:199]\n"
"v_mfma_f32_16x16x32_bf16 v[228:231], v[250:253], a[36:39], 0\n"
"v_mul_f32 v226, v6, v226\n"
"v_mfma_f32_16x16x32_bf16 v[196:199], a[180:183], a[108:111], v[196:199]\n"
"v_mul_f32 v227, v7, v227\n"
"v_cvt_pk_bf16_f32 v224, v224, v225\n"
"v_mfma_f32_16x16x32_bf16 v[200:203], a[176:179], a[112:115], v[200:203]\n"
"v_cvt_pk_bf16_f32 v225, v226, v227\n"
"ds_write_b64 v244, v[224:225] offset:18432\n"
"v_mfma_f32_16x16x32_bf16 v[200:203], a[180:183], a[116:119], v[200:203]\n"
"v_mul_f32 v228, v36, v228\n"
"v_mul_f32 v229, v37, v229\n"
"v_mfma_f32_16x16x32_bf16 v[204:207], a[176:179], a[120:123], v[204:207]\n"
"v_mfma_f32_16x16x32_bf16 v[224:227], v[252:255], a[68:71], 0\n"
"v_mfma_f32_16x16x32_bf16 v[204:207], a[180:183], a[124:127], v[204:207]\n"
"v_mul_f32 v230, v38, v230\n"
"v_mfma_f32_16x16x32_bf16 v[208:211], a[176:179], a[128:131], v[208:211]\n"
"v_mul_f32 v231, v39, v231\n"
"v_cvt_pk_bf16_f32 v228, v228, v229\n"
"v_mfma_f32_16x16x32_bf16 v[208:211], a[180:183], a[132:135], v[208:211]\n"
"v_cvt_pk_bf16_f32 v229, v230, v231\n"
"v_mfma_f32_16x16x32_bf16 v[212:215], a[176:179], a[136:139], v[212:215]\n"
"ds_write_b64 v244, v[228:229] offset:18440\n"
"v_add_u32 v249, 0, v248\n"
"v_mfma_f32_16x16x32_bf16 v[212:215], a[180:183], a[140:143], v[212:215]\n"
"s_waitcnt lgkmcnt(3)\n"
"global_store_dwordx4 v249, a[32:35], s[28:29] sc1\n"
"v_mfma_f32_16x16x32_bf16 v[216:219], a[176:179], a[144:147], v[216:219]\n"
"v_mul_f32 v224, v68, v224\n"
"v_mul_f32 v225, v69, v225\n"
"v_mfma_f32_16x16x32_bf16 v[216:219], a[180:183], a[148:151], v[216:219]\n"
"v_mfma_f32_16x16x32_bf16 v[228:231], v[250:253], a[68:71], 0\n"
"v_mul_f32 v226, v70, v226\n"
"v_mfma_f32_16x16x32_bf16 v[220:223], a[176:179], a[152:155], v[220:223]\n"
"global_load_dwordx4 a[176:179], v246, s[16:17] offset:0 sc1\n"
"v_mul_f32 v227, v71, v227\n"
"v_mfma_f32_16x16x32_bf16 v[220:223], a[180:183], a[156:159], v[220:223]\n"
"v_cvt_pk_bf16_f32 v224, v224, v225\n"
"v_mfma_f32_16x16x32_bf16 a[0:3], a[184:187], a[96:99], a[0:3]\n"
"global_load_dwordx4 a[180:183], v246, s[16:17] offset:1024 sc1\n"
"ds_read_b128 a[96:99], v240 offset:32768\n"
"v_mfma_f32_16x16x32_bf16 a[0:3], a[188:191], a[100:103], a[0:3]\n"
"ds_read_b128 a[100:103], v241 offset:32768\n"
"v_cvt_pk_bf16_f32 v225, v226, v227\n"
"v_mfma_f32_16x16x32_bf16 a[4:7], a[184:187], a[104:107], a[4:7]\n"
"ds_read_b128 a[104:107], v240 offset:34816\n"
"ds_write_b64 v245, v[224:225] offset:18432\n"
"v_mfma_f32_16x16x32_bf16 a[4:7], a[188:191], a[108:111], a[4:7]\n"
"ds_read_b128 a[108:111], v241 offset:34816\n"
"v_add_u32 v249, 16384, v248\n"
"v_mfma_f32_16x16x32_bf16 a[8:11], a[184:187], a[112:115], a[8:11]\n"
"ds_read_b128 a[112:115], v240 offset:36864\n"
"v_mfma_f32_16x16x32_bf16 a[8:11], a[188:191], a[116:119], a[8:11]\n"
"ds_read_b128 a[116:119], v241 offset:36864\n"
"v_mfma_f32_16x16x32_bf16 a[12:15], a[184:187], a[120:123], a[12:15]\n"
"ds_read_b128 a[120:123], v240 offset:38912\n"
"v_mfma_f32_16x16x32_bf16 a[12:15], a[188:191], a[124:127], a[12:15]\n"
"ds_read_b128 a[124:127], v241 offset:38912\n"
"v_mfma_f32_16x16x32_bf16 a[16:19], a[184:187], a[128:131], a[16:19]\n"
"ds_read_b128 a[128:131], v240 offset:40960\n"
"v_mfma_f32_16x16x32_bf16 a[16:19], a[188:191], a[132:135], a[16:19]\n"
"ds_read_b128 a[132:135], v241 offset:40960\n"
"v_mfma_f32_16x16x32_bf16 a[20:23], a[184:187], a[136:139], a[20:23]\n"
"ds_read_b128 a[136:139], v240 offset:43008\n"
"v_mfma_f32_16x16x32_bf16 a[20:23], a[188:191], a[140:143], a[20:23]\n"
"ds_read_b128 a[140:143], v241 offset:43008\n"
"v_mfma_f32_16x16x32_bf16 a[24:27], a[184:187], a[144:147], a[24:27]\n"
"ds_read_b128 a[144:147], v240 offset:45056\n"
"v_mfma_f32_16x16x32_bf16 a[24:27], a[188:191], a[148:151], a[24:27]\n"
"ds_read_b128 a[148:151], v241 offset:45056\n"
"v_mfma_f32_16x16x32_bf16 a[28:31], a[184:187], a[152:155], a[28:31]\n"
"global_load_dwordx4 a[184:187], v246, s[16:17] offset:2048 sc1\n"
"ds_read_b128 a[152:155], v240 offset:47104\n"
"v_mfma_f32_16x16x32_bf16 a[28:31], a[188:191], a[156:159], a[28:31]\n"
"ds_read_b128 a[156:159], v241 offset:47104\n"
"s_waitcnt vmcnt(14) lgkmcnt(15)\n"
"v_mfma_f32_16x16x32_bf16 v[128:131], a[192:195], a[96:99], v[128:131]\n"
"global_load_dwordx4 a[188:191], v246, s[16:17] offset:3072 sc1\n"
"v_mfma_f32_16x16x32_bf16 v[128:131], a[196:199], a[100:103], v[128:131]\n"
"s_waitcnt lgkmcnt(12)\n"
"v_mfma_f32_16x16x32_bf16 v[132:135], a[192:195], a[104:107], v[132:135]\n"
"v_add_u32 v246, 0x2000, v246\n"
"v_mfma_f32_16x16x32_bf16 v[132:135], a[196:199], a[108:111], v[132:135]\n"
"s_waitcnt lgkmcnt(10)\n"
"v_mfma_f32_16x16x32_bf16 v[136:139], a[192:195], a[112:115], v[136:139]\n"
"global_store_dwordx4 v249, a[64:67], s[28:29] sc1\n"
"v_mfma_f32_16x16x32_bf16 v[136:139], a[196:199], a[116:119], v[136:139]\n"
"v_mul_f32 v228, v100, v228\n"
"s_waitcnt lgkmcnt(8)\n"
"v_mfma_f32_16x16x32_bf16 v[140:143], a[192:195], a[120:123], v[140:143]\n"
"v_mul_f32 v229, v101, v229\n"
"v_mfma_f32_16x16x32_bf16 v[224:227], v[252:255], a[40:43], 0\n"
"v_mfma_f32_16x16x32_bf16 v[140:143], a[196:199], a[124:127], v[140:143]\n"
"v_mul_f32 v230, v102, v230\n"
"s_waitcnt lgkmcnt(6)\n"
"v_mfma_f32_16x16x32_bf16 v[144:147], a[192:195], a[128:131], v[144:147]\n"
"v_mul_f32 v231, v103, v231\n"
"v_cvt_pk_bf16_f32 v228, v228, v229\n"
"v_mfma_f32_16x16x32_bf16 v[144:147], a[196:199], a[132:135], v[144:147]\n"
"v_cvt_pk_bf16_f32 v229, v230, v231\n"
"s_waitcnt lgkmcnt(4)\n"
"v_mfma_f32_16x16x32_bf16 v[148:151], a[192:195], a[136:139], v[148:151]\n"
"ds_write_b64 v245, v[228:229] offset:18440\n"
"ds_read_b128 a[36:39], v234 offset:18432\n"
"v_mfma_f32_16x16x32_bf16 v[148:151], a[196:199], a[140:143], v[148:151]\n"
"ds_read_b128 a[68:71], v234 offset:19456\n"
"s_waitcnt lgkmcnt(5)\n"
"v_mfma_f32_16x16x32_bf16 v[152:155], a[192:195], a[144:147], v[152:155]\n"
"v_mul_f32 v224, v8, v224\n"
"v_mul_f32 v225, v9, v225\n"
"v_mfma_f32_16x16x32_bf16 v[152:155], a[196:199], a[148:151], v[152:155]\n"
"v_mfma_f32_16x16x32_bf16 v[228:231], v[250:253], a[40:43], 0\n"
"s_waitcnt lgkmcnt(3)\n"
"v_mfma_f32_16x16x32_bf16 v[156:159], a[192:195], a[152:155], v[156:159]\n"
"global_load_dwordx4 a[192:195], v246, s[16:17] offset:-4096 sc1\n"
"v_mul_f32 v226, v10, v226\n"
"v_mfma_f32_16x16x32_bf16 v[156:159], a[196:199], a[156:159], v[156:159]\n"
"v_mul_f32 v227, v11, v227\n"
"s_waitcnt vmcnt(15)\n"
"v_mfma_f32_16x16x32_bf16 v[160:163], a[200:203], a[96:99], v[160:163]\n"
"global_load_dwordx4 a[196:199], v246, s[16:17] offset:-3072 sc1\n"
"v_cvt_pk_bf16_f32 v224, v224, v225\n"
"v_mfma_f32_16x16x32_bf16 v[160:163], a[204:207], a[100:103], v[160:163]\n"
"v_cvt_pk_bf16_f32 v225, v226, v227\n"
"ds_write_b64 v244, v[224:225] offset:20480\n"
"v_mfma_f32_16x16x32_bf16 v[164:167], a[200:203], a[104:107], v[164:167]\n"
"v_mul_f32 v228, v40, v228\n"
"v_mul_f32 v229, v41, v229\n"
"v_mfma_f32_16x16x32_bf16 v[164:167], a[204:207], a[108:111], v[164:167]\n"
"v_mfma_f32_16x16x32_bf16 v[224:227], v[252:255], a[72:75], 0\n"
"v_mul_f32 v230, v42, v230\n"
"v_mfma_f32_16x16x32_bf16 v[168:171], a[200:203], a[112:115], v[168:171]\n"
"v_mul_f32 v231, v43, v231\n"
"v_cvt_pk_bf16_f32 v228, v228, v229\n"
"v_mfma_f32_16x16x32_bf16 v[168:171], a[204:207], a[116:119], v[168:171]\n"
"v_cvt_pk_bf16_f32 v229, v230, v231\n"
"ds_write_b64 v244, v[228:229] offset:20488\n"
"v_mfma_f32_16x16x32_bf16 v[172:175], a[200:203], a[120:123], v[172:175]\n"
"v_add_u32 v249, 32768, v248\n"
"v_mfma_f32_16x16x32_bf16 v[172:175], a[204:207], a[124:127], v[172:175]\n"
"s_waitcnt lgkmcnt(3)\n"
"global_store_dwordx4 v249, a[36:39], s[28:29] sc1\n"
"v_mfma_f32_16x16x32_bf16 v[176:179], a[200:203], a[128:131], v[176:179]\n"
"v_mul_f32 v224, v72, v224\n"
"v_mul_f32 v225, v73, v225\n"
"v_mfma_f32_16x16x32_bf16 v[176:179], a[204:207], a[132:135], v[176:179]\n"
"v_mfma_f32_16x16x32_bf16 v[228:231], v[250:253], a[72:75], 0\n"
"v_mul_f32 v226, v74, v226\n"
"v_mfma_f32_16x16x32_bf16 v[180:183], a[200:203], a[136:139], v[180:183]\n"
"v_mul_f32 v227, v75, v227\n"
"v_cvt_pk_bf16_f32 v224, v224, v225\n"
"v_mfma_f32_16x16x32_bf16 v[180:183], a[204:207], a[140:143], v[180:183]\n"
"v_cvt_pk_bf16_f32 v225, v226, v227\n"
"ds_write_b64 v245, v[224:225] offset:20480\n"
"v_mfma_f32_16x16x32_bf16 v[184:187], a[200:203], a[144:147], v[184:187]\n"
"v_add_u32 v249, 49152, v248\n"
"v_mfma_f32_16x16x32_bf16 v[184:187], a[204:207], a[148:151], v[184:187]\n"
"s_waitcnt lgkmcnt(3)\n"
"global_store_dwordx4 v249, a[68:71], s[28:29] sc1\n"
"v_mfma_f32_16x16x32_bf16 v[188:191], a[200:203], a[152:155], v[188:191]\n"
"global_load_dwordx4 a[200:203], v246, s[16:17] offset:-2048 sc1\n"
"v_mul_f32 v228, v104, v228\n"
"v_mfma_f32_16x16x32_bf16 v[188:191], a[204:207], a[156:159], v[188:191]\n"
"v_mul_f32 v229, v105, v229\n"
"s_waitcnt vmcnt(17)\n"
"v_mfma_f32_16x16x32_bf16 v[192:195], a[208:211], a[96:99], v[192:195]\n"
"global_load_dwordx4 a[204:207], v246, s[16:17] offset:-1024 sc1\n"
"v_mfma_f32_16x16x32_bf16 v[224:227], v[252:255], a[44:47], 0\n"
"v_mfma_f32_16x16x32_bf16 v[192:195], a[212:215], a[100:103], v[192:195]\n"
"v_mul_f32 v230, v106, v230\n"
"v_mul_f32 v231, v107, v231\n"
"v_mfma_f32_16x16x32_bf16 v[196:199], a[208:211], a[104:107], v[196:199]\n"
"v_cvt_pk_bf16_f32 v228, v228, v229\n"
"v_cvt_pk_bf16_f32 v229, v230, v231\n"
"v_mfma_f32_16x16x32_bf16 v[196:199], a[212:215], a[108:111], v[196:199]\n"
"ds_write_b64 v245, v[228:229] offset:20488\n"
"ds_read_b128 a[40:43], v234 offset:20480\n"
"v_mfma_f32_16x16x32_bf16 v[200:203], a[208:211], a[112:115], v[200:203]\n"
"ds_read_b128 a[72:75], v234 offset:21504\n"
"v_mul_f32 v224, v12, v224\n"
"v_mfma_f32_16x16x32_bf16 v[200:203], a[212:215], a[116:119], v[200:203]\n"
"v_mul_f32 v225, v13, v225\n"
"v_mfma_f32_16x16x32_bf16 v[228:231], v[250:253], a[44:47], 0\n"
"v_mfma_f32_16x16x32_bf16 v[204:207], a[208:211], a[120:123], v[204:207]\n"
"v_mul_f32 v226, v14, v226\n"
"v_mul_f32 v227, v15, v227\n"
"v_mfma_f32_16x16x32_bf16 v[204:207], a[212:215], a[124:127], v[204:207]\n"
"v_cvt_pk_bf16_f32 v224, v224, v225\n"
"v_cvt_pk_bf16_f32 v225, v226, v227\n"
"v_mfma_f32_16x16x32_bf16 v[208:211], a[208:211], a[128:131], v[208:211]\n"
"ds_write_b64 v244, v[224:225] offset:22528\n"
"v_mul_f32 v228, v44, v228\n"
"v_mfma_f32_16x16x32_bf16 v[208:211], a[212:215], a[132:135], v[208:211]\n"
"v_mul_f32 v229, v45, v229\n"
"v_mfma_f32_16x16x32_bf16 v[224:227], v[252:255], a[76:79], 0\n"
"v_mfma_f32_16x16x32_bf16 v[212:215], a[208:211], a[136:139], v[212:215]\n"
"v_mul_f32 v230, v46, v230\n"
"v_mul_f32 v231, v47, v231\n"
"v_mfma_f32_16x16x32_bf16 v[212:215], a[212:215], a[140:143], v[212:215]\n"
"v_cvt_pk_bf16_f32 v228, v228, v229\n"
"v_cvt_pk_bf16_f32 v229, v230, v231\n"
"v_mfma_f32_16x16x32_bf16 v[216:219], a[208:211], a[144:147], v[216:219]\n"
"ds_write_b64 v244, v[228:229] offset:22536\n"
"v_add_u32 v249, 65536, v248\n"
"v_mfma_f32_16x16x32_bf16 v[216:219], a[212:215], a[148:151], v[216:219]\n"
"s_waitcnt lgkmcnt(3)\n"
"global_store_dwordx4 v249, a[40:43], s[28:29] sc1\n"
"v_mfma_f32_16x16x32_bf16 v[220:223], a[208:211], a[152:155], v[220:223]\n"
"global_load_dwordx4 a[208:211], v246, s[16:17] offset:0 sc1\n"
"v_mul_f32 v224, v76, v224\n"
"v_mfma_f32_16x16x32_bf16 v[220:223], a[212:215], a[156:159], v[220:223]\n"
"v_mul_f32 v225, v77, v225\n"
"s_waitcnt vmcnt(18)\n"
"v_mfma_f32_16x16x32_bf16 a[0:3], a[216:219], a[96:99], a[0:3]\n"
"global_load_dwordx4 a[212:215], v246, s[16:17] offset:1024 sc1\n"
"ds_read_b128 a[96:99], v242 offset:0\n"
"v_mfma_f32_16x16x32_bf16 a[0:3], a[220:223], a[100:103], a[0:3]\n"
"ds_read_b128 a[100:103], v243 offset:0\n"
"v_mfma_f32_16x16x32_bf16 v[228:231], v[250:253], a[76:79], 0\n"
"v_mfma_f32_16x16x32_bf16 a[4:7], a[216:219], a[104:107], a[4:7]\n"
"ds_read_b128 a[104:107], v242 offset:2048\n"
"v_mul_f32 v226, v78, v226\n"
"v_mfma_f32_16x16x32_bf16 a[4:7], a[220:223], a[108:111], a[4:7]\n"
"ds_read_b128 a[108:111], v243 offset:2048\n"
"v_mul_f32 v227, v79, v227\n"
"v_mfma_f32_16x16x32_bf16 a[8:11], a[216:219], a[112:115], a[8:11]\n"
"ds_read_b128 a[112:115], v242 offset:4096\n"
"v_cvt_pk_bf16_f32 v224, v224, v225\n"
"v_mfma_f32_16x16x32_bf16 a[8:11], a[220:223], a[116:119], a[8:11]\n"
"ds_read_b128 a[116:119], v243 offset:4096\n"
"v_cvt_pk_bf16_f32 v225, v226, v227\n"
"v_mfma_f32_16x16x32_bf16 a[12:15], a[216:219], a[120:123], a[12:15]\n"
"ds_read_b128 a[120:123], v242 offset:6144\n"
"ds_write_b64 v245, v[224:225] offset:22528\n"
"v_mfma_f32_16x16x32_bf16 a[12:15], a[220:223], a[124:127], a[12:15]\n"
"ds_read_b128 a[124:127], v243 offset:6144\n"
"v_add_u32 v249, 81920, v248\n"
"v_mfma_f32_16x16x32_bf16 a[16:19], a[216:219], a[128:131], a[16:19]\n"
"ds_read_b128 a[128:131], v242 offset:8192\n"
"v_mfma_f32_16x16x32_bf16 a[16:19], a[220:223], a[132:135], a[16:19]\n"
"ds_read_b128 a[132:135], v243 offset:8192\n"
"v_mfma_f32_16x16x32_bf16 a[20:23], a[216:219], a[136:139], a[20:23]\n"
"ds_read_b128 a[136:139], v242 offset:10240\n"
"v_mfma_f32_16x16x32_bf16 a[20:23], a[220:223], a[140:143], a[20:23]\n"
"ds_read_b128 a[140:143], v243 offset:10240\n"
"v_mfma_f32_16x16x32_bf16 a[24:27], a[216:219], a[144:147], a[24:27]\n"
"ds_read_b128 a[144:147], v242 offset:12288\n"
"v_mfma_f32_16x16x32_bf16 a[24:27], a[220:223], a[148:151], a[24:27]\n"
"ds_read_b128 a[148:151], v243 offset:12288\n"
"v_mfma_f32_16x16x32_bf16 a[28:31], a[216:219], a[152:155], a[28:31]\n"
"global_load_dwordx4 a[216:219], v246, s[16:17] offset:2048 sc1\n"
"ds_read_b128 a[152:155], v242 offset:14336\n"
"v_mfma_f32_16x16x32_bf16 a[28:31], a[220:223], a[156:159], a[28:31]\n"
"ds_read_b128 a[156:159], v243 offset:14336\n"
"s_waitcnt vmcnt(18) lgkmcnt(15)\n"
"v_mfma_f32_16x16x32_bf16 v[128:131], a[160:163], a[96:99], v[128:131]\n"
"global_load_dwordx4 a[220:223], v246, s[16:17] offset:3072 sc1\n"
"v_mfma_f32_16x16x32_bf16 v[128:131], a[164:167], a[100:103], v[128:131]\n"
"s_waitcnt lgkmcnt(13)\n"
"v_mfma_f32_16x16x32_bf16 v[132:135], a[160:163], a[104:107], v[132:135]\n"
"v_add_u32 v246, 0x2000, v246\n"
"v_mfma_f32_16x16x32_bf16 v[132:135], a[164:167], a[108:111], v[132:135]\n"
"s_waitcnt lgkmcnt(11)\n"
"v_mfma_f32_16x16x32_bf16 v[136:139], a[160:163], a[112:115], v[136:139]\n"
"global_store_dwordx4 v249, a[72:75], s[28:29] sc1\n"
"v_mfma_f32_16x16x32_bf16 v[136:139], a[164:167], a[116:119], v[136:139]\n"
"v_mul_f32 v228, v108, v228\n"
"s_waitcnt lgkmcnt(8)\n"
"v_mfma_f32_16x16x32_bf16 v[140:143], a[160:163], a[120:123], v[140:143]\n"
"v_mul_f32 v229, v109, v229\n"
"v_mfma_f32_16x16x32_bf16 v[224:227], v[252:255], a[48:51], 0\n"
"v_mfma_f32_16x16x32_bf16 v[140:143], a[164:167], a[124:127], v[140:143]\n"
"v_mul_f32 v230, v110, v230\n"
"s_waitcnt lgkmcnt(6)\n"
"v_mfma_f32_16x16x32_bf16 v[144:147], a[160:163], a[128:131], v[144:147]\n"
"v_mul_f32 v231, v111, v231\n"
"v_cvt_pk_bf16_f32 v228, v228, v229\n"
"v_mfma_f32_16x16x32_bf16 v[144:147], a[164:167], a[132:135], v[144:147]\n"
"v_cvt_pk_bf16_f32 v229, v230, v231\n"
"s_waitcnt lgkmcnt(4)\n"
"v_mfma_f32_16x16x32_bf16 v[148:151], a[160:163], a[136:139], v[148:151]\n"
"ds_write_b64 v245, v[228:229] offset:22536\n"
"ds_read_b128 a[44:47], v234 offset:22528\n"
"v_mfma_f32_16x16x32_bf16 v[148:151], a[164:167], a[140:143], v[148:151]\n"
"ds_read_b128 a[76:79], v234 offset:23552\n"
"s_waitcnt lgkmcnt(5)\n"
"v_mfma_f32_16x16x32_bf16 v[152:155], a[160:163], a[144:147], v[152:155]\n"
"v_mul_f32 v224, v16, v224\n"
"v_mul_f32 v225, v17, v225\n"
"v_mfma_f32_16x16x32_bf16 v[152:155], a[164:167], a[148:151], v[152:155]\n"
"v_mfma_f32_16x16x32_bf16 v[228:231], v[250:253], a[48:51], 0\n"
"s_waitcnt lgkmcnt(3)\n"
"v_mfma_f32_16x16x32_bf16 v[156:159], a[160:163], a[152:155], v[156:159]\n"
"v_mul_f32 v226, v18, v226\n"
"v_mul_f32 v227, v19, v227\n"
"v_mfma_f32_16x16x32_bf16 v[156:159], a[164:167], a[156:159], v[156:159]\n"
"v_cvt_pk_bf16_f32 v224, v224, v225\n"
"s_waitcnt vmcnt(18)\n"
"v_mfma_f32_16x16x32_bf16 v[160:163], a[168:171], a[96:99], v[160:163]\n"
"v_cvt_pk_bf16_f32 v225, v226, v227\n"
"ds_write_b64 v244, v[224:225] offset:24576\n"
"v_mfma_f32_16x16x32_bf16 v[160:163], a[172:175], a[100:103], v[160:163]\n"
"v_mul_f32 v228, v48, v228\n"
"v_mul_f32 v229, v49, v229\n"
"v_mfma_f32_16x16x32_bf16 v[164:167], a[168:171], a[104:107], v[164:167]\n"
"v_mfma_f32_16x16x32_bf16 v[224:227], v[252:255], a[80:83], 0\n"
"v_mul_f32 v230, v50, v230\n"
"v_mfma_f32_16x16x32_bf16 v[164:167], a[172:175], a[108:111], v[164:167]\n"
"v_mul_f32 v231, v51, v231\n"
"v_cvt_pk_bf16_f32 v228, v228, v229\n"
"v_mfma_f32_16x16x32_bf16 v[168:171], a[168:171], a[112:115], v[168:171]\n"
"v_cvt_pk_bf16_f32 v229, v230, v231\n"
"ds_write_b64 v244, v[228:229] offset:24584\n"
"v_mfma_f32_16x16x32_bf16 v[168:171], a[172:175], a[116:119], v[168:171]\n"
"v_add_u32 v249, 98304, v248\n"
"v_mfma_f32_16x16x32_bf16 v[172:175], a[168:171], a[120:123], v[172:175]\n"
"s_waitcnt lgkmcnt(3)\n"
"global_store_dwordx4 v249, a[44:47], s[28:29] sc1\n"
"v_mfma_f32_16x16x32_bf16 v[172:175], a[172:175], a[124:127], v[172:175]\n"
"v_mul_f32 v224, v80, v224\n"
"v_mul_f32 v225, v81, v225\n"
"v_mfma_f32_16x16x32_bf16 v[176:179], a[168:171], a[128:131], v[176:179]\n"
"v_mfma_f32_16x16x32_bf16 v[228:231], v[250:253], a[80:83], 0\n"
"v_mul_f32 v226, v82, v226\n"
"v_mfma_f32_16x16x32_bf16 v[176:179], a[172:175], a[132:135], v[176:179]\n"
"v_mul_f32 v227, v83, v227\n"
"v_cvt_pk_bf16_f32 v224, v224, v225\n"
"v_mfma_f32_16x16x32_bf16 v[180:183], a[168:171], a[136:139], v[180:183]\n"
"v_cvt_pk_bf16_f32 v225, v226, v227\n"
"ds_write_b64 v245, v[224:225] offset:24576\n"
"v_mfma_f32_16x16x32_bf16 v[180:183], a[172:175], a[140:143], v[180:183]\n"
"v_add_u32 v249, 114688, v248\n"
"v_mfma_f32_16x16x32_bf16 v[184:187], a[168:171], a[144:147], v[184:187]\n"
"s_waitcnt lgkmcnt(3)\n"
"global_store_dwordx4 v249, a[76:79], s[28:29] sc1\n"
"v_mfma_f32_16x16x32_bf16 v[184:187], a[172:175], a[148:151], v[184:187]\n"
"v_mul_f32 v228, v112, v228\n"
"v_mul_f32 v229, v113, v229\n"
"v_mfma_f32_16x16x32_bf16 v[188:191], a[168:171], a[152:155], v[188:191]\n"
"v_mfma_f32_16x16x32_bf16 v[224:227], v[252:255], a[52:55], 0\n"
"v_mul_f32 v230, v114, v230\n"
"v_mfma_f32_16x16x32_bf16 v[188:191], a[172:175], a[156:159], v[188:191]\n"
"v_mul_f32 v231, v115, v231\n"
"s_waitcnt vmcnt(17)\n"
"v_mfma_f32_16x16x32_bf16 v[192:195], a[176:179], a[96:99], v[192:195]\n"
"v_cvt_pk_bf16_f32 v228, v228, v229\n"
"v_cvt_pk_bf16_f32 v229, v230, v231\n"
"v_mfma_f32_16x16x32_bf16 v[192:195], a[180:183], a[100:103], v[192:195]\n"
"ds_write_b64 v245, v[228:229] offset:24584\n"
"ds_read_b128 a[48:51], v234 offset:24576\n"
"v_mfma_f32_16x16x32_bf16 v[196:199], a[176:179], a[104:107], v[196:199]\n"
"ds_read_b128 a[80:83], v234 offset:25600\n"
"v_mul_f32 v224, v20, v224\n"
"v_mfma_f32_16x16x32_bf16 v[196:199], a[180:183], a[108:111], v[196:199]\n"
"v_mul_f32 v225, v21, v225\n"
"v_mfma_f32_16x16x32_bf16 v[228:231], v[250:253], a[52:55], 0\n"
"v_mfma_f32_16x16x32_bf16 v[200:203], a[176:179], a[112:115], v[200:203]\n"
"v_mul_f32 v226, v22, v226\n"
"v_mul_f32 v227, v23, v227\n"
"v_mfma_f32_16x16x32_bf16 v[200:203], a[180:183], a[116:119], v[200:203]\n"
"v_cvt_pk_bf16_f32 v224, v224, v225\n"
"v_cvt_pk_bf16_f32 v225, v226, v227\n"
"v_mfma_f32_16x16x32_bf16 v[204:207], a[176:179], a[120:123], v[204:207]\n"
"ds_write_b64 v244, v[224:225] offset:26624\n"
"v_mul_f32 v228, v52, v228\n"
"v_mfma_f32_16x16x32_bf16 v[204:207], a[180:183], a[124:127], v[204:207]\n"
"v_mul_f32 v229, v53, v229\n"
"v_mfma_f32_16x16x32_bf16 v[224:227], v[252:255], a[84:87], 0\n"
"v_mfma_f32_16x16x32_bf16 v[208:211], a[176:179], a[128:131], v[208:211]\n"
"v_mul_f32 v230, v54, v230\n"
"v_mul_f32 v231, v55, v231\n"
"v_mfma_f32_16x16x32_bf16 v[208:211], a[180:183], a[132:135], v[208:211]\n"
"v_cvt_pk_bf16_f32 v228, v228, v229\n"
"v_cvt_pk_bf16_f32 v229, v230, v231\n"
"v_mfma_f32_16x16x32_bf16 v[212:215], a[176:179], a[136:139], v[212:215]\n"
"ds_write_b64 v244, v[228:229] offset:26632\n"
"v_add_u32 v249, 131072, v248\n"
"v_mfma_f32_16x16x32_bf16 v[212:215], a[180:183], a[140:143], v[212:215]\n"
"s_waitcnt lgkmcnt(3)\n"
"global_store_dwordx4 v249, a[48:51], s[28:29] sc1\n"
"v_mfma_f32_16x16x32_bf16 v[216:219], a[176:179], a[144:147], v[216:219]\n"
"v_mul_f32 v224, v84, v224\n"
"v_mul_f32 v225, v85, v225\n"
"v_mfma_f32_16x16x32_bf16 v[216:219], a[180:183], a[148:151], v[216:219]\n"
"v_mfma_f32_16x16x32_bf16 v[228:231], v[250:253], a[84:87], 0\n"
"v_mul_f32 v226, v86, v226\n"
"v_mfma_f32_16x16x32_bf16 v[220:223], a[176:179], a[152:155], v[220:223]\n"
"v_mul_f32 v227, v87, v227\n"
"v_cvt_pk_bf16_f32 v224, v224, v225\n"
"v_mfma_f32_16x16x32_bf16 v[220:223], a[180:183], a[156:159], v[220:223]\n"
"v_cvt_pk_bf16_f32 v225, v226, v227\n"
"s_waitcnt vmcnt(16)\n"
"v_mfma_f32_16x16x32_bf16 a[0:3], a[184:187], a[96:99], a[0:3]\n"
"ds_read_b128 a[96:99], v242 offset:32768\n"
"ds_write_b64 v245, v[224:225] offset:26624\n"
"v_mfma_f32_16x16x32_bf16 a[0:3], a[188:191], a[100:103], a[0:3]\n"
"ds_read_b128 a[100:103], v243 offset:32768\n"
"v_add_u32 v249, 147456, v248\n"
"v_mfma_f32_16x16x32_bf16 a[4:7], a[184:187], a[104:107], a[4:7]\n"
"ds_read_b128 a[104:107], v242 offset:34816\n"
"v_mfma_f32_16x16x32_bf16 a[4:7], a[188:191], a[108:111], a[4:7]\n"
"ds_read_b128 a[108:111], v243 offset:34816\n"
"v_mfma_f32_16x16x32_bf16 a[8:11], a[184:187], a[112:115], a[8:11]\n"
"ds_read_b128 a[112:115], v242 offset:36864\n"
"v_mfma_f32_16x16x32_bf16 a[8:11], a[188:191], a[116:119], a[8:11]\n"
"ds_read_b128 a[116:119], v243 offset:36864\n"
"v_mfma_f32_16x16x32_bf16 a[12:15], a[184:187], a[120:123], a[12:15]\n"
"ds_read_b128 a[120:123], v242 offset:38912\n"
"v_mfma_f32_16x16x32_bf16 a[12:15], a[188:191], a[124:127], a[12:15]\n"
"ds_read_b128 a[124:127], v243 offset:38912\n"
"v_mfma_f32_16x16x32_bf16 a[16:19], a[184:187], a[128:131], a[16:19]\n"
"ds_read_b128 a[128:131], v242 offset:40960\n"
"v_mfma_f32_16x16x32_bf16 a[16:19], a[188:191], a[132:135], a[16:19]\n"
"ds_read_b128 a[132:135], v243 offset:40960\n"
"v_mfma_f32_16x16x32_bf16 a[20:23], a[184:187], a[136:139], a[20:23]\n"
"ds_read_b128 a[136:139], v242 offset:43008\n"
"v_mfma_f32_16x16x32_bf16 a[20:23], a[188:191], a[140:143], a[20:23]\n"
"ds_read_b128 a[140:143], v243 offset:43008\n"
"v_mfma_f32_16x16x32_bf16 a[24:27], a[184:187], a[144:147], a[24:27]\n"
"ds_read_b128 a[144:147], v242 offset:45056\n"
"v_mfma_f32_16x16x32_bf16 a[24:27], a[188:191], a[148:151], a[24:27]\n"
"ds_read_b128 a[148:151], v243 offset:45056\n"
"v_mfma_f32_16x16x32_bf16 a[28:31], a[184:187], a[152:155], a[28:31]\n"
"ds_read_b128 a[152:155], v242 offset:47104\n"
"v_mfma_f32_16x16x32_bf16 a[28:31], a[188:191], a[156:159], a[28:31]\n"
"ds_read_b128 a[156:159], v243 offset:47104\n"
"s_waitcnt vmcnt(13) lgkmcnt(14)\n"
"v_mfma_f32_16x16x32_bf16 v[128:131], a[192:195], a[96:99], v[128:131]\n"
"global_store_dwordx4 v249, a[80:83], s[28:29] sc1\n"
"v_mfma_f32_16x16x32_bf16 v[128:131], a[196:199], a[100:103], v[128:131]\n"
"v_mul_f32 v228, v116, v228\n"
"s_waitcnt lgkmcnt(12)\n"
"v_mfma_f32_16x16x32_bf16 v[132:135], a[192:195], a[104:107], v[132:135]\n"
"v_mul_f32 v229, v117, v229\n"
"v_mfma_f32_16x16x32_bf16 v[224:227], v[252:255], a[56:59], 0\n"
"v_mfma_f32_16x16x32_bf16 v[132:135], a[196:199], a[108:111], v[132:135]\n"
"v_mul_f32 v230, v118, v230\n"
"s_waitcnt lgkmcnt(10)\n"
"v_mfma_f32_16x16x32_bf16 v[136:139], a[192:195], a[112:115], v[136:139]\n"
"v_mul_f32 v231, v119, v231\n"
"v_cvt_pk_bf16_f32 v228, v228, v229\n"
"v_mfma_f32_16x16x32_bf16 v[136:139], a[196:199], a[116:119], v[136:139]\n"
"v_cvt_pk_bf16_f32 v229, v230, v231\n"
"s_waitcnt lgkmcnt(8)\n"
"v_mfma_f32_16x16x32_bf16 v[140:143], a[192:195], a[120:123], v[140:143]\n"
"ds_write_b64 v245, v[228:229] offset:26632\n"
"ds_read_b128 a[52:55], v234 offset:26624\n"
"v_mfma_f32_16x16x32_bf16 v[140:143], a[196:199], a[124:127], v[140:143]\n"
"ds_read_b128 a[84:87], v234 offset:27648\n"
"s_waitcnt lgkmcnt(9)\n"
"v_mfma_f32_16x16x32_bf16 v[144:147], a[192:195], a[128:131], v[144:147]\n"
"v_mul_f32 v224, v24, v224\n"
"v_mul_f32 v225, v25, v225\n"
"v_mfma_f32_16x16x32_bf16 v[144:147], a[196:199], a[132:135], v[144:147]\n"
"v_mfma_f32_16x16x32_bf16 v[228:231], v[250:253], a[56:59], 0\n"
"s_waitcnt lgkmcnt(7)\n"
"v_mfma_f32_16x16x32_bf16 v[148:151], a[192:195], a[136:139], v[148:151]\n"
"v_mul_f32 v226, v26, v226\n"
"v_mul_f32 v227, v27, v227\n"
"v_mfma_f32_16x16x32_bf16 v[148:151], a[196:199], a[140:143], v[148:151]\n"
"v_cvt_pk_bf16_f32 v224, v224, v225\n"
"s_waitcnt lgkmcnt(5)\n"
"v_mfma_f32_16x16x32_bf16 v[152:155], a[192:195], a[144:147], v[152:155]\n"
"v_cvt_pk_bf16_f32 v225, v226, v227\n"
"ds_write_b64 v244, v[224:225] offset:28672\n"
"v_mfma_f32_16x16x32_bf16 v[152:155], a[196:199], a[148:151], v[152:155]\n"
"v_mul_f32 v228, v56, v228\n"
"s_waitcnt lgkmcnt(4)\n"
"v_mfma_f32_16x16x32_bf16 v[156:159], a[192:195], a[152:155], v[156:159]\n"
"v_mul_f32 v229, v57, v229\n"
"v_mfma_f32_16x16x32_bf16 v[224:227], v[252:255], a[88:91], 0\n"
"v_mfma_f32_16x16x32_bf16 v[156:159], a[196:199], a[156:159], v[156:159]\n"
"v_mul_f32 v230, v58, v230\n"
"s_waitcnt vmcnt(10)\n"
"v_mfma_f32_16x16x32_bf16 v[160:163], a[200:203], a[96:99], v[160:163]\n"
"v_mul_f32 v231, v59, v231\n"
"v_cvt_pk_bf16_f32 v228, v228, v229\n"
"v_mfma_f32_16x16x32_bf16 v[160:163], a[204:207], a[100:103], v[160:163]\n"
"v_cvt_pk_bf16_f32 v229, v230, v231\n"
"ds_write_b64 v244, v[228:229] offset:28680\n"
"v_mfma_f32_16x16x32_bf16 v[164:167], a[200:203], a[104:107], v[164:167]\n"
"v_add_u32 v249, 163840, v248\n"
"v_mfma_f32_16x16x32_bf16 v[164:167], a[204:207], a[108:111], v[164:167]\n"
"s_waitcnt lgkmcnt(3)\n"
"global_store_dwordx4 v249, a[52:55], s[28:29] sc1\n"
"v_mfma_f32_16x16x32_bf16 v[168:171], a[200:203], a[112:115], v[168:171]\n"
"v_mul_f32 v224, v88, v224\n"
"v_mul_f32 v225, v89, v225\n"
"v_mfma_f32_16x16x32_bf16 v[168:171], a[204:207], a[116:119], v[168:171]\n"
"v_mfma_f32_16x16x32_bf16 v[228:231], v[250:253], a[88:91], 0\n"
"v_mul_f32 v226, v90, v226\n"
"v_mfma_f32_16x16x32_bf16 v[172:175], a[200:203], a[120:123], v[172:175]\n"
"v_mul_f32 v227, v91, v227\n"
"v_cvt_pk_bf16_f32 v224, v224, v225\n"
"v_mfma_f32_16x16x32_bf16 v[172:175], a[204:207], a[124:127], v[172:175]\n"
"v_cvt_pk_bf16_f32 v225, v226, v227\n"
"ds_write_b64 v245, v[224:225] offset:28672\n"
"v_mfma_f32_16x16x32_bf16 v[176:179], a[200:203], a[128:131], v[176:179]\n"
"v_add_u32 v249, 180224, v248\n"
"v_mfma_f32_16x16x32_bf16 v[176:179], a[204:207], a[132:135], v[176:179]\n"
"s_waitcnt lgkmcnt(3)\n"
"global_store_dwordx4 v249, a[84:87], s[28:29] sc1\n"
"v_mfma_f32_16x16x32_bf16 v[180:183], a[200:203], a[136:139], v[180:183]\n"
"v_mul_f32 v228, v120, v228\n"
"v_mul_f32 v229, v121, v229\n"
"v_mfma_f32_16x16x32_bf16 v[180:183], a[204:207], a[140:143], v[180:183]\n"
"v_mfma_f32_16x16x32_bf16 v[224:227], v[252:255], a[60:63], 0\n"
"v_mul_f32 v230, v122, v230\n"
"v_mfma_f32_16x16x32_bf16 v[184:187], a[200:203], a[144:147], v[184:187]\n"
"v_mul_f32 v231, v123, v231\n"
"v_cvt_pk_bf16_f32 v228, v228, v229\n"
"v_mfma_f32_16x16x32_bf16 v[184:187], a[204:207], a[148:151], v[184:187]\n"
"v_cvt_pk_bf16_f32 v229, v230, v231\n"
"ds_write_b64 v245, v[228:229] offset:28680\n"
"v_mfma_f32_16x16x32_bf16 v[188:191], a[200:203], a[152:155], v[188:191]\n"
"ds_read_b128 a[56:59], v234 offset:28672\n"
"ds_read_b128 a[88:91], v234 offset:29696\n"
"v_mfma_f32_16x16x32_bf16 v[188:191], a[204:207], a[156:159], v[188:191]\n"
"v_mul_f32 v224, v28, v224\n"
"s_waitcnt vmcnt(9)\n"
"v_mfma_f32_16x16x32_bf16 v[192:195], a[208:211], a[96:99], v[192:195]\n"
"v_mul_f32 v225, v29, v225\n"
"v_mfma_f32_16x16x32_bf16 v[228:231], v[250:253], a[60:63], 0\n"
"v_mfma_f32_16x16x32_bf16 v[192:195], a[212:215], a[100:103], v[192:195]\n"
"v_mul_f32 v226, v30, v226\n"
"v_mul_f32 v227, v31, v227\n"
"v_mfma_f32_16x16x32_bf16 v[196:199], a[208:211], a[104:107], v[196:199]\n"
"v_cvt_pk_bf16_f32 v224, v224, v225\n"
"v_cvt_pk_bf16_f32 v225, v226, v227\n"
"v_mfma_f32_16x16x32_bf16 v[196:199], a[212:215], a[108:111], v[196:199]\n"
"ds_write_b64 v244, v[224:225] offset:30720\n"
"v_mul_f32 v228, v60, v228\n"
"v_mfma_f32_16x16x32_bf16 v[200:203], a[208:211], a[112:115], v[200:203]\n"
"v_mul_f32 v229, v61, v229\n"
"v_mfma_f32_16x16x32_bf16 v[224:227], v[252:255], a[92:95], 0\n"
"v_mfma_f32_16x16x32_bf16 v[200:203], a[212:215], a[116:119], v[200:203]\n"
"v_mul_f32 v230, v62, v230\n"
"v_mul_f32 v231, v63, v231\n"
"v_mfma_f32_16x16x32_bf16 v[204:207], a[208:211], a[120:123], v[204:207]\n"
"v_cvt_pk_bf16_f32 v228, v228, v229\n"
"v_cvt_pk_bf16_f32 v229, v230, v231\n"
"v_mfma_f32_16x16x32_bf16 v[204:207], a[212:215], a[124:127], v[204:207]\n"
"ds_write_b64 v244, v[228:229] offset:30728\n"
"v_add_u32 v249, 196608, v248\n"
"v_mfma_f32_16x16x32_bf16 v[208:211], a[208:211], a[128:131], v[208:211]\n"
"s_waitcnt lgkmcnt(3)\n"
"global_store_dwordx4 v249, a[56:59], s[28:29] sc1\n"
"v_mfma_f32_16x16x32_bf16 v[208:211], a[212:215], a[132:135], v[208:211]\n"
"v_mul_f32 v224, v92, v224\n"
"v_mul_f32 v225, v93, v225\n"
"v_mfma_f32_16x16x32_bf16 v[212:215], a[208:211], a[136:139], v[212:215]\n"
"v_mfma_f32_16x16x32_bf16 v[228:231], v[250:253], a[92:95], 0\n"
"v_mul_f32 v226, v94, v226\n"
"v_mfma_f32_16x16x32_bf16 v[212:215], a[212:215], a[140:143], v[212:215]\n"
"v_mul_f32 v227, v95, v227\n"
"v_cvt_pk_bf16_f32 v224, v224, v225\n"
"v_mfma_f32_16x16x32_bf16 v[216:219], a[208:211], a[144:147], v[216:219]\n"
"v_cvt_pk_bf16_f32 v225, v226, v227\n"
"ds_write_b64 v245, v[224:225] offset:30720\n"
"v_mfma_f32_16x16x32_bf16 v[216:219], a[212:215], a[148:151], v[216:219]\n"
"v_add_u32 v249, 212992, v248\n"
"v_mfma_f32_16x16x32_bf16 v[220:223], a[208:211], a[152:155], v[220:223]\n"
"s_waitcnt lgkmcnt(3)\n"
"global_store_dwordx4 v249, a[88:91], s[28:29] sc1\n"
"v_mfma_f32_16x16x32_bf16 v[220:223], a[212:215], a[156:159], v[220:223]\n"
"v_mul_f32 v228, v124, v228\n"
"s_waitcnt vmcnt(9)\n"
"v_mfma_f32_16x16x32_bf16 a[0:3], a[216:219], a[96:99], a[0:3]\n"
"ds_read_b128 a[96:99], v236 offset:0\n"
"v_mul_f32 v229, v125, v229\n"
"v_mfma_f32_16x16x32_bf16 a[0:3], a[220:223], a[100:103], a[0:3]\n"
"ds_read_b128 a[100:103], v236 offset:1024\n"
"v_mul_f32 v230, v126, v230\n"
"v_mfma_f32_16x16x32_bf16 a[4:7], a[216:219], a[104:107], a[4:7]\n"
"ds_read_b128 a[104:107], v236 offset:2048\n"
"v_mul_f32 v231, v127, v231\n"
"v_mfma_f32_16x16x32_bf16 a[4:7], a[220:223], a[108:111], a[4:7]\n"
"ds_read_b128 a[108:111], v236 offset:3072\n"
"v_cvt_pk_bf16_f32 v228, v228, v229\n"
"v_mfma_f32_16x16x32_bf16 a[8:11], a[216:219], a[112:115], a[8:11]\n"
"ds_read_b128 a[112:115], v236 offset:4096\n"
"v_cvt_pk_bf16_f32 v229, v230, v231\n"
"v_mfma_f32_16x16x32_bf16 a[8:11], a[220:223], a[116:119], a[8:11]\n"
"ds_read_b128 a[116:119], v236 offset:5120\n"
"ds_write_b64 v245, v[228:229] offset:30728\n"
"v_mfma_f32_16x16x32_bf16 a[12:15], a[216:219], a[120:123], a[12:15]\n"
".Lpg22_%=:\n"
"s_cbranch_execz .Lretb_%=\n"
"ds_read_b128 a[120:123], v236 offset:6144\n"
"ds_read_b128 a[60:63], v234 offset:30720\n"
"v_mfma_f32_16x16x32_bf16 a[12:15], a[220:223], a[124:127], a[12:15]\n"
"ds_read_b128 a[124:127], v236 offset:7168\n"
"ds_read_b128 a[92:95], v234 offset:31744\n"
"v_mfma_f32_16x16x32_bf16 a[16:19], a[216:219], a[128:131], a[16:19]\n"
"v_mfma_f32_16x16x32_bf16 a[16:19], a[220:223], a[132:135], a[16:19]\n"
"v_mfma_f32_16x16x32_bf16 a[20:23], a[216:219], a[136:139], a[20:23]\n"
"v_mfma_f32_16x16x32_bf16 a[20:23], a[220:223], a[140:143], a[20:23]\n"
"v_mfma_f32_16x16x32_bf16 a[24:27], a[216:219], a[144:147], a[24:27]\n"
"v_mfma_f32_16x16x32_bf16 a[24:27], a[220:223], a[148:151], a[24:27]\n"
"v_mfma_f32_16x16x32_bf16 a[28:31], a[216:219], a[152:155], a[28:31]\n"
"v_mfma_f32_16x16x32_bf16 a[28:31], a[220:223], a[156:159], a[28:31]\n"
"v_add_u32 v249, 229376, v248\n"
"s_waitcnt lgkmcnt(2)\n"
"global_store_dwordx4 v249, a[60:63], s[28:29] sc1\n"
"v_add_u32 v249, 245760, v248\n"
"s_waitcnt lgkmcnt(0)\n"
"global_store_dwordx4 v249, a[92:95], s[28:29] sc1\n"
"s_mul_i32 s47, %1, 28672\n"
"v_add_u32 v246, s47, v247\n"
".p2align 3\n"
"v_mfma_f32_16x16x32_bf16 v[224:227], v[252:255], a[224:227], 0\n"
"v_mfma_f32_16x16x32_bf16 v[228:231], v[250:253], a[224:227], 0\n"
"v_mfma_f32_16x16x32_bf16 v[236:239], v[250:253], a[96:99], 0\n"
"s_nop 5\n"
"v_mul_f32 v224, v128, v224\n"
"v_mul_f32 v225, v129, v225\n"
"v_mul_f32 v226, v130, v226\n"
"v_mul_f32 v227, v131, v227\n"
"v_mfma_f32_16x16x32_bf16 v[232:235], v[252:255], a[96:99], 0\n"
"v_mul_f32 v228, v160, v228\n"
"v_mul_f32 v229, v161, v229\n"
"v_mul_f32 v230, v162, v230\n"
"v_mul_f32 v231, v163, v231\n"
"v_cvt_pk_bf16_f32 v224, v224, v225\n"
"v_cvt_pk_bf16_f32 v225, v226, v227\n"
"v_cvt_pk_bf16_f32 v226, v228, v229\n"
"v_cvt_pk_bf16_f32 v227, v230, v231\n"
"ds_write_b128 v244, v[224:227] offset:16384\n"
"v_mfma_f32_16x16x32_bf16 v[228:231], v[250:253], a[228:231], 0\n"
"v_mul_f32 v232, v192, v232\n"
"v_mul_f32 v233, v193, v233\n"
"v_mul_f32 v234, v194, v234\n"
"v_mul_f32 v235, v195, v235\n"
"v_mfma_f32_16x16x32_bf16 v[224:227], v[252:255], a[228:231], 0\n"
"v_accvgpr_read_b32 v249, a0\n"
"v_mul_f32_e64 v236, v249, v236\n"
"v_accvgpr_read_b32 v249, a1\n"
"v_mul_f32_e64 v237, v249, v237\n"
"v_accvgpr_read_b32 v249, a2\n"
"v_mul_f32_e64 v238, v249, v238\n"
"v_accvgpr_read_b32 v249, a3\n"
"v_mul_f32_e64 v239, v249, v239\n"
"v_cvt_pk_bf16_f32 v232, v232, v233\n"
"v_cvt_pk_bf16_f32 v233, v234, v235\n"
"v_cvt_pk_bf16_f32 v234, v236, v237\n"
"v_cvt_pk_bf16_f32 v235, v238, v239\n"
"ds_write_b128 v245, v[232:235] offset:16384\n"
"v_mfma_f32_16x16x32_bf16 v[236:239], v[250:253], a[100:103], 0\n"
"v_mul_f32 v224, v132, v224\n"
"v_mul_f32 v225, v133, v225\n"
"v_mul_f32 v226, v134, v226\n"
"v_mul_f32 v227, v135, v227\n"
"v_mfma_f32_16x16x32_bf16 v[232:235], v[252:255], a[100:103], 0\n"
"v_mul_f32 v228, v164, v228\n"
"v_mul_f32 v229, v165, v229\n"
"v_mul_f32 v230, v166, v230\n"
"v_mul_f32 v231, v167, v231\n"
"ds_read_b128 a[160:163], v246 offset:16384\n"
"v_cvt_pk_bf16_f32 v224, v224, v225\n"
"ds_read_b128 a[164:167], v246 offset:17408\n"
"v_cvt_pk_bf16_f32 v225, v226, v227\n"
"v_cvt_pk_bf16_f32 v226, v228, v229\n"
"v_cvt_pk_bf16_f32 v227, v230, v231\n"
"ds_write_b128 v244, v[224:227] offset:18432\n"
"v_mfma_f32_16x16x32_bf16 v[228:231], v[250:253], a[232:235], 0\n"
"v_mul_f32 v232, v196, v232\n"
"v_mul_f32 v233, v197, v233\n"
"v_mul_f32 v234, v198, v234\n"
"v_mul_f32 v235, v199, v235\n"
"v_mfma_f32_16x16x32_bf16 v[224:227], v[252:255], a[232:235], 0\n"
"v_accvgpr_read_b32 v249, a4\n"
"v_mul_f32_e64 v236, v249, v236\n"
"v_accvgpr_read_b32 v249, a5\n"
"v_mul_f32_e64 v237, v249, v237\n"
"v_accvgpr_read_b32 v249, a6\n"
"v_mul_f32_e64 v238, v249, v238\n"
"v_accvgpr_read_b32 v249, a7\n"
"v_mul_f32_e64 v239, v249, v239\n"
"v_add_u32 v247, 128, v248\n"
"s_waitcnt lgkmcnt(2)\n"
"global_store_dwordx4 v247, a[160:163], s[28:29] sc1\n"
"v_cvt_pk_bf16_f32 v232, v232, v233\n"
"v_cvt_pk_bf16_f32 v233, v234, v235\n"
"v_cvt_pk_bf16_f32 v234, v236, v237\n"
"v_cvt_pk_bf16_f32 v235, v238, v239\n"
"ds_write_b128 v245, v[232:235] offset:18432\n"
"v_mfma_f32_16x16x32_bf16 v[236:239], v[250:253], a[104:107], 0\n"
"v_mul_f32 v224, v136, v224\n"
"v_mul_f32 v225, v137, v225\n"
"v_mul_f32 v226, v138, v226\n"
"v_mul_f32 v227, v139, v227\n"
"v_mfma_f32_16x16x32_bf16 v[232:235], v[252:255], a[104:107], 0\n"
"v_mul_f32 v228, v168, v228\n"
"v_mul_f32 v229, v169, v229\n"
"v_mul_f32 v230, v170, v230\n"
"v_mul_f32 v231, v171, v231\n"
"v_add_u32 v247, 16512, v248\n"
"s_waitcnt lgkmcnt(2)\n"
"global_store_dwordx4 v247, a[164:167], s[28:29] sc1\n"
"v_cvt_pk_bf16_f32 v224, v224, v225\n"
"ds_read_b128 a[168:171], v246 offset:18432\n"
"v_cvt_pk_bf16_f32 v225, v226, v227\n"
"ds_read_b128 a[172:175], v246 offset:19456\n"
"v_cvt_pk_bf16_f32 v226, v228, v229\n"
"v_cvt_pk_bf16_f32 v227, v230, v231\n"
"ds_write_b128 v244, v[224:227] offset:20480\n"
"v_mfma_f32_16x16x32_bf16 v[228:231], v[250:253], a[236:239], 0\n"
"v_mul_f32 v232, v200, v232\n"
"v_mul_f32 v233, v201, v233\n"
"v_mul_f32 v234, v202, v234\n"
"v_mul_f32 v235, v203, v235\n"
"v_mfma_f32_16x16x32_bf16 v[224:227], v[252:255], a[236:239], 0\n"
"v_accvgpr_read_b32 v249, a8\n"
"v_mul_f32_e64 v236, v249, v236\n"
"v_accvgpr_read_b32 v249, a9\n"
"v_mul_f32_e64 v237, v249, v237\n"
"v_accvgpr_read_b32 v249, a10\n"
"v_mul_f32_e64 v238, v249, v238\n"
"v_accvgpr_read_b32 v249, a11\n"
"v_mul_f32_e64 v239, v249, v239\n"
"v_add_u32 v247, 32896, v248\n"
"s_waitcnt lgkmcnt(2)\n"
"global_store_dwordx4 v247, a[168:171], s[28:29] sc1\n"
"v_cvt_pk_bf16_f32 v232, v232, v233\n"
"v_cvt_pk_bf16_f32 v233, v234, v235\n"
"v_cvt_pk_bf16_f32 v234, v236, v237\n"
"v_cvt_pk_bf16_f32 v235, v238, v239\n"
"ds_write_b128 v245, v[232:235] offset:20480\n"
"v_mfma_f32_16x16x32_bf16 v[236:239], v[250:253], a[108:111], 0\n"
"v_mul_f32 v224, v140, v224\n"
"v_mul_f32 v225, v141, v225\n"
"v_mul_f32 v226, v142, v226\n"
"v_mul_f32 v227, v143, v227\n"
"v_mfma_f32_16x16x32_bf16 v[232:235], v[252:255], a[108:111], 0\n"
"v_mul_f32 v228, v172, v228\n"
"v_mul_f32 v229, v173, v229\n"
"v_mul_f32 v230, v174, v230\n"
"v_mul_f32 v231, v175, v231\n"
"v_add_u32 v247, 49280, v248\n"
"s_waitcnt lgkmcnt(2)\n"
"global_store_dwordx4 v247, a[172:175], s[28:29] sc1\n"
"v_cvt_pk_bf16_f32 v224, v224, v225\n"
"ds_read_b128 a[176:179], v246 offset:20480\n"
"v_cvt_pk_bf16_f32 v225, v226, v227\n"
"ds_read_b128 a[180:183], v246 offset:21504\n"
"v_cvt_pk_bf16_f32 v226, v228, v229\n"
"v_cvt_pk_bf16_f32 v227, v230, v231\n"
"ds_write_b128 v244, v[224:227] offset:22528\n"
"v_mfma_f32_16x16x32_bf16 v[228:231], v[250:253], a[240:243], 0\n"
"v_mul_f32 v232, v204, v232\n"
"v_mul_f32 v233, v205, v233\n"
"v_mul_f32 v234, v206, v234\n"
"v_mul_f32 v235, v207, v235\n"
"v_mfma_f32_16x16x32_bf16 v[224:227], v[252:255], a[240:243], 0\n"
"v_accvgpr_read_b32 v249, a12\n"
"v_mul_f32_e64 v236, v249, v236\n"
"v_accvgpr_read_b32 v249, a13\n"
"v_mul_f32_e64 v237, v249, v237\n"
"v_accvgpr_read_b32 v249, a14\n"
"v_mul_f32_e64 v238, v249, v238\n"
"v_accvgpr_read_b32 v249, a15\n"
"v_mul_f32_e64 v239, v249, v239\n"
"v_add_u32 v247, 65664, v248\n"
"s_waitcnt lgkmcnt(2)\n"
"global_store_dwordx4 v247, a[176:179], s[28:29] sc1\n"
"v_cvt_pk_bf16_f32 v232, v232, v233\n"
"v_cvt_pk_bf16_f32 v233, v234, v235\n"
"v_cvt_pk_bf16_f32 v234, v236, v237\n"
"v_cvt_pk_bf16_f32 v235, v238, v239\n"
"ds_write_b128 v245, v[232:235] offset:22528\n"
"v_mfma_f32_16x16x32_bf16 v[236:239], v[250:253], a[112:115], 0\n"
"v_mul_f32 v224, v144, v224\n"
"v_mul_f32 v225, v145, v225\n"
"v_mul_f32 v226, v146, v226\n"
"v_mul_f32 v227, v147, v227\n"
"v_mfma_f32_16x16x32_bf16 v[232:235], v[252:255], a[112:115], 0\n"
"v_mul_f32 v228, v176, v228\n"
"v_mul_f32 v229, v177, v229\n"
"v_mul_f32 v230, v178, v230\n"
"v_mul_f32 v231, v179, v231\n"
"v_add_u32 v247, 82048, v248\n"
"s_waitcnt lgkmcnt(2)\n"
"global_store_dwordx4 v247, a[180:183], s[28:29] sc1\n"
"v_cvt_pk_bf16_f32 v224, v224, v225\n"
"ds_read_b128 a[184:187], v246 offset:22528\n"
"v_cvt_pk_bf16_f32 v225, v226, v227\n"
"ds_read_b128 a[188:191], v246 offset:23552\n"
"v_cvt_pk_bf16_f32 v226, v228, v229\n"
"v_cvt_pk_bf16_f32 v227, v230, v231\n"
"ds_write_b128 v244, v[224:227] offset:24576\n"
"v_mfma_f32_16x16x32_bf16 v[228:231], v[250:253], a[244:247], 0\n"
"v_mul_f32 v232, v208, v232\n"
"v_mul_f32 v233, v209, v233\n"
"v_mul_f32 v234, v210, v234\n"
"v_mul_f32 v235, v211, v235\n"
"v_mfma_f32_16x16x32_bf16 v[224:227], v[252:255], a[244:247], 0\n"
"v_accvgpr_read_b32 v249, a16\n"
"v_mul_f32_e64 v236, v249, v236\n"
"v_accvgpr_read_b32 v249, a17\n"
"v_mul_f32_e64 v237, v249, v237\n"
"v_accvgpr_read_b32 v249, a18\n"
"v_mul_f32_e64 v238, v249, v238\n"
"v_accvgpr_read_b32 v249, a19\n"
"v_mul_f32_e64 v239, v249, v239\n"
"v_add_u32 v247, 98432, v248\n"
"s_waitcnt lgkmcnt(2)\n"
"global_store_dwordx4 v247, a[184:187], s[28:29] sc1\n"
"v_cvt_pk_bf16_f32 v232, v232, v233\n"
"v_cvt_pk_bf16_f32 v233, v234, v235\n"
"v_cvt_pk_bf16_f32 v234, v236, v237\n"
"v_cvt_pk_bf16_f32 v235, v238, v239\n"
"ds_write_b128 v245, v[232:235] offset:24576\n"
"v_mfma_f32_16x16x32_bf16 v[236:239], v[250:253], a[116:119], 0\n"
"v_mul_f32 v224, v148, v224\n"
"v_mul_f32 v225, v149, v225\n"
"v_mul_f32 v226, v150, v226\n"
"v_mul_f32 v227, v151, v227\n"
"v_mfma_f32_16x16x32_bf16 v[232:235], v[252:255], a[116:119], 0\n"
"v_mul_f32 v228, v180, v228\n"
"v_mul_f32 v229, v181, v229\n"
"v_mul_f32 v230, v182, v230\n"
"v_mul_f32 v231, v183, v231\n"
"v_add_u32 v247, 114816, v248\n"
"s_waitcnt lgkmcnt(2)\n"
"global_store_dwordx4 v247, a[188:191], s[28:29] sc1\n"
"v_cvt_pk_bf16_f32 v224, v224, v225\n"
"ds_read_b128 a[160:163], v246 offset:24576\n"
"v_cvt_pk_bf16_f32 v225, v226, v227\n"
"ds_read_b128 a[164:167], v246 offset:25600\n"
"v_cvt_pk_bf16_f32 v226, v228, v229\n"
"v_cvt_pk_bf16_f32 v227, v230, v231\n"
"ds_write_b128 v244, v[224:227] offset:26624\n"
"v_mfma_f32_16x16x32_bf16 v[228:231], v[250:253], a[248:251], 0\n"
"v_mul_f32 v232, v212, v232\n"
"v_mul_f32 v233, v213, v233\n"
"v_mul_f32 v234, v214, v234\n"
"v_mul_f32 v235, v215, v235\n"
"v_mfma_f32_16x16x32_bf16 v[224:227], v[252:255], a[248:251], 0\n"
"v_accvgpr_read_b32 v249, a20\n"
"v_mul_f32_e64 v236, v249, v236\n"
"v_accvgpr_read_b32 v249, a21\n"
"v_mul_f32_e64 v237, v249, v237\n"
"v_accvgpr_read_b32 v249, a22\n"
"v_mul_f32_e64 v238, v249, v238\n"
"v_accvgpr_read_b32 v249, a23\n"
"v_mul_f32_e64 v239, v249, v239\n"
"v_add_u32 v247, 131200, v248\n"
"s_waitcnt lgkmcnt(2)\n"
"global_store_dwordx4 v247, a[160:163], s[28:29] sc1\n"
"v_cvt_pk_bf16_f32 v232, v232, v233\n"
"v_cvt_pk_bf16_f32 v233, v234, v235\n"
"v_cvt_pk_bf16_f32 v234, v236, v237\n"
"v_cvt_pk_bf16_f32 v235, v238, v239\n"
"ds_write_b128 v245, v[232:235] offset:26624\n"
"v_mfma_f32_16x16x32_bf16 v[236:239], v[250:253], a[120:123], 0\n"
"v_mul_f32 v224, v152, v224\n"
"v_mul_f32 v225, v153, v225\n"
"v_mul_f32 v226, v154, v226\n"
"v_mul_f32 v227, v155, v227\n"
"v_mfma_f32_16x16x32_bf16 v[232:235], v[252:255], a[120:123], 0\n"
"v_mul_f32 v228, v184, v228\n"
"v_mul_f32 v229, v185, v229\n"
"v_mul_f32 v230, v186, v230\n"
"v_mul_f32 v231, v187, v231\n"
"v_add_u32 v247, 147584, v248\n"
"s_waitcnt lgkmcnt(2)\n"
"global_store_dwordx4 v247, a[164:167], s[28:29] sc1\n"
"v_cvt_pk_bf16_f32 v224, v224, v225\n"
"ds_read_b128 a[168:171], v246 offset:26624\n"
"v_cvt_pk_bf16_f32 v225, v226, v227\n"
"ds_read_b128 a[172:175], v246 offset:27648\n"
"v_cvt_pk_bf16_f32 v226, v228, v229\n"
"v_cvt_pk_bf16_f32 v227, v230, v231\n"
"ds_write_b128 v244, v[224:227] offset:28672\n"
"v_mfma_f32_16x16x32_bf16 v[228:231], v[250:253], a[252:255], 0\n"
"v_mul_f32 v232, v216, v232\n"
"v_mul_f32 v233, v217, v233\n"
"v_mul_f32 v234, v218, v234\n"
"v_mul_f32 v235, v219, v235\n"
"v_mfma_f32_16x16x32_bf16 v[224:227], v[252:255], a[252:255], 0\n"
"v_accvgpr_read_b32 v249, a24\n"
"v_mul_f32_e64 v236, v249, v236\n"
"v_accvgpr_read_b32 v249, a25\n"
"v_mul_f32_e64 v237, v249, v237\n"
"v_accvgpr_read_b32 v249, a26\n"
"v_mul_f32_e64 v238, v249, v238\n"
"v_accvgpr_read_b32 v249, a27\n"
"v_mul_f32_e64 v239, v249, v239\n"
"v_add_u32 v247, 163968, v248\n"
"s_waitcnt lgkmcnt(2)\n"
"global_store_dwordx4 v247, a[168:171], s[28:29] sc1\n"
"v_cvt_pk_bf16_f32 v232, v232, v233\n"
"v_cvt_pk_bf16_f32 v233, v234, v235\n"
"v_cvt_pk_bf16_f32 v234, v236, v237\n"
"v_cvt_pk_bf16_f32 v235, v238, v239\n"
"ds_write_b128 v245, v[232:235] offset:28672\n"
"v_mfma_f32_16x16x32_bf16 v[236:239], v[250:253], a[124:127], 0\n"
"v_mul_f32 v224, v156, v224\n"
"v_mul_f32 v225, v157, v225\n"
"v_mul_f32 v226, v158, v226\n"
"v_mul_f32 v227, v159, v227\n"
"v_mfma_f32_16x16x32_bf16 v[232:235], v[252:255], a[124:127], 0\n"
"v_mul_f32 v228, v188, v228\n"
"v_mul_f32 v229, v189, v229\n"
"v_mul_f32 v230, v190, v230\n"
"v_mul_f32 v231, v191, v231\n"
"v_add_u32 v247, 180352, v248\n"
"s_waitcnt lgkmcnt(2)\n"
"global_store_dwordx4 v247, a[172:175], s[28:29] sc1\n"
"v_cvt_pk_bf16_f32 v224, v224, v225\n"
"ds_read_b128 a[176:179], v246 offset:28672\n"
"v_cvt_pk_bf16_f32 v225, v226, v227\n"
"ds_read_b128 a[180:183], v246 offset:29696\n"
"v_cvt_pk_bf16_f32 v226, v228, v229\n"
"v_cvt_pk_bf16_f32 v227, v230, v231\n"
"ds_write_b128 v244, v[224:227] offset:30720\n"
"v_mul_f32 v232, v220, v232\n"
"v_mul_f32 v233, v221, v233\n"
"v_mul_f32 v234, v222, v234\n"
"v_mul_f32 v235, v223, v235\n"
"v_accvgpr_read_b32 v249, a28\n"
"v_mul_f32_e64 v236, v249, v236\n"
"v_accvgpr_read_b32 v249, a29\n"
"v_mul_f32_e64 v237, v249, v237\n"
"v_accvgpr_read_b32 v249, a30\n"
"v_mul_f32_e64 v238, v249, v238\n"
"v_accvgpr_read_b32 v249, a31\n"
"v_mul_f32_e64 v239, v249, v239\n"
"v_add_u32 v247, 196736, v248\n"
"s_waitcnt lgkmcnt(2)\n"
"global_store_dwordx4 v247, a[176:179], s[28:29] sc1\n"
"v_cvt_pk_bf16_f32 v232, v232, v233\n"
"v_cvt_pk_bf16_f32 v233, v234, v235\n"
"v_cvt_pk_bf16_f32 v234, v236, v237\n"
"v_cvt_pk_bf16_f32 v235, v238, v239\n"
"ds_write_b128 v245, v[232:235] offset:30720\n"
"v_add_u32 v247, 213120, v248\n"
"s_waitcnt lgkmcnt(2)\n"
"global_store_dwordx4 v247, a[180:183], s[28:29] sc1\n"
"ds_read_b128 a[184:187], v246 offset:30720\n"
"ds_read_b128 a[188:191], v246 offset:31744\n"
"v_add_u32 v247, 229504, v248\n"
"s_waitcnt lgkmcnt(1)\n"
"global_store_dwordx4 v247, a[184:187], s[28:29] sc1\n"
"v_add_u32 v247, 245888, v248\n"
"s_waitcnt lgkmcnt(0)\n"
"global_store_dwordx4 v247, a[188:191], s[28:29] sc1\n"
"s_branch .Lend_%=\n"
".Lwslow0_%=:\n"
"s_mov_b32 s47, 0x100000\n"
".Lwspin0_%=:\n"
"s_sleep 2\n"
"global_load_dword v235, v228, s[36:37] offset:0 sc1\n"
"s_waitcnt vmcnt(0)\n"
"v_cmp_ne_u32 vcc, v235, v230\n"
"s_nop 1\n"
"s_cmp_eq_u64 vcc, -1\n"
"s_cbranch_scc1 .Lwback0_%=\n"
"s_sub_u32 s47, s47, 1\n"
"s_cmp_lg_u32 s47, 0\n"
"s_cbranch_scc1 .Lwspin0_%=\n"
"s_branch .Lwback0_%=\n"
".Lwslow1_%=:\n"
"s_mov_b32 s47, 0x100000\n"
".Lwspin1_%=:\n"
"s_sleep 2\n"
"global_load_dword v235, v228, s[36:37] offset:4 sc1\n"
"s_waitcnt vmcnt(0)\n"
"v_cmp_ne_u32 vcc, v235, v231\n"
"s_nop 1\n"
"s_cmp_eq_u64 vcc, -1\n"
"s_cbranch_scc1 .Lwback1_%=\n"
"s_sub_u32 s47, s47, 1\n"
"s_cmp_lg_u32 s47, 0\n"
"s_cbranch_scc1 .Lwspin1_%=\n"
"s_branch .Lwback1_%=\n"
".Lslow1_%=:\n"
"s_mov_b32 s47, 0x100000\n"
".Lspin1_%=:\n"
"s_sleep 2\n"
"global_load_dword v239, v250, s[34:35] sc1\n"
"s_waitcnt vmcnt(0)\n"
"v_readfirstlane_b32 s46, v239\n"
"s_nop 3\n"
"s_sub_u32 s46, s46, s44\n"
"s_cmp_ge_u32 s46, 16256\n"
"s_cbranch_scc1 .Lback1_%=\n"
"s_sub_u32 s47, s47, 1\n"
"s_cmp_lg_u32 s47, 0\n"
"s_cbranch_scc1 .Lspin1_%=\n"
"s_branch .Lback1_%=\n"
".Lslow2_%=:\n"
"s_mov_b32 s47, 0x100000\n"
".Lspin2_%=:\n"
"s_sleep 2\n"
"global_load_dword v239, v250, s[34:35] sc1\n"
"s_waitcnt vmcnt(0)\n"
"v_readfirstlane_b32 s46, v239\n"
"s_nop 3\n"
"s_sub_u32 s46, s46, s44\n"
"s_cmp_ge_u32 s46, 32512\n"
"s_cbranch_scc1 .Lback2_%=\n"
"s_sub_u32 s47, s47, 1\n"
"s_cmp_lg_u32 s47, 0\n"
"s_cbranch_scc1 .Lspin2_%=\n"
"s_branch .Lback2_%=\n"
".Lslow3_%=:\n"
"s_mov_b32 s47, 0x100000\n"
".Lspin3_%=:\n"
"s_sleep 2\n"
"global_load_dword v239, v250, s[34:35] sc1\n"
"s_waitcnt vmcnt(0)\n"
"v_readfirstlane_b32 s46, v239\n"
"s_nop 3\n"
"s_sub_u32 s46, s46, s44\n"
"s_cmp_ge_u32 s46, 48768\n"
"s_cbranch_scc1 .Lback3_%=\n"
"s_sub_u32 s47, s47, 1\n"
"s_cmp_lg_u32 s47, 0\n"
"s_cbranch_scc1 .Lspin3_%=\n"
"s_branch .Lback3_%=\n"
".Lslow4_%=:\n"
"s_mov_b32 s47, 0x100000\n"
".Lspin4_%=:\n"
"s_sleep 2\n"
"global_load_dword v239, v250, s[34:35] sc1\n"
"s_waitcnt vmcnt(0)\n"
"v_readfirstlane_b32 s46, v239\n"
"s_nop 3\n"
"s_sub_u32 s46, s46, s44\n"
"s_cmp_ge_u32 s46, 65024\n"
"s_cbranch_scc1 .Lback4_%=\n"
"s_sub_u32 s47, s47, 1\n"
"s_cmp_lg_u32 s47, 0\n"
"s_cbranch_scc1 .Lspin4_%=\n"
"s_branch .Lback4_%=\n"
".Lslow5_%=:\n"
"s_mov_b32 s47, 0x100000\n"
".Lspin5_%=:\n"
"s_sleep 2\n"
"global_load_dword v239, v250, s[34:35] sc1\n"
"s_waitcnt vmcnt(0)\n"
"v_readfirstlane_b32 s46, v239\n"
"s_nop 3\n"
"s_sub_u32 s46, s46, s44\n"
"s_cmp_ge_u32 s46, 81280\n"
"s_cbranch_scc1 .Lback5_%=\n"
"s_sub_u32 s47, s47, 1\n"
"s_cmp_lg_u32 s47, 0\n"
"s_cbranch_scc1 .Lspin5_%=\n"
"s_branch .Lback5_%=\n"
".Lslow6_%=:\n"
"s_mov_b32 s47, 0x100000\n"
".Lspin6_%=:\n"
"s_sleep 2\n"
"global_load_dword v239, v250, s[34:35] sc1\n"
"s_waitcnt vmcnt(0)\n"
"v_readfirstlane_b32 s46, v239\n"
"s_nop 3\n"
"s_sub_u32 s46, s46, s44\n"
"s_cmp_ge_u32 s46, 97536\n"
"s_cbranch_scc1 .Lback6_%=\n"
"s_sub_u32 s47, s47, 1\n"
"s_cmp_lg_u32 s47, 0\n"
"s_cbranch_scc1 .Lspin6_%=\n"
"s_branch .Lback6_%=\n"
".Lslow7_%=:\n"
"s_mov_b32 s47, 0x100000\n"
".Lspin7_%=:\n"
"s_sleep 2\n"
"global_load_dword v239, v250, s[34:35] sc1\n"
"s_waitcnt vmcnt(0)\n"
"v_readfirstlane_b32 s46, v239\n"
"s_nop 3\n"
"s_sub_u32 s46, s46, s44\n"
"s_cmp_ge_u32 s46, 113792\n"
"s_cbranch_scc1 .Lback7_%=\n"
"s_sub_u32 s47, s47, 1\n"
"s_cmp_lg_u32 s47, 0\n"
"s_cbranch_scc1 .Lspin7_%=\n"
"s_branch .Lback7_%=\n"
".Lslow8_%=:\n"
"s_mov_b32 s47, 0x100000\n"
".Lspin8_%=:\n"
"s_sleep 2\n"
"global_load_dword v239, v250, s[34:35] sc1\n"
"s_waitcnt vmcnt(0)\n"
"v_readfirstlane_b32 s46, v239\n"
"s_nop 3\n"
"s_sub_u32 s46, s46, s44\n"
"s_cmp_ge_u32 s46, 130048\n"
"s_cbranch_scc1 .Lback8_%=\n"
"s_sub_u32 s47, s47, 1\n"
"s_cmp_lg_u32 s47, 0\n"
"s_cbranch_scc1 .Lspin8_%=\n"
"s_branch .Lback8_%=\n"
".Lslow9_%=:\n"
"s_mov_b32 s47, 0x100000\n"
".Lspin9_%=:\n"
"s_sleep 2\n"
"global_load_dword v239, v250, s[34:35] sc1\n"
"s_waitcnt vmcnt(0)\n"
"v_readfirstlane_b32 s46, v239\n"
"s_nop 3\n"
"s_sub_u32 s46, s46, s44\n"
"s_cmp_ge_u32 s46, 146304\n"
"s_cbranch_scc1 .Lback9_%=\n"
"s_sub_u32 s47, s47, 1\n"
"s_cmp_lg_u32 s47, 0\n"
"s_cbranch_scc1 .Lspin9_%=\n"
"s_branch .Lback9_%=\n"
".Lslow10_%=:\n"
"s_mov_b32 s47, 0x100000\n"
".Lspin10_%=:\n"
"s_sleep 2\n"
"global_load_dword v239, v250, s[34:35] sc1\n"
"s_waitcnt vmcnt(0)\n"
"v_readfirstlane_b32 s46, v239\n"
"s_nop 3\n"
"s_sub_u32 s46, s46, s44\n"
"s_cmp_ge_u32 s46, 162560\n"
"s_cbranch_scc1 .Lback10_%=\n"
"s_sub_u32 s47, s47, 1\n"
"s_cmp_lg_u32 s47, 0\n"
"s_cbranch_scc1 .Lspin10_%=\n"
"s_branch .Lback10_%=\n"
".Lfin_%=:\n"
".Lend_%=:\n"
:: "s"(ka), "s"(wid), "s"(blockIdx.x)
: "v0", "v1", "v2", "v3", "v4", "v5", "v6", "v7", "v8", "v9", "v10", "v11", "v12", "v13", "v14", "v15", "v16", "v17", "v18", "v19", "v20", "v21", "v22", "v23", "v24", "v25", "v26", "v27", "v28", "v29", "v30", "v31", "v32", "v33", "v34", "v35", "v36", "v37", "v38", "v39", "v40", "v41", "v42", "v43", "v44", "v45", "v46", "v47", "v48", "v49", "v50", "v51", "v52", "v53", "v54", "v55", "v56", "v57", "v58", "v59", "v60", "v61", "v62", "v63", "v64", "v65", "v66", "v67", "v68", "v69", "v70", "v71", "v72", "v73", "v74", "v75", "v76", "v77", "v78", "v79", "v80", "v81", "v82", "v83", "v84", "v85", "v86", "v87", "v88", "v89", "v90", "v91", "v92", "v93", "v94", "v95", "v96", "v97", "v98", "v99", "v100", "v101", "v102", "v103", "v104", "v105", "v106", "v107", "v108", "v109", "v110", "v111", "v112", "v113", "v114", "v115", "v116", "v117", "v118", "v119", "v120", "v121", "v122", "v123", "v124", "v125", "v126", "v127", "v128", "v129", "v130", "v131", "v132", "v133", "v134", "v135", "v136", "v137", "v138", "v139", "v140", "v141", "v142", "v143", "v144", "v145", "v146", "v147", "v148", "v149", "v150", "v151", "v152", "v153", "v154", "v155", "v156", "v157", "v158", "v159", "v160", "v161", "v162", "v163", "v164", "v165", "v166", "v167", "v168", "v169", "v170", "v171", "v172", "v173", "v174", "v175", "v176", "v177", "v178", "v179", "v180", "v181", "v182", "v183", "v184", "v185", "v186", "v187", "v188", "v189", "v190", "v191", "v192", "v193", "v194", "v195", "v196", "v197", "v198", "v199", "v200", "v201", "v202", "v203", "v204", "v205", "v206", "v207", "v208", "v209", "v210", "v211", "v212", "v213", "v214", "v215", "v216", "v217", "v218", "v219", "v220", "v221", "v222", "v223", "v224", "v225", "v226", "v227", "v228", "v229", "v230", "v231", "v232", "v233", "v234", "v235", "v236", "v237", "v238", "v239", "v240", "v241", "v242", "v243", "v244", "v245", "v246", "v247", "v248", "v249", "v250", "v251", "v252", "v253", "v254", "v255", "a0", "a1", "a2", "a3", "a4", "a5", "a6", "a7", "a8", "a9", "a10", "a11", "a12", "a13", "a14", "a15", "a16", "a17", "a18", "a19", "a20", "a21", "a22", "a23", "a24", "a25", "a26", "a27", "a28", "a29", "a30", "a31", "a32", "a33", "a34", "a35", "a36", "a37", "a38", "a39", "a40", "a41", "a42", "a43", "a44", "a45", "a46", "a47", "a48", "a49", "a50", "a51", "a52", "a53", "a54", "a55", "a56", "a57", "a58", "a59", "a60", "a61", "a62", "a63", "a64", "a65", "a66", "a67", "a68", "a69", "a70", "a71", "a72", "a73", "a74", "a75", "a76", "a77", "a78", "a79", "a80", "a81", "a82", "a83", "a84", "a85", "a86", "a87", "a88", "a89", "a90", "a91", "a92", "a93", "a94", "a95", "a96", "a97", "a98", "a99", "a100", "a101", "a102", "a103", "a104", "a105", "a106", "a107", "a108", "a109", "a110", "a111", "a112", "a113", "a114", "a115", "a116", "a117", "a118", "a119", "a120", "a121", "a122", "a123", "a124", "a125", "a126", "a127", "a128", "a129", "a130", "a131", "a132", "a133", "a134", "a135", "a136", "a137", "a138", "a139", "a140", "a141", "a142", "a143", "a144", "a145", "a146", "a147", "a148", "a149", "a150", "a151", "a152", "a153", "a154", "a155", "a156", "a157", "a158", "a159", "a160", "a161", "a162", "a163", "a164", "a165", "a166", "a167", "a168", "a169", "a170", "a171", "a172", "a173", "a174", "a175", "a176", "a177", "a178", "a179", "a180", "a181", "a182", "a183", "a184", "a185", "a186", "a187", "a188", "a189", "a190", "a191", "a192", "a193", "a194", "a195", "a196", "a197", "a198", "a199", "a200", "a201", "a202", "a203", "a204", "a205", "a206", "a207", "a208", "a209", "a210", "a211", "a212", "a213", "a214", "a215", "a216", "a217", "a218", "a219", "a220", "a221", "a222", "a223", "a224", "a225", "a226", "a227", "a228", "a229", "a230", "a231", "a232", "a233", "a234", "a235", "a236", "a237", "a238", "a239", "a240", "a241", "a242", "a243", "a244", "a245", "a246", "a247", "a248", "a249", "a250", "a251", "a252", "a253", "a254", "a255", "s4", "s5", "s6", "s7", "s8", "s9", "s10", "s11", "s12", "s13", "s14", "s15", "s16", "s17", "s18", "s19", "s20", "s21", "s22", "s23", "s24", "s25", "s26", "s27", "s28", "s29", "s30", "s31", "s34", "s35", "s36", "s37", "s38", "s39", "s40", "s41", "s42", "s43", "s44", "s45", "s46", "s47", "s48", "s49", "s50", "s51", "s52", "s53", "s54", "s55", "s56", "s57", "s58", "s59", "s60", "s61", "s62", "s63", "s64", "s65", "s66", "s67", "s68", "s69", "s70", "s71", "s72", "s73", "vcc", "scc", "memory");
}

extern "C" void kernel_launch(void* const* d_in, const int* in_sizes, int n_in, void* d_out, int out_size, void* d_ws, size_t ws_size, hipStream_t stream) {
  k_dcn<<<256, 256, 0, stream>>>(d_in[0], d_in[1], d_in[2], d_out, d_ws);
}
```
